```python
import torch

def _softmax(x, axis=-1):
    return torch.softmax(x, dim=axis)
def _gelu(x):
    return torch.nn.functional.gelu(x, approximate="tanh")
def _silu(x):
    return torch.nn.functional.silu(x)
def _sigmoid(x):
    return torch.sigmoid(x)
def _layernorm(x, axis=-1, eps=1e-5):
    m = x.mean(dim=axis, keepdim=True)
    v = x.var(dim=axis, keepdim=True, unbiased=False)
    return (x - m) / torch.sqrt(v + eps)
def _scatter_add(tgt, idx, src, axis=0):
    return tgt.clone().index_add_(axis, idx, src)
def _segment_sum(src, seg, n):
    out = torch.zeros((n,) + src.shape[1:], dtype=src.dtype)
    return out.index_add_(0, seg, src)
def _pad(x, pw):
    flat = tuple(v for pair in reversed(pw) for v in pair)
    return torch.nn.functional.pad(x, flat)

kernel_name = 'standalone__mha_block_cached__near_ridge'

def setup_inputs():

    _w = (lambda a: (lambda t: t.to(torch.bfloat16) if t.is_floating_point() else t)(torch.as_tensor(a)))
    B, Sq, D = 16, 160, 640
    H, Sk, Dh = 32, 512, 128
    return {
        "x": _w((lambda *s: torch.randn(*s, dtype=torch.bfloat16) * 0.8)(B, Sq, D)),
        "k_cache": _w((lambda *s: torch.randn(*s, dtype=torch.bfloat16) * 0.8)(B, H, Sk, Dh)),
        "v_cache": _w((lambda *s: torch.randn(*s, dtype=torch.bfloat16) * 0.05)(B, H, Sk, Dh)),
        "wq": _w((lambda *s: torch.randn(*s, dtype=torch.bfloat16) * 0.8)(D, H * Dh) * (D ** -0.5)),
        "wo": _w((lambda *s: torch.randn(*s, dtype=torch.bfloat16) * 0.05)(H * Dh, D) * ((H * Dh) ** -0.5)),
    }

def reference(x, k_cache, v_cache, wq, wo):
    B, Sq, D = x.shape
    H, Dh = 32, 128
    q = (lambda x, *d: x.permute(*d))((x @ wq).reshape(B, Sq, H, Dh), 0, 2, 1, 3)
    scores = torch.einsum("bhqd,bhkd->bhqk", q, k_cache) * (Dh ** -0.5)
    p = _softmax(scores, axis=-1)
    o = torch.einsum("bhqk,bhkd->bhqd", p, v_cache)
    return (lambda x, *d: x.permute(*d))(o, 0, 2, 1, 3).reshape(B, Sq, H * Dh) @ wo
```

```cpp
#include <hip/hip_runtime.h>
#include <hip/hip_bf16.h>
typedef unsigned short u16;
constexpr int B=16,SQ=160,D=640,H=32,SK=512,DH=128,HD=H*DH;
__device__ __forceinline__ float bf2f(u16 v){return __uint_as_float(((unsigned)v)<<16);}
__device__ __forceinline__ u16 f2bf(float f){unsigned u=__float_as_uint(f);u+=0x7fffu+((u>>16)&1u);return (u16)(u>>16);}
__global__ void nq(const u16* x,const u16* wq,u16* q){
  int n=blockIdx.x*256+threadIdx.x; int m=blockIdx.y;
  float a=0; for(int k=0;k<D;++k) a+=bf2f(x[m*D+k])*bf2f(wq[k*HD+n]);
  q[(size_t)m*HD+n]=f2bf(a);
}
__global__ void na(const u16* q,const u16* K,const u16* V,u16* o){
  int r=blockIdx.x, h=blockIdx.y, b=blockIdx.z, t=threadIdx.x;
  __shared__ float qs[DH], s[SK], red[128];
  qs[t]=bf2f(q[((size_t)b*SQ+r)*HD+h*DH+t]); __syncthreads();
  const u16* Kb=K+((size_t)b*H+h)*SK*DH; const u16* Vb=V+((size_t)b*H+h)*SK*DH;
  float mx=-1e30f;
  for(int j=t;j<SK;j+=128){float a=0;for(int d=0;d<DH;++d)a+=qs[d]*bf2f(Kb[j*DH+d]);
    a=bf2f(f2bf(a)); a=bf2f(f2bf(a*0.08838834764831845f)); s[j]=a; mx=fmaxf(mx,a);}
  red[t]=mx; __syncthreads();
  for(int o2=64;o2>0;o2>>=1){if(t<o2)red[t]=fmaxf(red[t],red[t+o2]);__syncthreads();}
  mx=red[0]; __syncthreads();
  float sm=0; for(int j=t;j<SK;j+=128){float e=expf(s[j]-mx); s[j]=e; sm+=e;}
  red[t]=sm; __syncthreads();
  for(int o2=64;o2>0;o2>>=1){if(t<o2)red[t]+=red[t+o2];__syncthreads();}
  sm=red[0];
  float a=0; for(int j=0;j<SK;++j) a+=bf2f(f2bf(s[j]/sm))*bf2f(Vb[j*DH+t]);
  o[((size_t)b*SQ+r)*HD+h*DH+t]=f2bf(a);
}
__global__ void no(const u16* o,const u16* wo,u16* out){
  int n=blockIdx.x*128+threadIdx.x; int m=blockIdx.y;
  float a=0; for(int k=0;k<HD;++k) a+=bf2f(o[(size_t)m*HD+k])*bf2f(wo[k*D+n]);
  out[m*D+n]=f2bf(a);
}
extern "C" void kernel_launch(void* const* d_in, const int* in_sizes, int n_in, void* d_out, int out_size, void* d_ws, size_t ws_size, hipStream_t stream){
  u16* q=(u16*)d_ws; u16* o=q+(size_t)B*SQ*HD;
  nq<<<dim3(HD/256,B*SQ),256,0,stream>>>((const u16*)d_in[0],(const u16*)d_in[3],q);
  na<<<dim3(SQ,H,B),128,0,stream>>>(q,(const u16*)d_in[1],(const u16*)d_in[2],o);
  no<<<dim3(D/128,B*SQ),128,0,stream>>>(o,(const u16*)d_in[4],(u16*)d_out);
}
```

```cpp
#include <hip/hip_runtime.h>
#include <hip/hip_bf16.h>

typedef unsigned short u16;
typedef unsigned u32x4 __attribute__((ext_vector_type(4)));
constexpr int SQ = 160, DM = 640, NH = 32, SK = 512, DH = 128, HD = NH * DH;

__device__ __forceinline__ float bf2f(u16 v) { return __uint_as_float(((unsigned)v) << 16); }
__device__ __forceinline__ unsigned pk2(float lo, float hi) {
  typedef float f2 __attribute__((ext_vector_type(2))); typedef __bf16 b2 __attribute__((ext_vector_type(2)));
  f2 v = {lo, hi}; b2 b = __builtin_convertvector(v, b2); return __builtin_bit_cast(unsigned, b);
}
__device__ __forceinline__ float rbf(float f) { return __uint_as_float(pk2(f, 0.f) << 16); }

__device__ __forceinline__ void slow_item(const u16* x, const u16* K, const u16* V, const u16* wq, char* ws, int b, int h, float* sm, const int t) {
  float* qs = sm; float* sc = sm + 128; float* red = sm + 640; float* os = sm + 896;
  const u16* Kb = K + ((size_t)b * NH + h) * SK * DH; const u16* Vb = V + ((size_t)b * NH + h) * SK * DH;
  __amdgpu_buffer_rsrc_t ro = __builtin_amdgcn_make_buffer_rsrc((void*)(ws + (size_t)b * 1310720), (short)0, 1310720, 0x00020000);
  for (int r = 0; r < SQ; ++r) {
    __syncthreads();
    if (t < DH) { float a = 0.f; for (int k = 0; k < DM; ++k) a += bf2f(x[((size_t)b * SQ + r) * DM + k]) * bf2f(wq[(size_t)k * HD + h * DH + t]); qs[t] = rbf(a); }
    __syncthreads();
    float mx = -__builtin_inff();
    for (int j = t; j < SK; j += 256) { float a = 0.f; for (int d = 0; d < DH; ++d) a += qs[d] * bf2f(Kb[j * DH + d]); a = rbf(rbf(a) * 0.08838834764831845f); sc[j] = a; mx = (a != a) ? a : fmaxf(mx, a); if (mx != mx) break; }
    red[t] = mx; __syncthreads();
    mx = red[0]; for (int i = 1; i < 256; ++i) { const float v = red[i]; mx = (v != v || mx != mx) ? __builtin_nanf("") : fmaxf(mx, v); }
    __syncthreads();
    float s = 0.f; for (int j = t; j < SK; j += 256) { const float e = expf(sc[j] - mx); sc[j] = e; s += e; }
    red[t] = s; __syncthreads();
    s = 0.f; for (int i = 0; i < 256; ++i) s += red[i];
    if (t < DH) { float a = 0.f; for (int j = 0; j < SK; ++j) a += rbf(sc[j] / s) * bf2f(Vb[j * DH + t]); os[t] = a; }
    __syncthreads();
    if (t < 16) { const int sq = t >> 2, g = t & 3; const float* o = os + 32 * sq + 4 * g;
      const u32x4 w = {pk2(o[0], o[1]), pk2(o[2], o[3]), pk2(o[16], o[17]), pk2(o[18], o[19])};
      __builtin_amdgcn_raw_buffer_store_b128(w, ro, (unsigned)((h * 4 + sq) * 10240 + (r >> 4) * 1024 + ((r & 15) + 16 * g) * 16), 0, 16  ); }
  }
  asm volatile("s_waitcnt vmcnt(0)" ::: "memory");
  __syncthreads();
}

__global__ __launch_bounds__(256, 1) void mha_fused(const u16*, const u16*, const u16*, const u16*, const u16*, char*, u16*) {
  __shared__ __attribute__((aligned(16))) char lds[163840];
  const unsigned wid = __builtin_amdgcn_readfirstlane(threadIdx.x >> 6);
  typedef const __attribute__((address_space(4))) unsigned long long* kptr;
  const kptr ka = (kptr)__builtin_amdgcn_kernarg_segment_ptr();
  unsigned flag, t0, t1;
  asm volatile(
    "LCODE0_%=:\n"
    "s_load_dwordx8 s[20:27], %[ka], 0x0\n"
    "s_load_dwordx4 s[28:31], %[ka], 0x20\n"
    "s_load_dwordx2 s[32:33], %[ka], 0x30\n"
    "v_mbcnt_lo_u32_b32 v0, -1, 0\n"
    "v_mbcnt_hi_u32_b32 v0, -1, v0\n"
    "s_mov_b32 s72, %[wid]\n"
    "s_bfe_u32 s74, %[blk], 0x10000\n"
    "s_lshl_b32 s74, s74, 3\n"
    "s_bfe_u32 s75, %[blk], 0x30003\n"
    "s_add_u32 s89, s74, s75\n"
    "s_bfe_u32 s74, %[blk], 0x20001\n"
    "s_lshl_b32 s75, s74, 2\n"
    "s_bfe_u32 s76, %[blk], 0x20006\n"
    "s_add_u32 s90, s75, s76\n"
    "s_lshl_b32 s73, s72, 12\n"
    "v_bfe_u32 v44, v0, 2, 3\n"
    "v_lshrrev_b32 v45, 5, v0\n"
    "v_lshlrev_b32 v45, 6, v45\n"
    "v_lshl_add_u32 v44, v44, 13, v45\n"
    "v_and_b32 v45, 3, v0\n"
    "v_lshl_add_u32 v44, v45, 4, v44\n"
    "v_add_u32 v32, 0xc00, v44\n"
    "v_add_u32 v33, 0x880, v44\n"
    "v_add_u32 v34, 0x500, v44\n"
    "v_add_u32 v35, 0x180, v44\n"
    "v_lshrrev_b32 v44, 3, v0\n"
    "v_mul_u32_u24 v44, 0x500, v44\n"
    "v_and_b32 v45, 7, v0\n"
    "v_lshrrev_b32 v46, 4, v0\n"
    "v_add_u32 v47, 0, v46\n"
    "v_xor_b32 v47, v47, v45\n"
    "v_lshl_add_u32 v47, v47, 4, v44\n"
    "v_add_u32 v36, 0, v47\n"
    "v_add_u32 v47, 4, v46\n"
    "v_xor_b32 v47, v47, v45\n"
    "v_lshl_add_u32 v47, v47, 4, v44\n"
    "v_add_u32 v37, 0x2400, v47\n"
    "v_add_u32 v47, 0, v46\n"
    "v_xor_b32 v47, v47, v45\n"
    "v_lshl_add_u32 v47, v47, 4, v44\n"
    "v_add_u32 v38, 0x4800, v47\n"
    "v_add_u32 v47, 4, v46\n"
    "v_xor_b32 v47, v47, v45\n"
    "v_lshl_add_u32 v47, v47, 4, v44\n"
    "v_add_u32 v39, 0x6c00, v47\n"
    "s_lshl_b32 s74, s90, 9\n"
    "s_mul_i32 s75, s89, 0x32000\n"
    "s_waitcnt lgkmcnt(0)\n"
    "s_load_dword s96, s[26:27], 0x0\n"
    "s_load_dword s97, s[20:21], 0x0\n"
    "s_mov_b64 s[40:41], s[26:27]\n"
    "s_add_u32 s40, s40, s74\n"
    "s_addc_u32 s41, s41, 0\n"
    "s_sub_u32 s40, s40, 0xc00\n"
    "s_subb_u32 s41, s41, 0\n"
    "s_sub_u32 s42, 0x500c00, s74\n"
    "s_mov_b32 s43, 0x20000\n"
    "s_mov_b64 s[36:37], s[20:21]\n"
    "s_add_u32 s36, s36, s75\n"
    "s_addc_u32 s37, s37, 0\n"
    "s_mov_b32 s38, 0x32000\n"
    "s_mov_b32 s39, 0x20000\n"
    "s_cmp_lt_u32 s72, 2\n"
    "s_cbranch_scc1 LPW_%=\n"
    "s_cmp_eq_u32 s72, 3\n"
    "s_cbranch_scc1 LPKV_%=\n"
    "s_mov_b32 s79, 0x1c000\n"
    "s_mov_b32 s60, 0\n"
    "s_mov_b32 s61, 0x3d000\n"
    "s_add_u32 m0, s79, 0\n"
    "s_add_u32 s76, s60, 0\n"
    "buffer_load_dwordx4 v36, s[36:39], s76 offen lds\n"
    "buffer_load_dwordx4 v37, s[36:39], s76 offen offset:1024 lds\n"
    "buffer_load_dwordx4 v38, s[36:39], s76 offen offset:2048 lds\n"
    "buffer_load_dwordx4 v39, s[36:39], s76 offen offset:3072 lds\n"
    "s_add_u32 m0, s79, 0x1000\n"
    "s_add_u32 s76, s60, 0xa000\n"
    "buffer_load_dwordx4 v36, s[36:39], s76 offen lds\n"
    "buffer_load_dwordx4 v37, s[36:39], s76 offen offset:1024 lds\n"
    "buffer_load_dwordx4 v38, s[36:39], s76 offen offset:2048 lds\n"
    "buffer_load_dwordx4 v39, s[36:39], s76 offen offset:3072 lds\n"
    "s_add_u32 m0, s79, 0x2000\n"
    "s_add_u32 s76, s60, 0x14000\n"
    "buffer_load_dwordx4 v36, s[36:39], s76 offen lds\n"
    "buffer_load_dwordx4 v37, s[36:39], s76 offen offset:1024 lds\n"
    "buffer_load_dwordx4 v38, s[36:39], s76 offen offset:2048 lds\n"
    "buffer_load_dwordx4 v39, s[36:39], s76 offen offset:3072 lds\n"
    "s_branch LC1_%=\n"
    "LPKV_%=:\n"
    "s_getpc_b64 s[94:95]\n"
    "LPC_%=:\n"
    "s_sub_u32 s94, s94, LPC_%=-LCODE0_%=\n"
    "s_subb_u32 s95, s95, 0\n"
    "s_lshr_b32 s74, %[blk], 3\n"
    "s_mul_i32 s74, s74, 0xa00\n"
    "v_min_u32 v44, 23, v0\n"
    "v_lshl_add_u32 v44, v44, 7, s74\n"
    "v_min_u32 v44, LCODE1_%=-LCODE0_%=-4, v44\n"
    "global_load_dword a136, v44, s[94:95]\n"
    "v_lshlrev_b32 v57, 7, v0\n"
    "v_lshrrev_b32 v58, 2, v0\n"
    "v_and_b32 v59, 3, v0\n"
    "v_lshlrev_b32 v58, 13, v58\n"
    "v_lshl_add_u32 v58, v59, 7, v58\n"
    "s_mul_i32 s74, s89, 0x50000\n"
    "s_lshl_b32 s75, s90, 9\n"
    "s_add_u32 s74, s74, s75\n"
    "s_mov_b64 s[64:65], s[26:27]\n"
    "s_add_u32 s64, s64, s74\n"
    "s_addc_u32 s65, s65, 0\n"
    "s_mov_b32 s66, 0x4e200\n"
    "s_mov_b32 s67, 0x20000\n"
    "s_mov_b32 s75, 0\n"
    "buffer_load_dword a136, v58, s[64:67], s75 offen\n"
    "s_mov_b32 s75, 0x20000\n"
    "buffer_load_dword a136, v58, s[64:67], s75 offen\n"
    "s_mov_b32 s75, 0x40000\n"
    "buffer_load_dword a136, v58, s[64:67], s75 offen\n"
    "s_mul_i32 s74, s89, 0x32000\n"
    "s_mul_i32 s75, s90, 0x3200\n"
    "s_add_u32 s74, s74, s75\n"
    "s_mov_b64 s[64:65], s[20:21]\n"
    "s_add_u32 s64, s64, s74\n"
    "s_addc_u32 s65, s65, 0\n"
    "s_mov_b32 s66, 0x3200\n"
    "s_mov_b32 s67, 0x20000\n"
    "s_mov_b32 s75, 0\n"
    "buffer_load_dword a136, v57, s[64:67], s75 offen\n"
    "s_mov_b32 s75, 0x2000\n"
    "buffer_load_dword a136, v57, s[64:67], s75 offen\n"
    "s_branch LC1_%=\n"
    "LPW_%=:\n"
    "s_lshl_b32 s78, s72, 16\n"
    "s_sub_u32 s78, s78, 0x20000\n"
    "s_add_u32 m0, s73, 0x14000\n"
    "s_add_u32 s78, s78, 0x20000\n"
    "buffer_load_dwordx4 v32, s[40:43], s78 offen lds\n"
    "buffer_load_dwordx4 v33, s[40:43], s78 offen offset:1024 lds\n"
    "buffer_load_dwordx4 v34, s[40:43], s78 offen offset:2048 lds\n"
    "buffer_load_dwordx4 v35, s[40:43], s78 offen offset:3072 lds\n"
    "s_add_u32 m0, s73, 0x16000\n"
    "s_add_u32 s78, s78, 0x20000\n"
    "buffer_load_dwordx4 v32, s[40:43], s78 offen lds\n"
    "buffer_load_dwordx4 v33, s[40:43], s78 offen offset:1024 lds\n"
    "buffer_load_dwordx4 v34, s[40:43], s78 offen offset:2048 lds\n"
    "buffer_load_dwordx4 v35, s[40:43], s78 offen offset:3072 lds\n"
    "s_add_u32 s79, s73, 0x1c000\n"
    "s_mul_i32 s60, s72, 0xa000\n"
    "s_lshl_b32 s61, s73, 1\n"
    "s_add_u32 s61, s61, 0x3d000\n"
    "s_add_u32 m0, s79, 0x3000\n"
    "s_add_u32 s76, s60, 0x1e000\n"
    "buffer_load_dwordx4 v36, s[36:39], s76 offen lds\n"
    "buffer_load_dwordx4 v37, s[36:39], s76 offen offset:1024 lds\n"
    "buffer_load_dwordx4 v38, s[36:39], s76 offen offset:2048 lds\n"
    "buffer_load_dwordx4 v39, s[36:39], s76 offen offset:3072 lds\n"
    "s_add_u32 m0, s73, 0x18000\n"
    "s_add_u32 s78, s78, 0x20000\n"
    "buffer_load_dwordx4 v32, s[40:43], s78 offen lds\n"
    "buffer_load_dwordx4 v33, s[40:43], s78 offen offset:1024 lds\n"
    "buffer_load_dwordx4 v34, s[40:43], s78 offen offset:2048 lds\n"
    "buffer_load_dwordx4 v35, s[40:43], s78 offen offset:3072 lds\n"
    "s_add_u32 m0, s73, 0x1a000\n"
    "s_add_u32 s78, s78, 0x20000\n"
    "buffer_load_dwordx4 v32, s[40:43], s78 offen lds\n"
    "buffer_load_dwordx4 v33, s[40:43], s78 offen offset:1024 lds\n"
    "buffer_load_dwordx4 v34, s[40:43], s78 offen offset:2048 lds\n"
    "buffer_load_dwordx4 v35, s[40:43], s78 offen offset:3072 lds\n"
    "LC1_%=:\n"
    "v_accvgpr_write_b32 a184, 0\n"
    "v_accvgpr_write_b32 a185, 0\n"
    "v_accvgpr_write_b32 a186, 0\n"
    "v_accvgpr_write_b32 a187, 0\n"
    "s_nop 1\n"
    "v_mfma_f32_32x32x16_bf16 v[96:111], a[184:187], a[184:187], 0\n"
    "v_accvgpr_write_b32 a188, 0\n"
    "v_accvgpr_write_b32 a189, 0\n"
    "v_accvgpr_write_b32 a190, 0\n"
    "v_accvgpr_write_b32 a191, 0\n"
    "v_accvgpr_write_b32 a228, 0\n"
    "v_accvgpr_write_b32 a229, 0\n"
    "v_accvgpr_write_b32 a230, 0\n"
    "v_accvgpr_write_b32 a231, 0\n"
    "v_mfma_f32_32x32x16_bf16 v[112:127], a[184:187], a[184:187], 0\n"
    "v_accvgpr_write_b32 a232, 0\n"
    "v_accvgpr_write_b32 a233, 0\n"
    "v_accvgpr_write_b32 a234, 0\n"
    "v_accvgpr_write_b32 a235, 0\n"
    "v_accvgpr_write_b32 a236, 0\n"
    "v_accvgpr_write_b32 a237, 0\n"
    "v_accvgpr_write_b32 a238, 0\n"
    "v_accvgpr_write_b32 a239, 0\n"
    "v_mfma_f32_32x32x16_bf16 v[128:143], a[184:187], a[184:187], 0\n"
    "v_accvgpr_write_b32 a240, 0\n"
    "v_accvgpr_write_b32 a241, 0\n"
    "v_accvgpr_write_b32 a242, 0\n"
    "v_accvgpr_write_b32 a243, 0\n"
    "v_accvgpr_write_b32 a244, 0\n"
    "v_accvgpr_write_b32 a245, 0\n"
    "v_accvgpr_write_b32 a246, 0\n"
    "v_accvgpr_write_b32 a247, 0\n"
    "v_mfma_f32_32x32x16_bf16 v[144:159], a[184:187], a[184:187], 0\n"
    "v_and_b32 v44, 15, v0\n"
    "v_lshrrev_b32 v45, 4, v0\n"
    "s_and_b32 s76, s72, 1\n"
    "s_lshr_b32 s77, s72, 1\n"
    "s_mul_i32 s74, s76, 0x2800\n"
    "s_add_u32 s74, s74, 0x1c000\n"
    "v_lshl_add_u32 v46, v44, 7, s74\n"
    "v_lshrrev_b32 v47, 1, v44\n"
    "v_mfma_f32_32x32x16_bf16 v[160:175], a[184:187], a[184:187], 0\n"
    "v_xor_b32 v47, v47, v45\n"
    "v_lshl_add_u32 v28, v47, 4, v46\n"
    "v_xor_b32 v47, 4, v47\n"
    "v_lshl_add_u32 v29, v47, 4, v46\n"
    "v_bfe_u32 v49, v0, 2, 2\n"
    "s_lshl_b32 s74, s77, 11\n"
    "s_add_u32 s74, s74, 0x10000\n"
    "v_lshl_add_u32 v46, v45, 12, s74\n"
    "v_mfma_f32_32x32x16_bf16 v[176:191], a[184:187], a[184:187], 0\n"
    "v_lshl_add_u32 v46, v49, 6, v46\n"
    "v_and_b32 v47, 3, v0\n"
    "v_lshl_add_u32 v30, v47, 4, v46\n"
    "s_lshl_b32 s88, s77, 14\n"
    "s_lshl_b32 s74, s89, 4\n"
    "s_add_u32 s74, s74, s90\n"
    "s_lshl_b32 s74, s74, 18\n"
    "s_mov_b64 s[44:45], s[22:23]\n"
    "v_mfma_f32_32x32x16_bf16 v[192:207], a[184:187], a[184:187], 0\n"
    "s_add_u32 s44, s44, s74\n"
    "s_addc_u32 s45, s45, 0\n"
    "s_mov_b32 s46, 0x40000\n"
    "s_mov_b32 s47, 0x20000\n"
    "s_mov_b64 s[48:49], s[24:25]\n"
    "s_add_u32 s48, s48, s74\n"
    "s_addc_u32 s49, s49, 0\n"
    "s_sub_u32 s48, s48, 0x400\n"
    "s_subb_u32 s49, s49, 0\n"
    "s_mov_b32 s50, 0x40400\n"
    "s_mov_b32 s51, 0x20000\n"
    "v_lshrrev_b32 v44, 4, v0\n"
    "v_and_b32 v45, 15, v0\n"
    "v_add_u32 v46, 0, v44\n"
    "v_xor_b32 v46, v46, v45\n"
    "v_lshlrev_b32 v46, 4, v46\n"
    "v_mfma_f32_32x32x16_bf16 v[208:223], a[184:187], a[184:187], 0\n"
    "v_lshl_add_u32 v8, v44, 8, v46\n"
    "v_add_u32 v46, 4, v44\n"
    "v_xor_b32 v46, v46, v45\n"
    "v_lshlrev_b32 v46, 4, v46\n"
    "v_lshl_add_u32 v9, v44, 8, v46\n"
    "v_add_u32 v46, 8, v44\n"
    "v_xor_b32 v46, v46, v45\n"
    "v_lshlrev_b32 v46, 4, v46\n"
    "v_mfma_f32_32x32x16_bf16 v[224:239], a[184:187], a[184:187], 0\n"
    "v_lshl_add_u32 v10, v44, 8, v46\n"
    "v_add_u32 v46, 12, v44\n"
    "v_xor_b32 v46, v46, v45\n"
    "v_lshlrev_b32 v46, 4, v46\n"
    "v_lshl_add_u32 v11, v44, 8, v46\n"
    "v_bfe_u32 v44, v0, 2, 3\n"
    "v_lshrrev_b32 v45, 5, v0\n"
    "v_lshlrev_b32 v45, 6, v45\n"
    "v_mfma_f32_32x32x16_bf16 v[240:255], a[184:187], a[184:187], 0\n"
    "v_lshl_add_u32 v44, v44, 8, v45\n"
    "v_bfe_u32 v45, v0, 4, 1\n"
    "v_lshlrev_b32 v45, 1, v45\n"
    "v_and_b32 v46, 3, v0\n"
    "v_xor_b32 v45, v45, v46\n"
    "v_lshl_add_u32 v44, v45, 4, v44\n"
    "v_add_u32 v12, 0x400, v44\n"
    "v_add_u32 v13, 0x80, v44\n"
    "s_mul_i32 s74, s89, 0x140000\n"
    "s_mov_b64 s[52:53], s[30:31]\n"
    "s_add_u32 s52, s52, s74\n"
    "s_addc_u32 s53, s53, 0\n"
    "s_mov_b32 s54, 0x140000\n"
    "s_mov_b32 s55, 0x20000\n"
    "s_mov_b64 s[68:69], s[30:31]\n"
    "s_add_u32 s68, s68, 0x3400000\n"
    "s_addc_u32 s69, s69, 0\n"
    "s_mov_b32 s70, 0x40000\n"
    "s_mov_b32 s71, 0x20000\n"
    "s_mov_b32 s82, 0x3e0293ee\n"
    "s_mov_b32 s83, 0x3e0293ee\n"
    "s_mov_b32 s84, 0x4b800000\n"
    "s_lshl_b32 s74, s90, 1\n"
    "s_lshr_b32 s75, s72, 1\n"
    "s_add_u32 s74, s74, s75\n"
    "s_mul_i32 s93, s74, 0xa000\n"
    "s_and_b32 s75, s72, 1\n"
    "s_mul_i32 s75, s75, 0x1400\n"
    "s_add_u32 s93, s93, s75\n"
    "s_lshr_b32 s74, s89, 3\n"
    "s_and_b32 s75, s89, 7\n"
    "s_lshl_b32 s75, s75, 3\n"
    "s_add_u32 s74, s74, s75\n"
    "v_bfe_u32 v44, v0, 2, 2\n"
    "v_lshlrev_b32 v44, 1, v44\n"
    "v_and_b32 v45, 3, v0\n"
    "v_lshl_add_u32 v44, v45, 6, v44\n"
    "v_add_u32 v44, s74, v44\n"
    "v_lshlrev_b32 v14, 10, v44\n"
    "v_lshlrev_b32 v1, 4, v0\n"
    "v_mov_b32 v15, 0\n"
    "buffer_load_dwordx2 v[16:17], v14, s[68:71], 0 offen sc1\n"
    "s_mov_b32 s80, 0\n"
    "s_mov_b32 s81, 0x5000\n"
    "s_waitcnt vmcnt(1) lgkmcnt(0)\n"
    "s_barrier\n"
    "s_cmp_eq_u32 s72, 2\n"
    "s_cbranch_scc1 LRX_%=\n"
    "s_cmp_eq_u32 s72, 3\n"
    "s_cbranch_scc1 LRKV_%=\n"
    "LQW_%=:\n"
    "v_mfma_f32_16x16x32_bf16 v[216:219], a[184:187], a[228:231], v[216:219]\n"
    "s_add_u32 s60, s60, 0x80\n"
    "s_sub_u32 s79, s61, s79\n"
    "ds_read_b128 a[208:211], v28\n"
    "v_mfma_f32_16x16x32_bf16 v[220:223], a[184:187], a[232:235], v[220:223]\n"
    "s_add_u32 m0, s73, 0x10000\n"
    "s_add_u32 s78, s78, 0x20000\n"
    "ds_read_b64_tr_b16 a[144:145], v30 offset:16384\n"
    "v_mfma_f32_16x16x32_bf16 v[224:227], a[184:187], a[236:239], v[224:227]\n"
    "ds_read_b64_tr_b16 a[146:147], v30 offset:16640\n"
    "ds_read_b128 a[212:215], v28 offset:2048\n"
    "v_mfma_f32_16x16x32_bf16 v[228:231], a[184:187], a[240:243], v[228:231]\n"
    "ds_read_b128 a[216:219], v28 offset:4096\n"
    "v_mfma_f32_16x16x32_bf16 v[232:235], a[184:187], a[244:247], v[232:235]\n"
    "ds_read_b128 a[220:223], v28 offset:6144\n"
    "v_mfma_f32_16x16x32_bf16 v[236:239], a[188:191], a[228:231], v[236:239]\n"
    "ds_read_b128 a[224:227], v28 offset:8192\n"
    "ds_read_b64_tr_b16 a[148:149], v30 offset:16392\n"
    "v_add_u32 v28, s81, v28\n"
    "v_mfma_f32_16x16x32_bf16 v[240:243], a[188:191], a[232:235], v[240:243]\n"
    "ds_read_b64_tr_b16 a[150:151], v30 offset:16648\n"
    "ds_read_b64_tr_b16 a[152:153], v30 offset:16896\n"
    "v_mfma_f32_16x16x32_bf16 v[244:247], a[188:191], a[236:239], v[244:247]\n"
    "buffer_load_dwordx4 v32, s[40:43], s78 offen lds\n"
    "v_mfma_f32_16x16x32_bf16 v[248:251], a[188:191], a[240:243], v[248:251]\n"
    "ds_read_b64_tr_b16 a[154:155], v30 offset:17152\n"
    "v_mfma_f32_16x16x32_bf16 v[252:255], a[188:191], a[244:247], v[252:255]\n"
    "s_waitcnt lgkmcnt(5)\n"
    "v_mfma_f32_16x16x32_bf16 v[96:99], a[144:147], a[208:211], v[96:99]\n"
    "buffer_load_dwordx4 v33, s[40:43], s78 offen offset:1024 lds\n"
    "v_mfma_f32_16x16x32_bf16 v[100:103], a[144:147], a[212:215], v[100:103]\n"
    "buffer_load_dwordx4 v34, s[40:43], s78 offen offset:2048 lds\n"
    "v_mfma_f32_16x16x32_bf16 v[104:107], a[144:147], a[216:219], v[104:107]\n"
    "v_mfma_f32_16x16x32_bf16 v[108:111], a[144:147], a[220:223], v[108:111]\n"
    "s_waitcnt lgkmcnt(0)\n"
    "ds_read_b64_tr_b16 a[156:157], v30 offset:16904\n"
    "ds_read_b64_tr_b16 a[158:159], v30 offset:17160\n"
    "v_mfma_f32_16x16x32_bf16 v[112:115], a[144:147], a[224:227], v[112:115]\n"
    "buffer_load_dwordx4 v35, s[40:43], s78 offen offset:3072 lds\n"
    "v_mfma_f32_16x16x32_bf16 v[116:119], a[148:151], a[208:211], v[116:119]\n"
    "s_add_u32 m0, s73, 0x12000\n"
    "s_add_u32 s78, s78, 0x20000\n"
    "v_mfma_f32_16x16x32_bf16 v[120:123], a[148:151], a[212:215], v[120:123]\n"
    "buffer_load_dwordx4 v32, s[40:43], s78 offen lds\n"
    "v_mfma_f32_16x16x32_bf16 v[124:127], a[148:151], a[216:219], v[124:127]\n"
    "v_mfma_f32_16x16x32_bf16 v[128:131], a[148:151], a[220:223], v[128:131]\n"
    "ds_read_b64_tr_b16 a[160:161], v30 offset:17408\n"
    "ds_read_b64_tr_b16 a[162:163], v30 offset:17664\n"
    "v_mfma_f32_16x16x32_bf16 v[132:135], a[148:151], a[224:227], v[132:135]\n"
    "buffer_load_dwordx4 v33, s[40:43], s78 offen offset:1024 lds\n"
    "v_mfma_f32_16x16x32_bf16 v[136:139], a[152:155], a[208:211], v[136:139]\n"
    "v_mfma_f32_16x16x32_bf16 v[140:143], a[152:155], a[212:215], v[140:143]\n"
    "v_mfma_f32_16x16x32_bf16 v[144:147], a[152:155], a[216:219], v[144:147]\n"
    "buffer_load_dwordx4 v34, s[40:43], s78 offen offset:2048 lds\n"
    "v_mfma_f32_16x16x32_bf16 v[148:151], a[152:155], a[220:223], v[148:151]\n"
    "ds_read_b64_tr_b16 a[164:165], v30 offset:17416\n"
    "ds_read_b64_tr_b16 a[166:167], v30 offset:17672\n"
    "v_mfma_f32_16x16x32_bf16 v[152:155], a[152:155], a[224:227], v[152:155]\n"
    "s_waitcnt lgkmcnt(2)\n"
    "v_mfma_f32_16x16x32_bf16 v[156:159], a[156:159], a[208:211], v[156:159]\n"
    "buffer_load_dwordx4 v35, s[40:43], s78 offen offset:3072 lds\n"
    "v_mfma_f32_16x16x32_bf16 v[160:163], a[156:159], a[212:215], v[160:163]\n"
    "s_add_u32 m0, s79, 0x3000\n"
    "s_add_u32 s76, s60, 0x1e000\n"
    "v_mfma_f32_16x16x32_bf16 v[164:167], a[156:159], a[216:219], v[164:167]\n"
    "v_mfma_f32_16x16x32_bf16 v[168:171], a[156:159], a[220:223], v[168:171]\n"
    "ds_read_b64_tr_b16 a[168:169], v30 offset:17920\n"
    "ds_read_b64_tr_b16 a[170:171], v30 offset:18176\n"
    "v_mfma_f32_16x16x32_bf16 v[172:175], a[156:159], a[224:227], v[172:175]\n"
    "buffer_load_dwordx4 v36, s[36:39], s76 offen lds\n"
    "v_mfma_f32_16x16x32_bf16 v[176:179], a[160:163], a[208:211], v[176:179]\n"
    "ds_read_b64_tr_b16 a[172:173], v30 offset:17928\n"
    "ds_read_b64_tr_b16 a[174:175], v30 offset:18184\n"
    "v_mfma_f32_16x16x32_bf16 v[180:183], a[160:163], a[212:215], v[180:183]\n"
    "buffer_load_dwordx4 v37, s[36:39], s76 offen offset:1024 lds\n"
    "v_mfma_f32_16x16x32_bf16 v[184:187], a[160:163], a[216:219], v[184:187]\n"
    "v_mfma_f32_16x16x32_bf16 v[188:191], a[160:163], a[220:223], v[188:191]\n"
    "v_mfma_f32_16x16x32_bf16 v[192:195], a[160:163], a[224:227], v[192:195]\n"
    "s_waitcnt lgkmcnt(2)\n"
    "v_mfma_f32_16x16x32_bf16 v[196:199], a[164:167], a[208:211], v[196:199]\n"
    "buffer_load_dwordx4 v38, s[36:39], s76 offen offset:2048 lds\n"
    "v_mfma_f32_16x16x32_bf16 v[200:203], a[164:167], a[212:215], v[200:203]\n"
    "v_mfma_f32_16x16x32_bf16 v[204:207], a[164:167], a[216:219], v[204:207]\n"
    "buffer_load_dwordx4 v39, s[36:39], s76 offen offset:3072 lds\n"
    "v_mfma_f32_16x16x32_bf16 v[208:211], a[164:167], a[220:223], v[208:211]\n"
    "v_mfma_f32_16x16x32_bf16 v[212:215], a[164:167], a[224:227], v[212:215]\n"
    "s_waitcnt vmcnt(12) lgkmcnt(0)\n"
    "s_barrier\n"
    "v_mfma_f32_16x16x32_bf16 v[216:219], a[168:171], a[208:211], v[216:219]\n"
    "s_add_u32 m0, s73, 0x14000\n"
    "s_add_u32 s78, s78, 0x20000\n"
    "ds_read_b128 a[228:231], v29\n"
    "v_mfma_f32_16x16x32_bf16 v[220:223], a[168:171], a[212:215], v[220:223]\n"
    "ds_read_b64_tr_b16 a[176:177], v30 offset:32768\n"
    "ds_read_b64_tr_b16 a[178:179], v30 offset:33024\n"
    "v_mfma_f32_16x16x32_bf16 v[224:227], a[168:171], a[216:219], v[224:227]\n"
    "ds_read_b128 a[232:235], v29 offset:2048\n"
    "v_mfma_f32_16x16x32_bf16 v[228:231], a[168:171], a[220:223], v[228:231]\n"
    "ds_read_b128 a[236:239], v29 offset:4096\n"
    "v_mfma_f32_16x16x32_bf16 v[232:235], a[168:171], a[224:227], v[232:235]\n"
    "ds_read_b128 a[240:243], v29 offset:6144\n"
    "v_mfma_f32_16x16x32_bf16 v[236:239], a[172:175], a[208:211], v[236:239]\n"
    "ds_read_b128 a[244:247], v29 offset:8192\n"
    "ds_read_b64_tr_b16 a[180:181], v30 offset:32776\n"
    "v_add_u32 v29, s81, v29\n"
    "v_mfma_f32_16x16x32_bf16 v[240:243], a[172:175], a[212:215], v[240:243]\n"
    "ds_read_b64_tr_b16 a[182:183], v30 offset:33032\n"
    "ds_read_b64_tr_b16 a[184:185], v30 offset:33280\n"
    "v_mfma_f32_16x16x32_bf16 v[244:247], a[172:175], a[216:219], v[244:247]\n"
    "buffer_load_dwordx4 v32, s[40:43], s78 offen lds\n"
    "v_mfma_f32_16x16x32_bf16 v[248:251], a[172:175], a[220:223], v[248:251]\n"
    "ds_read_b64_tr_b16 a[186:187], v30 offset:33536\n"
    "v_mfma_f32_16x16x32_bf16 v[252:255], a[172:175], a[224:227], v[252:255]\n"
    "s_waitcnt lgkmcnt(5)\n"
    "v_mfma_f32_16x16x32_bf16 v[96:99], a[176:179], a[228:231], v[96:99]\n"
    "buffer_load_dwordx4 v33, s[40:43], s78 offen offset:1024 lds\n"
    "v_mfma_f32_16x16x32_bf16 v[100:103], a[176:179], a[232:235], v[100:103]\n"
    "buffer_load_dwordx4 v34, s[40:43], s78 offen offset:2048 lds\n"
    "v_mfma_f32_16x16x32_bf16 v[104:107], a[176:179], a[236:239], v[104:107]\n"
    "v_mfma_f32_16x16x32_bf16 v[108:111], a[176:179], a[240:243], v[108:111]\n"
    "s_waitcnt lgkmcnt(0)\n"
    "ds_read_b64_tr_b16 a[188:189], v30 offset:33288\n"
    "ds_read_b64_tr_b16 a[190:191], v30 offset:33544\n"
    "v_mfma_f32_16x16x32_bf16 v[112:115], a[176:179], a[244:247], v[112:115]\n"
    "buffer_load_dwordx4 v35, s[40:43], s78 offen offset:3072 lds\n"
    "v_mfma_f32_16x16x32_bf16 v[116:119], a[180:183], a[228:231], v[116:119]\n"
    "s_add_u32 m0, s73, 0x16000\n"
    "s_add_u32 s78, s78, 0x20000\n"
    "v_mfma_f32_16x16x32_bf16 v[120:123], a[180:183], a[232:235], v[120:123]\n"
    "buffer_load_dwordx4 v32, s[40:43], s78 offen lds\n"
    "v_mfma_f32_16x16x32_bf16 v[124:127], a[180:183], a[236:239], v[124:127]\n"
    "v_mfma_f32_16x16x32_bf16 v[128:131], a[180:183], a[240:243], v[128:131]\n"
    "ds_read_b64_tr_b16 a[144:145], v30 offset:33792\n"
    "ds_read_b64_tr_b16 a[146:147], v30 offset:34048\n"
    "v_mfma_f32_16x16x32_bf16 v[132:135], a[180:183], a[244:247], v[132:135]\n"
    "buffer_load_dwordx4 v33, s[40:43], s78 offen offset:1024 lds\n"
    "v_mfma_f32_16x16x32_bf16 v[136:139], a[184:187], a[228:231], v[136:139]\n"
    "v_mfma_f32_16x16x32_bf16 v[140:143], a[184:187], a[232:235], v[140:143]\n"
    "v_mfma_f32_16x16x32_bf16 v[144:147], a[184:187], a[236:239], v[144:147]\n"
    "buffer_load_dwordx4 v34, s[40:43], s78 offen offset:2048 lds\n"
    "v_mfma_f32_16x16x32_bf16 v[148:151], a[184:187], a[240:243], v[148:151]\n"
    "ds_read_b64_tr_b16 a[148:149], v30 offset:33800\n"
    "ds_read_b64_tr_b16 a[150:151], v30 offset:34056\n"
    "v_mfma_f32_16x16x32_bf16 v[152:155], a[184:187], a[244:247], v[152:155]\n"
    "s_waitcnt lgkmcnt(2)\n"
    "v_mfma_f32_16x16x32_bf16 v[156:159], a[188:191], a[228:231], v[156:159]\n"
    "buffer_load_dwordx4 v35, s[40:43], s78 offen offset:3072 lds\n"
    "v_mfma_f32_16x16x32_bf16 v[160:163], a[188:191], a[232:235], v[160:163]\n"
    "v_mfma_f32_16x16x32_bf16 v[164:167], a[188:191], a[236:239], v[164:167]\n"
    "v_mfma_f32_16x16x32_bf16 v[168:171], a[188:191], a[240:243], v[168:171]\n"
    "ds_read_b64_tr_b16 a[152:153], v30 offset:34304\n"
    "ds_read_b64_tr_b16 a[154:155], v30 offset:34560\n"
    "v_mfma_f32_16x16x32_bf16 v[172:175], a[188:191], a[244:247], v[172:175]\n"
    "v_mfma_f32_16x16x32_bf16 v[176:179], a[144:147], a[228:231], v[176:179]\n"
    "ds_read_b64_tr_b16 a[156:157], v30 offset:34312\n"
    "ds_read_b64_tr_b16 a[158:159], v30 offset:34568\n"
    "v_mfma_f32_16x16x32_bf16 v[180:183], a[144:147], a[232:235], v[180:183]\n"
    "v_mfma_f32_16x16x32_bf16 v[184:187], a[144:147], a[236:239], v[184:187]\n"
    "v_mfma_f32_16x16x32_bf16 v[188:191], a[144:147], a[240:243], v[188:191]\n"
    "v_mfma_f32_16x16x32_bf16 v[192:195], a[144:147], a[244:247], v[192:195]\n"
    "s_waitcnt lgkmcnt(2)\n"
    "v_mfma_f32_16x16x32_bf16 v[196:199], a[148:151], a[228:231], v[196:199]\n"
    "v_mfma_f32_16x16x32_bf16 v[200:203], a[148:151], a[232:235], v[200:203]\n"
    "v_mfma_f32_16x16x32_bf16 v[204:207], a[148:151], a[236:239], v[204:207]\n"
    "v_mfma_f32_16x16x32_bf16 v[208:211], a[148:151], a[240:243], v[208:211]\n"
    "v_mfma_f32_16x16x32_bf16 v[212:215], a[148:151], a[244:247], v[212:215]\n"
    "s_waitcnt vmcnt(8) lgkmcnt(0)\n"
    "s_barrier\n"
    "v_mfma_f32_16x16x32_bf16 v[216:219], a[152:155], a[228:231], v[216:219]\n"
    "s_add_u32 s60, s60, 0x80\n"
    "s_sub_u32 s79, s61, s79\n"
    "ds_read_b128 a[208:211], v28\n"
    "v_mfma_f32_16x16x32_bf16 v[220:223], a[152:155], a[232:235], v[220:223]\n"
    "s_add_u32 m0, s73, 0x18000\n"
    "s_add_u32 s78, s78, 0x20000\n"
    "ds_read_b64_tr_b16 a[160:161], v30\n"
    "v_mfma_f32_16x16x32_bf16 v[224:227], a[152:155], a[236:239], v[224:227]\n"
    "ds_read_b64_tr_b16 a[162:163], v30 offset:256\n"
    "ds_read_b128 a[212:215], v28 offset:2048\n"
    "v_mfma_f32_16x16x32_bf16 v[228:231], a[152:155], a[240:243], v[228:231]\n"
    "ds_read_b128 a[216:219], v28 offset:4096\n"
    "v_mfma_f32_16x16x32_bf16 v[232:235], a[152:155], a[244:247], v[232:235]\n"
    "ds_read_b128 a[220:223], v28 offset:6144\n"
    "v_mfma_f32_16x16x32_bf16 v[236:239], a[156:159], a[228:231], v[236:239]\n"
    "ds_read_b128 a[224:227], v28 offset:8192\n"
    "ds_read_b64_tr_b16 a[164:165], v30 offset:8\n"
    "v_subrev_u32 v28, s81, v28\n"
    "v_mfma_f32_16x16x32_bf16 v[240:243], a[156:159], a[232:235], v[240:243]\n"
    "ds_read_b64_tr_b16 a[166:167], v30 offset:264\n"
    "ds_read_b64_tr_b16 a[168:169], v30 offset:512\n"
    "v_mfma_f32_16x16x32_bf16 v[244:247], a[156:159], a[236:239], v[244:247]\n"
    "buffer_load_dwordx4 v32, s[40:43], s78 offen lds\n"
    "v_mfma_f32_16x16x32_bf16 v[248:251], a[156:159], a[240:243], v[248:251]\n"
    "ds_read_b64_tr_b16 a[170:171], v30 offset:768\n"
    "v_mfma_f32_16x16x32_bf16 v[252:255], a[156:159], a[244:247], v[252:255]\n"
    "s_waitcnt lgkmcnt(5)\n"
    "v_mfma_f32_16x16x32_bf16 v[96:99], a[160:163], a[208:211], v[96:99]\n"
    "buffer_load_dwordx4 v33, s[40:43], s78 offen offset:1024 lds\n"
    "v_mfma_f32_16x16x32_bf16 v[100:103], a[160:163], a[212:215], v[100:103]\n"
    "buffer_load_dwordx4 v34, s[40:43], s78 offen offset:2048 lds\n"
    "v_mfma_f32_16x16x32_bf16 v[104:107], a[160:163], a[216:219], v[104:107]\n"
    "v_mfma_f32_16x16x32_bf16 v[108:111], a[160:163], a[220:223], v[108:111]\n"
    "s_waitcnt lgkmcnt(0)\n"
    "ds_read_b64_tr_b16 a[172:173], v30 offset:520\n"
    "ds_read_b64_tr_b16 a[174:175], v30 offset:776\n"
    "v_mfma_f32_16x16x32_bf16 v[112:115], a[160:163], a[224:227], v[112:115]\n"
    "buffer_load_dwordx4 v35, s[40:43], s78 offen offset:3072 lds\n"
    "v_mfma_f32_16x16x32_bf16 v[116:119], a[164:167], a[208:211], v[116:119]\n"
    "s_add_u32 m0, s73, 0x1a000\n"
    "s_add_u32 s78, s78, 0x20000\n"
    "v_mfma_f32_16x16x32_bf16 v[120:123], a[164:167], a[212:215], v[120:123]\n"
    "buffer_load_dwordx4 v32, s[40:43], s78 offen lds\n"
    "v_mfma_f32_16x16x32_bf16 v[124:127], a[164:167], a[216:219], v[124:127]\n"
    "v_mfma_f32_16x16x32_bf16 v[128:131], a[164:167], a[220:223], v[128:131]\n"
    "ds_read_b64_tr_b16 a[176:177], v30 offset:1024\n"
    "ds_read_b64_tr_b16 a[178:179], v30 offset:1280\n"
    "v_mfma_f32_16x16x32_bf16 v[132:135], a[164:167], a[224:227], v[132:135]\n"
    "buffer_load_dwordx4 v33, s[40:43], s78 offen offset:1024 lds\n"
    "v_mfma_f32_16x16x32_bf16 v[136:139], a[168:171], a[208:211], v[136:139]\n"
    "v_mfma_f32_16x16x32_bf16 v[140:143], a[168:171], a[212:215], v[140:143]\n"
    "v_mfma_f32_16x16x32_bf16 v[144:147], a[168:171], a[216:219], v[144:147]\n"
    "buffer_load_dwordx4 v34, s[40:43], s78 offen offset:2048 lds\n"
    "v_mfma_f32_16x16x32_bf16 v[148:151], a[168:171], a[220:223], v[148:151]\n"
    "ds_read_b64_tr_b16 a[180:181], v30 offset:1032\n"
    "ds_read_b64_tr_b16 a[182:183], v30 offset:1288\n"
    "v_mfma_f32_16x16x32_bf16 v[152:155], a[168:171], a[224:227], v[152:155]\n"
    "s_waitcnt lgkmcnt(2)\n"
    "v_mfma_f32_16x16x32_bf16 v[156:159], a[172:175], a[208:211], v[156:159]\n"
    "buffer_load_dwordx4 v35, s[40:43], s78 offen offset:3072 lds\n"
    "v_mfma_f32_16x16x32_bf16 v[160:163], a[172:175], a[212:215], v[160:163]\n"
    "s_add_u32 m0, s79, 0x3000\n"
    "s_add_u32 s76, s60, 0x1e000\n"
    "v_mfma_f32_16x16x32_bf16 v[164:167], a[172:175], a[216:219], v[164:167]\n"
    "v_mfma_f32_16x16x32_bf16 v[168:171], a[172:175], a[220:223], v[168:171]\n"
    "ds_read_b64_tr_b16 a[184:185], v30 offset:1536\n"
    "ds_read_b64_tr_b16 a[186:187], v30 offset:1792\n"
    "v_mfma_f32_16x16x32_bf16 v[172:175], a[172:175], a[224:227], v[172:175]\n"
    "buffer_load_dwordx4 v36, s[36:39], s76 offen lds\n"
    "v_mfma_f32_16x16x32_bf16 v[176:179], a[176:179], a[208:211], v[176:179]\n"
    "ds_read_b64_tr_b16 a[188:189], v30 offset:1544\n"
    "ds_read_b64_tr_b16 a[190:191], v30 offset:1800\n"
    "v_mfma_f32_16x16x32_bf16 v[180:183], a[176:179], a[212:215], v[180:183]\n"
    "buffer_load_dwordx4 v37, s[36:39], s76 offen offset:1024 lds\n"
    "v_mfma_f32_16x16x32_bf16 v[184:187], a[176:179], a[216:219], v[184:187]\n"
    "v_mfma_f32_16x16x32_bf16 v[188:191], a[176:179], a[220:223], v[188:191]\n"
    "v_mfma_f32_16x16x32_bf16 v[192:195], a[176:179], a[224:227], v[192:195]\n"
    "s_waitcnt lgkmcnt(2)\n"
    "v_mfma_f32_16x16x32_bf16 v[196:199], a[180:183], a[208:211], v[196:199]\n"
    "buffer_load_dwordx4 v38, s[36:39], s76 offen offset:2048 lds\n"
    "v_mfma_f32_16x16x32_bf16 v[200:203], a[180:183], a[212:215], v[200:203]\n"
    "v_mfma_f32_16x16x32_bf16 v[204:207], a[180:183], a[216:219], v[204:207]\n"
    "buffer_load_dwordx4 v39, s[36:39], s76 offen offset:3072 lds\n"
    "v_mfma_f32_16x16x32_bf16 v[208:211], a[180:183], a[220:223], v[208:211]\n"
    "v_mfma_f32_16x16x32_bf16 v[212:215], a[180:183], a[224:227], v[212:215]\n"
    "s_waitcnt vmcnt(12) lgkmcnt(0)\n"
    "s_barrier\n"
    "v_mfma_f32_16x16x32_bf16 v[216:219], a[184:187], a[208:211], v[216:219]\n"
    "s_add_u32 m0, s73, 0x10000\n"
    "s_add_u32 s78, s78, 0x20000\n"
    "ds_read_b128 a[228:231], v29\n"
    "v_mfma_f32_16x16x32_bf16 v[220:223], a[184:187], a[212:215], v[220:223]\n"
    "ds_read_b64_tr_b16 a[144:145], v30 offset:16384\n"
    "ds_read_b64_tr_b16 a[146:147], v30 offset:16640\n"
    "v_mfma_f32_16x16x32_bf16 v[224:227], a[184:187], a[216:219], v[224:227]\n"
    "ds_read_b128 a[232:235], v29 offset:2048\n"
    "v_mfma_f32_16x16x32_bf16 v[228:231], a[184:187], a[220:223], v[228:231]\n"
    "ds_read_b128 a[236:239], v29 offset:4096\n"
    "v_mfma_f32_16x16x32_bf16 v[232:235], a[184:187], a[224:227], v[232:235]\n"
    "ds_read_b128 a[240:243], v29 offset:6144\n"
    "v_mfma_f32_16x16x32_bf16 v[236:239], a[188:191], a[208:211], v[236:239]\n"
    "ds_read_b128 a[244:247], v29 offset:8192\n"
    "ds_read_b64_tr_b16 a[148:149], v30 offset:16392\n"
    "v_subrev_u32 v29, s81, v29\n"
    "v_mfma_f32_16x16x32_bf16 v[240:243], a[188:191], a[212:215], v[240:243]\n"
    "ds_read_b64_tr_b16 a[150:151], v30 offset:16648\n"
    "ds_read_b64_tr_b16 a[152:153], v30 offset:16896\n"
    "v_mfma_f32_16x16x32_bf16 v[244:247], a[188:191], a[216:219], v[244:247]\n"
    "buffer_load_dwordx4 v32, s[40:43], s78 offen lds\n"
    "v_mfma_f32_16x16x32_bf16 v[248:251], a[188:191], a[220:223], v[248:251]\n"
    "ds_read_b64_tr_b16 a[154:155], v30 offset:17152\n"
    "v_mfma_f32_16x16x32_bf16 v[252:255], a[188:191], a[224:227], v[252:255]\n"
    "s_waitcnt lgkmcnt(5)\n"
    "v_mfma_f32_16x16x32_bf16 v[96:99], a[144:147], a[228:231], v[96:99]\n"
    "buffer_load_dwordx4 v33, s[40:43], s78 offen offset:1024 lds\n"
    "v_mfma_f32_16x16x32_bf16 v[100:103], a[144:147], a[232:235], v[100:103]\n"
    "buffer_load_dwordx4 v34, s[40:43], s78 offen offset:2048 lds\n"
    "v_mfma_f32_16x16x32_bf16 v[104:107], a[144:147], a[236:239], v[104:107]\n"
    "v_mfma_f32_16x16x32_bf16 v[108:111], a[144:147], a[240:243], v[108:111]\n"
    "s_waitcnt lgkmcnt(0)\n"
    "ds_read_b64_tr_b16 a[156:157], v30 offset:16904\n"
    "ds_read_b64_tr_b16 a[158:159], v30 offset:17160\n"
    "v_mfma_f32_16x16x32_bf16 v[112:115], a[144:147], a[244:247], v[112:115]\n"
    "buffer_load_dwordx4 v35, s[40:43], s78 offen offset:3072 lds\n"
    "v_mfma_f32_16x16x32_bf16 v[116:119], a[148:151], a[228:231], v[116:119]\n"
    "s_add_u32 m0, s73, 0x12000\n"
    "s_add_u32 s78, s78, 0x20000\n"
    "v_mfma_f32_16x16x32_bf16 v[120:123], a[148:151], a[232:235], v[120:123]\n"
    "buffer_load_dwordx4 v32, s[40:43], s78 offen lds\n"
    "v_mfma_f32_16x16x32_bf16 v[124:127], a[148:151], a[236:239], v[124:127]\n"
    "v_mfma_f32_16x16x32_bf16 v[128:131], a[148:151], a[240:243], v[128:131]\n"
    "ds_read_b64_tr_b16 a[160:161], v30 offset:17408\n"
    "ds_read_b64_tr_b16 a[162:163], v30 offset:17664\n"
    "v_mfma_f32_16x16x32_bf16 v[132:135], a[148:151], a[244:247], v[132:135]\n"
    "buffer_load_dwordx4 v33, s[40:43], s78 offen offset:1024 lds\n"
    "v_mfma_f32_16x16x32_bf16 v[136:139], a[152:155], a[228:231], v[136:139]\n"
    "v_mfma_f32_16x16x32_bf16 v[140:143], a[152:155], a[232:235], v[140:143]\n"
    "v_mfma_f32_16x16x32_bf16 v[144:147], a[152:155], a[236:239], v[144:147]\n"
    "buffer_load_dwordx4 v34, s[40:43], s78 offen offset:2048 lds\n"
    "v_mfma_f32_16x16x32_bf16 v[148:151], a[152:155], a[240:243], v[148:151]\n"
    "ds_read_b64_tr_b16 a[164:165], v30 offset:17416\n"
    "ds_read_b64_tr_b16 a[166:167], v30 offset:17672\n"
    "v_mfma_f32_16x16x32_bf16 v[152:155], a[152:155], a[244:247], v[152:155]\n"
    "s_waitcnt lgkmcnt(2)\n"
    "v_mfma_f32_16x16x32_bf16 v[156:159], a[156:159], a[228:231], v[156:159]\n"
    "buffer_load_dwordx4 v35, s[40:43], s78 offen offset:3072 lds\n"
    "v_mfma_f32_16x16x32_bf16 v[160:163], a[156:159], a[232:235], v[160:163]\n"
    "v_mfma_f32_16x16x32_bf16 v[164:167], a[156:159], a[236:239], v[164:167]\n"
    "v_mfma_f32_16x16x32_bf16 v[168:171], a[156:159], a[240:243], v[168:171]\n"
    "ds_read_b64_tr_b16 a[168:169], v30 offset:17920\n"
    "ds_read_b64_tr_b16 a[170:171], v30 offset:18176\n"
    "v_mfma_f32_16x16x32_bf16 v[172:175], a[156:159], a[244:247], v[172:175]\n"
    "v_mfma_f32_16x16x32_bf16 v[176:179], a[160:163], a[228:231], v[176:179]\n"
    "ds_read_b64_tr_b16 a[172:173], v30 offset:17928\n"
    "ds_read_b64_tr_b16 a[174:175], v30 offset:18184\n"
    "v_mfma_f32_16x16x32_bf16 v[180:183], a[160:163], a[232:235], v[180:183]\n"
    "v_mfma_f32_16x16x32_bf16 v[184:187], a[160:163], a[236:239], v[184:187]\n"
    "v_mfma_f32_16x16x32_bf16 v[188:191], a[160:163], a[240:243], v[188:191]\n"
    "v_mfma_f32_16x16x32_bf16 v[192:195], a[160:163], a[244:247], v[192:195]\n"
    "s_waitcnt lgkmcnt(2)\n"
    "v_mfma_f32_16x16x32_bf16 v[196:199], a[164:167], a[228:231], v[196:199]\n"
    "v_mfma_f32_16x16x32_bf16 v[200:203], a[164:167], a[232:235], v[200:203]\n"
    "v_mfma_f32_16x16x32_bf16 v[204:207], a[164:167], a[236:239], v[204:207]\n"
    "v_mfma_f32_16x16x32_bf16 v[208:211], a[164:167], a[240:243], v[208:211]\n"
    "v_mfma_f32_16x16x32_bf16 v[212:215], a[164:167], a[244:247], v[212:215]\n"
    "s_waitcnt vmcnt(8) lgkmcnt(0)\n"
    "s_barrier\n"
    "v_mfma_f32_16x16x32_bf16 v[216:219], a[168:171], a[228:231], v[216:219]\n"
    "s_add_u32 s60, s60, 0x80\n"
    "s_sub_u32 s79, s61, s79\n"
    "ds_read_b128 a[208:211], v28\n"
    "v_mfma_f32_16x16x32_bf16 v[220:223], a[168:171], a[232:235], v[220:223]\n"
    "s_add_u32 m0, s73, 0x14000\n"
    "s_add_u32 s78, s78, 0x20000\n"
    "ds_read_b64_tr_b16 a[176:177], v30 offset:32768\n"
    "v_mfma_f32_16x16x32_bf16 v[224:227], a[168:171], a[236:239], v[224:227]\n"
    "ds_read_b64_tr_b16 a[178:179], v30 offset:33024\n"
    "ds_read_b128 a[212:215], v28 offset:2048\n"
    "v_mfma_f32_16x16x32_bf16 v[228:231], a[168:171], a[240:243], v[228:231]\n"
    "ds_read_b128 a[216:219], v28 offset:4096\n"
    "v_mfma_f32_16x16x32_bf16 v[232:235], a[168:171], a[244:247], v[232:235]\n"
    "ds_read_b128 a[220:223], v28 offset:6144\n"
    "v_mfma_f32_16x16x32_bf16 v[236:239], a[172:175], a[228:231], v[236:239]\n"
    "ds_read_b128 a[224:227], v28 offset:8192\n"
    "ds_read_b64_tr_b16 a[180:181], v30 offset:32776\n"
    "v_add_u32 v28, s81, v28\n"
    "v_mfma_f32_16x16x32_bf16 v[240:243], a[172:175], a[232:235], v[240:243]\n"
    "ds_read_b64_tr_b16 a[182:183], v30 offset:33032\n"
    "ds_read_b64_tr_b16 a[184:185], v30 offset:33280\n"
    "v_mfma_f32_16x16x32_bf16 v[244:247], a[172:175], a[236:239], v[244:247]\n"
    "buffer_load_dwordx4 v32, s[40:43], s78 offen lds\n"
    "v_mfma_f32_16x16x32_bf16 v[248:251], a[172:175], a[240:243], v[248:251]\n"
    "ds_read_b64_tr_b16 a[186:187], v30 offset:33536\n"
    "v_mfma_f32_16x16x32_bf16 v[252:255], a[172:175], a[244:247], v[252:255]\n"
    "s_waitcnt lgkmcnt(5)\n"
    "v_mfma_f32_16x16x32_bf16 v[96:99], a[176:179], a[208:211], v[96:99]\n"
    "buffer_load_dwordx4 v33, s[40:43], s78 offen offset:1024 lds\n"
    "v_mfma_f32_16x16x32_bf16 v[100:103], a[176:179], a[212:215], v[100:103]\n"
    "buffer_load_dwordx4 v34, s[40:43], s78 offen offset:2048 lds\n"
    "v_mfma_f32_16x16x32_bf16 v[104:107], a[176:179], a[216:219], v[104:107]\n"
    "v_mfma_f32_16x16x32_bf16 v[108:111], a[176:179], a[220:223], v[108:111]\n"
    "s_waitcnt lgkmcnt(0)\n"
    "ds_read_b64_tr_b16 a[188:189], v30 offset:33288\n"
    "ds_read_b64_tr_b16 a[190:191], v30 offset:33544\n"
    "v_mfma_f32_16x16x32_bf16 v[112:115], a[176:179], a[224:227], v[112:115]\n"
    "buffer_load_dwordx4 v35, s[40:43], s78 offen offset:3072 lds\n"
    "v_mfma_f32_16x16x32_bf16 v[116:119], a[180:183], a[208:211], v[116:119]\n"
    "s_add_u32 m0, s73, 0x16000\n"
    "s_add_u32 s78, s78, 0x20000\n"
    "v_mfma_f32_16x16x32_bf16 v[120:123], a[180:183], a[212:215], v[120:123]\n"
    "buffer_load_dwordx4 v32, s[40:43], s78 offen lds\n"
    "v_mfma_f32_16x16x32_bf16 v[124:127], a[180:183], a[216:219], v[124:127]\n"
    "v_mfma_f32_16x16x32_bf16 v[128:131], a[180:183], a[220:223], v[128:131]\n"
    "ds_read_b64_tr_b16 a[144:145], v30 offset:33792\n"
    "ds_read_b64_tr_b16 a[146:147], v30 offset:34048\n"
    "v_mfma_f32_16x16x32_bf16 v[132:135], a[180:183], a[224:227], v[132:135]\n"
    "buffer_load_dwordx4 v33, s[40:43], s78 offen offset:1024 lds\n"
    "v_mfma_f32_16x16x32_bf16 v[136:139], a[184:187], a[208:211], v[136:139]\n"
    "v_mfma_f32_16x16x32_bf16 v[140:143], a[184:187], a[212:215], v[140:143]\n"
    "v_mfma_f32_16x16x32_bf16 v[144:147], a[184:187], a[216:219], v[144:147]\n"
    "buffer_load_dwordx4 v34, s[40:43], s78 offen offset:2048 lds\n"
    "v_mfma_f32_16x16x32_bf16 v[148:151], a[184:187], a[220:223], v[148:151]\n"
    "ds_read_b64_tr_b16 a[148:149], v30 offset:33800\n"
    "ds_read_b64_tr_b16 a[150:151], v30 offset:34056\n"
    "v_mfma_f32_16x16x32_bf16 v[152:155], a[184:187], a[224:227], v[152:155]\n"
    "s_waitcnt lgkmcnt(2)\n"
    "v_mfma_f32_16x16x32_bf16 v[156:159], a[188:191], a[208:211], v[156:159]\n"
    "buffer_load_dwordx4 v35, s[40:43], s78 offen offset:3072 lds\n"
    "v_mfma_f32_16x16x32_bf16 v[160:163], a[188:191], a[212:215], v[160:163]\n"
    "s_add_u32 m0, s79, 0x3000\n"
    "s_add_u32 s76, s60, 0x1e000\n"
    "v_mfma_f32_16x16x32_bf16 v[164:167], a[188:191], a[216:219], v[164:167]\n"
    "v_mfma_f32_16x16x32_bf16 v[168:171], a[188:191], a[220:223], v[168:171]\n"
    "ds_read_b64_tr_b16 a[152:153], v30 offset:34304\n"
    "ds_read_b64_tr_b16 a[154:155], v30 offset:34560\n"
    "v_mfma_f32_16x16x32_bf16 v[172:175], a[188:191], a[224:227], v[172:175]\n"
    "buffer_load_dwordx4 v36, s[36:39], s76 offen lds\n"
    "v_mfma_f32_16x16x32_bf16 v[176:179], a[144:147], a[208:211], v[176:179]\n"
    "ds_read_b64_tr_b16 a[156:157], v30 offset:34312\n"
    "ds_read_b64_tr_b16 a[158:159], v30 offset:34568\n"
    "v_mfma_f32_16x16x32_bf16 v[180:183], a[144:147], a[212:215], v[180:183]\n"
    "buffer_load_dwordx4 v37, s[36:39], s76 offen offset:1024 lds\n"
    "v_mfma_f32_16x16x32_bf16 v[184:187], a[144:147], a[216:219], v[184:187]\n"
    "v_mfma_f32_16x16x32_bf16 v[188:191], a[144:147], a[220:223], v[188:191]\n"
    "v_mfma_f32_16x16x32_bf16 v[192:195], a[144:147], a[224:227], v[192:195]\n"
    "s_waitcnt lgkmcnt(2)\n"
    "v_mfma_f32_16x16x32_bf16 v[196:199], a[148:151], a[208:211], v[196:199]\n"
    "buffer_load_dwordx4 v38, s[36:39], s76 offen offset:2048 lds\n"
    "v_mfma_f32_16x16x32_bf16 v[200:203], a[148:151], a[212:215], v[200:203]\n"
    "v_mfma_f32_16x16x32_bf16 v[204:207], a[148:151], a[216:219], v[204:207]\n"
    "buffer_load_dwordx4 v39, s[36:39], s76 offen offset:3072 lds\n"
    "v_mfma_f32_16x16x32_bf16 v[208:211], a[148:151], a[220:223], v[208:211]\n"
    "v_mfma_f32_16x16x32_bf16 v[212:215], a[148:151], a[224:227], v[212:215]\n"
    "s_waitcnt vmcnt(12) lgkmcnt(0)\n"
    "s_barrier\n"
    "v_mfma_f32_16x16x32_bf16 v[216:219], a[152:155], a[208:211], v[216:219]\n"
    "s_add_u32 m0, s73, 0x18000\n"
    "s_add_u32 s78, s78, 0x20000\n"
    "ds_read_b128 a[228:231], v29\n"
    "v_mfma_f32_16x16x32_bf16 v[220:223], a[152:155], a[212:215], v[220:223]\n"
    "ds_read_b64_tr_b16 a[160:161], v30\n"
    "ds_read_b64_tr_b16 a[162:163], v30 offset:256\n"
    "v_mfma_f32_16x16x32_bf16 v[224:227], a[152:155], a[216:219], v[224:227]\n"
    "ds_read_b128 a[232:235], v29 offset:2048\n"
    "v_mfma_f32_16x16x32_bf16 v[228:231], a[152:155], a[220:223], v[228:231]\n"
    "ds_read_b128 a[236:239], v29 offset:4096\n"
    "v_mfma_f32_16x16x32_bf16 v[232:235], a[152:155], a[224:227], v[232:235]\n"
    "ds_read_b128 a[240:243], v29 offset:6144\n"
    "v_mfma_f32_16x16x32_bf16 v[236:239], a[156:159], a[208:211], v[236:239]\n"
    "ds_read_b128 a[244:247], v29 offset:8192\n"
    "ds_read_b64_tr_b16 a[164:165], v30 offset:8\n"
    "v_add_u32 v29, s81, v29\n"
    "v_mfma_f32_16x16x32_bf16 v[240:243], a[156:159], a[212:215], v[240:243]\n"
    "ds_read_b64_tr_b16 a[166:167], v30 offset:264\n"
    "ds_read_b64_tr_b16 a[168:169], v30 offset:512\n"
    "v_mfma_f32_16x16x32_bf16 v[244:247], a[156:159], a[216:219], v[244:247]\n"
    "buffer_load_dwordx4 v32, s[40:43], s78 offen lds\n"
    "v_mfma_f32_16x16x32_bf16 v[248:251], a[156:159], a[220:223], v[248:251]\n"
    "ds_read_b64_tr_b16 a[170:171], v30 offset:768\n"
    "v_mfma_f32_16x16x32_bf16 v[252:255], a[156:159], a[224:227], v[252:255]\n"
    "s_waitcnt lgkmcnt(5)\n"
    "v_mfma_f32_16x16x32_bf16 v[96:99], a[160:163], a[228:231], v[96:99]\n"
    "buffer_load_dwordx4 v33, s[40:43], s78 offen offset:1024 lds\n"
    "v_mfma_f32_16x16x32_bf16 v[100:103], a[160:163], a[232:235], v[100:103]\n"
    "buffer_load_dwordx4 v34, s[40:43], s78 offen offset:2048 lds\n"
    "v_mfma_f32_16x16x32_bf16 v[104:107], a[160:163], a[236:239], v[104:107]\n"
    "v_mfma_f32_16x16x32_bf16 v[108:111], a[160:163], a[240:243], v[108:111]\n"
    "s_waitcnt lgkmcnt(0)\n"
    "ds_read_b64_tr_b16 a[172:173], v30 offset:520\n"
    "ds_read_b64_tr_b16 a[174:175], v30 offset:776\n"
    "v_mfma_f32_16x16x32_bf16 v[112:115], a[160:163], a[244:247], v[112:115]\n"
    "buffer_load_dwordx4 v35, s[40:43], s78 offen offset:3072 lds\n"
    "v_mfma_f32_16x16x32_bf16 v[116:119], a[164:167], a[228:231], v[116:119]\n"
    "s_add_u32 m0, s73, 0x1a000\n"
    "s_add_u32 s78, s78, 0x20000\n"
    "v_mfma_f32_16x16x32_bf16 v[120:123], a[164:167], a[232:235], v[120:123]\n"
    "buffer_load_dwordx4 v32, s[40:43], s78 offen lds\n"
    "v_mfma_f32_16x16x32_bf16 v[124:127], a[164:167], a[236:239], v[124:127]\n"
    "v_mfma_f32_16x16x32_bf16 v[128:131], a[164:167], a[240:243], v[128:131]\n"
    "ds_read_b64_tr_b16 a[176:177], v30 offset:1024\n"
    "ds_read_b64_tr_b16 a[178:179], v30 offset:1280\n"
    "v_mfma_f32_16x16x32_bf16 v[132:135], a[164:167], a[244:247], v[132:135]\n"
    "buffer_load_dwordx4 v33, s[40:43], s78 offen offset:1024 lds\n"
    "v_mfma_f32_16x16x32_bf16 v[136:139], a[168:171], a[228:231], v[136:139]\n"
    "v_mfma_f32_16x16x32_bf16 v[140:143], a[168:171], a[232:235], v[140:143]\n"
    "v_mfma_f32_16x16x32_bf16 v[144:147], a[168:171], a[236:239], v[144:147]\n"
    "buffer_load_dwordx4 v34, s[40:43], s78 offen offset:2048 lds\n"
    "v_mfma_f32_16x16x32_bf16 v[148:151], a[168:171], a[240:243], v[148:151]\n"
    "ds_read_b64_tr_b16 a[180:181], v30 offset:1032\n"
    "ds_read_b64_tr_b16 a[182:183], v30 offset:1288\n"
    "v_mfma_f32_16x16x32_bf16 v[152:155], a[168:171], a[244:247], v[152:155]\n"
    "s_waitcnt lgkmcnt(2)\n"
    "ds_read_b64_tr_b16 a[184:185], v30 offset:1536\n"
    "ds_read_b64_tr_b16 a[186:187], v30 offset:1792\n"
    "v_mfma_f32_16x16x32_bf16 v[156:159], a[172:175], a[228:231], v[156:159]\n"
    "buffer_load_dwordx4 v35, s[40:43], s78 offen offset:3072 lds\n"
    "v_mfma_f32_16x16x32_bf16 v[160:163], a[172:175], a[232:235], v[160:163]\n"
    "v_mfma_f32_16x16x32_bf16 v[164:167], a[172:175], a[236:239], v[164:167]\n"
    "v_mfma_f32_16x16x32_bf16 v[168:171], a[172:175], a[240:243], v[168:171]\n"
    "v_mfma_f32_16x16x32_bf16 v[172:175], a[172:175], a[244:247], v[172:175]\n"
    "ds_read_b64_tr_b16 a[188:189], v30 offset:1544\n"
    "ds_read_b64_tr_b16 a[190:191], v30 offset:1800\n"
    "v_mfma_f32_16x16x32_bf16 v[176:179], a[176:179], a[228:231], v[176:179]\n"
    "v_mfma_f32_16x16x32_bf16 v[180:183], a[176:179], a[232:235], v[180:183]\n"
    "v_mfma_f32_16x16x32_bf16 v[184:187], a[176:179], a[236:239], v[184:187]\n"
    "v_mfma_f32_16x16x32_bf16 v[188:191], a[176:179], a[240:243], v[188:191]\n"
    "v_mfma_f32_16x16x32_bf16 v[192:195], a[176:179], a[244:247], v[192:195]\n"
    "s_waitcnt lgkmcnt(4)\n"
    "v_mfma_f32_16x16x32_bf16 v[196:199], a[180:183], a[228:231], v[196:199]\n"
    "v_mfma_f32_16x16x32_bf16 v[200:203], a[180:183], a[232:235], v[200:203]\n"
    "v_mfma_f32_16x16x32_bf16 v[204:207], a[180:183], a[236:239], v[204:207]\n"
    "v_mfma_f32_16x16x32_bf16 v[208:211], a[180:183], a[240:243], v[208:211]\n"
    "v_mfma_f32_16x16x32_bf16 v[212:215], a[180:183], a[244:247], v[212:215]\n"
    "s_sub_u32 s81, 0, s81\n"
    "s_add_u32 s80, s80, 1\n"
    "s_cmp_lt_u32 s80, 3\n"
    "s_waitcnt vmcnt(8) lgkmcnt(0)\n"
    "s_barrier\n"
    "s_cbranch_scc1 LQW_%=\n"
    "v_mfma_f32_16x16x32_bf16 v[216:219], a[184:187], a[228:231], v[216:219]\n"
    "ds_read_b128 a[208:211], v28\n"
    "v_and_b32 v44, 15, v0\n"
    "v_mfma_f32_16x16x32_bf16 v[220:223], a[184:187], a[232:235], v[220:223]\n"
    "ds_read_b64_tr_b16 a[144:145], v30 offset:16384\n"
    "v_lshrrev_b32 v45, 4, v0\n"
    "v_mfma_f32_16x16x32_bf16 v[224:227], a[184:187], a[236:239], v[224:227]\n"
    "ds_read_b64_tr_b16 a[146:147], v30 offset:16640\n"
    "v_lshlrev_b32 v46, 8, v44\n"
    "v_mfma_f32_16x16x32_bf16 v[228:231], a[184:187], a[240:243], v[228:231]\n"
    "ds_read_b128 a[212:215], v28 offset:2048\n"
    "v_add_u32 v46, s88, v46\n"
    "v_mfma_f32_16x16x32_bf16 v[232:235], a[184:187], a[244:247], v[232:235]\n"
    "ds_read_b128 a[216:219], v28 offset:4096\n"
    "v_xor_b32 v47, v45, v44\n"
    "v_mfma_f32_16x16x32_bf16 v[236:239], a[188:191], a[228:231], v[236:239]\n"
    "ds_read_b128 a[220:223], v28 offset:6144\n"
    "v_xor_b32 v48, 0, v47\n"
    "v_mfma_f32_16x16x32_bf16 v[240:243], a[188:191], a[232:235], v[240:243]\n"
    "s_add_u32 m0, s73, 0x10000\n"
    "s_add_u32 s86, s73, 0x4000\n"
    "ds_read_b128 a[224:227], v28 offset:8192\n"
    "v_mfma_f32_16x16x32_bf16 v[244:247], a[188:191], a[236:239], v[244:247]\n"
    "ds_read_b64_tr_b16 a[148:149], v30 offset:16392\n"
    "v_lshl_add_u32 v2, v48, 4, v46\n"
    "v_mfma_f32_16x16x32_bf16 v[248:251], a[188:191], a[240:243], v[248:251]\n"
    "ds_read_b64_tr_b16 a[150:151], v30 offset:16648\n"
    "v_xor_b32 v48, 4, v47\n"
    "v_mfma_f32_16x16x32_bf16 v[252:255], a[188:191], a[244:247], v[252:255]\n"
    "s_waitcnt lgkmcnt(4)\n"
    "ds_read_b64_tr_b16 a[152:153], v30 offset:16896\n"
    "v_lshl_add_u32 v3, v48, 4, v46\n"
    "v_mfma_f32_16x16x32_bf16 v[96:99], a[144:147], a[208:211], v[96:99]\n"
    "ds_read_b64_tr_b16 a[154:155], v30 offset:17152\n"
    "v_xor_b32 v48, 8, v47\n"
    "v_mfma_f32_16x16x32_bf16 v[100:103], a[144:147], a[212:215], v[100:103]\n"
    "v_lshl_add_u32 v4, v48, 4, v46\n"
    "v_xor_b32 v48, 12, v47\n"
    "v_mfma_f32_16x16x32_bf16 v[104:107], a[144:147], a[216:219], v[104:107]\n"
    "s_waitcnt lgkmcnt(4)\n"
    "v_lshl_add_u32 v5, v48, 4, v46\n"
    "v_lshrrev_b32 v46, 5, v0\n"
    "v_mfma_f32_16x16x32_bf16 v[108:111], a[144:147], a[220:223], v[108:111]\n"
    "ds_read_b64_tr_b16 a[156:157], v30 offset:16904\n"
    "v_lshlrev_b32 v46, 11, v46\n"
    "v_mfma_f32_16x16x32_bf16 v[112:115], a[144:147], a[224:227], v[112:115]\n"
    "s_waitcnt lgkmcnt(3)\n"
    "ds_read_b64_tr_b16 a[158:159], v30 offset:17160\n"
    "v_add_u32 v46, s88, v46\n"
    "v_mfma_f32_16x16x32_bf16 v[116:119], a[148:151], a[208:211], v[116:119]\n"
    "v_add_u32 v46, 0x8000, v46\n"
    "v_bfe_u32 v47, v0, 2, 3\n"
    "v_mfma_f32_16x16x32_bf16 v[120:123], a[148:151], a[212:215], v[120:123]\n"
    "v_lshl_add_u32 v46, v47, 6, v46\n"
    "v_and_b32 v47, 3, v0\n"
    "v_mfma_f32_16x16x32_bf16 v[124:127], a[148:151], a[216:219], v[124:127]\n"
    "buffer_load_dwordx4 v8, s[44:47], s86 offen nt lds\n"
    "v_mfma_f32_16x16x32_bf16 v[128:131], a[148:151], a[220:223], v[128:131]\n"
    "ds_read_b64_tr_b16 a[160:161], v30 offset:17408\n"
    "v_lshl_add_u32 v46, v47, 3, v46\n"
    "v_mfma_f32_16x16x32_bf16 v[132:135], a[148:151], a[224:227], v[132:135]\n"
    "s_waitcnt lgkmcnt(1)\n"
    "ds_read_b64_tr_b16 a[162:163], v30 offset:17664\n"
    "v_bfe_u32 v47, v0, 4, 1\n"
    "v_mfma_f32_16x16x32_bf16 v[136:139], a[152:155], a[208:211], v[136:139]\n"
    "v_lshl_add_u32 v6, v47, 5, v46\n"
    "v_xor_b32 v7, 32, v6\n"
    "v_mfma_f32_16x16x32_bf16 v[140:143], a[152:155], a[212:215], v[140:143]\n"
    "v_mov_b32 v46, 0x3f803f80\n"
    "v_accvgpr_write_b32 a132, v46\n"
    "v_mfma_f32_16x16x32_bf16 v[144:147], a[152:155], a[216:219], v[144:147]\n"
    "v_accvgpr_write_b32 a133, v46\n"
    "v_accvgpr_write_b32 a134, v46\n"
    "v_mfma_f32_16x16x32_bf16 v[148:151], a[152:155], a[220:223], v[148:151]\n"
    "ds_read_b64_tr_b16 a[164:165], v30 offset:17416\n"
    "v_accvgpr_write_b32 a135, v46\n"
    "v_mfma_f32_16x16x32_bf16 v[152:155], a[152:155], a[224:227], v[152:155]\n"
    "ds_read_b64_tr_b16 a[166:167], v30 offset:17672\n"
    "v_add_u32 v44, 0, v0\n"
    "v_mfma_f32_16x16x32_bf16 v[156:159], a[156:159], a[208:211], v[156:159]\n"
    "v_mul_u32_u24 v45, 0xccd, v44\n"
    "v_lshrrev_b32 v45, 16, v45\n"
    "v_mfma_f32_16x16x32_bf16 v[160:163], a[156:159], a[212:215], v[160:163]\n"
    "buffer_load_dwordx4 v9, s[44:47], s86 offen offset:1024 nt lds\n"
    "v_mfma_f32_16x16x32_bf16 v[164:167], a[156:159], a[216:219], v[164:167]\n"
    "v_mul_u32_u24 v46, 20, v45\n"
    "v_sub_u32 v46, v44, v46\n"
    "v_mfma_f32_16x16x32_bf16 v[168:171], a[156:159], a[220:223], v[168:171]\n"
    "ds_read_b64_tr_b16 a[168:169], v30 offset:17920\n"
    "v_lshrrev_b32 v47, 1, v45\n"
    "v_mfma_f32_16x16x32_bf16 v[172:175], a[156:159], a[224:227], v[172:175]\n"
    "s_waitcnt lgkmcnt(1)\n"
    "ds_read_b64_tr_b16 a[170:171], v30 offset:18176\n"
    "v_and_b32 v47, 2, v47\n"
    "v_mfma_f32_16x16x32_bf16 v[176:179], a[160:163], a[208:211], v[176:179]\n"
    "ds_read_b64_tr_b16 a[172:173], v30 offset:17928\n"
    "v_xor_b32 v46, v46, v47\n"
    "v_mfma_f32_16x16x32_bf16 v[180:183], a[160:163], a[212:215], v[180:183]\n"
    "ds_read_b64_tr_b16 a[174:175], v30 offset:18184\n"
    "v_mul_u32_u24 v45, 0x500, v45\n"
    "v_mfma_f32_16x16x32_bf16 v[184:187], a[160:163], a[216:219], v[184:187]\n"
    "v_lshl_add_u32 v45, v46, 4, v45\n"
    "v_add_u32 v23, 0xc00, v45\n"
    "v_mfma_f32_16x16x32_bf16 v[188:191], a[160:163], a[220:223], v[188:191]\n"
    "v_add_u32 v44, 64, v0\n"
    "v_mul_u32_u24 v45, 0xccd, v44\n"
    "v_mfma_f32_16x16x32_bf16 v[192:195], a[160:163], a[224:227], v[192:195]\n"
    "v_lshrrev_b32 v45, 16, v45\n"
    "v_mul_u32_u24 v46, 20, v45\n"
    "v_mfma_f32_16x16x32_bf16 v[196:199], a[164:167], a[208:211], v[196:199]\n"
    "buffer_load_dwordx4 v10, s[44:47], s86 offen offset:2048 nt lds\n"
    "v_mfma_f32_16x16x32_bf16 v[200:203], a[164:167], a[212:215], v[200:203]\n"
    "v_sub_u32 v46, v44, v46\n"
    "v_lshrrev_b32 v47, 1, v45\n"
    "v_mfma_f32_16x16x32_bf16 v[204:207], a[164:167], a[216:219], v[204:207]\n"
    "v_and_b32 v47, 2, v47\n"
    "v_xor_b32 v46, v46, v47\n"
    "v_mfma_f32_16x16x32_bf16 v[208:211], a[164:167], a[220:223], v[208:211]\n"
    "v_mul_u32_u24 v45, 0x500, v45\n"
    "v_lshl_add_u32 v45, v46, 4, v45\n"
    "v_mfma_f32_16x16x32_bf16 v[212:215], a[164:167], a[224:227], v[212:215]\n"
    "s_waitcnt vmcnt(3) lgkmcnt(0)\n"
    "s_barrier\n"
    "v_mfma_f32_16x16x32_bf16 v[216:219], a[168:171], a[208:211], v[216:219]\n"
    "ds_read_b128 a[228:231], v29\n"
    "v_add_u32 v24, 0x800, v45\n"
    "v_mfma_f32_16x16x32_bf16 v[220:223], a[168:171], a[212:215], v[220:223]\n"
    "ds_read_b64_tr_b16 a[176:177], v30 offset:32768\n"
    "v_add_u32 v44, 0x80, v0\n"
    "v_mfma_f32_16x16x32_bf16 v[224:227], a[168:171], a[216:219], v[224:227]\n"
    "ds_read_b64_tr_b16 a[178:179], v30 offset:33024\n"
    "v_mul_u32_u24 v45, 0xccd, v44\n"
    "v_mfma_f32_16x16x32_bf16 v[228:231], a[168:171], a[220:223], v[228:231]\n"
    "ds_read_b128 a[232:235], v29 offset:2048\n"
    "v_lshrrev_b32 v45, 16, v45\n"
    "v_mfma_f32_16x16x32_bf16 v[232:235], a[168:171], a[224:227], v[232:235]\n"
    "ds_read_b128 a[236:239], v29 offset:4096\n"
    "v_mul_u32_u24 v46, 20, v45\n"
    "v_mfma_f32_16x16x32_bf16 v[236:239], a[172:175], a[208:211], v[236:239]\n"
    "ds_read_b128 a[240:243], v29 offset:6144\n"
    "v_sub_u32 v46, v44, v46\n"
    "v_mfma_f32_16x16x32_bf16 v[240:243], a[172:175], a[212:215], v[240:243]\n"
    "ds_read_b128 a[244:247], v29 offset:8192\n"
    "v_lshrrev_b32 v47, 1, v45\n"
    "v_mfma_f32_16x16x32_bf16 v[244:247], a[172:175], a[216:219], v[244:247]\n"
    "ds_read_b64_tr_b16 a[180:181], v30 offset:32776\n"
    "v_and_b32 v47, 2, v47\n"
    "v_mfma_f32_16x16x32_bf16 v[248:251], a[172:175], a[220:223], v[248:251]\n"
    "ds_read_b64_tr_b16 a[182:183], v30 offset:33032\n"
    "v_xor_b32 v46, v46, v47\n"
    "v_mfma_f32_16x16x32_bf16 v[252:255], a[172:175], a[224:227], v[252:255]\n"
    "s_waitcnt lgkmcnt(4)\n"
    "ds_read_b64_tr_b16 a[184:185], v30 offset:33280\n"
    "v_mul_u32_u24 v45, 0x500, v45\n"
    "v_mfma_f32_16x16x32_bf16 v[96:99], a[176:179], a[228:231], v[96:99]\n"
    "ds_read_b64_tr_b16 a[186:187], v30 offset:33536\n"
    "v_lshl_add_u32 v45, v46, 4, v45\n"
    "v_mfma_f32_16x16x32_bf16 v[100:103], a[176:179], a[232:235], v[100:103]\n"
    "buffer_load_dwordx4 v11, s[44:47], s86 offen offset:3072 nt lds\n"
    "v_mfma_f32_16x16x32_bf16 v[104:107], a[176:179], a[236:239], v[104:107]\n"
    "s_waitcnt lgkmcnt(4)\n"
    "v_add_u32 v25, 0x400, v45\n"
    "v_add_u32 v44, 0xc0, v0\n"
    "v_mfma_f32_16x16x32_bf16 v[108:111], a[176:179], a[240:243], v[108:111]\n"
    "s_add_u32 m0, s73, 0x14000\n"
    "s_add_u32 s86, s73, 0x24000\n"
    "ds_read_b64_tr_b16 a[188:189], v30 offset:33288\n"
    "v_mfma_f32_16x16x32_bf16 v[112:115], a[176:179], a[244:247], v[112:115]\n"
    "s_waitcnt lgkmcnt(3)\n"
    "ds_read_b64_tr_b16 a[190:191], v30 offset:33544\n"
    "v_mul_u32_u24 v45, 0xccd, v44\n"
    "v_mfma_f32_16x16x32_bf16 v[116:119], a[180:183], a[228:231], v[116:119]\n"
    "v_lshrrev_b32 v45, 16, v45\n"
    "v_mul_u32_u24 v46, 20, v45\n"
    "v_mfma_f32_16x16x32_bf16 v[120:123], a[180:183], a[232:235], v[120:123]\n"
    "v_sub_u32 v46, v44, v46\n"
    "v_lshrrev_b32 v47, 1, v45\n"
    "v_mfma_f32_16x16x32_bf16 v[124:127], a[180:183], a[236:239], v[124:127]\n"
    "v_and_b32 v47, 2, v47\n"
    "v_xor_b32 v46, v46, v47\n"
    "v_mfma_f32_16x16x32_bf16 v[128:131], a[180:183], a[240:243], v[128:131]\n"
    "ds_read_b64_tr_b16 a[144:145], v30 offset:33792\n"
    "v_mul_u32_u24 v45, 0x500, v45\n"
    "v_mfma_f32_16x16x32_bf16 v[132:135], a[180:183], a[244:247], v[132:135]\n"
    "s_waitcnt lgkmcnt(1)\n"
    "ds_read_b64_tr_b16 a[146:147], v30 offset:34048\n"
    "v_lshl_add_u32 v45, v46, 4, v45\n"
    "v_mfma_f32_16x16x32_bf16 v[136:139], a[184:187], a[228:231], v[136:139]\n"
    "v_add_u32 v26, 0, v45\n"
    "v_add_u32 v44, 0x100, v0\n"
    "v_mfma_f32_16x16x32_bf16 v[140:143], a[184:187], a[232:235], v[140:143]\n"
    "v_mul_u32_u24 v45, 0xccd, v44\n"
    "v_lshrrev_b32 v45, 16, v45\n"
    "v_mfma_f32_16x16x32_bf16 v[144:147], a[184:187], a[236:239], v[144:147]\n"
    "v_mul_u32_u24 v46, 20, v45\n"
    "v_sub_u32 v46, v44, v46\n"
    "v_mfma_f32_16x16x32_bf16 v[148:151], a[184:187], a[240:243], v[148:151]\n"
    "ds_read_b64_tr_b16 a[148:149], v30 offset:33800\n"
    "v_lshrrev_b32 v47, 1, v45\n"
    "v_mfma_f32_16x16x32_bf16 v[152:155], a[184:187], a[244:247], v[152:155]\n"
    "ds_read_b64_tr_b16 a[150:151], v30 offset:34056\n"
    "v_and_b32 v47, 2, v47\n"
    "v_mfma_f32_16x16x32_bf16 v[156:159], a[188:191], a[228:231], v[156:159]\n"
    "v_xor_b32 v46, v46, v47\n"
    "v_mul_u32_u24 v45, 0x500, v45\n"
    "v_mfma_f32_16x16x32_bf16 v[160:163], a[188:191], a[232:235], v[160:163]\n"
    "v_lshl_add_u32 v45, v46, 4, v45\n"
    "v_add_u32 v27, 0xc00, v45\n"
    "v_mfma_f32_16x16x32_bf16 v[164:167], a[188:191], a[236:239], v[164:167]\n"
    "buffer_load_dwordx4 v8, s[44:47], s86 offen nt lds\n"
    "v_mfma_f32_16x16x32_bf16 v[168:171], a[188:191], a[240:243], v[168:171]\n"
    "ds_read_b64_tr_b16 a[152:153], v30 offset:34304\n"
    "ds_read_b64_tr_b16 a[154:155], v30 offset:34560\n"
    "v_mfma_f32_16x16x32_bf16 v[172:175], a[188:191], a[244:247], v[172:175]\n"
    "s_waitcnt lgkmcnt(2)\n"
    "v_mfma_f32_16x16x32_bf16 v[176:179], a[144:147], a[228:231], v[176:179]\n"
    "buffer_load_dwordx4 v9, s[44:47], s86 offen offset:1024 nt lds\n"
    "v_mfma_f32_16x16x32_bf16 v[180:183], a[144:147], a[232:235], v[180:183]\n"
    "buffer_load_dwordx4 v10, s[44:47], s86 offen offset:2048 nt lds\n"
    "v_mfma_f32_16x16x32_bf16 v[184:187], a[144:147], a[236:239], v[184:187]\n"
    "buffer_load_dwordx4 v11, s[44:47], s86 offen offset:3072 nt lds\n"
    "v_mfma_f32_16x16x32_bf16 v[188:191], a[144:147], a[240:243], v[188:191]\n"
    "ds_read_b64_tr_b16 a[156:157], v30 offset:34312\n"
    "ds_read_b64_tr_b16 a[158:159], v30 offset:34568\n"
    "v_mfma_f32_16x16x32_bf16 v[192:195], a[144:147], a[244:247], v[192:195]\n"
    "v_mfma_f32_16x16x32_bf16 v[196:199], a[148:151], a[228:231], v[196:199]\n"
    "v_mfma_f32_16x16x32_bf16 v[200:203], a[148:151], a[232:235], v[200:203]\n"
    "v_mfma_f32_16x16x32_bf16 v[204:207], a[148:151], a[236:239], v[204:207]\n"
    "v_mfma_f32_16x16x32_bf16 v[208:211], a[148:151], a[240:243], v[208:211]\n"
    "v_mfma_f32_16x16x32_bf16 v[212:215], a[148:151], a[244:247], v[212:215]\n"
    "s_waitcnt lgkmcnt(0)\n"
    "v_mfma_f32_16x16x32_bf16 v[216:219], a[152:155], a[228:231], v[216:219]\n"
    "v_mfma_f32_16x16x32_bf16 v[220:223], a[152:155], a[232:235], v[220:223]\n"
    "v_mfma_f32_16x16x32_bf16 v[224:227], a[152:155], a[236:239], v[224:227]\n"
    "v_mfma_f32_16x16x32_bf16 v[228:231], a[152:155], a[240:243], v[228:231]\n"
    "v_mfma_f32_16x16x32_bf16 v[232:235], a[152:155], a[244:247], v[232:235]\n"
    "v_mfma_f32_16x16x32_bf16 v[236:239], a[156:159], a[228:231], v[236:239]\n"
    "v_mfma_f32_16x16x32_bf16 v[240:243], a[156:159], a[232:235], v[240:243]\n"
    "v_mfma_f32_16x16x32_bf16 v[244:247], a[156:159], a[236:239], v[244:247]\n"
    "v_mfma_f32_16x16x32_bf16 v[248:251], a[156:159], a[240:243], v[248:251]\n"
    "v_mfma_f32_16x16x32_bf16 v[252:255], a[156:159], a[244:247], v[252:255]\n"
    "s_branch LJOIN_%=\n"
    "LRX_%=:\n"
    "LQX_%=:\n"
    "v_mfma_f32_16x16x32_bf16 v[216:219], a[184:187], a[228:231], v[216:219]\n"
    "s_add_u32 s60, s60, 0x80\n"
    "s_sub_u32 s79, s61, s79\n"
    "ds_read_b128 a[208:211], v28\n"
    "v_mfma_f32_16x16x32_bf16 v[220:223], a[184:187], a[232:235], v[220:223]\n"
    "s_add_u32 m0, s79, 0\n"
    "s_add_u32 s76, s60, 0\n"
    "ds_read_b64_tr_b16 a[144:145], v30 offset:16384\n"
    "v_mfma_f32_16x16x32_bf16 v[224:227], a[184:187], a[236:239], v[224:227]\n"
    "ds_read_b64_tr_b16 a[146:147], v30 offset:16640\n"
    "ds_read_b128 a[212:215], v28 offset:2048\n"
    "v_mfma_f32_16x16x32_bf16 v[228:231], a[184:187], a[240:243], v[228:231]\n"
    "ds_read_b128 a[216:219], v28 offset:4096\n"
    "v_mfma_f32_16x16x32_bf16 v[232:235], a[184:187], a[244:247], v[232:235]\n"
    "ds_read_b128 a[220:223], v28 offset:6144\n"
    "v_mfma_f32_16x16x32_bf16 v[236:239], a[188:191], a[228:231], v[236:239]\n"
    "ds_read_b128 a[224:227], v28 offset:8192\n"
    "ds_read_b64_tr_b16 a[148:149], v30 offset:16392\n"
    "v_add_u32 v28, s81, v28\n"
    "v_mfma_f32_16x16x32_bf16 v[240:243], a[188:191], a[232:235], v[240:243]\n"
    "ds_read_b64_tr_b16 a[150:151], v30 offset:16648\n"
    "ds_read_b64_tr_b16 a[152:153], v30 offset:16896\n"
    "v_mfma_f32_16x16x32_bf16 v[244:247], a[188:191], a[236:239], v[244:247]\n"
    "buffer_load_dwordx4 v36, s[36:39], s76 offen lds\n"
    "v_mfma_f32_16x16x32_bf16 v[248:251], a[188:191], a[240:243], v[248:251]\n"
    "ds_read_b64_tr_b16 a[154:155], v30 offset:17152\n"
    "v_mfma_f32_16x16x32_bf16 v[252:255], a[188:191], a[244:247], v[252:255]\n"
    "s_waitcnt lgkmcnt(5)\n"
    "v_mfma_f32_16x16x32_bf16 v[96:99], a[144:147], a[208:211], v[96:99]\n"
    "buffer_load_dwordx4 v37, s[36:39], s76 offen offset:1024 lds\n"
    "v_mfma_f32_16x16x32_bf16 v[100:103], a[144:147], a[212:215], v[100:103]\n"
    "buffer_load_dwordx4 v38, s[36:39], s76 offen offset:2048 lds\n"
    "v_mfma_f32_16x16x32_bf16 v[104:107], a[144:147], a[216:219], v[104:107]\n"
    "v_mfma_f32_16x16x32_bf16 v[108:111], a[144:147], a[220:223], v[108:111]\n"
    "s_waitcnt lgkmcnt(0)\n"
    "ds_read_b64_tr_b16 a[156:157], v30 offset:16904\n"
    "ds_read_b64_tr_b16 a[158:159], v30 offset:17160\n"
    "v_mfma_f32_16x16x32_bf16 v[112:115], a[144:147], a[224:227], v[112:115]\n"
    "buffer_load_dwordx4 v39, s[36:39], s76 offen offset:3072 lds\n"
    "v_mfma_f32_16x16x32_bf16 v[116:119], a[148:151], a[208:211], v[116:119]\n"
    "s_add_u32 m0, s79, 0x1000\n"
    "s_add_u32 s76, s60, 0xa000\n"
    "v_mfma_f32_16x16x32_bf16 v[120:123], a[148:151], a[212:215], v[120:123]\n"
    "buffer_load_dwordx4 v36, s[36:39], s76 offen lds\n"
    "v_mfma_f32_16x16x32_bf16 v[124:127], a[148:151], a[216:219], v[124:127]\n"
    "v_mfma_f32_16x16x32_bf16 v[128:131], a[148:151], a[220:223], v[128:131]\n"
    "ds_read_b64_tr_b16 a[160:161], v30 offset:17408\n"
    "ds_read_b64_tr_b16 a[162:163], v30 offset:17664\n"
    "v_mfma_f32_16x16x32_bf16 v[132:135], a[148:151], a[224:227], v[132:135]\n"
    "buffer_load_dwordx4 v37, s[36:39], s76 offen offset:1024 lds\n"
    "v_mfma_f32_16x16x32_bf16 v[136:139], a[152:155], a[208:211], v[136:139]\n"
    "v_mfma_f32_16x16x32_bf16 v[140:143], a[152:155], a[212:215], v[140:143]\n"
    "v_mfma_f32_16x16x32_bf16 v[144:147], a[152:155], a[216:219], v[144:147]\n"
    "buffer_load_dwordx4 v38, s[36:39], s76 offen offset:2048 lds\n"
    "v_mfma_f32_16x16x32_bf16 v[148:151], a[152:155], a[220:223], v[148:151]\n"
    "ds_read_b64_tr_b16 a[164:165], v30 offset:17416\n"
    "ds_read_b64_tr_b16 a[166:167], v30 offset:17672\n"
    "v_mfma_f32_16x16x32_bf16 v[152:155], a[152:155], a[224:227], v[152:155]\n"
    "s_waitcnt lgkmcnt(2)\n"
    "v_mfma_f32_16x16x32_bf16 v[156:159], a[156:159], a[208:211], v[156:159]\n"
    "buffer_load_dwordx4 v39, s[36:39], s76 offen offset:3072 lds\n"
    "v_mfma_f32_16x16x32_bf16 v[160:163], a[156:159], a[212:215], v[160:163]\n"
    "s_add_u32 m0, s79, 0x2000\n"
    "s_add_u32 s76, s60, 0x14000\n"
    "v_mfma_f32_16x16x32_bf16 v[164:167], a[156:159], a[216:219], v[164:167]\n"
    "v_mfma_f32_16x16x32_bf16 v[168:171], a[156:159], a[220:223], v[168:171]\n"
    "ds_read_b64_tr_b16 a[168:169], v30 offset:17920\n"
    "ds_read_b64_tr_b16 a[170:171], v30 offset:18176\n"
    "v_mfma_f32_16x16x32_bf16 v[172:175], a[156:159], a[224:227], v[172:175]\n"
    "buffer_load_dwordx4 v36, s[36:39], s76 offen lds\n"
    "v_mfma_f32_16x16x32_bf16 v[176:179], a[160:163], a[208:211], v[176:179]\n"
    "ds_read_b64_tr_b16 a[172:173], v30 offset:17928\n"
    "ds_read_b64_tr_b16 a[174:175], v30 offset:18184\n"
    "v_mfma_f32_16x16x32_bf16 v[180:183], a[160:163], a[212:215], v[180:183]\n"
    "buffer_load_dwordx4 v37, s[36:39], s76 offen offset:1024 lds\n"
    "v_mfma_f32_16x16x32_bf16 v[184:187], a[160:163], a[216:219], v[184:187]\n"
    "v_mfma_f32_16x16x32_bf16 v[188:191], a[160:163], a[220:223], v[188:191]\n"
    "v_mfma_f32_16x16x32_bf16 v[192:195], a[160:163], a[224:227], v[192:195]\n"
    "s_waitcnt lgkmcnt(2)\n"
    "v_mfma_f32_16x16x32_bf16 v[196:199], a[164:167], a[208:211], v[196:199]\n"
    "buffer_load_dwordx4 v38, s[36:39], s76 offen offset:2048 lds\n"
    "v_mfma_f32_16x16x32_bf16 v[200:203], a[164:167], a[212:215], v[200:203]\n"
    "v_mfma_f32_16x16x32_bf16 v[204:207], a[164:167], a[216:219], v[204:207]\n"
    "buffer_load_dwordx4 v39, s[36:39], s76 offen offset:3072 lds\n"
    "v_mfma_f32_16x16x32_bf16 v[208:211], a[164:167], a[220:223], v[208:211]\n"
    "v_mfma_f32_16x16x32_bf16 v[212:215], a[164:167], a[224:227], v[212:215]\n"
    "s_waitcnt lgkmcnt(0)\n"
    "s_barrier\n"
    "v_mfma_f32_16x16x32_bf16 v[216:219], a[168:171], a[208:211], v[216:219]\n"
    "ds_read_b128 a[228:231], v29\n"
    "ds_read_b64_tr_b16 a[176:177], v30 offset:32768\n"
    "v_mfma_f32_16x16x32_bf16 v[220:223], a[168:171], a[212:215], v[220:223]\n"
    "ds_read_b64_tr_b16 a[178:179], v30 offset:33024\n"
    "ds_read_b128 a[232:235], v29 offset:2048\n"
    "v_mfma_f32_16x16x32_bf16 v[224:227], a[168:171], a[216:219], v[224:227]\n"
    "ds_read_b128 a[236:239], v29 offset:4096\n"
    "v_mfma_f32_16x16x32_bf16 v[228:231], a[168:171], a[220:223], v[228:231]\n"
    "ds_read_b128 a[240:243], v29 offset:6144\n"
    "v_mfma_f32_16x16x32_bf16 v[232:235], a[168:171], a[224:227], v[232:235]\n"
    "ds_read_b128 a[244:247], v29 offset:8192\n"
    "ds_read_b64_tr_b16 a[180:181], v30 offset:32776\n"
    "v_add_u32 v29, s81, v29\n"
    "v_mfma_f32_16x16x32_bf16 v[236:239], a[172:175], a[208:211], v[236:239]\n"
    "ds_read_b64_tr_b16 a[182:183], v30 offset:33032\n"
    "v_mfma_f32_16x16x32_bf16 v[240:243], a[172:175], a[212:215], v[240:243]\n"
    "v_mfma_f32_16x16x32_bf16 v[244:247], a[172:175], a[216:219], v[244:247]\n"
    "v_mfma_f32_16x16x32_bf16 v[248:251], a[172:175], a[220:223], v[248:251]\n"
    "ds_read_b64_tr_b16 a[184:185], v30 offset:33280\n"
    "ds_read_b64_tr_b16 a[186:187], v30 offset:33536\n"
    "v_mfma_f32_16x16x32_bf16 v[252:255], a[172:175], a[224:227], v[252:255]\n"
    "s_waitcnt lgkmcnt(4)\n"
    "v_mfma_f32_16x16x32_bf16 v[96:99], a[176:179], a[228:231], v[96:99]\n"
    "v_mfma_f32_16x16x32_bf16 v[100:103], a[176:179], a[232:235], v[100:103]\n"
    "v_mfma_f32_16x16x32_bf16 v[104:107], a[176:179], a[236:239], v[104:107]\n"
    "v_mfma_f32_16x16x32_bf16 v[108:111], a[176:179], a[240:243], v[108:111]\n"
    "ds_read_b64_tr_b16 a[188:189], v30 offset:33288\n"
    "ds_read_b64_tr_b16 a[190:191], v30 offset:33544\n"
    "v_mfma_f32_16x16x32_bf16 v[112:115], a[176:179], a[244:247], v[112:115]\n"
    "s_waitcnt lgkmcnt(2)\n"
    "v_mfma_f32_16x16x32_bf16 v[116:119], a[180:183], a[228:231], v[116:119]\n"
    "v_mfma_f32_16x16x32_bf16 v[120:123], a[180:183], a[232:235], v[120:123]\n"
    "v_mfma_f32_16x16x32_bf16 v[124:127], a[180:183], a[236:239], v[124:127]\n"
    "v_mfma_f32_16x16x32_bf16 v[128:131], a[180:183], a[240:243], v[128:131]\n"
    "ds_read_b64_tr_b16 a[144:145], v30 offset:33792\n"
    "ds_read_b64_tr_b16 a[146:147], v30 offset:34048\n"
    "v_mfma_f32_16x16x32_bf16 v[132:135], a[180:183], a[244:247], v[132:135]\n"
    "v_mfma_f32_16x16x32_bf16 v[136:139], a[184:187], a[228:231], v[136:139]\n"
    "v_mfma_f32_16x16x32_bf16 v[140:143], a[184:187], a[232:235], v[140:143]\n"
    "v_mfma_f32_16x16x32_bf16 v[144:147], a[184:187], a[236:239], v[144:147]\n"
    "v_mfma_f32_16x16x32_bf16 v[148:151], a[184:187], a[240:243], v[148:151]\n"
    "ds_read_b64_tr_b16 a[148:149], v30 offset:33800\n"
    "ds_read_b64_tr_b16 a[150:151], v30 offset:34056\n"
    "v_mfma_f32_16x16x32_bf16 v[152:155], a[184:187], a[244:247], v[152:155]\n"
    "s_waitcnt lgkmcnt(2)\n"
    "v_mfma_f32_16x16x32_bf16 v[156:159], a[188:191], a[228:231], v[156:159]\n"
    "v_mfma_f32_16x16x32_bf16 v[160:163], a[188:191], a[232:235], v[160:163]\n"
    "v_mfma_f32_16x16x32_bf16 v[164:167], a[188:191], a[236:239], v[164:167]\n"
    "v_mfma_f32_16x16x32_bf16 v[168:171], a[188:191], a[240:243], v[168:171]\n"
    "ds_read_b64_tr_b16 a[152:153], v30 offset:34304\n"
    "ds_read_b64_tr_b16 a[154:155], v30 offset:34560\n"
    "v_mfma_f32_16x16x32_bf16 v[172:175], a[188:191], a[244:247], v[172:175]\n"
    "v_mfma_f32_16x16x32_bf16 v[176:179], a[144:147], a[228:231], v[176:179]\n"
    "ds_read_b64_tr_b16 a[156:157], v30 offset:34312\n"
    "ds_read_b64_tr_b16 a[158:159], v30 offset:34568\n"
    "v_mfma_f32_16x16x32_bf16 v[180:183], a[144:147], a[232:235], v[180:183]\n"
    "v_mfma_f32_16x16x32_bf16 v[184:187], a[144:147], a[236:239], v[184:187]\n"
    "v_mfma_f32_16x16x32_bf16 v[188:191], a[144:147], a[240:243], v[188:191]\n"
    "v_mfma_f32_16x16x32_bf16 v[192:195], a[144:147], a[244:247], v[192:195]\n"
    "s_waitcnt lgkmcnt(2)\n"
    "v_mfma_f32_16x16x32_bf16 v[196:199], a[148:151], a[228:231], v[196:199]\n"
    "v_mfma_f32_16x16x32_bf16 v[200:203], a[148:151], a[232:235], v[200:203]\n"
    "v_mfma_f32_16x16x32_bf16 v[204:207], a[148:151], a[236:239], v[204:207]\n"
    "v_mfma_f32_16x16x32_bf16 v[208:211], a[148:151], a[240:243], v[208:211]\n"
    "v_mfma_f32_16x16x32_bf16 v[212:215], a[148:151], a[244:247], v[212:215]\n"
    "s_waitcnt vmcnt(0) lgkmcnt(0)\n"
    "s_barrier\n"
    "v_mfma_f32_16x16x32_bf16 v[216:219], a[152:155], a[228:231], v[216:219]\n"
    "s_add_u32 s60, s60, 0x80\n"
    "s_sub_u32 s79, s61, s79\n"
    "ds_read_b128 a[208:211], v28\n"
    "v_mfma_f32_16x16x32_bf16 v[220:223], a[152:155], a[232:235], v[220:223]\n"
    "s_add_u32 m0, s79, 0\n"
    "s_add_u32 s76, s60, 0\n"
    "ds_read_b64_tr_b16 a[160:161], v30\n"
    "v_mfma_f32_16x16x32_bf16 v[224:227], a[152:155], a[236:239], v[224:227]\n"
    "ds_read_b64_tr_b16 a[162:163], v30 offset:256\n"
    "ds_read_b128 a[212:215], v28 offset:2048\n"
    "v_mfma_f32_16x16x32_bf16 v[228:231], a[152:155], a[240:243], v[228:231]\n"
    "ds_read_b128 a[216:219], v28 offset:4096\n"
    "v_mfma_f32_16x16x32_bf16 v[232:235], a[152:155], a[244:247], v[232:235]\n"
    "ds_read_b128 a[220:223], v28 offset:6144\n"
    "v_mfma_f32_16x16x32_bf16 v[236:239], a[156:159], a[228:231], v[236:239]\n"
    "ds_read_b128 a[224:227], v28 offset:8192\n"
    "ds_read_b64_tr_b16 a[164:165], v30 offset:8\n"
    "v_subrev_u32 v28, s81, v28\n"
    "v_mfma_f32_16x16x32_bf16 v[240:243], a[156:159], a[232:235], v[240:243]\n"
    "ds_read_b64_tr_b16 a[166:167], v30 offset:264\n"
    "ds_read_b64_tr_b16 a[168:169], v30 offset:512\n"
    "v_mfma_f32_16x16x32_bf16 v[244:247], a[156:159], a[236:239], v[244:247]\n"
    "buffer_load_dwordx4 v36, s[36:39], s76 offen lds\n"
    "v_mfma_f32_16x16x32_bf16 v[248:251], a[156:159], a[240:243], v[248:251]\n"
    "ds_read_b64_tr_b16 a[170:171], v30 offset:768\n"
    "v_mfma_f32_16x16x32_bf16 v[252:255], a[156:159], a[244:247], v[252:255]\n"
    "s_waitcnt lgkmcnt(5)\n"
    "v_mfma_f32_16x16x32_bf16 v[96:99], a[160:163], a[208:211], v[96:99]\n"
    "buffer_load_dwordx4 v37, s[36:39], s76 offen offset:1024 lds\n"
    "v_mfma_f32_16x16x32_bf16 v[100:103], a[160:163], a[212:215], v[100:103]\n"
    "buffer_load_dwordx4 v38, s[36:39], s76 offen offset:2048 lds\n"
    "v_mfma_f32_16x16x32_bf16 v[104:107], a[160:163], a[216:219], v[104:107]\n"
    "v_mfma_f32_16x16x32_bf16 v[108:111], a[160:163], a[220:223], v[108:111]\n"
    "s_waitcnt lgkmcnt(0)\n"
    "ds_read_b64_tr_b16 a[172:173], v30 offset:520\n"
    "ds_read_b64_tr_b16 a[174:175], v30 offset:776\n"
    "v_mfma_f32_16x16x32_bf16 v[112:115], a[160:163], a[224:227], v[112:115]\n"
    "buffer_load_dwordx4 v39, s[36:39], s76 offen offset:3072 lds\n"
    "v_mfma_f32_16x16x32_bf16 v[116:119], a[164:167], a[208:211], v[116:119]\n"
    "s_add_u32 m0, s79, 0x1000\n"
    "s_add_u32 s76, s60, 0xa000\n"
    "v_mfma_f32_16x16x32_bf16 v[120:123], a[164:167], a[212:215], v[120:123]\n"
    "buffer_load_dwordx4 v36, s[36:39], s76 offen lds\n"
    "v_mfma_f32_16x16x32_bf16 v[124:127], a[164:167], a[216:219], v[124:127]\n"
    "v_mfma_f32_16x16x32_bf16 v[128:131], a[164:167], a[220:223], v[128:131]\n"
    "ds_read_b64_tr_b16 a[176:177], v30 offset:1024\n"
    "ds_read_b64_tr_b16 a[178:179], v30 offset:1280\n"
    "v_mfma_f32_16x16x32_bf16 v[132:135], a[164:167], a[224:227], v[132:135]\n"
    "buffer_load_dwordx4 v37, s[36:39], s76 offen offset:1024 lds\n"
    "v_mfma_f32_16x16x32_bf16 v[136:139], a[168:171], a[208:211], v[136:139]\n"
    "v_mfma_f32_16x16x32_bf16 v[140:143], a[168:171], a[212:215], v[140:143]\n"
    "v_mfma_f32_16x16x32_bf16 v[144:147], a[168:171], a[216:219], v[144:147]\n"
    "buffer_load_dwordx4 v38, s[36:39], s76 offen offset:2048 lds\n"
    "v_mfma_f32_16x16x32_bf16 v[148:151], a[168:171], a[220:223], v[148:151]\n"
    "ds_read_b64_tr_b16 a[180:181], v30 offset:1032\n"
    "ds_read_b64_tr_b16 a[182:183], v30 offset:1288\n"
    "v_mfma_f32_16x16x32_bf16 v[152:155], a[168:171], a[224:227], v[152:155]\n"
    "s_waitcnt lgkmcnt(2)\n"
    "v_mfma_f32_16x16x32_bf16 v[156:159], a[172:175], a[208:211], v[156:159]\n"
    "buffer_load_dwordx4 v39, s[36:39], s76 offen offset:3072 lds\n"
    "v_mfma_f32_16x16x32_bf16 v[160:163], a[172:175], a[212:215], v[160:163]\n"
    "s_add_u32 m0, s79, 0x2000\n"
    "s_add_u32 s76, s60, 0x14000\n"
    "v_mfma_f32_16x16x32_bf16 v[164:167], a[172:175], a[216:219], v[164:167]\n"
    "v_mfma_f32_16x16x32_bf16 v[168:171], a[172:175], a[220:223], v[168:171]\n"
    "ds_read_b64_tr_b16 a[184:185], v30 offset:1536\n"
    "ds_read_b64_tr_b16 a[186:187], v30 offset:1792\n"
    "v_mfma_f32_16x16x32_bf16 v[172:175], a[172:175], a[224:227], v[172:175]\n"
    "buffer_load_dwordx4 v36, s[36:39], s76 offen lds\n"
    "v_mfma_f32_16x16x32_bf16 v[176:179], a[176:179], a[208:211], v[176:179]\n"
    "ds_read_b64_tr_b16 a[188:189], v30 offset:1544\n"
    "ds_read_b64_tr_b16 a[190:191], v30 offset:1800\n"
    "v_mfma_f32_16x16x32_bf16 v[180:183], a[176:179], a[212:215], v[180:183]\n"
    "buffer_load_dwordx4 v37, s[36:39], s76 offen offset:1024 lds\n"
    "v_mfma_f32_16x16x32_bf16 v[184:187], a[176:179], a[216:219], v[184:187]\n"
    "v_mfma_f32_16x16x32_bf16 v[188:191], a[176:179], a[220:223], v[188:191]\n"
    "v_mfma_f32_16x16x32_bf16 v[192:195], a[176:179], a[224:227], v[192:195]\n"
    "s_waitcnt lgkmcnt(2)\n"
    "v_mfma_f32_16x16x32_bf16 v[196:199], a[180:183], a[208:211], v[196:199]\n"
    "buffer_load_dwordx4 v38, s[36:39], s76 offen offset:2048 lds\n"
    "v_mfma_f32_16x16x32_bf16 v[200:203], a[180:183], a[212:215], v[200:203]\n"
    "v_mfma_f32_16x16x32_bf16 v[204:207], a[180:183], a[216:219], v[204:207]\n"
    "buffer_load_dwordx4 v39, s[36:39], s76 offen offset:3072 lds\n"
    "v_mfma_f32_16x16x32_bf16 v[208:211], a[180:183], a[220:223], v[208:211]\n"
    "v_mfma_f32_16x16x32_bf16 v[212:215], a[180:183], a[224:227], v[212:215]\n"
    "s_waitcnt lgkmcnt(0)\n"
    "s_barrier\n"
    "v_mfma_f32_16x16x32_bf16 v[216:219], a[184:187], a[208:211], v[216:219]\n"
    "ds_read_b128 a[228:231], v29\n"
    "ds_read_b64_tr_b16 a[144:145], v30 offset:16384\n"
    "v_mfma_f32_16x16x32_bf16 v[220:223], a[184:187], a[212:215], v[220:223]\n"
    "ds_read_b64_tr_b16 a[146:147], v30 offset:16640\n"
    "ds_read_b128 a[232:235], v29 offset:2048\n"
    "v_mfma_f32_16x16x32_bf16 v[224:227], a[184:187], a[216:219], v[224:227]\n"
    "ds_read_b128 a[236:239], v29 offset:4096\n"
    "v_mfma_f32_16x16x32_bf16 v[228:231], a[184:187], a[220:223], v[228:231]\n"
    "ds_read_b128 a[240:243], v29 offset:6144\n"
    "v_mfma_f32_16x16x32_bf16 v[232:235], a[184:187], a[224:227], v[232:235]\n"
    "ds_read_b128 a[244:247], v29 offset:8192\n"
    "ds_read_b64_tr_b16 a[148:149], v30 offset:16392\n"
    "v_subrev_u32 v29, s81, v29\n"
    "v_mfma_f32_16x16x32_bf16 v[236:239], a[188:191], a[208:211], v[236:239]\n"
    "ds_read_b64_tr_b16 a[150:151], v30 offset:16648\n"
    "v_mfma_f32_16x16x32_bf16 v[240:243], a[188:191], a[212:215], v[240:243]\n"
    "v_mfma_f32_16x16x32_bf16 v[244:247], a[188:191], a[216:219], v[244:247]\n"
    "v_mfma_f32_16x16x32_bf16 v[248:251], a[188:191], a[220:223], v[248:251]\n"
    "ds_read_b64_tr_b16 a[152:153], v30 offset:16896\n"
    "ds_read_b64_tr_b16 a[154:155], v30 offset:17152\n"
    "v_mfma_f32_16x16x32_bf16 v[252:255], a[188:191], a[224:227], v[252:255]\n"
    "s_waitcnt lgkmcnt(4)\n"
    "v_mfma_f32_16x16x32_bf16 v[96:99], a[144:147], a[228:231], v[96:99]\n"
    "v_mfma_f32_16x16x32_bf16 v[100:103], a[144:147], a[232:235], v[100:103]\n"
    "v_mfma_f32_16x16x32_bf16 v[104:107], a[144:147], a[236:239], v[104:107]\n"
    "v_mfma_f32_16x16x32_bf16 v[108:111], a[144:147], a[240:243], v[108:111]\n"
    "ds_read_b64_tr_b16 a[156:157], v30 offset:16904\n"
    "ds_read_b64_tr_b16 a[158:159], v30 offset:17160\n"
    "v_mfma_f32_16x16x32_bf16 v[112:115], a[144:147], a[244:247], v[112:115]\n"
    "s_waitcnt lgkmcnt(2)\n"
    "v_mfma_f32_16x16x32_bf16 v[116:119], a[148:151], a[228:231], v[116:119]\n"
    "v_mfma_f32_16x16x32_bf16 v[120:123], a[148:151], a[232:235], v[120:123]\n"
    "v_mfma_f32_16x16x32_bf16 v[124:127], a[148:151], a[236:239], v[124:127]\n"
    "v_mfma_f32_16x16x32_bf16 v[128:131], a[148:151], a[240:243], v[128:131]\n"
    "ds_read_b64_tr_b16 a[160:161], v30 offset:17408\n"
    "ds_read_b64_tr_b16 a[162:163], v30 offset:17664\n"
    "v_mfma_f32_16x16x32_bf16 v[132:135], a[148:151], a[244:247], v[132:135]\n"
    "v_mfma_f32_16x16x32_bf16 v[136:139], a[152:155], a[228:231], v[136:139]\n"
    "v_mfma_f32_16x16x32_bf16 v[140:143], a[152:155], a[232:235], v[140:143]\n"
    "v_mfma_f32_16x16x32_bf16 v[144:147], a[152:155], a[236:239], v[144:147]\n"
    "v_mfma_f32_16x16x32_bf16 v[148:151], a[152:155], a[240:243], v[148:151]\n"
    "ds_read_b64_tr_b16 a[164:165], v30 offset:17416\n"
    "ds_read_b64_tr_b16 a[166:167], v30 offset:17672\n"
    "v_mfma_f32_16x16x32_bf16 v[152:155], a[152:155], a[244:247], v[152:155]\n"
    "s_waitcnt lgkmcnt(2)\n"
    "v_mfma_f32_16x16x32_bf16 v[156:159], a[156:159], a[228:231], v[156:159]\n"
    "v_mfma_f32_16x16x32_bf16 v[160:163], a[156:159], a[232:235], v[160:163]\n"
    "v_mfma_f32_16x16x32_bf16 v[164:167], a[156:159], a[236:239], v[164:167]\n"
    "v_mfma_f32_16x16x32_bf16 v[168:171], a[156:159], a[240:243], v[168:171]\n"
    "ds_read_b64_tr_b16 a[168:169], v30 offset:17920\n"
    "ds_read_b64_tr_b16 a[170:171], v30 offset:18176\n"
    "v_mfma_f32_16x16x32_bf16 v[172:175], a[156:159], a[244:247], v[172:175]\n"
    "v_mfma_f32_16x16x32_bf16 v[176:179], a[160:163], a[228:231], v[176:179]\n"
    "ds_read_b64_tr_b16 a[172:173], v30 offset:17928\n"
    "ds_read_b64_tr_b16 a[174:175], v30 offset:18184\n"
    "v_mfma_f32_16x16x32_bf16 v[180:183], a[160:163], a[232:235], v[180:183]\n"
    "v_mfma_f32_16x16x32_bf16 v[184:187], a[160:163], a[236:239], v[184:187]\n"
    "v_mfma_f32_16x16x32_bf16 v[188:191], a[160:163], a[240:243], v[188:191]\n"
    "v_mfma_f32_16x16x32_bf16 v[192:195], a[160:163], a[244:247], v[192:195]\n"
    "s_waitcnt lgkmcnt(2)\n"
    "v_mfma_f32_16x16x32_bf16 v[196:199], a[164:167], a[228:231], v[196:199]\n"
    "v_mfma_f32_16x16x32_bf16 v[200:203], a[164:167], a[232:235], v[200:203]\n"
    "v_mfma_f32_16x16x32_bf16 v[204:207], a[164:167], a[236:239], v[204:207]\n"
    "v_mfma_f32_16x16x32_bf16 v[208:211], a[164:167], a[240:243], v[208:211]\n"
    "v_mfma_f32_16x16x32_bf16 v[212:215], a[164:167], a[244:247], v[212:215]\n"
    "s_waitcnt vmcnt(0) lgkmcnt(0)\n"
    "s_barrier\n"
    "v_mfma_f32_16x16x32_bf16 v[216:219], a[168:171], a[228:231], v[216:219]\n"
    "s_add_u32 s60, s60, 0x80\n"
    "s_sub_u32 s79, s61, s79\n"
    "ds_read_b128 a[208:211], v28\n"
    "v_mfma_f32_16x16x32_bf16 v[220:223], a[168:171], a[232:235], v[220:223]\n"
    "s_add_u32 m0, s79, 0\n"
    "s_add_u32 s76, s60, 0\n"
    "ds_read_b64_tr_b16 a[176:177], v30 offset:32768\n"
    "v_mfma_f32_16x16x32_bf16 v[224:227], a[168:171], a[236:239], v[224:227]\n"
    "ds_read_b64_tr_b16 a[178:179], v30 offset:33024\n"
    "ds_read_b128 a[212:215], v28 offset:2048\n"
    "v_mfma_f32_16x16x32_bf16 v[228:231], a[168:171], a[240:243], v[228:231]\n"
    "ds_read_b128 a[216:219], v28 offset:4096\n"
    "v_mfma_f32_16x16x32_bf16 v[232:235], a[168:171], a[244:247], v[232:235]\n"
    "ds_read_b128 a[220:223], v28 offset:6144\n"
    "v_mfma_f32_16x16x32_bf16 v[236:239], a[172:175], a[228:231], v[236:239]\n"
    "ds_read_b128 a[224:227], v28 offset:8192\n"
    "ds_read_b64_tr_b16 a[180:181], v30 offset:32776\n"
    "v_add_u32 v28, s81, v28\n"
    "v_mfma_f32_16x16x32_bf16 v[240:243], a[172:175], a[232:235], v[240:243]\n"
    "ds_read_b64_tr_b16 a[182:183], v30 offset:33032\n"
    "ds_read_b64_tr_b16 a[184:185], v30 offset:33280\n"
    "v_mfma_f32_16x16x32_bf16 v[244:247], a[172:175], a[236:239], v[244:247]\n"
    "buffer_load_dwordx4 v36, s[36:39], s76 offen lds\n"
    "v_mfma_f32_16x16x32_bf16 v[248:251], a[172:175], a[240:243], v[248:251]\n"
    "ds_read_b64_tr_b16 a[186:187], v30 offset:33536\n"
    "v_mfma_f32_16x16x32_bf16 v[252:255], a[172:175], a[244:247], v[252:255]\n"
    "s_waitcnt lgkmcnt(5)\n"
    "v_mfma_f32_16x16x32_bf16 v[96:99], a[176:179], a[208:211], v[96:99]\n"
    "buffer_load_dwordx4 v37, s[36:39], s76 offen offset:1024 lds\n"
    "v_mfma_f32_16x16x32_bf16 v[100:103], a[176:179], a[212:215], v[100:103]\n"
    "buffer_load_dwordx4 v38, s[36:39], s76 offen offset:2048 lds\n"
    "v_mfma_f32_16x16x32_bf16 v[104:107], a[176:179], a[216:219], v[104:107]\n"
    "v_mfma_f32_16x16x32_bf16 v[108:111], a[176:179], a[220:223], v[108:111]\n"
    "s_waitcnt lgkmcnt(0)\n"
    "ds_read_b64_tr_b16 a[188:189], v30 offset:33288\n"
    "ds_read_b64_tr_b16 a[190:191], v30 offset:33544\n"
    "v_mfma_f32_16x16x32_bf16 v[112:115], a[176:179], a[224:227], v[112:115]\n"
    "buffer_load_dwordx4 v39, s[36:39], s76 offen offset:3072 lds\n"
    "v_mfma_f32_16x16x32_bf16 v[116:119], a[180:183], a[208:211], v[116:119]\n"
    "s_add_u32 m0, s79, 0x1000\n"
    "s_add_u32 s76, s60, 0xa000\n"
    "v_mfma_f32_16x16x32_bf16 v[120:123], a[180:183], a[212:215], v[120:123]\n"
    "buffer_load_dwordx4 v36, s[36:39], s76 offen lds\n"
    "v_mfma_f32_16x16x32_bf16 v[124:127], a[180:183], a[216:219], v[124:127]\n"
    "v_mfma_f32_16x16x32_bf16 v[128:131], a[180:183], a[220:223], v[128:131]\n"
    "ds_read_b64_tr_b16 a[144:145], v30 offset:33792\n"
    "ds_read_b64_tr_b16 a[146:147], v30 offset:34048\n"
    "v_mfma_f32_16x16x32_bf16 v[132:135], a[180:183], a[224:227], v[132:135]\n"
    "buffer_load_dwordx4 v37, s[36:39], s76 offen offset:1024 lds\n"
    "v_mfma_f32_16x16x32_bf16 v[136:139], a[184:187], a[208:211], v[136:139]\n"
    "v_mfma_f32_16x16x32_bf16 v[140:143], a[184:187], a[212:215], v[140:143]\n"
    "v_mfma_f32_16x16x32_bf16 v[144:147], a[184:187], a[216:219], v[144:147]\n"
    "buffer_load_dwordx4 v38, s[36:39], s76 offen offset:2048 lds\n"
    "v_mfma_f32_16x16x32_bf16 v[148:151], a[184:187], a[220:223], v[148:151]\n"
    "ds_read_b64_tr_b16 a[148:149], v30 offset:33800\n"
    "ds_read_b64_tr_b16 a[150:151], v30 offset:34056\n"
    "v_mfma_f32_16x16x32_bf16 v[152:155], a[184:187], a[224:227], v[152:155]\n"
    "s_waitcnt lgkmcnt(2)\n"
    "v_mfma_f32_16x16x32_bf16 v[156:159], a[188:191], a[208:211], v[156:159]\n"
    "buffer_load_dwordx4 v39, s[36:39], s76 offen offset:3072 lds\n"
    "v_mfma_f32_16x16x32_bf16 v[160:163], a[188:191], a[212:215], v[160:163]\n"
    "s_add_u32 m0, s79, 0x2000\n"
    "s_add_u32 s76, s60, 0x14000\n"
    "v_mfma_f32_16x16x32_bf16 v[164:167], a[188:191], a[216:219], v[164:167]\n"
    "v_mfma_f32_16x16x32_bf16 v[168:171], a[188:191], a[220:223], v[168:171]\n"
    "ds_read_b64_tr_b16 a[152:153], v30 offset:34304\n"
    "ds_read_b64_tr_b16 a[154:155], v30 offset:34560\n"
    "v_mfma_f32_16x16x32_bf16 v[172:175], a[188:191], a[224:227], v[172:175]\n"
    "buffer_load_dwordx4 v36, s[36:39], s76 offen lds\n"
    "v_mfma_f32_16x16x32_bf16 v[176:179], a[144:147], a[208:211], v[176:179]\n"
    "ds_read_b64_tr_b16 a[156:157], v30 offset:34312\n"
    "ds_read_b64_tr_b16 a[158:159], v30 offset:34568\n"
    "v_mfma_f32_16x16x32_bf16 v[180:183], a[144:147], a[212:215], v[180:183]\n"
    "buffer_load_dwordx4 v37, s[36:39], s76 offen offset:1024 lds\n"
    "v_mfma_f32_16x16x32_bf16 v[184:187], a[144:147], a[216:219], v[184:187]\n"
    "v_mfma_f32_16x16x32_bf16 v[188:191], a[144:147], a[220:223], v[188:191]\n"
    "v_mfma_f32_16x16x32_bf16 v[192:195], a[144:147], a[224:227], v[192:195]\n"
    "s_waitcnt lgkmcnt(2)\n"
    "v_mfma_f32_16x16x32_bf16 v[196:199], a[148:151], a[208:211], v[196:199]\n"
    "buffer_load_dwordx4 v38, s[36:39], s76 offen offset:2048 lds\n"
    "v_mfma_f32_16x16x32_bf16 v[200:203], a[148:151], a[212:215], v[200:203]\n"
    "v_mfma_f32_16x16x32_bf16 v[204:207], a[148:151], a[216:219], v[204:207]\n"
    "buffer_load_dwordx4 v39, s[36:39], s76 offen offset:3072 lds\n"
    "v_mfma_f32_16x16x32_bf16 v[208:211], a[148:151], a[220:223], v[208:211]\n"
    "v_mfma_f32_16x16x32_bf16 v[212:215], a[148:151], a[224:227], v[212:215]\n"
    "s_waitcnt lgkmcnt(0)\n"
    "s_barrier\n"
    "v_mfma_f32_16x16x32_bf16 v[216:219], a[152:155], a[208:211], v[216:219]\n"
    "ds_read_b128 a[228:231], v29\n"
    "ds_read_b64_tr_b16 a[160:161], v30\n"
    "v_mfma_f32_16x16x32_bf16 v[220:223], a[152:155], a[212:215], v[220:223]\n"
    "ds_read_b64_tr_b16 a[162:163], v30 offset:256\n"
    "ds_read_b128 a[232:235], v29 offset:2048\n"
    "v_mfma_f32_16x16x32_bf16 v[224:227], a[152:155], a[216:219], v[224:227]\n"
    "ds_read_b128 a[236:239], v29 offset:4096\n"
    "v_mfma_f32_16x16x32_bf16 v[228:231], a[152:155], a[220:223], v[228:231]\n"
    "ds_read_b128 a[240:243], v29 offset:6144\n"
    "v_mfma_f32_16x16x32_bf16 v[232:235], a[152:155], a[224:227], v[232:235]\n"
    "ds_read_b128 a[244:247], v29 offset:8192\n"
    "ds_read_b64_tr_b16 a[164:165], v30 offset:8\n"
    "v_add_u32 v29, s81, v29\n"
    "v_mfma_f32_16x16x32_bf16 v[236:239], a[156:159], a[208:211], v[236:239]\n"
    "ds_read_b64_tr_b16 a[166:167], v30 offset:264\n"
    "v_mfma_f32_16x16x32_bf16 v[240:243], a[156:159], a[212:215], v[240:243]\n"
    "v_mfma_f32_16x16x32_bf16 v[244:247], a[156:159], a[216:219], v[244:247]\n"
    "v_mfma_f32_16x16x32_bf16 v[248:251], a[156:159], a[220:223], v[248:251]\n"
    "ds_read_b64_tr_b16 a[168:169], v30 offset:512\n"
    "ds_read_b64_tr_b16 a[170:171], v30 offset:768\n"
    "v_mfma_f32_16x16x32_bf16 v[252:255], a[156:159], a[224:227], v[252:255]\n"
    "s_waitcnt lgkmcnt(4)\n"
    "v_mfma_f32_16x16x32_bf16 v[96:99], a[160:163], a[228:231], v[96:99]\n"
    "v_mfma_f32_16x16x32_bf16 v[100:103], a[160:163], a[232:235], v[100:103]\n"
    "v_mfma_f32_16x16x32_bf16 v[104:107], a[160:163], a[236:239], v[104:107]\n"
    "v_mfma_f32_16x16x32_bf16 v[108:111], a[160:163], a[240:243], v[108:111]\n"
    "ds_read_b64_tr_b16 a[172:173], v30 offset:520\n"
    "ds_read_b64_tr_b16 a[174:175], v30 offset:776\n"
    "v_mfma_f32_16x16x32_bf16 v[112:115], a[160:163], a[244:247], v[112:115]\n"
    "s_waitcnt lgkmcnt(2)\n"
    "v_mfma_f32_16x16x32_bf16 v[116:119], a[164:167], a[228:231], v[116:119]\n"
    "v_mfma_f32_16x16x32_bf16 v[120:123], a[164:167], a[232:235], v[120:123]\n"
    "v_mfma_f32_16x16x32_bf16 v[124:127], a[164:167], a[236:239], v[124:127]\n"
    "v_mfma_f32_16x16x32_bf16 v[128:131], a[164:167], a[240:243], v[128:131]\n"
    "ds_read_b64_tr_b16 a[176:177], v30 offset:1024\n"
    "ds_read_b64_tr_b16 a[178:179], v30 offset:1280\n"
    "v_mfma_f32_16x16x32_bf16 v[132:135], a[164:167], a[244:247], v[132:135]\n"
    "v_mfma_f32_16x16x32_bf16 v[136:139], a[168:171], a[228:231], v[136:139]\n"
    "v_mfma_f32_16x16x32_bf16 v[140:143], a[168:171], a[232:235], v[140:143]\n"
    "v_mfma_f32_16x16x32_bf16 v[144:147], a[168:171], a[236:239], v[144:147]\n"
    "v_mfma_f32_16x16x32_bf16 v[148:151], a[168:171], a[240:243], v[148:151]\n"
    "ds_read_b64_tr_b16 a[180:181], v30 offset:1032\n"
    "ds_read_b64_tr_b16 a[182:183], v30 offset:1288\n"
    "v_mfma_f32_16x16x32_bf16 v[152:155], a[168:171], a[244:247], v[152:155]\n"
    "s_waitcnt lgkmcnt(2)\n"
    "ds_read_b64_tr_b16 a[184:185], v30 offset:1536\n"
    "ds_read_b64_tr_b16 a[186:187], v30 offset:1792\n"
    "v_mfma_f32_16x16x32_bf16 v[156:159], a[172:175], a[228:231], v[156:159]\n"
    "v_mfma_f32_16x16x32_bf16 v[160:163], a[172:175], a[232:235], v[160:163]\n"
    "v_mfma_f32_16x16x32_bf16 v[164:167], a[172:175], a[236:239], v[164:167]\n"
    "v_mfma_f32_16x16x32_bf16 v[168:171], a[172:175], a[240:243], v[168:171]\n"
    "v_mfma_f32_16x16x32_bf16 v[172:175], a[172:175], a[244:247], v[172:175]\n"
    "ds_read_b64_tr_b16 a[188:189], v30 offset:1544\n"
    "ds_read_b64_tr_b16 a[190:191], v30 offset:1800\n"
    "v_mfma_f32_16x16x32_bf16 v[176:179], a[176:179], a[228:231], v[176:179]\n"
    "v_mfma_f32_16x16x32_bf16 v[180:183], a[176:179], a[232:235], v[180:183]\n"
    "v_mfma_f32_16x16x32_bf16 v[184:187], a[176:179], a[236:239], v[184:187]\n"
    "v_mfma_f32_16x16x32_bf16 v[188:191], a[176:179], a[240:243], v[188:191]\n"
    "v_mfma_f32_16x16x32_bf16 v[192:195], a[176:179], a[244:247], v[192:195]\n"
    "s_waitcnt lgkmcnt(4)\n"
    "v_mfma_f32_16x16x32_bf16 v[196:199], a[180:183], a[228:231], v[196:199]\n"
    "v_mfma_f32_16x16x32_bf16 v[200:203], a[180:183], a[232:235], v[200:203]\n"
    "v_mfma_f32_16x16x32_bf16 v[204:207], a[180:183], a[236:239], v[204:207]\n"
    "v_mfma_f32_16x16x32_bf16 v[208:211], a[180:183], a[240:243], v[208:211]\n"
    "v_mfma_f32_16x16x32_bf16 v[212:215], a[180:183], a[244:247], v[212:215]\n"
    "s_sub_u32 s81, 0, s81\n"
    "s_add_u32 s80, s80, 1\n"
    "s_cmp_lt_u32 s80, 3\n"
    "s_waitcnt vmcnt(0) lgkmcnt(0)\n"
    "s_barrier\n"
    "s_cbranch_scc1 LQX_%=\n"
    "v_mfma_f32_16x16x32_bf16 v[216:219], a[184:187], a[228:231], v[216:219]\n"
    "ds_read_b128 a[208:211], v28\n"
    "v_and_b32 v44, 15, v0\n"
    "v_mfma_f32_16x16x32_bf16 v[220:223], a[184:187], a[232:235], v[220:223]\n"
    "ds_read_b64_tr_b16 a[144:145], v30 offset:16384\n"
    "v_lshrrev_b32 v45, 4, v0\n"
    "v_mfma_f32_16x16x32_bf16 v[224:227], a[184:187], a[236:239], v[224:227]\n"
    "ds_read_b64_tr_b16 a[146:147], v30 offset:16640\n"
    "v_lshlrev_b32 v46, 8, v44\n"
    "v_mfma_f32_16x16x32_bf16 v[228:231], a[184:187], a[240:243], v[228:231]\n"
    "ds_read_b128 a[212:215], v28 offset:2048\n"
    "v_add_u32 v46, s88, v46\n"
    "v_mfma_f32_16x16x32_bf16 v[232:235], a[184:187], a[244:247], v[232:235]\n"
    "ds_read_b128 a[216:219], v28 offset:4096\n"
    "v_xor_b32 v47, v45, v44\n"
    "v_mfma_f32_16x16x32_bf16 v[236:239], a[188:191], a[228:231], v[236:239]\n"
    "ds_read_b128 a[220:223], v28 offset:6144\n"
    "v_xor_b32 v48, 0, v47\n"
    "v_mfma_f32_16x16x32_bf16 v[240:243], a[188:191], a[232:235], v[240:243]\n"
    "s_add_u32 m0, s73, 0x10000\n"
    "s_add_u32 s86, s73, 0x4000\n"
    "ds_read_b128 a[224:227], v28 offset:8192\n"
    "v_mfma_f32_16x16x32_bf16 v[244:247], a[188:191], a[236:239], v[244:247]\n"
    "ds_read_b64_tr_b16 a[148:149], v30 offset:16392\n"
    "v_lshl_add_u32 v2, v48, 4, v46\n"
    "v_mfma_f32_16x16x32_bf16 v[248:251], a[188:191], a[240:243], v[248:251]\n"
    "ds_read_b64_tr_b16 a[150:151], v30 offset:16648\n"
    "v_xor_b32 v48, 4, v47\n"
    "v_mfma_f32_16x16x32_bf16 v[252:255], a[188:191], a[244:247], v[252:255]\n"
    "s_waitcnt lgkmcnt(4)\n"
    "ds_read_b64_tr_b16 a[152:153], v30 offset:16896\n"
    "v_lshl_add_u32 v3, v48, 4, v46\n"
    "v_mfma_f32_16x16x32_bf16 v[96:99], a[144:147], a[208:211], v[96:99]\n"
    "ds_read_b64_tr_b16 a[154:155], v30 offset:17152\n"
    "v_xor_b32 v48, 8, v47\n"
    "v_mfma_f32_16x16x32_bf16 v[100:103], a[144:147], a[212:215], v[100:103]\n"
    "v_lshl_add_u32 v4, v48, 4, v46\n"
    "v_xor_b32 v48, 12, v47\n"
    "v_mfma_f32_16x16x32_bf16 v[104:107], a[144:147], a[216:219], v[104:107]\n"
    "s_waitcnt lgkmcnt(4)\n"
    "v_lshl_add_u32 v5, v48, 4, v46\n"
    "v_lshrrev_b32 v46, 5, v0\n"
    "v_mfma_f32_16x16x32_bf16 v[108:111], a[144:147], a[220:223], v[108:111]\n"
    "ds_read_b64_tr_b16 a[156:157], v30 offset:16904\n"
    "v_lshlrev_b32 v46, 11, v46\n"
    "v_mfma_f32_16x16x32_bf16 v[112:115], a[144:147], a[224:227], v[112:115]\n"
    "s_waitcnt lgkmcnt(3)\n"
    "ds_read_b64_tr_b16 a[158:159], v30 offset:17160\n"
    "v_add_u32 v46, s88, v46\n"
    "v_mfma_f32_16x16x32_bf16 v[116:119], a[148:151], a[208:211], v[116:119]\n"
    "v_add_u32 v46, 0x8000, v46\n"
    "v_bfe_u32 v47, v0, 2, 3\n"
    "v_mfma_f32_16x16x32_bf16 v[120:123], a[148:151], a[212:215], v[120:123]\n"
    "v_lshl_add_u32 v46, v47, 6, v46\n"
    "v_and_b32 v47, 3, v0\n"
    "v_mfma_f32_16x16x32_bf16 v[124:127], a[148:151], a[216:219], v[124:127]\n"
    "buffer_load_dwordx4 v8, s[44:47], s86 offen nt lds\n"
    "v_mfma_f32_16x16x32_bf16 v[128:131], a[148:151], a[220:223], v[128:131]\n"
    "ds_read_b64_tr_b16 a[160:161], v30 offset:17408\n"
    "v_lshl_add_u32 v46, v47, 3, v46\n"
    "v_mfma_f32_16x16x32_bf16 v[132:135], a[148:151], a[224:227], v[132:135]\n"
    "s_waitcnt lgkmcnt(1)\n"
    "ds_read_b64_tr_b16 a[162:163], v30 offset:17664\n"
    "v_bfe_u32 v47, v0, 4, 1\n"
    "v_mfma_f32_16x16x32_bf16 v[136:139], a[152:155], a[208:211], v[136:139]\n"
    "v_lshl_add_u32 v6, v47, 5, v46\n"
    "v_xor_b32 v7, 32, v6\n"
    "v_mfma_f32_16x16x32_bf16 v[140:143], a[152:155], a[212:215], v[140:143]\n"
    "v_mov_b32 v46, 0x3f803f80\n"
    "v_accvgpr_write_b32 a132, v46\n"
    "v_mfma_f32_16x16x32_bf16 v[144:147], a[152:155], a[216:219], v[144:147]\n"
    "v_accvgpr_write_b32 a133, v46\n"
    "v_accvgpr_write_b32 a134, v46\n"
    "v_mfma_f32_16x16x32_bf16 v[148:151], a[152:155], a[220:223], v[148:151]\n"
    "ds_read_b64_tr_b16 a[164:165], v30 offset:17416\n"
    "v_accvgpr_write_b32 a135, v46\n"
    "v_mfma_f32_16x16x32_bf16 v[152:155], a[152:155], a[224:227], v[152:155]\n"
    "ds_read_b64_tr_b16 a[166:167], v30 offset:17672\n"
    "v_add_u32 v44, 0, v0\n"
    "v_mfma_f32_16x16x32_bf16 v[156:159], a[156:159], a[208:211], v[156:159]\n"
    "v_mul_u32_u24 v45, 0xccd, v44\n"
    "v_lshrrev_b32 v45, 16, v45\n"
    "v_mfma_f32_16x16x32_bf16 v[160:163], a[156:159], a[212:215], v[160:163]\n"
    "buffer_load_dwordx4 v9, s[44:47], s86 offen offset:1024 nt lds\n"
    "v_mfma_f32_16x16x32_bf16 v[164:167], a[156:159], a[216:219], v[164:167]\n"
    "v_mul_u32_u24 v46, 20, v45\n"
    "v_sub_u32 v46, v44, v46\n"
    "v_mfma_f32_16x16x32_bf16 v[168:171], a[156:159], a[220:223], v[168:171]\n"
    "ds_read_b64_tr_b16 a[168:169], v30 offset:17920\n"
    "v_lshrrev_b32 v47, 1, v45\n"
    "v_mfma_f32_16x16x32_bf16 v[172:175], a[156:159], a[224:227], v[172:175]\n"
    "s_waitcnt lgkmcnt(1)\n"
    "ds_read_b64_tr_b16 a[170:171], v30 offset:18176\n"
    "v_and_b32 v47, 2, v47\n"
    "v_mfma_f32_16x16x32_bf16 v[176:179], a[160:163], a[208:211], v[176:179]\n"
    "ds_read_b64_tr_b16 a[172:173], v30 offset:17928\n"
    "v_xor_b32 v46, v46, v47\n"
    "v_mfma_f32_16x16x32_bf16 v[180:183], a[160:163], a[212:215], v[180:183]\n"
    "ds_read_b64_tr_b16 a[174:175], v30 offset:18184\n"
    "v_mul_u32_u24 v45, 0x500, v45\n"
    "v_mfma_f32_16x16x32_bf16 v[184:187], a[160:163], a[216:219], v[184:187]\n"
    "v_lshl_add_u32 v45, v46, 4, v45\n"
    "v_add_u32 v23, 0xc00, v45\n"
    "v_mfma_f32_16x16x32_bf16 v[188:191], a[160:163], a[220:223], v[188:191]\n"
    "v_add_u32 v44, 64, v0\n"
    "v_mul_u32_u24 v45, 0xccd, v44\n"
    "v_mfma_f32_16x16x32_bf16 v[192:195], a[160:163], a[224:227], v[192:195]\n"
    "v_lshrrev_b32 v45, 16, v45\n"
    "v_mul_u32_u24 v46, 20, v45\n"
    "v_mfma_f32_16x16x32_bf16 v[196:199], a[164:167], a[208:211], v[196:199]\n"
    "buffer_load_dwordx4 v10, s[44:47], s86 offen offset:2048 nt lds\n"
    "v_mfma_f32_16x16x32_bf16 v[200:203], a[164:167], a[212:215], v[200:203]\n"
    "v_sub_u32 v46, v44, v46\n"
    "v_lshrrev_b32 v47, 1, v45\n"
    "v_mfma_f32_16x16x32_bf16 v[204:207], a[164:167], a[216:219], v[204:207]\n"
    "v_and_b32 v47, 2, v47\n"
    "v_xor_b32 v46, v46, v47\n"
    "v_mfma_f32_16x16x32_bf16 v[208:211], a[164:167], a[220:223], v[208:211]\n"
    "v_mul_u32_u24 v45, 0x500, v45\n"
    "v_lshl_add_u32 v45, v46, 4, v45\n"
    "v_mfma_f32_16x16x32_bf16 v[212:215], a[164:167], a[224:227], v[212:215]\n"
    "s_waitcnt lgkmcnt(0)\n"
    "s_barrier\n"
    "v_mfma_f32_16x16x32_bf16 v[216:219], a[168:171], a[208:211], v[216:219]\n"
    "ds_read_b128 a[228:231], v29\n"
    "v_add_u32 v24, 0x800, v45\n"
    "v_mfma_f32_16x16x32_bf16 v[220:223], a[168:171], a[212:215], v[220:223]\n"
    "ds_read_b64_tr_b16 a[176:177], v30 offset:32768\n"
    "v_add_u32 v44, 0x80, v0\n"
    "v_mfma_f32_16x16x32_bf16 v[224:227], a[168:171], a[216:219], v[224:227]\n"
    "ds_read_b64_tr_b16 a[178:179], v30 offset:33024\n"
    "v_mul_u32_u24 v45, 0xccd, v44\n"
    "v_mfma_f32_16x16x32_bf16 v[228:231], a[168:171], a[220:223], v[228:231]\n"
    "ds_read_b128 a[232:235], v29 offset:2048\n"
    "v_lshrrev_b32 v45, 16, v45\n"
    "v_mfma_f32_16x16x32_bf16 v[232:235], a[168:171], a[224:227], v[232:235]\n"
    "ds_read_b128 a[236:239], v29 offset:4096\n"
    "v_mul_u32_u24 v46, 20, v45\n"
    "v_mfma_f32_16x16x32_bf16 v[236:239], a[172:175], a[208:211], v[236:239]\n"
    "ds_read_b128 a[240:243], v29 offset:6144\n"
    "v_sub_u32 v46, v44, v46\n"
    "v_mfma_f32_16x16x32_bf16 v[240:243], a[172:175], a[212:215], v[240:243]\n"
    "ds_read_b128 a[244:247], v29 offset:8192\n"
    "v_lshrrev_b32 v47, 1, v45\n"
    "v_mfma_f32_16x16x32_bf16 v[244:247], a[172:175], a[216:219], v[244:247]\n"
    "ds_read_b64_tr_b16 a[180:181], v30 offset:32776\n"
    "v_and_b32 v47, 2, v47\n"
    "v_mfma_f32_16x16x32_bf16 v[248:251], a[172:175], a[220:223], v[248:251]\n"
    "ds_read_b64_tr_b16 a[182:183], v30 offset:33032\n"
    "v_xor_b32 v46, v46, v47\n"
    "v_mfma_f32_16x16x32_bf16 v[252:255], a[172:175], a[224:227], v[252:255]\n"
    "s_waitcnt lgkmcnt(4)\n"
    "ds_read_b64_tr_b16 a[184:185], v30 offset:33280\n"
    "v_mul_u32_u24 v45, 0x500, v45\n"
    "v_mfma_f32_16x16x32_bf16 v[96:99], a[176:179], a[228:231], v[96:99]\n"
    "ds_read_b64_tr_b16 a[186:187], v30 offset:33536\n"
    "v_lshl_add_u32 v45, v46, 4, v45\n"
    "v_mfma_f32_16x16x32_bf16 v[100:103], a[176:179], a[232:235], v[100:103]\n"
    "buffer_load_dwordx4 v11, s[44:47], s86 offen offset:3072 nt lds\n"
    "v_mfma_f32_16x16x32_bf16 v[104:107], a[176:179], a[236:239], v[104:107]\n"
    "s_waitcnt lgkmcnt(4)\n"
    "v_add_u32 v25, 0x400, v45\n"
    "v_add_u32 v44, 0xc0, v0\n"
    "v_mfma_f32_16x16x32_bf16 v[108:111], a[176:179], a[240:243], v[108:111]\n"
    "s_add_u32 m0, s73, 0x14000\n"
    "s_add_u32 s86, s73, 0x24000\n"
    "ds_read_b64_tr_b16 a[188:189], v30 offset:33288\n"
    "v_mfma_f32_16x16x32_bf16 v[112:115], a[176:179], a[244:247], v[112:115]\n"
    "s_waitcnt lgkmcnt(3)\n"
    "ds_read_b64_tr_b16 a[190:191], v30 offset:33544\n"
    "v_mul_u32_u24 v45, 0xccd, v44\n"
    "v_mfma_f32_16x16x32_bf16 v[116:119], a[180:183], a[228:231], v[116:119]\n"
    "v_lshrrev_b32 v45, 16, v45\n"
    "v_mul_u32_u24 v46, 20, v45\n"
    "v_mfma_f32_16x16x32_bf16 v[120:123], a[180:183], a[232:235], v[120:123]\n"
    "v_sub_u32 v46, v44, v46\n"
    "v_lshrrev_b32 v47, 1, v45\n"
    "v_mfma_f32_16x16x32_bf16 v[124:127], a[180:183], a[236:239], v[124:127]\n"
    "v_and_b32 v47, 2, v47\n"
    "v_xor_b32 v46, v46, v47\n"
    "v_mfma_f32_16x16x32_bf16 v[128:131], a[180:183], a[240:243], v[128:131]\n"
    "ds_read_b64_tr_b16 a[144:145], v30 offset:33792\n"
    "v_mul_u32_u24 v45, 0x500, v45\n"
    "v_mfma_f32_16x16x32_bf16 v[132:135], a[180:183], a[244:247], v[132:135]\n"
    "s_waitcnt lgkmcnt(1)\n"
    "ds_read_b64_tr_b16 a[146:147], v30 offset:34048\n"
    "v_lshl_add_u32 v45, v46, 4, v45\n"
    "v_mfma_f32_16x16x32_bf16 v[136:139], a[184:187], a[228:231], v[136:139]\n"
    "v_add_u32 v26, 0, v45\n"
    "v_add_u32 v44, 0x100, v0\n"
    "v_mfma_f32_16x16x32_bf16 v[140:143], a[184:187], a[232:235], v[140:143]\n"
    "v_mul_u32_u24 v45, 0xccd, v44\n"
    "v_lshrrev_b32 v45, 16, v45\n"
    "v_mfma_f32_16x16x32_bf16 v[144:147], a[184:187], a[236:239], v[144:147]\n"
    "v_mul_u32_u24 v46, 20, v45\n"
    "v_sub_u32 v46, v44, v46\n"
    "v_mfma_f32_16x16x32_bf16 v[148:151], a[184:187], a[240:243], v[148:151]\n"
    "ds_read_b64_tr_b16 a[148:149], v30 offset:33800\n"
    "v_lshrrev_b32 v47, 1, v45\n"
    "v_mfma_f32_16x16x32_bf16 v[152:155], a[184:187], a[244:247], v[152:155]\n"
    "ds_read_b64_tr_b16 a[150:151], v30 offset:34056\n"
    "v_and_b32 v47, 2, v47\n"
    "v_mfma_f32_16x16x32_bf16 v[156:159], a[188:191], a[228:231], v[156:159]\n"
    "v_xor_b32 v46, v46, v47\n"
    "v_mul_u32_u24 v45, 0x500, v45\n"
    "v_mfma_f32_16x16x32_bf16 v[160:163], a[188:191], a[232:235], v[160:163]\n"
    "v_lshl_add_u32 v45, v46, 4, v45\n"
    "v_add_u32 v27, 0xc00, v45\n"
    "v_mfma_f32_16x16x32_bf16 v[164:167], a[188:191], a[236:239], v[164:167]\n"
    "buffer_load_dwordx4 v8, s[44:47], s86 offen nt lds\n"
    "v_mfma_f32_16x16x32_bf16 v[168:171], a[188:191], a[240:243], v[168:171]\n"
    "ds_read_b64_tr_b16 a[152:153], v30 offset:34304\n"
    "ds_read_b64_tr_b16 a[154:155], v30 offset:34560\n"
    "v_mfma_f32_16x16x32_bf16 v[172:175], a[188:191], a[244:247], v[172:175]\n"
    "s_waitcnt lgkmcnt(2)\n"
    "v_mfma_f32_16x16x32_bf16 v[176:179], a[144:147], a[228:231], v[176:179]\n"
    "buffer_load_dwordx4 v9, s[44:47], s86 offen offset:1024 nt lds\n"
    "v_mfma_f32_16x16x32_bf16 v[180:183], a[144:147], a[232:235], v[180:183]\n"
    "buffer_load_dwordx4 v10, s[44:47], s86 offen offset:2048 nt lds\n"
    "v_mfma_f32_16x16x32_bf16 v[184:187], a[144:147], a[236:239], v[184:187]\n"
    "buffer_load_dwordx4 v11, s[44:47], s86 offen offset:3072 nt lds\n"
    "v_mfma_f32_16x16x32_bf16 v[188:191], a[144:147], a[240:243], v[188:191]\n"
    "ds_read_b64_tr_b16 a[156:157], v30 offset:34312\n"
    "ds_read_b64_tr_b16 a[158:159], v30 offset:34568\n"
    "v_mfma_f32_16x16x32_bf16 v[192:195], a[144:147], a[244:247], v[192:195]\n"
    "v_mfma_f32_16x16x32_bf16 v[196:199], a[148:151], a[228:231], v[196:199]\n"
    "v_mfma_f32_16x16x32_bf16 v[200:203], a[148:151], a[232:235], v[200:203]\n"
    "v_mfma_f32_16x16x32_bf16 v[204:207], a[148:151], a[236:239], v[204:207]\n"
    "v_mfma_f32_16x16x32_bf16 v[208:211], a[148:151], a[240:243], v[208:211]\n"
    "v_mfma_f32_16x16x32_bf16 v[212:215], a[148:151], a[244:247], v[212:215]\n"
    "s_waitcnt lgkmcnt(0)\n"
    "v_mfma_f32_16x16x32_bf16 v[216:219], a[152:155], a[228:231], v[216:219]\n"
    "v_mfma_f32_16x16x32_bf16 v[220:223], a[152:155], a[232:235], v[220:223]\n"
    "v_mfma_f32_16x16x32_bf16 v[224:227], a[152:155], a[236:239], v[224:227]\n"
    "v_mfma_f32_16x16x32_bf16 v[228:231], a[152:155], a[240:243], v[228:231]\n"
    "v_mfma_f32_16x16x32_bf16 v[232:235], a[152:155], a[244:247], v[232:235]\n"
    "v_mfma_f32_16x16x32_bf16 v[236:239], a[156:159], a[228:231], v[236:239]\n"
    "v_mfma_f32_16x16x32_bf16 v[240:243], a[156:159], a[232:235], v[240:243]\n"
    "v_mfma_f32_16x16x32_bf16 v[244:247], a[156:159], a[236:239], v[244:247]\n"
    "v_mfma_f32_16x16x32_bf16 v[248:251], a[156:159], a[240:243], v[248:251]\n"
    "v_mfma_f32_16x16x32_bf16 v[252:255], a[156:159], a[244:247], v[252:255]\n"
    "s_branch LJOIN_%=\n"
    "LRKV_%=:\n"
    "s_lshl_b32 s74, s89, 4\n"
    "s_add_u32 s74, s74, s90\n"
    "s_lshl_b32 s74, s74, 18\n"
    "v_mov_b32 v45, s74\n"
    "global_load_dword a136, v45, s[22:23]\n"
    "global_load_dword a136, v45, s[24:25]\n"
    "v_min_u32 v46, 2, v0\n"
    "v_lshlrev_b32 v46, 21, v46\n"
    "global_load_dword a136, v46, s[28:29]\n"
    "s_mul_i32 s74, s89, 0x140000\n"
    "s_mul_i32 s75, s90, 0x14000\n"
    "s_add_u32 s74, s74, s75\n"
    "v_mov_b32 v47, s74\n"
    "global_load_dword a136, v47, s[30:31]\n"
    "s_lshl_b32 s74, s89, 20\n"
    "s_and_b32 s75, s90, 3\n"
    "s_lshl_b32 s75, s75, 18\n"
    "s_add_u32 s74, s74, s75\n"
    "s_lshr_b32 s75, s90, 2\n"
    "s_lshl_b32 s75, s75, 16\n"
    "s_add_u32 s74, s74, s75\n"
    "s_add_u32 s74, s74, 0x1600000\n"
    "v_mov_b32 v48, s74\n"
    "global_load_dword a136, v48, s[30:31]\n"
    "s_mul_i32 s74, s89, 0x32000\n"
    "v_mov_b32 v49, s74\n"
    "global_load_dword a136, v49, s[32:33]\n"
    "v_mfma_f32_16x16x32_bf16 v[216:219], a[184:187], a[228:231], v[216:219]\n"
    "ds_read_b128 a[208:211], v28\n"
    "ds_read_b64_tr_b16 a[144:145], v30 offset:16384\n"
    "v_mfma_f32_16x16x32_bf16 v[220:223], a[184:187], a[232:235], v[220:223]\n"
    "ds_read_b64_tr_b16 a[146:147], v30 offset:16640\n"
    "ds_read_b128 a[212:215], v28 offset:2048\n"
    "v_mfma_f32_16x16x32_bf16 v[224:227], a[184:187], a[236:239], v[224:227]\n"
    "ds_read_b128 a[216:219], v28 offset:4096\n"
    "v_mfma_f32_16x16x32_bf16 v[228:231], a[184:187], a[240:243], v[228:231]\n"
    "ds_read_b128 a[220:223], v28 offset:6144\n"
    "v_mfma_f32_16x16x32_bf16 v[232:235], a[184:187], a[244:247], v[232:235]\n"
    "ds_read_b128 a[224:227], v28 offset:8192\n"
    "ds_read_b64_tr_b16 a[148:149], v30 offset:16392\n"
    "v_add_u32 v28, s81, v28\n"
    "v_mfma_f32_16x16x32_bf16 v[236:239], a[188:191], a[228:231], v[236:239]\n"
    "ds_read_b64_tr_b16 a[150:151], v30 offset:16648\n"
    "v_mfma_f32_16x16x32_bf16 v[240:243], a[188:191], a[232:235], v[240:243]\n"
    "v_mfma_f32_16x16x32_bf16 v[244:247], a[188:191], a[236:239], v[244:247]\n"
    "v_mfma_f32_16x16x32_bf16 v[248:251], a[188:191], a[240:243], v[248:251]\n"
    "ds_read_b64_tr_b16 a[152:153], v30 offset:16896\n"
    "ds_read_b64_tr_b16 a[154:155], v30 offset:17152\n"
    "v_mfma_f32_16x16x32_bf16 v[252:255], a[188:191], a[244:247], v[252:255]\n"
    "s_waitcnt lgkmcnt(4)\n"
    "v_mfma_f32_16x16x32_bf16 v[96:99], a[144:147], a[208:211], v[96:99]\n"
    "v_mfma_f32_16x16x32_bf16 v[100:103], a[144:147], a[212:215], v[100:103]\n"
    "v_mfma_f32_16x16x32_bf16 v[104:107], a[144:147], a[216:219], v[104:107]\n"
    "v_mfma_f32_16x16x32_bf16 v[108:111], a[144:147], a[220:223], v[108:111]\n"
    "ds_read_b64_tr_b16 a[156:157], v30 offset:16904\n"
    "ds_read_b64_tr_b16 a[158:159], v30 offset:17160\n"
    "v_mfma_f32_16x16x32_bf16 v[112:115], a[144:147], a[224:227], v[112:115]\n"
    "s_waitcnt lgkmcnt(2)\n"
    "v_mfma_f32_16x16x32_bf16 v[116:119], a[148:151], a[208:211], v[116:119]\n"
    "v_mfma_f32_16x16x32_bf16 v[120:123], a[148:151], a[212:215], v[120:123]\n"
    "v_mfma_f32_16x16x32_bf16 v[124:127], a[148:151], a[216:219], v[124:127]\n"
    "v_mfma_f32_16x16x32_bf16 v[128:131], a[148:151], a[220:223], v[128:131]\n"
    "ds_read_b64_tr_b16 a[160:161], v30 offset:17408\n"
    "ds_read_b64_tr_b16 a[162:163], v30 offset:17664\n"
    "v_mfma_f32_16x16x32_bf16 v[132:135], a[148:151], a[224:227], v[132:135]\n"
    "v_mfma_f32_16x16x32_bf16 v[136:139], a[152:155], a[208:211], v[136:139]\n"
    "v_mfma_f32_16x16x32_bf16 v[140:143], a[152:155], a[212:215], v[140:143]\n"
    "v_mfma_f32_16x16x32_bf16 v[144:147], a[152:155], a[216:219], v[144:147]\n"
    "v_mfma_f32_16x16x32_bf16 v[148:151], a[152:155], a[220:223], v[148:151]\n"
    "ds_read_b64_tr_b16 a[164:165], v30 offset:17416\n"
    "ds_read_b64_tr_b16 a[166:167], v30 offset:17672\n"
    "v_mfma_f32_16x16x32_bf16 v[152:155], a[152:155], a[224:227], v[152:155]\n"
    "s_waitcnt lgkmcnt(2)\n"
    "v_mfma_f32_16x16x32_bf16 v[156:159], a[156:159], a[208:211], v[156:159]\n"
    "v_mfma_f32_16x16x32_bf16 v[160:163], a[156:159], a[212:215], v[160:163]\n"
    "v_mfma_f32_16x16x32_bf16 v[164:167], a[156:159], a[216:219], v[164:167]\n"
    "v_mfma_f32_16x16x32_bf16 v[168:171], a[156:159], a[220:223], v[168:171]\n"
    "ds_read_b64_tr_b16 a[168:169], v30 offset:17920\n"
    "ds_read_b64_tr_b16 a[170:171], v30 offset:18176\n"
    "v_mfma_f32_16x16x32_bf16 v[172:175], a[156:159], a[224:227], v[172:175]\n"
    "v_mfma_f32_16x16x32_bf16 v[176:179], a[160:163], a[208:211], v[176:179]\n"
    "ds_read_b64_tr_b16 a[172:173], v30 offset:17928\n"
    "ds_read_b64_tr_b16 a[174:175], v30 offset:18184\n"
    "v_mfma_f32_16x16x32_bf16 v[180:183], a[160:163], a[212:215], v[180:183]\n"
    "v_mfma_f32_16x16x32_bf16 v[184:187], a[160:163], a[216:219], v[184:187]\n"
    "v_mfma_f32_16x16x32_bf16 v[188:191], a[160:163], a[220:223], v[188:191]\n"
    "v_mfma_f32_16x16x32_bf16 v[192:195], a[160:163], a[224:227], v[192:195]\n"
    "s_waitcnt lgkmcnt(2)\n"
    "v_mfma_f32_16x16x32_bf16 v[196:199], a[164:167], a[208:211], v[196:199]\n"
    "v_mfma_f32_16x16x32_bf16 v[200:203], a[164:167], a[212:215], v[200:203]\n"
    "v_mfma_f32_16x16x32_bf16 v[204:207], a[164:167], a[216:219], v[204:207]\n"
    "v_mfma_f32_16x16x32_bf16 v[208:211], a[164:167], a[220:223], v[208:211]\n"
    "v_mfma_f32_16x16x32_bf16 v[212:215], a[164:167], a[224:227], v[212:215]\n"
    "s_waitcnt lgkmcnt(0)\n"
    "s_barrier\n"
    "v_mfma_f32_16x16x32_bf16 v[216:219], a[168:171], a[208:211], v[216:219]\n"
    "ds_read_b128 a[228:231], v29\n"
    "ds_read_b64_tr_b16 a[176:177], v30 offset:32768\n"
    "v_mfma_f32_16x16x32_bf16 v[220:223], a[168:171], a[212:215], v[220:223]\n"
    "ds_read_b64_tr_b16 a[178:179], v30 offset:33024\n"
    "ds_read_b128 a[232:235], v29 offset:2048\n"
    "v_mfma_f32_16x16x32_bf16 v[224:227], a[168:171], a[216:219], v[224:227]\n"
    "ds_read_b128 a[236:239], v29 offset:4096\n"
    "v_mfma_f32_16x16x32_bf16 v[228:231], a[168:171], a[220:223], v[228:231]\n"
    "ds_read_b128 a[240:243], v29 offset:6144\n"
    "v_mfma_f32_16x16x32_bf16 v[232:235], a[168:171], a[224:227], v[232:235]\n"
    "ds_read_b128 a[244:247], v29 offset:8192\n"
    "ds_read_b64_tr_b16 a[180:181], v30 offset:32776\n"
    "v_add_u32 v29, s81, v29\n"
    "v_mfma_f32_16x16x32_bf16 v[236:239], a[172:175], a[208:211], v[236:239]\n"
    "ds_read_b64_tr_b16 a[182:183], v30 offset:33032\n"
    "v_mfma_f32_16x16x32_bf16 v[240:243], a[172:175], a[212:215], v[240:243]\n"
    "v_mfma_f32_16x16x32_bf16 v[244:247], a[172:175], a[216:219], v[244:247]\n"
    "v_mfma_f32_16x16x32_bf16 v[248:251], a[172:175], a[220:223], v[248:251]\n"
    "ds_read_b64_tr_b16 a[184:185], v30 offset:33280\n"
    "ds_read_b64_tr_b16 a[186:187], v30 offset:33536\n"
    "v_mfma_f32_16x16x32_bf16 v[252:255], a[172:175], a[224:227], v[252:255]\n"
    "s_waitcnt lgkmcnt(4)\n"
    "v_mfma_f32_16x16x32_bf16 v[96:99], a[176:179], a[228:231], v[96:99]\n"
    "v_mfma_f32_16x16x32_bf16 v[100:103], a[176:179], a[232:235], v[100:103]\n"
    "v_mfma_f32_16x16x32_bf16 v[104:107], a[176:179], a[236:239], v[104:107]\n"
    "v_mfma_f32_16x16x32_bf16 v[108:111], a[176:179], a[240:243], v[108:111]\n"
    "ds_read_b64_tr_b16 a[188:189], v30 offset:33288\n"
    "ds_read_b64_tr_b16 a[190:191], v30 offset:33544\n"
    "v_mfma_f32_16x16x32_bf16 v[112:115], a[176:179], a[244:247], v[112:115]\n"
    "s_waitcnt lgkmcnt(2)\n"
    "v_mfma_f32_16x16x32_bf16 v[116:119], a[180:183], a[228:231], v[116:119]\n"
    "v_mfma_f32_16x16x32_bf16 v[120:123], a[180:183], a[232:235], v[120:123]\n"
    "v_mfma_f32_16x16x32_bf16 v[124:127], a[180:183], a[236:239], v[124:127]\n"
    "v_mfma_f32_16x16x32_bf16 v[128:131], a[180:183], a[240:243], v[128:131]\n"
    "ds_read_b64_tr_b16 a[144:145], v30 offset:33792\n"
    "ds_read_b64_tr_b16 a[146:147], v30 offset:34048\n"
    "v_mfma_f32_16x16x32_bf16 v[132:135], a[180:183], a[244:247], v[132:135]\n"
    "v_mfma_f32_16x16x32_bf16 v[136:139], a[184:187], a[228:231], v[136:139]\n"
    "v_mfma_f32_16x16x32_bf16 v[140:143], a[184:187], a[232:235], v[140:143]\n"
    "v_mfma_f32_16x16x32_bf16 v[144:147], a[184:187], a[236:239], v[144:147]\n"
    "v_mfma_f32_16x16x32_bf16 v[148:151], a[184:187], a[240:243], v[148:151]\n"
    "ds_read_b64_tr_b16 a[148:149], v30 offset:33800\n"
    "ds_read_b64_tr_b16 a[150:151], v30 offset:34056\n"
    "v_mfma_f32_16x16x32_bf16 v[152:155], a[184:187], a[244:247], v[152:155]\n"
    "s_waitcnt lgkmcnt(2)\n"
    "v_mfma_f32_16x16x32_bf16 v[156:159], a[188:191], a[228:231], v[156:159]\n"
    "v_mfma_f32_16x16x32_bf16 v[160:163], a[188:191], a[232:235], v[160:163]\n"
    "v_mfma_f32_16x16x32_bf16 v[164:167], a[188:191], a[236:239], v[164:167]\n"
    "v_mfma_f32_16x16x32_bf16 v[168:171], a[188:191], a[240:243], v[168:171]\n"
    "ds_read_b64_tr_b16 a[152:153], v30 offset:34304\n"
    "ds_read_b64_tr_b16 a[154:155], v30 offset:34560\n"
    "v_mfma_f32_16x16x32_bf16 v[172:175], a[188:191], a[244:247], v[172:175]\n"
    "v_mfma_f32_16x16x32_bf16 v[176:179], a[144:147], a[228:231], v[176:179]\n"
    "ds_read_b64_tr_b16 a[156:157], v30 offset:34312\n"
    "ds_read_b64_tr_b16 a[158:159], v30 offset:34568\n"
    "v_mfma_f32_16x16x32_bf16 v[180:183], a[144:147], a[232:235], v[180:183]\n"
    "v_mfma_f32_16x16x32_bf16 v[184:187], a[144:147], a[236:239], v[184:187]\n"
    "v_mfma_f32_16x16x32_bf16 v[188:191], a[144:147], a[240:243], v[188:191]\n"
    "v_mfma_f32_16x16x32_bf16 v[192:195], a[144:147], a[244:247], v[192:195]\n"
    "s_waitcnt lgkmcnt(2)\n"
    "v_mfma_f32_16x16x32_bf16 v[196:199], a[148:151], a[228:231], v[196:199]\n"
    "v_mfma_f32_16x16x32_bf16 v[200:203], a[148:151], a[232:235], v[200:203]\n"
    "v_mfma_f32_16x16x32_bf16 v[204:207], a[148:151], a[236:239], v[204:207]\n"
    "v_mfma_f32_16x16x32_bf16 v[208:211], a[148:151], a[240:243], v[208:211]\n"
    "v_mfma_f32_16x16x32_bf16 v[212:215], a[148:151], a[244:247], v[212:215]\n"
    "s_waitcnt lgkmcnt(0)\n"
    "s_barrier\n"
    "v_mfma_f32_16x16x32_bf16 v[216:219], a[152:155], a[228:231], v[216:219]\n"
    "ds_read_b128 a[208:211], v28\n"
    "ds_read_b64_tr_b16 a[160:161], v30\n"
    "v_mfma_f32_16x16x32_bf16 v[220:223], a[152:155], a[232:235], v[220:223]\n"
    "ds_read_b64_tr_b16 a[162:163], v30 offset:256\n"
    "ds_read_b128 a[212:215], v28 offset:2048\n"
    "v_mfma_f32_16x16x32_bf16 v[224:227], a[152:155], a[236:239], v[224:227]\n"
    "ds_read_b128 a[216:219], v28 offset:4096\n"
    "v_mfma_f32_16x16x32_bf16 v[228:231], a[152:155], a[240:243], v[228:231]\n"
    "ds_read_b128 a[220:223], v28 offset:6144\n"
    "v_mfma_f32_16x16x32_bf16 v[232:235], a[152:155], a[244:247], v[232:235]\n"
    "ds_read_b128 a[224:227], v28 offset:8192\n"
    "ds_read_b64_tr_b16 a[164:165], v30 offset:8\n"
    "v_subrev_u32 v28, s81, v28\n"
    "v_mfma_f32_16x16x32_bf16 v[236:239], a[156:159], a[228:231], v[236:239]\n"
    "ds_read_b64_tr_b16 a[166:167], v30 offset:264\n"
    "v_mfma_f32_16x16x32_bf16 v[240:243], a[156:159], a[232:235], v[240:243]\n"
    "s_mov_b32 m0, 0\n"
    "v_mfma_f32_16x16x32_bf16 v[244:247], a[156:159], a[236:239], v[244:247]\n"
    "s_mov_b32 s86, 0\n"
    "v_mfma_f32_16x16x32_bf16 v[248:251], a[156:159], a[240:243], v[248:251]\n"
    "ds_read_b64_tr_b16 a[168:169], v30 offset:512\n"
    "ds_read_b64_tr_b16 a[170:171], v30 offset:768\n"
    "v_mfma_f32_16x16x32_bf16 v[252:255], a[156:159], a[244:247], v[252:255]\n"
    "s_waitcnt lgkmcnt(4)\n"
    "v_mfma_f32_16x16x32_bf16 v[96:99], a[160:163], a[208:211], v[96:99]\n"
    "v_mfma_f32_16x16x32_bf16 v[100:103], a[160:163], a[212:215], v[100:103]\n"
    "v_mfma_f32_16x16x32_bf16 v[104:107], a[160:163], a[216:219], v[104:107]\n"
    "buffer_load_dwordx4 v8, s[44:47], s86 offen nt lds\n"
    "v_mfma_f32_16x16x32_bf16 v[108:111], a[160:163], a[220:223], v[108:111]\n"
    "ds_read_b64_tr_b16 a[172:173], v30 offset:520\n"
    "ds_read_b64_tr_b16 a[174:175], v30 offset:776\n"
    "v_mfma_f32_16x16x32_bf16 v[112:115], a[160:163], a[224:227], v[112:115]\n"
    "s_waitcnt lgkmcnt(2)\n"
    "v_mfma_f32_16x16x32_bf16 v[116:119], a[164:167], a[208:211], v[116:119]\n"
    "v_mfma_f32_16x16x32_bf16 v[120:123], a[164:167], a[212:215], v[120:123]\n"
    "v_mfma_f32_16x16x32_bf16 v[124:127], a[164:167], a[216:219], v[124:127]\n"
    "v_mfma_f32_16x16x32_bf16 v[128:131], a[164:167], a[220:223], v[128:131]\n"
    "ds_read_b64_tr_b16 a[176:177], v30 offset:1024\n"
    "ds_read_b64_tr_b16 a[178:179], v30 offset:1280\n"
    "v_mfma_f32_16x16x32_bf16 v[132:135], a[164:167], a[224:227], v[132:135]\n"
    "buffer_load_dwordx4 v9, s[44:47], s86 offen offset:1024 nt lds\n"
    "v_mfma_f32_16x16x32_bf16 v[136:139], a[168:171], a[208:211], v[136:139]\n"
    "v_mfma_f32_16x16x32_bf16 v[140:143], a[168:171], a[212:215], v[140:143]\n"
    "v_mfma_f32_16x16x32_bf16 v[144:147], a[168:171], a[216:219], v[144:147]\n"
    "v_mfma_f32_16x16x32_bf16 v[148:151], a[168:171], a[220:223], v[148:151]\n"
    "ds_read_b64_tr_b16 a[180:181], v30 offset:1032\n"
    "ds_read_b64_tr_b16 a[182:183], v30 offset:1288\n"
    "v_mfma_f32_16x16x32_bf16 v[152:155], a[168:171], a[224:227], v[152:155]\n"
    "s_waitcnt lgkmcnt(2)\n"
    "v_mfma_f32_16x16x32_bf16 v[156:159], a[172:175], a[208:211], v[156:159]\n"
    "v_mfma_f32_16x16x32_bf16 v[160:163], a[172:175], a[212:215], v[160:163]\n"
    "buffer_load_dwordx4 v10, s[44:47], s86 offen offset:2048 nt lds\n"
    "v_mfma_f32_16x16x32_bf16 v[164:167], a[172:175], a[216:219], v[164:167]\n"
    "v_mfma_f32_16x16x32_bf16 v[168:171], a[172:175], a[220:223], v[168:171]\n"
    "ds_read_b64_tr_b16 a[184:185], v30 offset:1536\n"
    "ds_read_b64_tr_b16 a[186:187], v30 offset:1792\n"
    "v_mfma_f32_16x16x32_bf16 v[172:175], a[172:175], a[224:227], v[172:175]\n"
    "v_mfma_f32_16x16x32_bf16 v[176:179], a[176:179], a[208:211], v[176:179]\n"
    "ds_read_b64_tr_b16 a[188:189], v30 offset:1544\n"
    "ds_read_b64_tr_b16 a[190:191], v30 offset:1800\n"
    "v_mfma_f32_16x16x32_bf16 v[180:183], a[176:179], a[212:215], v[180:183]\n"
    "v_mfma_f32_16x16x32_bf16 v[184:187], a[176:179], a[216:219], v[184:187]\n"
    "v_mfma_f32_16x16x32_bf16 v[188:191], a[176:179], a[220:223], v[188:191]\n"
    "buffer_load_dwordx4 v11, s[44:47], s86 offen offset:3072 nt lds\n"
    "v_mfma_f32_16x16x32_bf16 v[192:195], a[176:179], a[224:227], v[192:195]\n"
    "s_waitcnt lgkmcnt(2)\n"
    "v_mfma_f32_16x16x32_bf16 v[196:199], a[180:183], a[208:211], v[196:199]\n"
    "v_mfma_f32_16x16x32_bf16 v[200:203], a[180:183], a[212:215], v[200:203]\n"
    "v_mfma_f32_16x16x32_bf16 v[204:207], a[180:183], a[216:219], v[204:207]\n"
    "v_mfma_f32_16x16x32_bf16 v[208:211], a[180:183], a[220:223], v[208:211]\n"
    "v_mfma_f32_16x16x32_bf16 v[212:215], a[180:183], a[224:227], v[212:215]\n"
    "s_waitcnt lgkmcnt(0)\n"
    "s_barrier\n"
    "v_mfma_f32_16x16x32_bf16 v[216:219], a[184:187], a[208:211], v[216:219]\n"
    "ds_read_b128 a[228:231], v29\n"
    "ds_read_b64_tr_b16 a[144:145], v30 offset:16384\n"
    "v_mfma_f32_16x16x32_bf16 v[220:223], a[184:187], a[212:215], v[220:223]\n"
    "ds_read_b64_tr_b16 a[146:147], v30 offset:16640\n"
    "ds_read_b128 a[232:235], v29 offset:2048\n"
    "v_mfma_f32_16x16x32_bf16 v[224:227], a[184:187], a[216:219], v[224:227]\n"
    "ds_read_b128 a[236:239], v29 offset:4096\n"
    "v_mfma_f32_16x16x32_bf16 v[228:231], a[184:187], a[220:223], v[228:231]\n"
    "ds_read_b128 a[240:243], v29 offset:6144\n"
    "v_mfma_f32_16x16x32_bf16 v[232:235], a[184:187], a[224:227], v[232:235]\n"
    "ds_read_b128 a[244:247], v29 offset:8192\n"
    "ds_read_b64_tr_b16 a[148:149], v30 offset:16392\n"
    "v_subrev_u32 v29, s81, v29\n"
    "v_mfma_f32_16x16x32_bf16 v[236:239], a[188:191], a[208:211], v[236:239]\n"
    "ds_read_b64_tr_b16 a[150:151], v30 offset:16648\n"
    "v_mfma_f32_16x16x32_bf16 v[240:243], a[188:191], a[212:215], v[240:243]\n"
    "s_mov_b32 m0, 0x1000\n"
    "v_mfma_f32_16x16x32_bf16 v[244:247], a[188:191], a[216:219], v[244:247]\n"
    "s_mov_b32 s86, 0x1000\n"
    "v_mfma_f32_16x16x32_bf16 v[248:251], a[188:191], a[220:223], v[248:251]\n"
    "ds_read_b64_tr_b16 a[152:153], v30 offset:16896\n"
    "ds_read_b64_tr_b16 a[154:155], v30 offset:17152\n"
    "v_mfma_f32_16x16x32_bf16 v[252:255], a[188:191], a[224:227], v[252:255]\n"
    "s_waitcnt lgkmcnt(4)\n"
    "v_mfma_f32_16x16x32_bf16 v[96:99], a[144:147], a[228:231], v[96:99]\n"
    "v_mfma_f32_16x16x32_bf16 v[100:103], a[144:147], a[232:235], v[100:103]\n"
    "v_mfma_f32_16x16x32_bf16 v[104:107], a[144:147], a[236:239], v[104:107]\n"
    "buffer_load_dwordx4 v8, s[44:47], s86 offen nt lds\n"
    "v_mfma_f32_16x16x32_bf16 v[108:111], a[144:147], a[240:243], v[108:111]\n"
    "ds_read_b64_tr_b16 a[156:157], v30 offset:16904\n"
    "ds_read_b64_tr_b16 a[158:159], v30 offset:17160\n"
    "v_mfma_f32_16x16x32_bf16 v[112:115], a[144:147], a[244:247], v[112:115]\n"
    "s_waitcnt lgkmcnt(2)\n"
    "v_mfma_f32_16x16x32_bf16 v[116:119], a[148:151], a[228:231], v[116:119]\n"
    "v_mfma_f32_16x16x32_bf16 v[120:123], a[148:151], a[232:235], v[120:123]\n"
    "v_mfma_f32_16x16x32_bf16 v[124:127], a[148:151], a[236:239], v[124:127]\n"
    "v_mfma_f32_16x16x32_bf16 v[128:131], a[148:151], a[240:243], v[128:131]\n"
    "ds_read_b64_tr_b16 a[160:161], v30 offset:17408\n"
    "ds_read_b64_tr_b16 a[162:163], v30 offset:17664\n"
    "v_mfma_f32_16x16x32_bf16 v[132:135], a[148:151], a[244:247], v[132:135]\n"
    "buffer_load_dwordx4 v9, s[44:47], s86 offen offset:1024 nt lds\n"
    "v_mfma_f32_16x16x32_bf16 v[136:139], a[152:155], a[228:231], v[136:139]\n"
    "v_mfma_f32_16x16x32_bf16 v[140:143], a[152:155], a[232:235], v[140:143]\n"
    "v_mfma_f32_16x16x32_bf16 v[144:147], a[152:155], a[236:239], v[144:147]\n"
    "v_mfma_f32_16x16x32_bf16 v[148:151], a[152:155], a[240:243], v[148:151]\n"
    "ds_read_b64_tr_b16 a[164:165], v30 offset:17416\n"
    "ds_read_b64_tr_b16 a[166:167], v30 offset:17672\n"
    "v_mfma_f32_16x16x32_bf16 v[152:155], a[152:155], a[244:247], v[152:155]\n"
    "s_waitcnt lgkmcnt(2)\n"
    "v_mfma_f32_16x16x32_bf16 v[156:159], a[156:159], a[228:231], v[156:159]\n"
    "v_mfma_f32_16x16x32_bf16 v[160:163], a[156:159], a[232:235], v[160:163]\n"
    "buffer_load_dwordx4 v10, s[44:47], s86 offen offset:2048 nt lds\n"
    "v_mfma_f32_16x16x32_bf16 v[164:167], a[156:159], a[236:239], v[164:167]\n"
    "v_mfma_f32_16x16x32_bf16 v[168:171], a[156:159], a[240:243], v[168:171]\n"
    "ds_read_b64_tr_b16 a[168:169], v30 offset:17920\n"
    "ds_read_b64_tr_b16 a[170:171], v30 offset:18176\n"
    "v_mfma_f32_16x16x32_bf16 v[172:175], a[156:159], a[244:247], v[172:175]\n"
    "v_mfma_f32_16x16x32_bf16 v[176:179], a[160:163], a[228:231], v[176:179]\n"
    "ds_read_b64_tr_b16 a[172:173], v30 offset:17928\n"
    "ds_read_b64_tr_b16 a[174:175], v30 offset:18184\n"
    "v_mfma_f32_16x16x32_bf16 v[180:183], a[160:163], a[232:235], v[180:183]\n"
    "v_mfma_f32_16x16x32_bf16 v[184:187], a[160:163], a[236:239], v[184:187]\n"
    "v_mfma_f32_16x16x32_bf16 v[188:191], a[160:163], a[240:243], v[188:191]\n"
    "buffer_load_dwordx4 v11, s[44:47], s86 offen offset:3072 nt lds\n"
    "v_mfma_f32_16x16x32_bf16 v[192:195], a[160:163], a[244:247], v[192:195]\n"
    "s_waitcnt lgkmcnt(2)\n"
    "v_mfma_f32_16x16x32_bf16 v[196:199], a[164:167], a[228:231], v[196:199]\n"
    "v_mfma_f32_16x16x32_bf16 v[200:203], a[164:167], a[232:235], v[200:203]\n"
    "v_mfma_f32_16x16x32_bf16 v[204:207], a[164:167], a[236:239], v[204:207]\n"
    "v_mfma_f32_16x16x32_bf16 v[208:211], a[164:167], a[240:243], v[208:211]\n"
    "v_mfma_f32_16x16x32_bf16 v[212:215], a[164:167], a[244:247], v[212:215]\n"
    "s_waitcnt lgkmcnt(0)\n"
    "s_barrier\n"
    "v_mfma_f32_16x16x32_bf16 v[216:219], a[168:171], a[228:231], v[216:219]\n"
    "ds_read_b128 a[208:211], v28\n"
    "ds_read_b64_tr_b16 a[176:177], v30 offset:32768\n"
    "v_mfma_f32_16x16x32_bf16 v[220:223], a[168:171], a[232:235], v[220:223]\n"
    "ds_read_b64_tr_b16 a[178:179], v30 offset:33024\n"
    "ds_read_b128 a[212:215], v28 offset:2048\n"
    "v_mfma_f32_16x16x32_bf16 v[224:227], a[168:171], a[236:239], v[224:227]\n"
    "ds_read_b128 a[216:219], v28 offset:4096\n"
    "v_mfma_f32_16x16x32_bf16 v[228:231], a[168:171], a[240:243], v[228:231]\n"
    "ds_read_b128 a[220:223], v28 offset:6144\n"
    "v_mfma_f32_16x16x32_bf16 v[232:235], a[168:171], a[244:247], v[232:235]\n"
    "ds_read_b128 a[224:227], v28 offset:8192\n"
    "ds_read_b64_tr_b16 a[180:181], v30 offset:32776\n"
    "v_add_u32 v28, s81, v28\n"
    "v_mfma_f32_16x16x32_bf16 v[236:239], a[172:175], a[228:231], v[236:239]\n"
    "ds_read_b64_tr_b16 a[182:183], v30 offset:33032\n"
    "v_mfma_f32_16x16x32_bf16 v[240:243], a[172:175], a[232:235], v[240:243]\n"
    "s_mov_b32 m0, 0x2000\n"
    "v_mfma_f32_16x16x32_bf16 v[244:247], a[172:175], a[236:239], v[244:247]\n"
    "s_mov_b32 s86, 0x2000\n"
    "v_mfma_f32_16x16x32_bf16 v[248:251], a[172:175], a[240:243], v[248:251]\n"
    "ds_read_b64_tr_b16 a[184:185], v30 offset:33280\n"
    "ds_read_b64_tr_b16 a[186:187], v30 offset:33536\n"
    "v_mfma_f32_16x16x32_bf16 v[252:255], a[172:175], a[244:247], v[252:255]\n"
    "s_waitcnt lgkmcnt(4)\n"
    "v_mfma_f32_16x16x32_bf16 v[96:99], a[176:179], a[208:211], v[96:99]\n"
    "v_mfma_f32_16x16x32_bf16 v[100:103], a[176:179], a[212:215], v[100:103]\n"
    "v_mfma_f32_16x16x32_bf16 v[104:107], a[176:179], a[216:219], v[104:107]\n"
    "buffer_load_dwordx4 v8, s[44:47], s86 offen nt lds\n"
    "v_mfma_f32_16x16x32_bf16 v[108:111], a[176:179], a[220:223], v[108:111]\n"
    "ds_read_b64_tr_b16 a[188:189], v30 offset:33288\n"
    "ds_read_b64_tr_b16 a[190:191], v30 offset:33544\n"
    "v_mfma_f32_16x16x32_bf16 v[112:115], a[176:179], a[224:227], v[112:115]\n"
    "s_waitcnt lgkmcnt(2)\n"
    "v_mfma_f32_16x16x32_bf16 v[116:119], a[180:183], a[208:211], v[116:119]\n"
    "v_mfma_f32_16x16x32_bf16 v[120:123], a[180:183], a[212:215], v[120:123]\n"
    "v_mfma_f32_16x16x32_bf16 v[124:127], a[180:183], a[216:219], v[124:127]\n"
    "v_mfma_f32_16x16x32_bf16 v[128:131], a[180:183], a[220:223], v[128:131]\n"
    "ds_read_b64_tr_b16 a[144:145], v30 offset:33792\n"
    "ds_read_b64_tr_b16 a[146:147], v30 offset:34048\n"
    "v_mfma_f32_16x16x32_bf16 v[132:135], a[180:183], a[224:227], v[132:135]\n"
    "buffer_load_dwordx4 v9, s[44:47], s86 offen offset:1024 nt lds\n"
    "v_mfma_f32_16x16x32_bf16 v[136:139], a[184:187], a[208:211], v[136:139]\n"
    "v_mfma_f32_16x16x32_bf16 v[140:143], a[184:187], a[212:215], v[140:143]\n"
    "v_mfma_f32_16x16x32_bf16 v[144:147], a[184:187], a[216:219], v[144:147]\n"
    "v_mfma_f32_16x16x32_bf16 v[148:151], a[184:187], a[220:223], v[148:151]\n"
    "ds_read_b64_tr_b16 a[148:149], v30 offset:33800\n"
    "ds_read_b64_tr_b16 a[150:151], v30 offset:34056\n"
    "v_mfma_f32_16x16x32_bf16 v[152:155], a[184:187], a[224:227], v[152:155]\n"
    "s_waitcnt lgkmcnt(2)\n"
    "v_mfma_f32_16x16x32_bf16 v[156:159], a[188:191], a[208:211], v[156:159]\n"
    "v_mfma_f32_16x16x32_bf16 v[160:163], a[188:191], a[212:215], v[160:163]\n"
    "buffer_load_dwordx4 v10, s[44:47], s86 offen offset:2048 nt lds\n"
    "v_mfma_f32_16x16x32_bf16 v[164:167], a[188:191], a[216:219], v[164:167]\n"
    "v_mfma_f32_16x16x32_bf16 v[168:171], a[188:191], a[220:223], v[168:171]\n"
    "ds_read_b64_tr_b16 a[152:153], v30 offset:34304\n"
    "ds_read_b64_tr_b16 a[154:155], v30 offset:34560\n"
    "v_mfma_f32_16x16x32_bf16 v[172:175], a[188:191], a[224:227], v[172:175]\n"
    "v_mfma_f32_16x16x32_bf16 v[176:179], a[144:147], a[208:211], v[176:179]\n"
    "ds_read_b64_tr_b16 a[156:157], v30 offset:34312\n"
    "ds_read_b64_tr_b16 a[158:159], v30 offset:34568\n"
    "v_mfma_f32_16x16x32_bf16 v[180:183], a[144:147], a[212:215], v[180:183]\n"
    "v_mfma_f32_16x16x32_bf16 v[184:187], a[144:147], a[216:219], v[184:187]\n"
    "v_mfma_f32_16x16x32_bf16 v[188:191], a[144:147], a[220:223], v[188:191]\n"
    "buffer_load_dwordx4 v11, s[44:47], s86 offen offset:3072 nt lds\n"
    "v_mfma_f32_16x16x32_bf16 v[192:195], a[144:147], a[224:227], v[192:195]\n"
    "s_waitcnt lgkmcnt(2)\n"
    "v_mfma_f32_16x16x32_bf16 v[196:199], a[148:151], a[208:211], v[196:199]\n"
    "v_mfma_f32_16x16x32_bf16 v[200:203], a[148:151], a[212:215], v[200:203]\n"
    "v_mfma_f32_16x16x32_bf16 v[204:207], a[148:151], a[216:219], v[204:207]\n"
    "v_mfma_f32_16x16x32_bf16 v[208:211], a[148:151], a[220:223], v[208:211]\n"
    "v_mfma_f32_16x16x32_bf16 v[212:215], a[148:151], a[224:227], v[212:215]\n"
    "s_waitcnt lgkmcnt(0)\n"
    "s_barrier\n"
    "v_mfma_f32_16x16x32_bf16 v[216:219], a[152:155], a[208:211], v[216:219]\n"
    "ds_read_b128 a[228:231], v29\n"
    "ds_read_b64_tr_b16 a[160:161], v30\n"
    "v_mfma_f32_16x16x32_bf16 v[220:223], a[152:155], a[212:215], v[220:223]\n"
    "ds_read_b64_tr_b16 a[162:163], v30 offset:256\n"
    "ds_read_b128 a[232:235], v29 offset:2048\n"
    "v_mfma_f32_16x16x32_bf16 v[224:227], a[152:155], a[216:219], v[224:227]\n"
    "ds_read_b128 a[236:239], v29 offset:4096\n"
    "v_mfma_f32_16x16x32_bf16 v[228:231], a[152:155], a[220:223], v[228:231]\n"
    "ds_read_b128 a[240:243], v29 offset:6144\n"
    "v_mfma_f32_16x16x32_bf16 v[232:235], a[152:155], a[224:227], v[232:235]\n"
    "ds_read_b128 a[244:247], v29 offset:8192\n"
    "ds_read_b64_tr_b16 a[164:165], v30 offset:8\n"
    "v_add_u32 v29, s81, v29\n"
    "v_mfma_f32_16x16x32_bf16 v[236:239], a[156:159], a[208:211], v[236:239]\n"
    "ds_read_b64_tr_b16 a[166:167], v30 offset:264\n"
    "v_mfma_f32_16x16x32_bf16 v[240:243], a[156:159], a[212:215], v[240:243]\n"
    "s_mov_b32 m0, 0x3000\n"
    "v_mfma_f32_16x16x32_bf16 v[244:247], a[156:159], a[216:219], v[244:247]\n"
    "s_mov_b32 s86, 0x3000\n"
    "v_mfma_f32_16x16x32_bf16 v[248:251], a[156:159], a[220:223], v[248:251]\n"
    "ds_read_b64_tr_b16 a[168:169], v30 offset:512\n"
    "ds_read_b64_tr_b16 a[170:171], v30 offset:768\n"
    "v_mfma_f32_16x16x32_bf16 v[252:255], a[156:159], a[224:227], v[252:255]\n"
    "s_waitcnt lgkmcnt(4)\n"
    "v_mfma_f32_16x16x32_bf16 v[96:99], a[160:163], a[228:231], v[96:99]\n"
    "v_mfma_f32_16x16x32_bf16 v[100:103], a[160:163], a[232:235], v[100:103]\n"
    "v_mfma_f32_16x16x32_bf16 v[104:107], a[160:163], a[236:239], v[104:107]\n"
    "buffer_load_dwordx4 v8, s[44:47], s86 offen nt lds\n"
    "v_mfma_f32_16x16x32_bf16 v[108:111], a[160:163], a[240:243], v[108:111]\n"
    "ds_read_b64_tr_b16 a[172:173], v30 offset:520\n"
    "ds_read_b64_tr_b16 a[174:175], v30 offset:776\n"
    "v_mfma_f32_16x16x32_bf16 v[112:115], a[160:163], a[244:247], v[112:115]\n"
    "s_waitcnt lgkmcnt(2)\n"
    "v_mfma_f32_16x16x32_bf16 v[116:119], a[164:167], a[228:231], v[116:119]\n"
    "v_mfma_f32_16x16x32_bf16 v[120:123], a[164:167], a[232:235], v[120:123]\n"
    "v_mfma_f32_16x16x32_bf16 v[124:127], a[164:167], a[236:239], v[124:127]\n"
    "v_mfma_f32_16x16x32_bf16 v[128:131], a[164:167], a[240:243], v[128:131]\n"
    "ds_read_b64_tr_b16 a[176:177], v30 offset:1024\n"
    "ds_read_b64_tr_b16 a[178:179], v30 offset:1280\n"
    "v_mfma_f32_16x16x32_bf16 v[132:135], a[164:167], a[244:247], v[132:135]\n"
    "buffer_load_dwordx4 v9, s[44:47], s86 offen offset:1024 nt lds\n"
    "v_mfma_f32_16x16x32_bf16 v[136:139], a[168:171], a[228:231], v[136:139]\n"
    "v_mfma_f32_16x16x32_bf16 v[140:143], a[168:171], a[232:235], v[140:143]\n"
    "v_mfma_f32_16x16x32_bf16 v[144:147], a[168:171], a[236:239], v[144:147]\n"
    "v_mfma_f32_16x16x32_bf16 v[148:151], a[168:171], a[240:243], v[148:151]\n"
    "ds_read_b64_tr_b16 a[180:181], v30 offset:1032\n"
    "ds_read_b64_tr_b16 a[182:183], v30 offset:1288\n"
    "v_mfma_f32_16x16x32_bf16 v[152:155], a[168:171], a[244:247], v[152:155]\n"
    "s_waitcnt lgkmcnt(2)\n"
    "ds_read_b64_tr_b16 a[184:185], v30 offset:1536\n"
    "ds_read_b64_tr_b16 a[186:187], v30 offset:1792\n"
    "v_mfma_f32_16x16x32_bf16 v[156:159], a[172:175], a[228:231], v[156:159]\n"
    "v_mfma_f32_16x16x32_bf16 v[160:163], a[172:175], a[232:235], v[160:163]\n"
    "buffer_load_dwordx4 v10, s[44:47], s86 offen offset:2048 nt lds\n"
    "v_mfma_f32_16x16x32_bf16 v[164:167], a[172:175], a[236:239], v[164:167]\n"
    "v_mfma_f32_16x16x32_bf16 v[168:171], a[172:175], a[240:243], v[168:171]\n"
    "v_mfma_f32_16x16x32_bf16 v[172:175], a[172:175], a[244:247], v[172:175]\n"
    "ds_read_b64_tr_b16 a[188:189], v30 offset:1544\n"
    "ds_read_b64_tr_b16 a[190:191], v30 offset:1800\n"
    "v_mfma_f32_16x16x32_bf16 v[176:179], a[176:179], a[228:231], v[176:179]\n"
    "v_mfma_f32_16x16x32_bf16 v[180:183], a[176:179], a[232:235], v[180:183]\n"
    "v_mfma_f32_16x16x32_bf16 v[184:187], a[176:179], a[236:239], v[184:187]\n"
    "v_mfma_f32_16x16x32_bf16 v[188:191], a[176:179], a[240:243], v[188:191]\n"
    "buffer_load_dwordx4 v11, s[44:47], s86 offen offset:3072 nt lds\n"
    "v_mfma_f32_16x16x32_bf16 v[192:195], a[176:179], a[244:247], v[192:195]\n"
    "s_waitcnt lgkmcnt(4)\n"
    "v_mfma_f32_16x16x32_bf16 v[196:199], a[180:183], a[228:231], v[196:199]\n"
    "v_mfma_f32_16x16x32_bf16 v[200:203], a[180:183], a[232:235], v[200:203]\n"
    "v_mfma_f32_16x16x32_bf16 v[204:207], a[180:183], a[236:239], v[204:207]\n"
    "v_mfma_f32_16x16x32_bf16 v[208:211], a[180:183], a[240:243], v[208:211]\n"
    "v_mfma_f32_16x16x32_bf16 v[212:215], a[180:183], a[244:247], v[212:215]\n"
    "s_sub_u32 s81, 0, s81\n"
    "s_add_u32 s80, s80, 1\n"
    "s_cmp_lt_u32 s80, 3\n"
    "s_waitcnt lgkmcnt(0)\n"
    "s_barrier\n"
    "v_mfma_f32_16x16x32_bf16 v[216:219], a[184:187], a[228:231], v[216:219]\n"
    "ds_read_b128 a[208:211], v28\n"
    "ds_read_b64_tr_b16 a[144:145], v30 offset:16384\n"
    "v_mfma_f32_16x16x32_bf16 v[220:223], a[184:187], a[232:235], v[220:223]\n"
    "ds_read_b64_tr_b16 a[146:147], v30 offset:16640\n"
    "ds_read_b128 a[212:215], v28 offset:2048\n"
    "v_mfma_f32_16x16x32_bf16 v[224:227], a[184:187], a[236:239], v[224:227]\n"
    "ds_read_b128 a[216:219], v28 offset:4096\n"
    "v_mfma_f32_16x16x32_bf16 v[228:231], a[184:187], a[240:243], v[228:231]\n"
    "ds_read_b128 a[220:223], v28 offset:6144\n"
    "v_mfma_f32_16x16x32_bf16 v[232:235], a[184:187], a[244:247], v[232:235]\n"
    "ds_read_b128 a[224:227], v28 offset:8192\n"
    "ds_read_b64_tr_b16 a[148:149], v30 offset:16392\n"
    "v_add_u32 v28, s81, v28\n"
    "v_mfma_f32_16x16x32_bf16 v[236:239], a[188:191], a[228:231], v[236:239]\n"
    "ds_read_b64_tr_b16 a[150:151], v30 offset:16648\n"
    "v_mfma_f32_16x16x32_bf16 v[240:243], a[188:191], a[232:235], v[240:243]\n"
    "s_mov_b32 m0, 0x4000\n"
    "v_mfma_f32_16x16x32_bf16 v[244:247], a[188:191], a[236:239], v[244:247]\n"
    "s_mov_b32 s86, 0x20000\n"
    "v_mfma_f32_16x16x32_bf16 v[248:251], a[188:191], a[240:243], v[248:251]\n"
    "ds_read_b64_tr_b16 a[152:153], v30 offset:16896\n"
    "ds_read_b64_tr_b16 a[154:155], v30 offset:17152\n"
    "v_mfma_f32_16x16x32_bf16 v[252:255], a[188:191], a[244:247], v[252:255]\n"
    "s_waitcnt lgkmcnt(4)\n"
    "v_mfma_f32_16x16x32_bf16 v[96:99], a[144:147], a[208:211], v[96:99]\n"
    "v_mfma_f32_16x16x32_bf16 v[100:103], a[144:147], a[212:215], v[100:103]\n"
    "v_mfma_f32_16x16x32_bf16 v[104:107], a[144:147], a[216:219], v[104:107]\n"
    "buffer_load_dwordx4 v8, s[44:47], s86 offen nt lds\n"
    "v_mfma_f32_16x16x32_bf16 v[108:111], a[144:147], a[220:223], v[108:111]\n"
    "ds_read_b64_tr_b16 a[156:157], v30 offset:16904\n"
    "ds_read_b64_tr_b16 a[158:159], v30 offset:17160\n"
    "v_mfma_f32_16x16x32_bf16 v[112:115], a[144:147], a[224:227], v[112:115]\n"
    "s_waitcnt lgkmcnt(2)\n"
    "v_mfma_f32_16x16x32_bf16 v[116:119], a[148:151], a[208:211], v[116:119]\n"
    "v_mfma_f32_16x16x32_bf16 v[120:123], a[148:151], a[212:215], v[120:123]\n"
    "v_mfma_f32_16x16x32_bf16 v[124:127], a[148:151], a[216:219], v[124:127]\n"
    "v_mfma_f32_16x16x32_bf16 v[128:131], a[148:151], a[220:223], v[128:131]\n"
    "ds_read_b64_tr_b16 a[160:161], v30 offset:17408\n"
    "ds_read_b64_tr_b16 a[162:163], v30 offset:17664\n"
    "v_mfma_f32_16x16x32_bf16 v[132:135], a[148:151], a[224:227], v[132:135]\n"
    "buffer_load_dwordx4 v9, s[44:47], s86 offen offset:1024 nt lds\n"
    "v_mfma_f32_16x16x32_bf16 v[136:139], a[152:155], a[208:211], v[136:139]\n"
    "v_mfma_f32_16x16x32_bf16 v[140:143], a[152:155], a[212:215], v[140:143]\n"
    "v_mfma_f32_16x16x32_bf16 v[144:147], a[152:155], a[216:219], v[144:147]\n"
    "v_mfma_f32_16x16x32_bf16 v[148:151], a[152:155], a[220:223], v[148:151]\n"
    "ds_read_b64_tr_b16 a[164:165], v30 offset:17416\n"
    "ds_read_b64_tr_b16 a[166:167], v30 offset:17672\n"
    "v_mfma_f32_16x16x32_bf16 v[152:155], a[152:155], a[224:227], v[152:155]\n"
    "s_waitcnt lgkmcnt(2)\n"
    "v_mfma_f32_16x16x32_bf16 v[156:159], a[156:159], a[208:211], v[156:159]\n"
    "v_mfma_f32_16x16x32_bf16 v[160:163], a[156:159], a[212:215], v[160:163]\n"
    "buffer_load_dwordx4 v10, s[44:47], s86 offen offset:2048 nt lds\n"
    "v_mfma_f32_16x16x32_bf16 v[164:167], a[156:159], a[216:219], v[164:167]\n"
    "v_mfma_f32_16x16x32_bf16 v[168:171], a[156:159], a[220:223], v[168:171]\n"
    "ds_read_b64_tr_b16 a[168:169], v30 offset:17920\n"
    "ds_read_b64_tr_b16 a[170:171], v30 offset:18176\n"
    "v_mfma_f32_16x16x32_bf16 v[172:175], a[156:159], a[224:227], v[172:175]\n"
    "v_mfma_f32_16x16x32_bf16 v[176:179], a[160:163], a[208:211], v[176:179]\n"
    "ds_read_b64_tr_b16 a[172:173], v30 offset:17928\n"
    "ds_read_b64_tr_b16 a[174:175], v30 offset:18184\n"
    "v_mfma_f32_16x16x32_bf16 v[180:183], a[160:163], a[212:215], v[180:183]\n"
    "v_mfma_f32_16x16x32_bf16 v[184:187], a[160:163], a[216:219], v[184:187]\n"
    "v_mfma_f32_16x16x32_bf16 v[188:191], a[160:163], a[220:223], v[188:191]\n"
    "buffer_load_dwordx4 v11, s[44:47], s86 offen offset:3072 nt lds\n"
    "v_mfma_f32_16x16x32_bf16 v[192:195], a[160:163], a[224:227], v[192:195]\n"
    "s_waitcnt lgkmcnt(2)\n"
    "v_mfma_f32_16x16x32_bf16 v[196:199], a[164:167], a[208:211], v[196:199]\n"
    "v_mfma_f32_16x16x32_bf16 v[200:203], a[164:167], a[212:215], v[200:203]\n"
    "v_mfma_f32_16x16x32_bf16 v[204:207], a[164:167], a[216:219], v[204:207]\n"
    "v_mfma_f32_16x16x32_bf16 v[208:211], a[164:167], a[220:223], v[208:211]\n"
    "v_mfma_f32_16x16x32_bf16 v[212:215], a[164:167], a[224:227], v[212:215]\n"
    "s_waitcnt lgkmcnt(0)\n"
    "s_barrier\n"
    "v_mfma_f32_16x16x32_bf16 v[216:219], a[168:171], a[208:211], v[216:219]\n"
    "ds_read_b128 a[228:231], v29\n"
    "ds_read_b64_tr_b16 a[176:177], v30 offset:32768\n"
    "v_mfma_f32_16x16x32_bf16 v[220:223], a[168:171], a[212:215], v[220:223]\n"
    "ds_read_b64_tr_b16 a[178:179], v30 offset:33024\n"
    "ds_read_b128 a[232:235], v29 offset:2048\n"
    "v_mfma_f32_16x16x32_bf16 v[224:227], a[168:171], a[216:219], v[224:227]\n"
    "ds_read_b128 a[236:239], v29 offset:4096\n"
    "v_mfma_f32_16x16x32_bf16 v[228:231], a[168:171], a[220:223], v[228:231]\n"
    "ds_read_b128 a[240:243], v29 offset:6144\n"
    "v_mfma_f32_16x16x32_bf16 v[232:235], a[168:171], a[224:227], v[232:235]\n"
    "ds_read_b128 a[244:247], v29 offset:8192\n"
    "ds_read_b64_tr_b16 a[180:181], v30 offset:32776\n"
    "v_add_u32 v29, s81, v29\n"
    "v_mfma_f32_16x16x32_bf16 v[236:239], a[172:175], a[208:211], v[236:239]\n"
    "ds_read_b64_tr_b16 a[182:183], v30 offset:33032\n"
    "v_mfma_f32_16x16x32_bf16 v[240:243], a[172:175], a[212:215], v[240:243]\n"
    "s_mov_b32 m0, 0x5000\n"
    "v_mfma_f32_16x16x32_bf16 v[244:247], a[172:175], a[216:219], v[244:247]\n"
    "s_mov_b32 s86, 0x21000\n"
    "v_mfma_f32_16x16x32_bf16 v[248:251], a[172:175], a[220:223], v[248:251]\n"
    "ds_read_b64_tr_b16 a[184:185], v30 offset:33280\n"
    "ds_read_b64_tr_b16 a[186:187], v30 offset:33536\n"
    "v_mfma_f32_16x16x32_bf16 v[252:255], a[172:175], a[224:227], v[252:255]\n"
    "s_waitcnt lgkmcnt(4)\n"
    "v_mfma_f32_16x16x32_bf16 v[96:99], a[176:179], a[228:231], v[96:99]\n"
    "v_mfma_f32_16x16x32_bf16 v[100:103], a[176:179], a[232:235], v[100:103]\n"
    "v_mfma_f32_16x16x32_bf16 v[104:107], a[176:179], a[236:239], v[104:107]\n"
    "buffer_load_dwordx4 v8, s[44:47], s86 offen nt lds\n"
    "v_mfma_f32_16x16x32_bf16 v[108:111], a[176:179], a[240:243], v[108:111]\n"
    "ds_read_b64_tr_b16 a[188:189], v30 offset:33288\n"
    "ds_read_b64_tr_b16 a[190:191], v30 offset:33544\n"
    "v_mfma_f32_16x16x32_bf16 v[112:115], a[176:179], a[244:247], v[112:115]\n"
    "s_waitcnt lgkmcnt(2)\n"
    "v_mfma_f32_16x16x32_bf16 v[116:119], a[180:183], a[228:231], v[116:119]\n"
    "v_mfma_f32_16x16x32_bf16 v[120:123], a[180:183], a[232:235], v[120:123]\n"
    "v_mfma_f32_16x16x32_bf16 v[124:127], a[180:183], a[236:239], v[124:127]\n"
    "v_mfma_f32_16x16x32_bf16 v[128:131], a[180:183], a[240:243], v[128:131]\n"
    "ds_read_b64_tr_b16 a[144:145], v30 offset:33792\n"
    "ds_read_b64_tr_b16 a[146:147], v30 offset:34048\n"
    "v_mfma_f32_16x16x32_bf16 v[132:135], a[180:183], a[244:247], v[132:135]\n"
    "buffer_load_dwordx4 v9, s[44:47], s86 offen offset:1024 nt lds\n"
    "v_mfma_f32_16x16x32_bf16 v[136:139], a[184:187], a[228:231], v[136:139]\n"
    "v_mfma_f32_16x16x32_bf16 v[140:143], a[184:187], a[232:235], v[140:143]\n"
    "v_mfma_f32_16x16x32_bf16 v[144:147], a[184:187], a[236:239], v[144:147]\n"
    "v_mfma_f32_16x16x32_bf16 v[148:151], a[184:187], a[240:243], v[148:151]\n"
    "ds_read_b64_tr_b16 a[148:149], v30 offset:33800\n"
    "ds_read_b64_tr_b16 a[150:151], v30 offset:34056\n"
    "v_mfma_f32_16x16x32_bf16 v[152:155], a[184:187], a[244:247], v[152:155]\n"
    "s_waitcnt lgkmcnt(2)\n"
    "v_mfma_f32_16x16x32_bf16 v[156:159], a[188:191], a[228:231], v[156:159]\n"
    "v_mfma_f32_16x16x32_bf16 v[160:163], a[188:191], a[232:235], v[160:163]\n"
    "buffer_load_dwordx4 v10, s[44:47], s86 offen offset:2048 nt lds\n"
    "v_mfma_f32_16x16x32_bf16 v[164:167], a[188:191], a[236:239], v[164:167]\n"
    "v_mfma_f32_16x16x32_bf16 v[168:171], a[188:191], a[240:243], v[168:171]\n"
    "ds_read_b64_tr_b16 a[152:153], v30 offset:34304\n"
    "ds_read_b64_tr_b16 a[154:155], v30 offset:34560\n"
    "v_mfma_f32_16x16x32_bf16 v[172:175], a[188:191], a[244:247], v[172:175]\n"
    "v_mfma_f32_16x16x32_bf16 v[176:179], a[144:147], a[228:231], v[176:179]\n"
    "ds_read_b64_tr_b16 a[156:157], v30 offset:34312\n"
    "ds_read_b64_tr_b16 a[158:159], v30 offset:34568\n"
    "v_mfma_f32_16x16x32_bf16 v[180:183], a[144:147], a[232:235], v[180:183]\n"
    "v_mfma_f32_16x16x32_bf16 v[184:187], a[144:147], a[236:239], v[184:187]\n"
    "v_mfma_f32_16x16x32_bf16 v[188:191], a[144:147], a[240:243], v[188:191]\n"
    "buffer_load_dwordx4 v11, s[44:47], s86 offen offset:3072 nt lds\n"
    "v_mfma_f32_16x16x32_bf16 v[192:195], a[144:147], a[244:247], v[192:195]\n"
    "s_waitcnt lgkmcnt(2)\n"
    "v_mfma_f32_16x16x32_bf16 v[196:199], a[148:151], a[228:231], v[196:199]\n"
    "v_mfma_f32_16x16x32_bf16 v[200:203], a[148:151], a[232:235], v[200:203]\n"
    "v_mfma_f32_16x16x32_bf16 v[204:207], a[148:151], a[236:239], v[204:207]\n"
    "v_mfma_f32_16x16x32_bf16 v[208:211], a[148:151], a[240:243], v[208:211]\n"
    "v_mfma_f32_16x16x32_bf16 v[212:215], a[148:151], a[244:247], v[212:215]\n"
    "s_waitcnt lgkmcnt(0)\n"
    "s_barrier\n"
    "v_mfma_f32_16x16x32_bf16 v[216:219], a[152:155], a[228:231], v[216:219]\n"
    "ds_read_b128 a[208:211], v28\n"
    "ds_read_b64_tr_b16 a[160:161], v30\n"
    "v_mfma_f32_16x16x32_bf16 v[220:223], a[152:155], a[232:235], v[220:223]\n"
    "ds_read_b64_tr_b16 a[162:163], v30 offset:256\n"
    "ds_read_b128 a[212:215], v28 offset:2048\n"
    "v_mfma_f32_16x16x32_bf16 v[224:227], a[152:155], a[236:239], v[224:227]\n"
    "ds_read_b128 a[216:219], v28 offset:4096\n"
    "v_mfma_f32_16x16x32_bf16 v[228:231], a[152:155], a[240:243], v[228:231]\n"
    "ds_read_b128 a[220:223], v28 offset:6144\n"
    "v_mfma_f32_16x16x32_bf16 v[232:235], a[152:155], a[244:247], v[232:235]\n"
    "ds_read_b128 a[224:227], v28 offset:8192\n"
    "ds_read_b64_tr_b16 a[164:165], v30 offset:8\n"
    "v_subrev_u32 v28, s81, v28\n"
    "v_mfma_f32_16x16x32_bf16 v[236:239], a[156:159], a[228:231], v[236:239]\n"
    "ds_read_b64_tr_b16 a[166:167], v30 offset:264\n"
    "v_mfma_f32_16x16x32_bf16 v[240:243], a[156:159], a[232:235], v[240:243]\n"
    "s_mov_b32 m0, 0x6000\n"
    "v_mfma_f32_16x16x32_bf16 v[244:247], a[156:159], a[236:239], v[244:247]\n"
    "s_mov_b32 s86, 0x22000\n"
    "v_mfma_f32_16x16x32_bf16 v[248:251], a[156:159], a[240:243], v[248:251]\n"
    "ds_read_b64_tr_b16 a[168:169], v30 offset:512\n"
    "ds_read_b64_tr_b16 a[170:171], v30 offset:768\n"
    "v_mfma_f32_16x16x32_bf16 v[252:255], a[156:159], a[244:247], v[252:255]\n"
    "s_waitcnt lgkmcnt(4)\n"
    "v_mfma_f32_16x16x32_bf16 v[96:99], a[160:163], a[208:211], v[96:99]\n"
    "v_mfma_f32_16x16x32_bf16 v[100:103], a[160:163], a[212:215], v[100:103]\n"
    "v_mfma_f32_16x16x32_bf16 v[104:107], a[160:163], a[216:219], v[104:107]\n"
    "buffer_load_dwordx4 v8, s[44:47], s86 offen nt lds\n"
    "v_mfma_f32_16x16x32_bf16 v[108:111], a[160:163], a[220:223], v[108:111]\n"
    "ds_read_b64_tr_b16 a[172:173], v30 offset:520\n"
    "ds_read_b64_tr_b16 a[174:175], v30 offset:776\n"
    "v_mfma_f32_16x16x32_bf16 v[112:115], a[160:163], a[224:227], v[112:115]\n"
    "s_waitcnt lgkmcnt(2)\n"
    "v_mfma_f32_16x16x32_bf16 v[116:119], a[164:167], a[208:211], v[116:119]\n"
    "v_mfma_f32_16x16x32_bf16 v[120:123], a[164:167], a[212:215], v[120:123]\n"
    "v_mfma_f32_16x16x32_bf16 v[124:127], a[164:167], a[216:219], v[124:127]\n"
    "v_mfma_f32_16x16x32_bf16 v[128:131], a[164:167], a[220:223], v[128:131]\n"
    "ds_read_b64_tr_b16 a[176:177], v30 offset:1024\n"
    "ds_read_b64_tr_b16 a[178:179], v30 offset:1280\n"
    "v_mfma_f32_16x16x32_bf16 v[132:135], a[164:167], a[224:227], v[132:135]\n"
    "buffer_load_dwordx4 v9, s[44:47], s86 offen offset:1024 nt lds\n"
    "v_mfma_f32_16x16x32_bf16 v[136:139], a[168:171], a[208:211], v[136:139]\n"
    "v_mfma_f32_16x16x32_bf16 v[140:143], a[168:171], a[212:215], v[140:143]\n"
    "v_mfma_f32_16x16x32_bf16 v[144:147], a[168:171], a[216:219], v[144:147]\n"
    "v_mfma_f32_16x16x32_bf16 v[148:151], a[168:171], a[220:223], v[148:151]\n"
    "ds_read_b64_tr_b16 a[180:181], v30 offset:1032\n"
    "ds_read_b64_tr_b16 a[182:183], v30 offset:1288\n"
    "v_mfma_f32_16x16x32_bf16 v[152:155], a[168:171], a[224:227], v[152:155]\n"
    "s_waitcnt lgkmcnt(2)\n"
    "v_mfma_f32_16x16x32_bf16 v[156:159], a[172:175], a[208:211], v[156:159]\n"
    "v_mfma_f32_16x16x32_bf16 v[160:163], a[172:175], a[212:215], v[160:163]\n"
    "buffer_load_dwordx4 v10, s[44:47], s86 offen offset:2048 nt lds\n"
    "v_mfma_f32_16x16x32_bf16 v[164:167], a[172:175], a[216:219], v[164:167]\n"
    "v_mfma_f32_16x16x32_bf16 v[168:171], a[172:175], a[220:223], v[168:171]\n"
    "ds_read_b64_tr_b16 a[184:185], v30 offset:1536\n"
    "ds_read_b64_tr_b16 a[186:187], v30 offset:1792\n"
    "v_mfma_f32_16x16x32_bf16 v[172:175], a[172:175], a[224:227], v[172:175]\n"
    "v_mfma_f32_16x16x32_bf16 v[176:179], a[176:179], a[208:211], v[176:179]\n"
    "ds_read_b64_tr_b16 a[188:189], v30 offset:1544\n"
    "ds_read_b64_tr_b16 a[190:191], v30 offset:1800\n"
    "v_mfma_f32_16x16x32_bf16 v[180:183], a[176:179], a[212:215], v[180:183]\n"
    "v_mfma_f32_16x16x32_bf16 v[184:187], a[176:179], a[216:219], v[184:187]\n"
    "v_mfma_f32_16x16x32_bf16 v[188:191], a[176:179], a[220:223], v[188:191]\n"
    "buffer_load_dwordx4 v11, s[44:47], s86 offen offset:3072 nt lds\n"
    "v_mfma_f32_16x16x32_bf16 v[192:195], a[176:179], a[224:227], v[192:195]\n"
    "s_waitcnt lgkmcnt(2)\n"
    "v_mfma_f32_16x16x32_bf16 v[196:199], a[180:183], a[208:211], v[196:199]\n"
    "v_mfma_f32_16x16x32_bf16 v[200:203], a[180:183], a[212:215], v[200:203]\n"
    "v_mfma_f32_16x16x32_bf16 v[204:207], a[180:183], a[216:219], v[204:207]\n"
    "v_mfma_f32_16x16x32_bf16 v[208:211], a[180:183], a[220:223], v[208:211]\n"
    "v_mfma_f32_16x16x32_bf16 v[212:215], a[180:183], a[224:227], v[212:215]\n"
    "s_waitcnt lgkmcnt(0)\n"
    "s_barrier\n"
    "v_mfma_f32_16x16x32_bf16 v[216:219], a[184:187], a[208:211], v[216:219]\n"
    "ds_read_b128 a[228:231], v29\n"
    "ds_read_b64_tr_b16 a[144:145], v30 offset:16384\n"
    "v_mfma_f32_16x16x32_bf16 v[220:223], a[184:187], a[212:215], v[220:223]\n"
    "ds_read_b64_tr_b16 a[146:147], v30 offset:16640\n"
    "ds_read_b128 a[232:235], v29 offset:2048\n"
    "v_mfma_f32_16x16x32_bf16 v[224:227], a[184:187], a[216:219], v[224:227]\n"
    "ds_read_b128 a[236:239], v29 offset:4096\n"
    "v_mfma_f32_16x16x32_bf16 v[228:231], a[184:187], a[220:223], v[228:231]\n"
    "ds_read_b128 a[240:243], v29 offset:6144\n"
    "v_mfma_f32_16x16x32_bf16 v[232:235], a[184:187], a[224:227], v[232:235]\n"
    "ds_read_b128 a[244:247], v29 offset:8192\n"
    "ds_read_b64_tr_b16 a[148:149], v30 offset:16392\n"
    "v_subrev_u32 v29, s81, v29\n"
    "v_mfma_f32_16x16x32_bf16 v[236:239], a[188:191], a[208:211], v[236:239]\n"
    "ds_read_b64_tr_b16 a[150:151], v30 offset:16648\n"
    "v_mfma_f32_16x16x32_bf16 v[240:243], a[188:191], a[212:215], v[240:243]\n"
    "s_mov_b32 m0, 0x7000\n"
    "v_mfma_f32_16x16x32_bf16 v[244:247], a[188:191], a[216:219], v[244:247]\n"
    "s_mov_b32 s86, 0x23000\n"
    "v_mfma_f32_16x16x32_bf16 v[248:251], a[188:191], a[220:223], v[248:251]\n"
    "ds_read_b64_tr_b16 a[152:153], v30 offset:16896\n"
    "ds_read_b64_tr_b16 a[154:155], v30 offset:17152\n"
    "v_mfma_f32_16x16x32_bf16 v[252:255], a[188:191], a[224:227], v[252:255]\n"
    "s_waitcnt lgkmcnt(4)\n"
    "v_mfma_f32_16x16x32_bf16 v[96:99], a[144:147], a[228:231], v[96:99]\n"
    "v_mfma_f32_16x16x32_bf16 v[100:103], a[144:147], a[232:235], v[100:103]\n"
    "v_mfma_f32_16x16x32_bf16 v[104:107], a[144:147], a[236:239], v[104:107]\n"
    "buffer_load_dwordx4 v8, s[44:47], s86 offen nt lds\n"
    "v_mfma_f32_16x16x32_bf16 v[108:111], a[144:147], a[240:243], v[108:111]\n"
    "ds_read_b64_tr_b16 a[156:157], v30 offset:16904\n"
    "ds_read_b64_tr_b16 a[158:159], v30 offset:17160\n"
    "v_mfma_f32_16x16x32_bf16 v[112:115], a[144:147], a[244:247], v[112:115]\n"
    "s_waitcnt lgkmcnt(2)\n"
    "v_mfma_f32_16x16x32_bf16 v[116:119], a[148:151], a[228:231], v[116:119]\n"
    "v_mfma_f32_16x16x32_bf16 v[120:123], a[148:151], a[232:235], v[120:123]\n"
    "v_mfma_f32_16x16x32_bf16 v[124:127], a[148:151], a[236:239], v[124:127]\n"
    "v_mfma_f32_16x16x32_bf16 v[128:131], a[148:151], a[240:243], v[128:131]\n"
    "ds_read_b64_tr_b16 a[160:161], v30 offset:17408\n"
    "ds_read_b64_tr_b16 a[162:163], v30 offset:17664\n"
    "v_mfma_f32_16x16x32_bf16 v[132:135], a[148:151], a[244:247], v[132:135]\n"
    "buffer_load_dwordx4 v9, s[44:47], s86 offen offset:1024 nt lds\n"
    "v_mfma_f32_16x16x32_bf16 v[136:139], a[152:155], a[228:231], v[136:139]\n"
    "v_mfma_f32_16x16x32_bf16 v[140:143], a[152:155], a[232:235], v[140:143]\n"
    "v_mfma_f32_16x16x32_bf16 v[144:147], a[152:155], a[236:239], v[144:147]\n"
    "v_mfma_f32_16x16x32_bf16 v[148:151], a[152:155], a[240:243], v[148:151]\n"
    "ds_read_b64_tr_b16 a[164:165], v30 offset:17416\n"
    "ds_read_b64_tr_b16 a[166:167], v30 offset:17672\n"
    "v_mfma_f32_16x16x32_bf16 v[152:155], a[152:155], a[244:247], v[152:155]\n"
    "s_waitcnt lgkmcnt(2)\n"
    "v_mfma_f32_16x16x32_bf16 v[156:159], a[156:159], a[228:231], v[156:159]\n"
    "v_mfma_f32_16x16x32_bf16 v[160:163], a[156:159], a[232:235], v[160:163]\n"
    "buffer_load_dwordx4 v10, s[44:47], s86 offen offset:2048 nt lds\n"
    "v_mfma_f32_16x16x32_bf16 v[164:167], a[156:159], a[236:239], v[164:167]\n"
    "v_mfma_f32_16x16x32_bf16 v[168:171], a[156:159], a[240:243], v[168:171]\n"
    "ds_read_b64_tr_b16 a[168:169], v30 offset:17920\n"
    "ds_read_b64_tr_b16 a[170:171], v30 offset:18176\n"
    "v_mfma_f32_16x16x32_bf16 v[172:175], a[156:159], a[244:247], v[172:175]\n"
    "v_mfma_f32_16x16x32_bf16 v[176:179], a[160:163], a[228:231], v[176:179]\n"
    "ds_read_b64_tr_b16 a[172:173], v30 offset:17928\n"
    "ds_read_b64_tr_b16 a[174:175], v30 offset:18184\n"
    "v_mfma_f32_16x16x32_bf16 v[180:183], a[160:163], a[232:235], v[180:183]\n"
    "v_mfma_f32_16x16x32_bf16 v[184:187], a[160:163], a[236:239], v[184:187]\n"
    "v_mfma_f32_16x16x32_bf16 v[188:191], a[160:163], a[240:243], v[188:191]\n"
    "buffer_load_dwordx4 v11, s[44:47], s86 offen offset:3072 nt lds\n"
    "v_mfma_f32_16x16x32_bf16 v[192:195], a[160:163], a[244:247], v[192:195]\n"
    "s_waitcnt lgkmcnt(2)\n"
    "v_mfma_f32_16x16x32_bf16 v[196:199], a[164:167], a[228:231], v[196:199]\n"
    "v_mfma_f32_16x16x32_bf16 v[200:203], a[164:167], a[232:235], v[200:203]\n"
    "v_mfma_f32_16x16x32_bf16 v[204:207], a[164:167], a[236:239], v[204:207]\n"
    "v_mfma_f32_16x16x32_bf16 v[208:211], a[164:167], a[240:243], v[208:211]\n"
    "v_mfma_f32_16x16x32_bf16 v[212:215], a[164:167], a[244:247], v[212:215]\n"
    "s_waitcnt lgkmcnt(0)\n"
    "s_barrier\n"
    "v_mfma_f32_16x16x32_bf16 v[216:219], a[168:171], a[228:231], v[216:219]\n"
    "ds_read_b128 a[208:211], v28\n"
    "ds_read_b64_tr_b16 a[176:177], v30 offset:32768\n"
    "v_mfma_f32_16x16x32_bf16 v[220:223], a[168:171], a[232:235], v[220:223]\n"
    "ds_read_b64_tr_b16 a[178:179], v30 offset:33024\n"
    "ds_read_b128 a[212:215], v28 offset:2048\n"
    "v_mfma_f32_16x16x32_bf16 v[224:227], a[168:171], a[236:239], v[224:227]\n"
    "ds_read_b128 a[216:219], v28 offset:4096\n"
    "v_mfma_f32_16x16x32_bf16 v[228:231], a[168:171], a[240:243], v[228:231]\n"
    "ds_read_b128 a[220:223], v28 offset:6144\n"
    "v_mfma_f32_16x16x32_bf16 v[232:235], a[168:171], a[244:247], v[232:235]\n"
    "ds_read_b128 a[224:227], v28 offset:8192\n"
    "ds_read_b64_tr_b16 a[180:181], v30 offset:32776\n"
    "v_add_u32 v28, s81, v28\n"
    "v_mfma_f32_16x16x32_bf16 v[236:239], a[172:175], a[228:231], v[236:239]\n"
    "ds_read_b64_tr_b16 a[182:183], v30 offset:33032\n"
    "v_mfma_f32_16x16x32_bf16 v[240:243], a[172:175], a[232:235], v[240:243]\n"
    "s_lshr_b32 s74, s90, 2\n"
    "v_mfma_f32_16x16x32_bf16 v[244:247], a[172:175], a[236:239], v[244:247]\n"
    "s_lshl_b32 s74, s74, 6\n"
    "v_mfma_f32_16x16x32_bf16 v[248:251], a[172:175], a[240:243], v[248:251]\n"
    "s_lshl_b32 s75, s89, 2\n"
    "ds_read_b64_tr_b16 a[184:185], v30 offset:33280\n"
    "ds_read_b64_tr_b16 a[186:187], v30 offset:33536\n"
    "v_mfma_f32_16x16x32_bf16 v[252:255], a[172:175], a[244:247], v[252:255]\n"
    "s_waitcnt lgkmcnt(4)\n"
    "v_mfma_f32_16x16x32_bf16 v[96:99], a[176:179], a[208:211], v[96:99]\n"
    "s_add_u32 s74, s74, s75\n"
    "v_mfma_f32_16x16x32_bf16 v[100:103], a[176:179], a[212:215], v[100:103]\n"
    "s_and_b32 s75, s90, 3\n"
    "v_mfma_f32_16x16x32_bf16 v[104:107], a[176:179], a[216:219], v[104:107]\n"
    "s_add_u32 s74, s74, s75\n"
    "v_mfma_f32_16x16x32_bf16 v[108:111], a[176:179], a[220:223], v[108:111]\n"
    "s_mul_i32 s74, s74, 0x5000\n"
    "ds_read_b64_tr_b16 a[188:189], v30 offset:33288\n"
    "ds_read_b64_tr_b16 a[190:191], v30 offset:33544\n"
    "v_mfma_f32_16x16x32_bf16 v[112:115], a[176:179], a[224:227], v[112:115]\n"
    "s_waitcnt lgkmcnt(2)\n"
    "v_mfma_f32_16x16x32_bf16 v[116:119], a[180:183], a[208:211], v[116:119]\n"
    "s_mov_b64 s[64:65], s[28:29]\n"
    "v_mfma_f32_16x16x32_bf16 v[120:123], a[180:183], a[212:215], v[120:123]\n"
    "s_add_u32 s64, s64, s74\n"
    "s_addc_u32 s65, s65, 0\n"
    "v_mfma_f32_16x16x32_bf16 v[124:127], a[180:183], a[216:219], v[124:127]\n"
    "s_mov_b32 s66, 0x5000\n"
    "v_mfma_f32_16x16x32_bf16 v[128:131], a[180:183], a[220:223], v[128:131]\n"
    "s_mov_b32 s67, 0x20000\n"
    "ds_read_b64_tr_b16 a[144:145], v30 offset:33792\n"
    "ds_read_b64_tr_b16 a[146:147], v30 offset:34048\n"
    "v_mfma_f32_16x16x32_bf16 v[132:135], a[180:183], a[224:227], v[132:135]\n"
    "s_mov_b32 s98, 0\n"
    "v_mfma_f32_16x16x32_bf16 v[136:139], a[184:187], a[208:211], v[136:139]\n"
    "buffer_load_dword a136, v57, s[64:67], s98 offen\n"
    "v_mfma_f32_16x16x32_bf16 v[140:143], a[184:187], a[212:215], v[140:143]\n"
    "v_mfma_f32_16x16x32_bf16 v[144:147], a[184:187], a[216:219], v[144:147]\n"
    "v_mfma_f32_16x16x32_bf16 v[148:151], a[184:187], a[220:223], v[148:151]\n"
    "ds_read_b64_tr_b16 a[148:149], v30 offset:33800\n"
    "ds_read_b64_tr_b16 a[150:151], v30 offset:34056\n"
    "v_mfma_f32_16x16x32_bf16 v[152:155], a[184:187], a[224:227], v[152:155]\n"
    "s_waitcnt lgkmcnt(2)\n"
    "v_mfma_f32_16x16x32_bf16 v[156:159], a[188:191], a[208:211], v[156:159]\n"
    "v_mfma_f32_16x16x32_bf16 v[160:163], a[188:191], a[212:215], v[160:163]\n"
    "v_mfma_f32_16x16x32_bf16 v[164:167], a[188:191], a[216:219], v[164:167]\n"
    "v_mfma_f32_16x16x32_bf16 v[168:171], a[188:191], a[220:223], v[168:171]\n"
    "ds_read_b64_tr_b16 a[152:153], v30 offset:34304\n"
    "ds_read_b64_tr_b16 a[154:155], v30 offset:34560\n"
    "v_mfma_f32_16x16x32_bf16 v[172:175], a[188:191], a[224:227], v[172:175]\n"
    "v_mfma_f32_16x16x32_bf16 v[176:179], a[144:147], a[208:211], v[176:179]\n"
    "ds_read_b64_tr_b16 a[156:157], v30 offset:34312\n"
    "ds_read_b64_tr_b16 a[158:159], v30 offset:34568\n"
    "v_mfma_f32_16x16x32_bf16 v[180:183], a[144:147], a[212:215], v[180:183]\n"
    "v_mfma_f32_16x16x32_bf16 v[184:187], a[144:147], a[216:219], v[184:187]\n"
    "v_mfma_f32_16x16x32_bf16 v[188:191], a[144:147], a[220:223], v[188:191]\n"
    "v_mfma_f32_16x16x32_bf16 v[192:195], a[144:147], a[224:227], v[192:195]\n"
    "s_waitcnt lgkmcnt(2)\n"
    "v_mfma_f32_16x16x32_bf16 v[196:199], a[148:151], a[208:211], v[196:199]\n"
    "v_mfma_f32_16x16x32_bf16 v[200:203], a[148:151], a[212:215], v[200:203]\n"
    "v_mfma_f32_16x16x32_bf16 v[204:207], a[148:151], a[216:219], v[204:207]\n"
    "v_mfma_f32_16x16x32_bf16 v[208:211], a[148:151], a[220:223], v[208:211]\n"
    "v_mfma_f32_16x16x32_bf16 v[212:215], a[148:151], a[224:227], v[212:215]\n"
    "s_waitcnt lgkmcnt(0)\n"
    "s_barrier\n"
    "v_mfma_f32_16x16x32_bf16 v[216:219], a[152:155], a[208:211], v[216:219]\n"
    "ds_read_b128 a[228:231], v29\n"
    "ds_read_b64_tr_b16 a[160:161], v30\n"
    "v_mfma_f32_16x16x32_bf16 v[220:223], a[152:155], a[212:215], v[220:223]\n"
    "ds_read_b64_tr_b16 a[162:163], v30 offset:256\n"
    "ds_read_b128 a[232:235], v29 offset:2048\n"
    "v_mfma_f32_16x16x32_bf16 v[224:227], a[152:155], a[216:219], v[224:227]\n"
    "ds_read_b128 a[236:239], v29 offset:4096\n"
    "v_mfma_f32_16x16x32_bf16 v[228:231], a[152:155], a[220:223], v[228:231]\n"
    "ds_read_b128 a[240:243], v29 offset:6144\n"
    "v_mfma_f32_16x16x32_bf16 v[232:235], a[152:155], a[224:227], v[232:235]\n"
    "ds_read_b128 a[244:247], v29 offset:8192\n"
    "ds_read_b64_tr_b16 a[164:165], v30 offset:8\n"
    "v_add_u32 v29, s81, v29\n"
    "v_mfma_f32_16x16x32_bf16 v[236:239], a[156:159], a[208:211], v[236:239]\n"
    "ds_read_b64_tr_b16 a[166:167], v30 offset:264\n"
    "v_mfma_f32_16x16x32_bf16 v[240:243], a[156:159], a[212:215], v[240:243]\n"
    "s_mov_b32 s98, 0x2000\n"
    "v_mfma_f32_16x16x32_bf16 v[244:247], a[156:159], a[216:219], v[244:247]\n"
    "v_mfma_f32_16x16x32_bf16 v[248:251], a[156:159], a[220:223], v[248:251]\n"
    "ds_read_b64_tr_b16 a[168:169], v30 offset:512\n"
    "ds_read_b64_tr_b16 a[170:171], v30 offset:768\n"
    "v_mfma_f32_16x16x32_bf16 v[252:255], a[156:159], a[224:227], v[252:255]\n"
    "s_waitcnt lgkmcnt(4)\n"
    "v_mfma_f32_16x16x32_bf16 v[96:99], a[160:163], a[228:231], v[96:99]\n"
    "v_mfma_f32_16x16x32_bf16 v[100:103], a[160:163], a[232:235], v[100:103]\n"
    "v_mfma_f32_16x16x32_bf16 v[104:107], a[160:163], a[236:239], v[104:107]\n"
    "buffer_load_dword a136, v57, s[64:67], s98 offen\n"
    "v_mfma_f32_16x16x32_bf16 v[108:111], a[160:163], a[240:243], v[108:111]\n"
    "ds_read_b64_tr_b16 a[172:173], v30 offset:520\n"
    "ds_read_b64_tr_b16 a[174:175], v30 offset:776\n"
    "v_mfma_f32_16x16x32_bf16 v[112:115], a[160:163], a[244:247], v[112:115]\n"
    "s_waitcnt lgkmcnt(2)\n"
    "v_mfma_f32_16x16x32_bf16 v[116:119], a[164:167], a[228:231], v[116:119]\n"
    "v_mfma_f32_16x16x32_bf16 v[120:123], a[164:167], a[232:235], v[120:123]\n"
    "v_mfma_f32_16x16x32_bf16 v[124:127], a[164:167], a[236:239], v[124:127]\n"
    "v_mfma_f32_16x16x32_bf16 v[128:131], a[164:167], a[240:243], v[128:131]\n"
    "ds_read_b64_tr_b16 a[176:177], v30 offset:1024\n"
    "ds_read_b64_tr_b16 a[178:179], v30 offset:1280\n"
    "v_mfma_f32_16x16x32_bf16 v[132:135], a[164:167], a[244:247], v[132:135]\n"
    "v_mfma_f32_16x16x32_bf16 v[136:139], a[168:171], a[228:231], v[136:139]\n"
    "v_mfma_f32_16x16x32_bf16 v[140:143], a[168:171], a[232:235], v[140:143]\n"
    "v_mfma_f32_16x16x32_bf16 v[144:147], a[168:171], a[236:239], v[144:147]\n"
    "v_mfma_f32_16x16x32_bf16 v[148:151], a[168:171], a[240:243], v[148:151]\n"
    "ds_read_b64_tr_b16 a[180:181], v30 offset:1032\n"
    "ds_read_b64_tr_b16 a[182:183], v30 offset:1288\n"
    "v_mfma_f32_16x16x32_bf16 v[152:155], a[168:171], a[244:247], v[152:155]\n"
    "s_waitcnt lgkmcnt(2)\n"
    "ds_read_b64_tr_b16 a[184:185], v30 offset:1536\n"
    "ds_read_b64_tr_b16 a[186:187], v30 offset:1792\n"
    "v_mfma_f32_16x16x32_bf16 v[156:159], a[172:175], a[228:231], v[156:159]\n"
    "v_mfma_f32_16x16x32_bf16 v[160:163], a[172:175], a[232:235], v[160:163]\n"
    "v_mfma_f32_16x16x32_bf16 v[164:167], a[172:175], a[236:239], v[164:167]\n"
    "v_mfma_f32_16x16x32_bf16 v[168:171], a[172:175], a[240:243], v[168:171]\n"
    "v_mfma_f32_16x16x32_bf16 v[172:175], a[172:175], a[244:247], v[172:175]\n"
    "ds_read_b64_tr_b16 a[188:189], v30 offset:1544\n"
    "ds_read_b64_tr_b16 a[190:191], v30 offset:1800\n"
    "v_mfma_f32_16x16x32_bf16 v[176:179], a[176:179], a[228:231], v[176:179]\n"
    "v_mfma_f32_16x16x32_bf16 v[180:183], a[176:179], a[232:235], v[180:183]\n"
    "v_mfma_f32_16x16x32_bf16 v[184:187], a[176:179], a[236:239], v[184:187]\n"
    "v_mfma_f32_16x16x32_bf16 v[188:191], a[176:179], a[240:243], v[188:191]\n"
    "v_mfma_f32_16x16x32_bf16 v[192:195], a[176:179], a[244:247], v[192:195]\n"
    "s_waitcnt lgkmcnt(4)\n"
    "v_mfma_f32_16x16x32_bf16 v[196:199], a[180:183], a[228:231], v[196:199]\n"
    "v_mfma_f32_16x16x32_bf16 v[200:203], a[180:183], a[232:235], v[200:203]\n"
    "v_mfma_f32_16x16x32_bf16 v[204:207], a[180:183], a[236:239], v[204:207]\n"
    "v_mfma_f32_16x16x32_bf16 v[208:211], a[180:183], a[240:243], v[208:211]\n"
    "v_mfma_f32_16x16x32_bf16 v[212:215], a[180:183], a[244:247], v[212:215]\n"
    "s_sub_u32 s81, 0, s81\n"
    "s_add_u32 s80, s80, 1\n"
    "s_cmp_lt_u32 s80, 3\n"
    "s_waitcnt lgkmcnt(0)\n"
    "s_barrier\n"
    "v_mfma_f32_16x16x32_bf16 v[216:219], a[184:187], a[228:231], v[216:219]\n"
    "ds_read_b128 a[208:211], v28\n"
    "ds_read_b64_tr_b16 a[144:145], v30 offset:16384\n"
    "v_mfma_f32_16x16x32_bf16 v[220:223], a[184:187], a[232:235], v[220:223]\n"
    "ds_read_b64_tr_b16 a[146:147], v30 offset:16640\n"
    "ds_read_b128 a[212:215], v28 offset:2048\n"
    "v_mfma_f32_16x16x32_bf16 v[224:227], a[184:187], a[236:239], v[224:227]\n"
    "ds_read_b128 a[216:219], v28 offset:4096\n"
    "v_mfma_f32_16x16x32_bf16 v[228:231], a[184:187], a[240:243], v[228:231]\n"
    "ds_read_b128 a[220:223], v28 offset:6144\n"
    "v_mfma_f32_16x16x32_bf16 v[232:235], a[184:187], a[244:247], v[232:235]\n"
    "ds_read_b128 a[224:227], v28 offset:8192\n"
    "ds_read_b64_tr_b16 a[148:149], v30 offset:16392\n"
    "v_add_u32 v28, s81, v28\n"
    "v_mfma_f32_16x16x32_bf16 v[236:239], a[188:191], a[228:231], v[236:239]\n"
    "ds_read_b64_tr_b16 a[150:151], v30 offset:16648\n"
    "v_mfma_f32_16x16x32_bf16 v[240:243], a[188:191], a[232:235], v[240:243]\n"
    "s_mov_b32 s98, 0x4000\n"
    "v_mfma_f32_16x16x32_bf16 v[244:247], a[188:191], a[236:239], v[244:247]\n"
    "v_mfma_f32_16x16x32_bf16 v[248:251], a[188:191], a[240:243], v[248:251]\n"
    "ds_read_b64_tr_b16 a[152:153], v30 offset:16896\n"
    "ds_read_b64_tr_b16 a[154:155], v30 offset:17152\n"
    "v_mfma_f32_16x16x32_bf16 v[252:255], a[188:191], a[244:247], v[252:255]\n"
    "s_waitcnt lgkmcnt(4)\n"
    "v_mfma_f32_16x16x32_bf16 v[96:99], a[144:147], a[208:211], v[96:99]\n"
    "v_mfma_f32_16x16x32_bf16 v[100:103], a[144:147], a[212:215], v[100:103]\n"
    "v_mfma_f32_16x16x32_bf16 v[104:107], a[144:147], a[216:219], v[104:107]\n"
    "buffer_load_dword a136, v57, s[64:67], s98 offen\n"
    "v_mfma_f32_16x16x32_bf16 v[108:111], a[144:147], a[220:223], v[108:111]\n"
    "ds_read_b64_tr_b16 a[156:157], v30 offset:16904\n"
    "ds_read_b64_tr_b16 a[158:159], v30 offset:17160\n"
    "v_mfma_f32_16x16x32_bf16 v[112:115], a[144:147], a[224:227], v[112:115]\n"
    "s_waitcnt lgkmcnt(2)\n"
    "v_mfma_f32_16x16x32_bf16 v[116:119], a[148:151], a[208:211], v[116:119]\n"
    "v_mfma_f32_16x16x32_bf16 v[120:123], a[148:151], a[212:215], v[120:123]\n"
    "v_mfma_f32_16x16x32_bf16 v[124:127], a[148:151], a[216:219], v[124:127]\n"
    "v_mfma_f32_16x16x32_bf16 v[128:131], a[148:151], a[220:223], v[128:131]\n"
    "ds_read_b64_tr_b16 a[160:161], v30 offset:17408\n"
    "ds_read_b64_tr_b16 a[162:163], v30 offset:17664\n"
    "v_mfma_f32_16x16x32_bf16 v[132:135], a[148:151], a[224:227], v[132:135]\n"
    "v_mfma_f32_16x16x32_bf16 v[136:139], a[152:155], a[208:211], v[136:139]\n"
    "v_mfma_f32_16x16x32_bf16 v[140:143], a[152:155], a[212:215], v[140:143]\n"
    "v_mfma_f32_16x16x32_bf16 v[144:147], a[152:155], a[216:219], v[144:147]\n"
    "v_mfma_f32_16x16x32_bf16 v[148:151], a[152:155], a[220:223], v[148:151]\n"
    "ds_read_b64_tr_b16 a[164:165], v30 offset:17416\n"
    "ds_read_b64_tr_b16 a[166:167], v30 offset:17672\n"
    "v_mfma_f32_16x16x32_bf16 v[152:155], a[152:155], a[224:227], v[152:155]\n"
    "s_waitcnt lgkmcnt(2)\n"
    "v_mfma_f32_16x16x32_bf16 v[156:159], a[156:159], a[208:211], v[156:159]\n"
    "v_mfma_f32_16x16x32_bf16 v[160:163], a[156:159], a[212:215], v[160:163]\n"
    "v_mfma_f32_16x16x32_bf16 v[164:167], a[156:159], a[216:219], v[164:167]\n"
    "v_mfma_f32_16x16x32_bf16 v[168:171], a[156:159], a[220:223], v[168:171]\n"
    "ds_read_b64_tr_b16 a[168:169], v30 offset:17920\n"
    "ds_read_b64_tr_b16 a[170:171], v30 offset:18176\n"
    "v_mfma_f32_16x16x32_bf16 v[172:175], a[156:159], a[224:227], v[172:175]\n"
    "v_mfma_f32_16x16x32_bf16 v[176:179], a[160:163], a[208:211], v[176:179]\n"
    "ds_read_b64_tr_b16 a[172:173], v30 offset:17928\n"
    "ds_read_b64_tr_b16 a[174:175], v30 offset:18184\n"
    "v_mfma_f32_16x16x32_bf16 v[180:183], a[160:163], a[212:215], v[180:183]\n"
    "v_mfma_f32_16x16x32_bf16 v[184:187], a[160:163], a[216:219], v[184:187]\n"
    "v_mfma_f32_16x16x32_bf16 v[188:191], a[160:163], a[220:223], v[188:191]\n"
    "v_mfma_f32_16x16x32_bf16 v[192:195], a[160:163], a[224:227], v[192:195]\n"
    "s_waitcnt lgkmcnt(2)\n"
    "v_mfma_f32_16x16x32_bf16 v[196:199], a[164:167], a[208:211], v[196:199]\n"
    "v_mfma_f32_16x16x32_bf16 v[200:203], a[164:167], a[212:215], v[200:203]\n"
    "v_mfma_f32_16x16x32_bf16 v[204:207], a[164:167], a[216:219], v[204:207]\n"
    "v_mfma_f32_16x16x32_bf16 v[208:211], a[164:167], a[220:223], v[208:211]\n"
    "v_mfma_f32_16x16x32_bf16 v[212:215], a[164:167], a[224:227], v[212:215]\n"
    "s_waitcnt lgkmcnt(0)\n"
    "s_barrier\n"
    "v_mfma_f32_16x16x32_bf16 v[216:219], a[168:171], a[208:211], v[216:219]\n"
    "ds_read_b128 a[228:231], v29\n"
    "ds_read_b64_tr_b16 a[176:177], v30 offset:32768\n"
    "v_mfma_f32_16x16x32_bf16 v[220:223], a[168:171], a[212:215], v[220:223]\n"
    "ds_read_b64_tr_b16 a[178:179], v30 offset:33024\n"
    "ds_read_b128 a[232:235], v29 offset:2048\n"
    "v_mfma_f32_16x16x32_bf16 v[224:227], a[168:171], a[216:219], v[224:227]\n"
    "ds_read_b128 a[236:239], v29 offset:4096\n"
    "v_mfma_f32_16x16x32_bf16 v[228:231], a[168:171], a[220:223], v[228:231]\n"
    "ds_read_b128 a[240:243], v29 offset:6144\n"
    "v_mfma_f32_16x16x32_bf16 v[232:235], a[168:171], a[224:227], v[232:235]\n"
    "ds_read_b128 a[244:247], v29 offset:8192\n"
    "ds_read_b64_tr_b16 a[180:181], v30 offset:32776\n"
    "v_add_u32 v29, s81, v29\n"
    "v_mfma_f32_16x16x32_bf16 v[236:239], a[172:175], a[208:211], v[236:239]\n"
    "ds_read_b64_tr_b16 a[182:183], v30 offset:33032\n"
    "v_mfma_f32_16x16x32_bf16 v[240:243], a[172:175], a[212:215], v[240:243]\n"
    "v_mfma_f32_16x16x32_bf16 v[244:247], a[172:175], a[216:219], v[244:247]\n"
    "v_mfma_f32_16x16x32_bf16 v[248:251], a[172:175], a[220:223], v[248:251]\n"
    "ds_read_b64_tr_b16 a[184:185], v30 offset:33280\n"
    "ds_read_b64_tr_b16 a[186:187], v30 offset:33536\n"
    "v_mfma_f32_16x16x32_bf16 v[252:255], a[172:175], a[224:227], v[252:255]\n"
    "s_waitcnt lgkmcnt(4)\n"
    "v_mfma_f32_16x16x32_bf16 v[96:99], a[176:179], a[228:231], v[96:99]\n"
    "v_mfma_f32_16x16x32_bf16 v[100:103], a[176:179], a[232:235], v[100:103]\n"
    "v_mfma_f32_16x16x32_bf16 v[104:107], a[176:179], a[236:239], v[104:107]\n"
    "v_mfma_f32_16x16x32_bf16 v[108:111], a[176:179], a[240:243], v[108:111]\n"
    "ds_read_b64_tr_b16 a[188:189], v30 offset:33288\n"
    "ds_read_b64_tr_b16 a[190:191], v30 offset:33544\n"
    "v_mfma_f32_16x16x32_bf16 v[112:115], a[176:179], a[244:247], v[112:115]\n"
    "s_waitcnt lgkmcnt(2)\n"
    "v_mfma_f32_16x16x32_bf16 v[116:119], a[180:183], a[228:231], v[116:119]\n"
    "v_mfma_f32_16x16x32_bf16 v[120:123], a[180:183], a[232:235], v[120:123]\n"
    "v_mfma_f32_16x16x32_bf16 v[124:127], a[180:183], a[236:239], v[124:127]\n"
    "v_mfma_f32_16x16x32_bf16 v[128:131], a[180:183], a[240:243], v[128:131]\n"
    "ds_read_b64_tr_b16 a[144:145], v30 offset:33792\n"
    "ds_read_b64_tr_b16 a[146:147], v30 offset:34048\n"
    "v_mfma_f32_16x16x32_bf16 v[132:135], a[180:183], a[244:247], v[132:135]\n"
    "v_mfma_f32_16x16x32_bf16 v[136:139], a[184:187], a[228:231], v[136:139]\n"
    "v_mfma_f32_16x16x32_bf16 v[140:143], a[184:187], a[232:235], v[140:143]\n"
    "v_mfma_f32_16x16x32_bf16 v[144:147], a[184:187], a[236:239], v[144:147]\n"
    "v_mfma_f32_16x16x32_bf16 v[148:151], a[184:187], a[240:243], v[148:151]\n"
    "ds_read_b64_tr_b16 a[148:149], v30 offset:33800\n"
    "ds_read_b64_tr_b16 a[150:151], v30 offset:34056\n"
    "v_mfma_f32_16x16x32_bf16 v[152:155], a[184:187], a[244:247], v[152:155]\n"
    "s_waitcnt lgkmcnt(2)\n"
    "v_mfma_f32_16x16x32_bf16 v[156:159], a[188:191], a[228:231], v[156:159]\n"
    "v_mfma_f32_16x16x32_bf16 v[160:163], a[188:191], a[232:235], v[160:163]\n"
    "v_mfma_f32_16x16x32_bf16 v[164:167], a[188:191], a[236:239], v[164:167]\n"
    "v_mfma_f32_16x16x32_bf16 v[168:171], a[188:191], a[240:243], v[168:171]\n"
    "ds_read_b64_tr_b16 a[152:153], v30 offset:34304\n"
    "ds_read_b64_tr_b16 a[154:155], v30 offset:34560\n"
    "v_mfma_f32_16x16x32_bf16 v[172:175], a[188:191], a[244:247], v[172:175]\n"
    "v_mfma_f32_16x16x32_bf16 v[176:179], a[144:147], a[228:231], v[176:179]\n"
    "ds_read_b64_tr_b16 a[156:157], v30 offset:34312\n"
    "ds_read_b64_tr_b16 a[158:159], v30 offset:34568\n"
    "v_mfma_f32_16x16x32_bf16 v[180:183], a[144:147], a[232:235], v[180:183]\n"
    "v_mfma_f32_16x16x32_bf16 v[184:187], a[144:147], a[236:239], v[184:187]\n"
    "v_mfma_f32_16x16x32_bf16 v[188:191], a[144:147], a[240:243], v[188:191]\n"
    "v_mfma_f32_16x16x32_bf16 v[192:195], a[144:147], a[244:247], v[192:195]\n"
    "s_waitcnt lgkmcnt(2)\n"
    "v_mfma_f32_16x16x32_bf16 v[196:199], a[148:151], a[228:231], v[196:199]\n"
    "v_mfma_f32_16x16x32_bf16 v[200:203], a[148:151], a[232:235], v[200:203]\n"
    "v_mfma_f32_16x16x32_bf16 v[204:207], a[148:151], a[236:239], v[204:207]\n"
    "v_mfma_f32_16x16x32_bf16 v[208:211], a[148:151], a[240:243], v[208:211]\n"
    "v_mfma_f32_16x16x32_bf16 v[212:215], a[148:151], a[244:247], v[212:215]\n"
    "s_waitcnt lgkmcnt(0)\n"
    "s_barrier\n"
    "v_mfma_f32_16x16x32_bf16 v[216:219], a[152:155], a[228:231], v[216:219]\n"
    "ds_read_b128 a[208:211], v28\n"
    "ds_read_b64_tr_b16 a[160:161], v30\n"
    "v_mfma_f32_16x16x32_bf16 v[220:223], a[152:155], a[232:235], v[220:223]\n"
    "ds_read_b64_tr_b16 a[162:163], v30 offset:256\n"
    "ds_read_b128 a[212:215], v28 offset:2048\n"
    "v_mfma_f32_16x16x32_bf16 v[224:227], a[152:155], a[236:239], v[224:227]\n"
    "ds_read_b128 a[216:219], v28 offset:4096\n"
    "v_mfma_f32_16x16x32_bf16 v[228:231], a[152:155], a[240:243], v[228:231]\n"
    "ds_read_b128 a[220:223], v28 offset:6144\n"
    "v_mfma_f32_16x16x32_bf16 v[232:235], a[152:155], a[244:247], v[232:235]\n"
    "ds_read_b128 a[224:227], v28 offset:8192\n"
    "ds_read_b64_tr_b16 a[164:165], v30 offset:8\n"
    "v_subrev_u32 v28, s81, v28\n"
    "v_mfma_f32_16x16x32_bf16 v[236:239], a[156:159], a[228:231], v[236:239]\n"
    "ds_read_b64_tr_b16 a[166:167], v30 offset:264\n"
    "v_mfma_f32_16x16x32_bf16 v[240:243], a[156:159], a[232:235], v[240:243]\n"
    "v_mfma_f32_16x16x32_bf16 v[244:247], a[156:159], a[236:239], v[244:247]\n"
    "v_mfma_f32_16x16x32_bf16 v[248:251], a[156:159], a[240:243], v[248:251]\n"
    "ds_read_b64_tr_b16 a[168:169], v30 offset:512\n"
    "ds_read_b64_tr_b16 a[170:171], v30 offset:768\n"
    "v_mfma_f32_16x16x32_bf16 v[252:255], a[156:159], a[244:247], v[252:255]\n"
    "s_waitcnt lgkmcnt(4)\n"
    "v_mfma_f32_16x16x32_bf16 v[96:99], a[160:163], a[208:211], v[96:99]\n"
    "v_mfma_f32_16x16x32_bf16 v[100:103], a[160:163], a[212:215], v[100:103]\n"
    "v_mfma_f32_16x16x32_bf16 v[104:107], a[160:163], a[216:219], v[104:107]\n"
    "v_mfma_f32_16x16x32_bf16 v[108:111], a[160:163], a[220:223], v[108:111]\n"
    "ds_read_b64_tr_b16 a[172:173], v30 offset:520\n"
    "ds_read_b64_tr_b16 a[174:175], v30 offset:776\n"
    "v_mfma_f32_16x16x32_bf16 v[112:115], a[160:163], a[224:227], v[112:115]\n"
    "s_waitcnt lgkmcnt(2)\n"
    "v_mfma_f32_16x16x32_bf16 v[116:119], a[164:167], a[208:211], v[116:119]\n"
    "v_mfma_f32_16x16x32_bf16 v[120:123], a[164:167], a[212:215], v[120:123]\n"
    "v_mfma_f32_16x16x32_bf16 v[124:127], a[164:167], a[216:219], v[124:127]\n"
    "v_mfma_f32_16x16x32_bf16 v[128:131], a[164:167], a[220:223], v[128:131]\n"
    "ds_read_b64_tr_b16 a[176:177], v30 offset:1024\n"
    "ds_read_b64_tr_b16 a[178:179], v30 offset:1280\n"
    "v_mfma_f32_16x16x32_bf16 v[132:135], a[164:167], a[224:227], v[132:135]\n"
    "v_mfma_f32_16x16x32_bf16 v[136:139], a[168:171], a[208:211], v[136:139]\n"
    "v_mfma_f32_16x16x32_bf16 v[140:143], a[168:171], a[212:215], v[140:143]\n"
    "v_mfma_f32_16x16x32_bf16 v[144:147], a[168:171], a[216:219], v[144:147]\n"
    "v_mfma_f32_16x16x32_bf16 v[148:151], a[168:171], a[220:223], v[148:151]\n"
    "ds_read_b64_tr_b16 a[180:181], v30 offset:1032\n"
    "ds_read_b64_tr_b16 a[182:183], v30 offset:1288\n"
    "v_mfma_f32_16x16x32_bf16 v[152:155], a[168:171], a[224:227], v[152:155]\n"
    "s_waitcnt lgkmcnt(2)\n"
    "v_mfma_f32_16x16x32_bf16 v[156:159], a[172:175], a[208:211], v[156:159]\n"
    "v_mfma_f32_16x16x32_bf16 v[160:163], a[172:175], a[212:215], v[160:163]\n"
    "v_mfma_f32_16x16x32_bf16 v[164:167], a[172:175], a[216:219], v[164:167]\n"
    "v_mfma_f32_16x16x32_bf16 v[168:171], a[172:175], a[220:223], v[168:171]\n"
    "ds_read_b64_tr_b16 a[184:185], v30 offset:1536\n"
    "ds_read_b64_tr_b16 a[186:187], v30 offset:1792\n"
    "v_mfma_f32_16x16x32_bf16 v[172:175], a[172:175], a[224:227], v[172:175]\n"
    "v_mfma_f32_16x16x32_bf16 v[176:179], a[176:179], a[208:211], v[176:179]\n"
    "ds_read_b64_tr_b16 a[188:189], v30 offset:1544\n"
    "ds_read_b64_tr_b16 a[190:191], v30 offset:1800\n"
    "v_mfma_f32_16x16x32_bf16 v[180:183], a[176:179], a[212:215], v[180:183]\n"
    "v_mfma_f32_16x16x32_bf16 v[184:187], a[176:179], a[216:219], v[184:187]\n"
    "v_mfma_f32_16x16x32_bf16 v[188:191], a[176:179], a[220:223], v[188:191]\n"
    "v_mfma_f32_16x16x32_bf16 v[192:195], a[176:179], a[224:227], v[192:195]\n"
    "s_waitcnt lgkmcnt(2)\n"
    "v_mfma_f32_16x16x32_bf16 v[196:199], a[180:183], a[208:211], v[196:199]\n"
    "v_mfma_f32_16x16x32_bf16 v[200:203], a[180:183], a[212:215], v[200:203]\n"
    "v_mfma_f32_16x16x32_bf16 v[204:207], a[180:183], a[216:219], v[204:207]\n"
    "v_mfma_f32_16x16x32_bf16 v[208:211], a[180:183], a[220:223], v[208:211]\n"
    "v_mfma_f32_16x16x32_bf16 v[212:215], a[180:183], a[224:227], v[212:215]\n"
    "s_waitcnt lgkmcnt(0)\n"
    "s_barrier\n"
    "v_mfma_f32_16x16x32_bf16 v[216:219], a[184:187], a[208:211], v[216:219]\n"
    "ds_read_b128 a[228:231], v29\n"
    "ds_read_b64_tr_b16 a[144:145], v30 offset:16384\n"
    "v_mfma_f32_16x16x32_bf16 v[220:223], a[184:187], a[212:215], v[220:223]\n"
    "ds_read_b64_tr_b16 a[146:147], v30 offset:16640\n"
    "ds_read_b128 a[232:235], v29 offset:2048\n"
    "v_mfma_f32_16x16x32_bf16 v[224:227], a[184:187], a[216:219], v[224:227]\n"
    "ds_read_b128 a[236:239], v29 offset:4096\n"
    "v_mfma_f32_16x16x32_bf16 v[228:231], a[184:187], a[220:223], v[228:231]\n"
    "ds_read_b128 a[240:243], v29 offset:6144\n"
    "v_mfma_f32_16x16x32_bf16 v[232:235], a[184:187], a[224:227], v[232:235]\n"
    "ds_read_b128 a[244:247], v29 offset:8192\n"
    "ds_read_b64_tr_b16 a[148:149], v30 offset:16392\n"
    "v_subrev_u32 v29, s81, v29\n"
    "v_mfma_f32_16x16x32_bf16 v[236:239], a[188:191], a[208:211], v[236:239]\n"
    "ds_read_b64_tr_b16 a[150:151], v30 offset:16648\n"
    "v_mfma_f32_16x16x32_bf16 v[240:243], a[188:191], a[212:215], v[240:243]\n"
    "s_mov_b32 m0, 0x8000\n"
    "v_mfma_f32_16x16x32_bf16 v[244:247], a[188:191], a[216:219], v[244:247]\n"
    "s_mov_b32 s87, 0\n"
    "v_mfma_f32_16x16x32_bf16 v[248:251], a[188:191], a[220:223], v[248:251]\n"
    "ds_read_b64_tr_b16 a[152:153], v30 offset:16896\n"
    "ds_read_b64_tr_b16 a[154:155], v30 offset:17152\n"
    "v_mfma_f32_16x16x32_bf16 v[252:255], a[188:191], a[224:227], v[252:255]\n"
    "s_waitcnt lgkmcnt(4)\n"
    "v_mfma_f32_16x16x32_bf16 v[96:99], a[144:147], a[228:231], v[96:99]\n"
    "v_mfma_f32_16x16x32_bf16 v[100:103], a[144:147], a[232:235], v[100:103]\n"
    "v_mfma_f32_16x16x32_bf16 v[104:107], a[144:147], a[236:239], v[104:107]\n"
    "buffer_load_dwordx4 v12, s[48:51], s87 offen nt lds\n"
    "v_mfma_f32_16x16x32_bf16 v[108:111], a[144:147], a[240:243], v[108:111]\n"
    "ds_read_b64_tr_b16 a[156:157], v30 offset:16904\n"
    "ds_read_b64_tr_b16 a[158:159], v30 offset:17160\n"
    "v_mfma_f32_16x16x32_bf16 v[112:115], a[144:147], a[244:247], v[112:115]\n"
    "s_waitcnt lgkmcnt(2)\n"
    "v_mfma_f32_16x16x32_bf16 v[116:119], a[148:151], a[228:231], v[116:119]\n"
    "buffer_load_dwordx4 v13, s[48:51], s87 offen offset:1024 nt lds\n"
    "v_mfma_f32_16x16x32_bf16 v[120:123], a[148:151], a[232:235], v[120:123]\n"
    "v_mfma_f32_16x16x32_bf16 v[124:127], a[148:151], a[236:239], v[124:127]\n"
    "v_mfma_f32_16x16x32_bf16 v[128:131], a[148:151], a[240:243], v[128:131]\n"
    "ds_read_b64_tr_b16 a[160:161], v30 offset:17408\n"
    "ds_read_b64_tr_b16 a[162:163], v30 offset:17664\n"
    "v_mfma_f32_16x16x32_bf16 v[132:135], a[148:151], a[244:247], v[132:135]\n"
    "buffer_load_dwordx4 v12, s[48:51], s87 offen offset:2048 nt lds\n"
    "v_mfma_f32_16x16x32_bf16 v[136:139], a[152:155], a[228:231], v[136:139]\n"
    "v_mfma_f32_16x16x32_bf16 v[140:143], a[152:155], a[232:235], v[140:143]\n"
    "buffer_load_dwordx4 v13, s[48:51], s87 offen offset:3072 nt lds\n"
    "v_mfma_f32_16x16x32_bf16 v[144:147], a[152:155], a[236:239], v[144:147]\n"
    "s_mov_b32 m0, 0x9000\n"
    "v_mfma_f32_16x16x32_bf16 v[148:151], a[152:155], a[240:243], v[148:151]\n"
    "s_mov_b32 s87, 0x1000\n"
    "ds_read_b64_tr_b16 a[164:165], v30 offset:17416\n"
    "ds_read_b64_tr_b16 a[166:167], v30 offset:17672\n"
    "v_mfma_f32_16x16x32_bf16 v[152:155], a[152:155], a[244:247], v[152:155]\n"
    "s_waitcnt lgkmcnt(2)\n"
    "v_mfma_f32_16x16x32_bf16 v[156:159], a[156:159], a[228:231], v[156:159]\n"
    "buffer_load_dwordx4 v12, s[48:51], s87 offen nt lds\n"
    "v_mfma_f32_16x16x32_bf16 v[160:163], a[156:159], a[232:235], v[160:163]\n"
    "v_mfma_f32_16x16x32_bf16 v[164:167], a[156:159], a[236:239], v[164:167]\n"
    "buffer_load_dwordx4 v13, s[48:51], s87 offen offset:1024 nt lds\n"
    "v_mfma_f32_16x16x32_bf16 v[168:171], a[156:159], a[240:243], v[168:171]\n"
    "ds_read_b64_tr_b16 a[168:169], v30 offset:17920\n"
    "ds_read_b64_tr_b16 a[170:171], v30 offset:18176\n"
    "v_mfma_f32_16x16x32_bf16 v[172:175], a[156:159], a[244:247], v[172:175]\n"
    "v_mfma_f32_16x16x32_bf16 v[176:179], a[160:163], a[228:231], v[176:179]\n"
    "ds_read_b64_tr_b16 a[172:173], v30 offset:17928\n"
    "ds_read_b64_tr_b16 a[174:175], v30 offset:18184\n"
    "v_mfma_f32_16x16x32_bf16 v[180:183], a[160:163], a[232:235], v[180:183]\n"
    "buffer_load_dwordx4 v12, s[48:51], s87 offen offset:2048 nt lds\n"
    "v_mfma_f32_16x16x32_bf16 v[184:187], a[160:163], a[236:239], v[184:187]\n"
    "v_mfma_f32_16x16x32_bf16 v[188:191], a[160:163], a[240:243], v[188:191]\n"
    "buffer_load_dwordx4 v13, s[48:51], s87 offen offset:3072 nt lds\n"
    "v_mfma_f32_16x16x32_bf16 v[192:195], a[160:163], a[244:247], v[192:195]\n"
    "s_waitcnt lgkmcnt(2)\n"
    "v_mfma_f32_16x16x32_bf16 v[196:199], a[164:167], a[228:231], v[196:199]\n"
    "s_mov_b32 m0, 0xa000\n"
    "v_mfma_f32_16x16x32_bf16 v[200:203], a[164:167], a[232:235], v[200:203]\n"
    "s_mov_b32 s87, 0x2000\n"
    "v_mfma_f32_16x16x32_bf16 v[204:207], a[164:167], a[236:239], v[204:207]\n"
    "buffer_load_dwordx4 v12, s[48:51], s87 offen nt lds\n"
    "v_mfma_f32_16x16x32_bf16 v[208:211], a[164:167], a[240:243], v[208:211]\n"
    "v_mfma_f32_16x16x32_bf16 v[212:215], a[164:167], a[244:247], v[212:215]\n"
    "s_waitcnt lgkmcnt(0)\n"
    "s_barrier\n"
    "v_mfma_f32_16x16x32_bf16 v[216:219], a[168:171], a[228:231], v[216:219]\n"
    "ds_read_b128 a[208:211], v28\n"
    "ds_read_b64_tr_b16 a[176:177], v30 offset:32768\n"
    "v_mfma_f32_16x16x32_bf16 v[220:223], a[168:171], a[232:235], v[220:223]\n"
    "ds_read_b64_tr_b16 a[178:179], v30 offset:33024\n"
    "ds_read_b128 a[212:215], v28 offset:2048\n"
    "v_mfma_f32_16x16x32_bf16 v[224:227], a[168:171], a[236:239], v[224:227]\n"
    "ds_read_b128 a[216:219], v28 offset:4096\n"
    "v_mfma_f32_16x16x32_bf16 v[228:231], a[168:171], a[240:243], v[228:231]\n"
    "ds_read_b128 a[220:223], v28 offset:6144\n"
    "v_mfma_f32_16x16x32_bf16 v[232:235], a[168:171], a[244:247], v[232:235]\n"
    "ds_read_b128 a[224:227], v28 offset:8192\n"
    "ds_read_b64_tr_b16 a[180:181], v30 offset:32776\n"
    "v_add_u32 v28, s81, v28\n"
    "v_mfma_f32_16x16x32_bf16 v[236:239], a[172:175], a[228:231], v[236:239]\n"
    "ds_read_b64_tr_b16 a[182:183], v30 offset:33032\n"
    "v_mfma_f32_16x16x32_bf16 v[240:243], a[172:175], a[232:235], v[240:243]\n"
    "buffer_load_dwordx4 v13, s[48:51], s87 offen offset:1024 nt lds\n"
    "v_mfma_f32_16x16x32_bf16 v[244:247], a[172:175], a[236:239], v[244:247]\n"
    "buffer_load_dwordx4 v12, s[48:51], s87 offen offset:2048 nt lds\n"
    "v_mfma_f32_16x16x32_bf16 v[248:251], a[172:175], a[240:243], v[248:251]\n"
    "ds_read_b64_tr_b16 a[184:185], v30 offset:33280\n"
    "ds_read_b64_tr_b16 a[186:187], v30 offset:33536\n"
    "v_mfma_f32_16x16x32_bf16 v[252:255], a[172:175], a[244:247], v[252:255]\n"
    "s_waitcnt lgkmcnt(4)\n"
    "v_mfma_f32_16x16x32_bf16 v[96:99], a[176:179], a[208:211], v[96:99]\n"
    "buffer_load_dwordx4 v13, s[48:51], s87 offen offset:3072 nt lds\n"
    "v_mfma_f32_16x16x32_bf16 v[100:103], a[176:179], a[212:215], v[100:103]\n"
    "s_mov_b32 m0, 0xb000\n"
    "v_mfma_f32_16x16x32_bf16 v[104:107], a[176:179], a[216:219], v[104:107]\n"
    "s_mov_b32 s87, 0x3000\n"
    "v_mfma_f32_16x16x32_bf16 v[108:111], a[176:179], a[220:223], v[108:111]\n"
    "ds_read_b64_tr_b16 a[188:189], v30 offset:33288\n"
    "ds_read_b64_tr_b16 a[190:191], v30 offset:33544\n"
    "v_mfma_f32_16x16x32_bf16 v[112:115], a[176:179], a[224:227], v[112:115]\n"
    "s_waitcnt lgkmcnt(2)\n"
    "v_mfma_f32_16x16x32_bf16 v[116:119], a[180:183], a[208:211], v[116:119]\n"
    "buffer_load_dwordx4 v12, s[48:51], s87 offen nt lds\n"
    "v_mfma_f32_16x16x32_bf16 v[120:123], a[180:183], a[212:215], v[120:123]\n"
    "buffer_load_dwordx4 v13, s[48:51], s87 offen offset:1024 nt lds\n"
    "v_mfma_f32_16x16x32_bf16 v[124:127], a[180:183], a[216:219], v[124:127]\n"
    "v_mfma_f32_16x16x32_bf16 v[128:131], a[180:183], a[220:223], v[128:131]\n"
    "ds_read_b64_tr_b16 a[144:145], v30 offset:33792\n"
    "ds_read_b64_tr_b16 a[146:147], v30 offset:34048\n"
    "v_mfma_f32_16x16x32_bf16 v[132:135], a[180:183], a[224:227], v[132:135]\n"
    "buffer_load_dwordx4 v12, s[48:51], s87 offen offset:2048 nt lds\n"
    "v_mfma_f32_16x16x32_bf16 v[136:139], a[184:187], a[208:211], v[136:139]\n"
    "v_mfma_f32_16x16x32_bf16 v[140:143], a[184:187], a[212:215], v[140:143]\n"
    "buffer_load_dwordx4 v13, s[48:51], s87 offen offset:3072 nt lds\n"
    "v_mfma_f32_16x16x32_bf16 v[144:147], a[184:187], a[216:219], v[144:147]\n"
    "s_mov_b32 m0, 0xc000\n"
    "v_mfma_f32_16x16x32_bf16 v[148:151], a[184:187], a[220:223], v[148:151]\n"
    "s_mov_b32 s87, 0x20000\n"
    "ds_read_b64_tr_b16 a[148:149], v30 offset:33800\n"
    "ds_read_b64_tr_b16 a[150:151], v30 offset:34056\n"
    "v_mfma_f32_16x16x32_bf16 v[152:155], a[184:187], a[224:227], v[152:155]\n"
    "s_waitcnt lgkmcnt(2)\n"
    "v_mfma_f32_16x16x32_bf16 v[156:159], a[188:191], a[208:211], v[156:159]\n"
    "buffer_load_dwordx4 v12, s[48:51], s87 offen nt lds\n"
    "v_mfma_f32_16x16x32_bf16 v[160:163], a[188:191], a[212:215], v[160:163]\n"
    "v_mfma_f32_16x16x32_bf16 v[164:167], a[188:191], a[216:219], v[164:167]\n"
    "buffer_load_dwordx4 v13, s[48:51], s87 offen offset:1024 nt lds\n"
    "v_mfma_f32_16x16x32_bf16 v[168:171], a[188:191], a[220:223], v[168:171]\n"
    "ds_read_b64_tr_b16 a[152:153], v30 offset:34304\n"
    "ds_read_b64_tr_b16 a[154:155], v30 offset:34560\n"
    "v_mfma_f32_16x16x32_bf16 v[172:175], a[188:191], a[224:227], v[172:175]\n"
    "v_mfma_f32_16x16x32_bf16 v[176:179], a[144:147], a[208:211], v[176:179]\n"
    "ds_read_b64_tr_b16 a[156:157], v30 offset:34312\n"
    "ds_read_b64_tr_b16 a[158:159], v30 offset:34568\n"
    "v_mfma_f32_16x16x32_bf16 v[180:183], a[144:147], a[212:215], v[180:183]\n"
    "buffer_load_dwordx4 v12, s[48:51], s87 offen offset:2048 nt lds\n"
    "v_mfma_f32_16x16x32_bf16 v[184:187], a[144:147], a[216:219], v[184:187]\n"
    "v_mfma_f32_16x16x32_bf16 v[188:191], a[144:147], a[220:223], v[188:191]\n"
    "buffer_load_dwordx4 v13, s[48:51], s87 offen offset:3072 nt lds\n"
    "v_mfma_f32_16x16x32_bf16 v[192:195], a[144:147], a[224:227], v[192:195]\n"
    "s_waitcnt lgkmcnt(2)\n"
    "v_mfma_f32_16x16x32_bf16 v[196:199], a[148:151], a[208:211], v[196:199]\n"
    "s_mov_b32 m0, 0xd000\n"
    "v_mfma_f32_16x16x32_bf16 v[200:203], a[148:151], a[212:215], v[200:203]\n"
    "s_mov_b32 s87, 0x21000\n"
    "v_mfma_f32_16x16x32_bf16 v[204:207], a[148:151], a[216:219], v[204:207]\n"
    "buffer_load_dwordx4 v12, s[48:51], s87 offen nt lds\n"
    "v_mfma_f32_16x16x32_bf16 v[208:211], a[148:151], a[220:223], v[208:211]\n"
    "v_mfma_f32_16x16x32_bf16 v[212:215], a[148:151], a[224:227], v[212:215]\n"
    "s_waitcnt lgkmcnt(0)\n"
    "s_barrier\n"
    "v_mfma_f32_16x16x32_bf16 v[216:219], a[152:155], a[208:211], v[216:219]\n"
    "ds_read_b128 a[228:231], v29\n"
    "ds_read_b64_tr_b16 a[160:161], v30\n"
    "v_mfma_f32_16x16x32_bf16 v[220:223], a[152:155], a[212:215], v[220:223]\n"
    "ds_read_b64_tr_b16 a[162:163], v30 offset:256\n"
    "ds_read_b128 a[232:235], v29 offset:2048\n"
    "v_mfma_f32_16x16x32_bf16 v[224:227], a[152:155], a[216:219], v[224:227]\n"
    "ds_read_b128 a[236:239], v29 offset:4096\n"
    "v_mfma_f32_16x16x32_bf16 v[228:231], a[152:155], a[220:223], v[228:231]\n"
    "ds_read_b128 a[240:243], v29 offset:6144\n"
    "v_mfma_f32_16x16x32_bf16 v[232:235], a[152:155], a[224:227], v[232:235]\n"
    "ds_read_b128 a[244:247], v29 offset:8192\n"
    "ds_read_b64_tr_b16 a[164:165], v30 offset:8\n"
    "v_add_u32 v29, s81, v29\n"
    "v_mfma_f32_16x16x32_bf16 v[236:239], a[156:159], a[208:211], v[236:239]\n"
    "ds_read_b64_tr_b16 a[166:167], v30 offset:264\n"
    "v_mfma_f32_16x16x32_bf16 v[240:243], a[156:159], a[212:215], v[240:243]\n"
    "buffer_load_dwordx4 v13, s[48:51], s87 offen offset:1024 nt lds\n"
    "v_mfma_f32_16x16x32_bf16 v[244:247], a[156:159], a[216:219], v[244:247]\n"
    "buffer_load_dwordx4 v12, s[48:51], s87 offen offset:2048 nt lds\n"
    "v_mfma_f32_16x16x32_bf16 v[248:251], a[156:159], a[220:223], v[248:251]\n"
    "ds_read_b64_tr_b16 a[168:169], v30 offset:512\n"
    "ds_read_b64_tr_b16 a[170:171], v30 offset:768\n"
    "v_mfma_f32_16x16x32_bf16 v[252:255], a[156:159], a[224:227], v[252:255]\n"
    "s_waitcnt lgkmcnt(4)\n"
    "v_mfma_f32_16x16x32_bf16 v[96:99], a[160:163], a[228:231], v[96:99]\n"
    "buffer_load_dwordx4 v13, s[48:51], s87 offen offset:3072 nt lds\n"
    "v_mfma_f32_16x16x32_bf16 v[100:103], a[160:163], a[232:235], v[100:103]\n"
    "s_mov_b32 m0, 0xe000\n"
    "v_mfma_f32_16x16x32_bf16 v[104:107], a[160:163], a[236:239], v[104:107]\n"
    "s_mov_b32 s87, 0x22000\n"
    "v_mfma_f32_16x16x32_bf16 v[108:111], a[160:163], a[240:243], v[108:111]\n"
    "ds_read_b64_tr_b16 a[172:173], v30 offset:520\n"
    "ds_read_b64_tr_b16 a[174:175], v30 offset:776\n"
    "v_mfma_f32_16x16x32_bf16 v[112:115], a[160:163], a[244:247], v[112:115]\n"
    "s_waitcnt lgkmcnt(2)\n"
    "v_mfma_f32_16x16x32_bf16 v[116:119], a[164:167], a[228:231], v[116:119]\n"
    "buffer_load_dwordx4 v12, s[48:51], s87 offen nt lds\n"
    "v_mfma_f32_16x16x32_bf16 v[120:123], a[164:167], a[232:235], v[120:123]\n"
    "buffer_load_dwordx4 v13, s[48:51], s87 offen offset:1024 nt lds\n"
    "v_mfma_f32_16x16x32_bf16 v[124:127], a[164:167], a[236:239], v[124:127]\n"
    "v_mfma_f32_16x16x32_bf16 v[128:131], a[164:167], a[240:243], v[128:131]\n"
    "ds_read_b64_tr_b16 a[176:177], v30 offset:1024\n"
    "ds_read_b64_tr_b16 a[178:179], v30 offset:1280\n"
    "v_mfma_f32_16x16x32_bf16 v[132:135], a[164:167], a[244:247], v[132:135]\n"
    "buffer_load_dwordx4 v12, s[48:51], s87 offen offset:2048 nt lds\n"
    "v_mfma_f32_16x16x32_bf16 v[136:139], a[168:171], a[228:231], v[136:139]\n"
    "v_mfma_f32_16x16x32_bf16 v[140:143], a[168:171], a[232:235], v[140:143]\n"
    "buffer_load_dwordx4 v13, s[48:51], s87 offen offset:3072 nt lds\n"
    "v_mfma_f32_16x16x32_bf16 v[144:147], a[168:171], a[236:239], v[144:147]\n"
    "s_mov_b32 m0, 0xf000\n"
    "v_mfma_f32_16x16x32_bf16 v[148:151], a[168:171], a[240:243], v[148:151]\n"
    "s_mov_b32 s87, 0x23000\n"
    "ds_read_b64_tr_b16 a[180:181], v30 offset:1032\n"
    "ds_read_b64_tr_b16 a[182:183], v30 offset:1288\n"
    "v_mfma_f32_16x16x32_bf16 v[152:155], a[168:171], a[244:247], v[152:155]\n"
    "s_waitcnt lgkmcnt(2)\n"
    "ds_read_b64_tr_b16 a[184:185], v30 offset:1536\n"
    "ds_read_b64_tr_b16 a[186:187], v30 offset:1792\n"
    "v_mfma_f32_16x16x32_bf16 v[156:159], a[172:175], a[228:231], v[156:159]\n"
    "buffer_load_dwordx4 v12, s[48:51], s87 offen nt lds\n"
    "v_mfma_f32_16x16x32_bf16 v[160:163], a[172:175], a[232:235], v[160:163]\n"
    "v_mfma_f32_16x16x32_bf16 v[164:167], a[172:175], a[236:239], v[164:167]\n"
    "buffer_load_dwordx4 v13, s[48:51], s87 offen offset:1024 nt lds\n"
    "v_mfma_f32_16x16x32_bf16 v[168:171], a[172:175], a[240:243], v[168:171]\n"
    "v_mfma_f32_16x16x32_bf16 v[172:175], a[172:175], a[244:247], v[172:175]\n"
    "ds_read_b64_tr_b16 a[188:189], v30 offset:1544\n"
    "ds_read_b64_tr_b16 a[190:191], v30 offset:1800\n"
    "v_mfma_f32_16x16x32_bf16 v[176:179], a[176:179], a[228:231], v[176:179]\n"
    "buffer_load_dwordx4 v12, s[48:51], s87 offen offset:2048 nt lds\n"
    "v_mfma_f32_16x16x32_bf16 v[180:183], a[176:179], a[232:235], v[180:183]\n"
    "v_mfma_f32_16x16x32_bf16 v[184:187], a[176:179], a[236:239], v[184:187]\n"
    "v_mfma_f32_16x16x32_bf16 v[188:191], a[176:179], a[240:243], v[188:191]\n"
    "buffer_load_dwordx4 v13, s[48:51], s87 offen offset:3072 nt lds\n"
    "v_mfma_f32_16x16x32_bf16 v[192:195], a[176:179], a[244:247], v[192:195]\n"
    "s_waitcnt lgkmcnt(4)\n"
    "v_mfma_f32_16x16x32_bf16 v[196:199], a[180:183], a[228:231], v[196:199]\n"
    "v_mfma_f32_16x16x32_bf16 v[200:203], a[180:183], a[232:235], v[200:203]\n"
    "v_mfma_f32_16x16x32_bf16 v[204:207], a[180:183], a[236:239], v[204:207]\n"
    "v_mfma_f32_16x16x32_bf16 v[208:211], a[180:183], a[240:243], v[208:211]\n"
    "v_mfma_f32_16x16x32_bf16 v[212:215], a[180:183], a[244:247], v[212:215]\n"
    "s_sub_u32 s81, 0, s81\n"
    "s_add_u32 s80, s80, 1\n"
    "s_cmp_lt_u32 s80, 3\n"
    "s_waitcnt lgkmcnt(0)\n"
    "s_barrier\n"
    "v_mfma_f32_16x16x32_bf16 v[216:219], a[184:187], a[228:231], v[216:219]\n"
    "ds_read_b128 a[208:211], v28\n"
    "v_and_b32 v44, 15, v0\n"
    "v_mfma_f32_16x16x32_bf16 v[220:223], a[184:187], a[232:235], v[220:223]\n"
    "ds_read_b64_tr_b16 a[144:145], v30 offset:16384\n"
    "v_lshrrev_b32 v45, 4, v0\n"
    "v_mfma_f32_16x16x32_bf16 v[224:227], a[184:187], a[236:239], v[224:227]\n"
    "ds_read_b64_tr_b16 a[146:147], v30 offset:16640\n"
    "v_lshlrev_b32 v46, 8, v44\n"
    "v_mfma_f32_16x16x32_bf16 v[228:231], a[184:187], a[240:243], v[228:231]\n"
    "ds_read_b128 a[212:215], v28 offset:2048\n"
    "v_add_u32 v46, s88, v46\n"
    "v_mfma_f32_16x16x32_bf16 v[232:235], a[184:187], a[244:247], v[232:235]\n"
    "ds_read_b128 a[216:219], v28 offset:4096\n"
    "v_xor_b32 v47, v45, v44\n"
    "v_mfma_f32_16x16x32_bf16 v[236:239], a[188:191], a[228:231], v[236:239]\n"
    "ds_read_b128 a[220:223], v28 offset:6144\n"
    "v_xor_b32 v48, 0, v47\n"
    "v_mfma_f32_16x16x32_bf16 v[240:243], a[188:191], a[232:235], v[240:243]\n"
    "s_add_u32 m0, s73, 0x10000\n"
    "ds_read_b128 a[224:227], v28 offset:8192\n"
    "v_lshl_add_u32 v2, v48, 4, v46\n"
    "v_mfma_f32_16x16x32_bf16 v[244:247], a[188:191], a[236:239], v[244:247]\n"
    "s_add_u32 s86, s73, 0x4000\n"
    "ds_read_b64_tr_b16 a[148:149], v30 offset:16392\n"
    "v_xor_b32 v48, 4, v47\n"
    "v_mfma_f32_16x16x32_bf16 v[248:251], a[188:191], a[240:243], v[248:251]\n"
    "ds_read_b64_tr_b16 a[150:151], v30 offset:16648\n"
    "v_lshl_add_u32 v3, v48, 4, v46\n"
    "v_mfma_f32_16x16x32_bf16 v[252:255], a[188:191], a[244:247], v[252:255]\n"
    "s_waitcnt lgkmcnt(4)\n"
    "ds_read_b64_tr_b16 a[152:153], v30 offset:16896\n"
    "v_xor_b32 v48, 8, v47\n"
    "v_mfma_f32_16x16x32_bf16 v[96:99], a[144:147], a[208:211], v[96:99]\n"
    "ds_read_b64_tr_b16 a[154:155], v30 offset:17152\n"
    "v_lshl_add_u32 v4, v48, 4, v46\n"
    "v_mfma_f32_16x16x32_bf16 v[100:103], a[144:147], a[212:215], v[100:103]\n"
    "v_xor_b32 v48, 12, v47\n"
    "v_lshl_add_u32 v5, v48, 4, v46\n"
    "v_mfma_f32_16x16x32_bf16 v[104:107], a[144:147], a[216:219], v[104:107]\n"
    "s_waitcnt lgkmcnt(4)\n"
    "v_lshrrev_b32 v46, 5, v0\n"
    "v_lshlrev_b32 v46, 11, v46\n"
    "v_mfma_f32_16x16x32_bf16 v[108:111], a[144:147], a[220:223], v[108:111]\n"
    "ds_read_b64_tr_b16 a[156:157], v30 offset:16904\n"
    "v_add_u32 v46, s88, v46\n"
    "v_mfma_f32_16x16x32_bf16 v[112:115], a[144:147], a[224:227], v[112:115]\n"
    "s_waitcnt lgkmcnt(3)\n"
    "ds_read_b64_tr_b16 a[158:159], v30 offset:17160\n"
    "v_add_u32 v46, 0x8000, v46\n"
    "v_mfma_f32_16x16x32_bf16 v[116:119], a[148:151], a[208:211], v[116:119]\n"
    "v_bfe_u32 v47, v0, 2, 3\n"
    "v_lshl_add_u32 v46, v47, 6, v46\n"
    "v_mfma_f32_16x16x32_bf16 v[120:123], a[148:151], a[212:215], v[120:123]\n"
    "v_and_b32 v47, 3, v0\n"
    "v_lshl_add_u32 v46, v47, 3, v46\n"
    "v_mfma_f32_16x16x32_bf16 v[124:127], a[148:151], a[216:219], v[124:127]\n"
    "buffer_load_dwordx4 v8, s[44:47], s86 offen nt lds\n"
    "v_mfma_f32_16x16x32_bf16 v[128:131], a[148:151], a[220:223], v[128:131]\n"
    "ds_read_b64_tr_b16 a[160:161], v30 offset:17408\n"
    "v_bfe_u32 v47, v0, 4, 1\n"
    "v_mfma_f32_16x16x32_bf16 v[132:135], a[148:151], a[224:227], v[132:135]\n"
    "s_waitcnt lgkmcnt(1)\n"
    "ds_read_b64_tr_b16 a[162:163], v30 offset:17664\n"
    "v_lshl_add_u32 v6, v47, 5, v46\n"
    "v_mfma_f32_16x16x32_bf16 v[136:139], a[152:155], a[208:211], v[136:139]\n"
    "v_xor_b32 v7, 32, v6\n"
    "v_mov_b32 v46, 0x3f803f80\n"
    "v_mfma_f32_16x16x32_bf16 v[140:143], a[152:155], a[212:215], v[140:143]\n"
    "v_accvgpr_write_b32 a132, v46\n"
    "v_accvgpr_write_b32 a133, v46\n"
    "v_mfma_f32_16x16x32_bf16 v[144:147], a[152:155], a[216:219], v[144:147]\n"
    "v_accvgpr_write_b32 a134, v46\n"
    "v_accvgpr_write_b32 a135, v46\n"
    "v_mfma_f32_16x16x32_bf16 v[148:151], a[152:155], a[220:223], v[148:151]\n"
    "ds_read_b64_tr_b16 a[164:165], v30 offset:17416\n"
    "v_add_u32 v44, 0, v0\n"
    "v_mfma_f32_16x16x32_bf16 v[152:155], a[152:155], a[224:227], v[152:155]\n"
    "ds_read_b64_tr_b16 a[166:167], v30 offset:17672\n"
    "v_mul_u32_u24 v45, 0xccd, v44\n"
    "v_mfma_f32_16x16x32_bf16 v[156:159], a[156:159], a[208:211], v[156:159]\n"
    "v_lshrrev_b32 v45, 16, v45\n"
    "v_mul_u32_u24 v46, 20, v45\n"
    "v_mfma_f32_16x16x32_bf16 v[160:163], a[156:159], a[212:215], v[160:163]\n"
    "buffer_load_dwordx4 v9, s[44:47], s86 offen offset:1024 nt lds\n"
    "v_mfma_f32_16x16x32_bf16 v[164:167], a[156:159], a[216:219], v[164:167]\n"
    "v_sub_u32 v46, v44, v46\n"
    "v_lshrrev_b32 v47, 1, v45\n"
    "v_mfma_f32_16x16x32_bf16 v[168:171], a[156:159], a[220:223], v[168:171]\n"
    "ds_read_b64_tr_b16 a[168:169], v30 offset:17920\n"
    "v_and_b32 v47, 2, v47\n"
    "v_mfma_f32_16x16x32_bf16 v[172:175], a[156:159], a[224:227], v[172:175]\n"
    "s_waitcnt lgkmcnt(1)\n"
    "ds_read_b64_tr_b16 a[170:171], v30 offset:18176\n"
    "v_xor_b32 v46, v46, v47\n"
    "v_mfma_f32_16x16x32_bf16 v[176:179], a[160:163], a[208:211], v[176:179]\n"
    "ds_read_b64_tr_b16 a[172:173], v30 offset:17928\n"
    "v_mul_u32_u24 v45, 0x500, v45\n"
    "v_mfma_f32_16x16x32_bf16 v[180:183], a[160:163], a[212:215], v[180:183]\n"
    "ds_read_b64_tr_b16 a[174:175], v30 offset:18184\n"
    "v_lshl_add_u32 v45, v46, 4, v45\n"
    "v_mfma_f32_16x16x32_bf16 v[184:187], a[160:163], a[216:219], v[184:187]\n"
    "v_add_u32 v23, 0xc00, v45\n"
    "v_add_u32 v44, 64, v0\n"
    "v_mfma_f32_16x16x32_bf16 v[188:191], a[160:163], a[220:223], v[188:191]\n"
    "v_mul_u32_u24 v45, 0xccd, v44\n"
    "v_lshrrev_b32 v45, 16, v45\n"
    "v_mfma_f32_16x16x32_bf16 v[192:195], a[160:163], a[224:227], v[192:195]\n"
    "v_mul_u32_u24 v46, 20, v45\n"
    "v_sub_u32 v46, v44, v46\n"
    "v_mfma_f32_16x16x32_bf16 v[196:199], a[164:167], a[208:211], v[196:199]\n"
    "buffer_load_dwordx4 v10, s[44:47], s86 offen offset:2048 nt lds\n"
    "v_mfma_f32_16x16x32_bf16 v[200:203], a[164:167], a[212:215], v[200:203]\n"
    "v_lshrrev_b32 v47, 1, v45\n"
    "v_and_b32 v47, 2, v47\n"
    "v_mfma_f32_16x16x32_bf16 v[204:207], a[164:167], a[216:219], v[204:207]\n"
    "v_xor_b32 v46, v46, v47\n"
    "v_mul_u32_u24 v45, 0x500, v45\n"
    "v_mfma_f32_16x16x32_bf16 v[208:211], a[164:167], a[220:223], v[208:211]\n"
    "v_lshl_add_u32 v45, v46, 4, v45\n"
    "v_add_u32 v24, 0x800, v45\n"
    "v_mfma_f32_16x16x32_bf16 v[212:215], a[164:167], a[224:227], v[212:215]\n"
    "s_waitcnt lgkmcnt(0)\n"
    "s_barrier\n"
    "v_mfma_f32_16x16x32_bf16 v[216:219], a[168:171], a[208:211], v[216:219]\n"
    "ds_read_b128 a[228:231], v29\n"
    "v_add_u32 v44, 0x80, v0\n"
    "v_mfma_f32_16x16x32_bf16 v[220:223], a[168:171], a[212:215], v[220:223]\n"
    "ds_read_b64_tr_b16 a[176:177], v30 offset:32768\n"
    "v_mul_u32_u24 v45, 0xccd, v44\n"
    "v_mfma_f32_16x16x32_bf16 v[224:227], a[168:171], a[216:219], v[224:227]\n"
    "ds_read_b64_tr_b16 a[178:179], v30 offset:33024\n"
    "v_lshrrev_b32 v45, 16, v45\n"
    "v_mfma_f32_16x16x32_bf16 v[228:231], a[168:171], a[220:223], v[228:231]\n"
    "ds_read_b128 a[232:235], v29 offset:2048\n"
    "v_mul_u32_u24 v46, 20, v45\n"
    "v_mfma_f32_16x16x32_bf16 v[232:235], a[168:171], a[224:227], v[232:235]\n"
    "ds_read_b128 a[236:239], v29 offset:4096\n"
    "v_sub_u32 v46, v44, v46\n"
    "v_mfma_f32_16x16x32_bf16 v[236:239], a[172:175], a[208:211], v[236:239]\n"
    "ds_read_b128 a[240:243], v29 offset:6144\n"
    "v_lshrrev_b32 v47, 1, v45\n"
    "v_mfma_f32_16x16x32_bf16 v[240:243], a[172:175], a[212:215], v[240:243]\n"
    "ds_read_b128 a[244:247], v29 offset:8192\n"
    "v_and_b32 v47, 2, v47\n"
    "v_mfma_f32_16x16x32_bf16 v[244:247], a[172:175], a[216:219], v[244:247]\n"
    "ds_read_b64_tr_b16 a[180:181], v30 offset:32776\n"
    "v_xor_b32 v46, v46, v47\n"
    "v_mfma_f32_16x16x32_bf16 v[248:251], a[172:175], a[220:223], v[248:251]\n"
    "ds_read_b64_tr_b16 a[182:183], v30 offset:33032\n"
    "v_mul_u32_u24 v45, 0x500, v45\n"
    "v_mfma_f32_16x16x32_bf16 v[252:255], a[172:175], a[224:227], v[252:255]\n"
    "s_waitcnt lgkmcnt(4)\n"
    "ds_read_b64_tr_b16 a[184:185], v30 offset:33280\n"
    "v_lshl_add_u32 v45, v46, 4, v45\n"
    "v_mfma_f32_16x16x32_bf16 v[96:99], a[176:179], a[228:231], v[96:99]\n"
    "ds_read_b64_tr_b16 a[186:187], v30 offset:33536\n"
    "v_add_u32 v25, 0x400, v45\n"
    "v_mfma_f32_16x16x32_bf16 v[100:103], a[176:179], a[232:235], v[100:103]\n"
    "buffer_load_dwordx4 v11, s[44:47], s86 offen offset:3072 nt lds\n"
    "v_mfma_f32_16x16x32_bf16 v[104:107], a[176:179], a[236:239], v[104:107]\n"
    "s_waitcnt lgkmcnt(4)\n"
    "v_add_u32 v44, 0xc0, v0\n"
    "v_mul_u32_u24 v45, 0xccd, v44\n"
    "v_mfma_f32_16x16x32_bf16 v[108:111], a[176:179], a[240:243], v[108:111]\n"
    "s_add_u32 m0, s73, 0x14000\n"
    "ds_read_b64_tr_b16 a[188:189], v30 offset:33288\n"
    "v_lshrrev_b32 v45, 16, v45\n"
    "v_mfma_f32_16x16x32_bf16 v[112:115], a[176:179], a[244:247], v[112:115]\n"
    "s_waitcnt lgkmcnt(3)\n"
    "ds_read_b64_tr_b16 a[190:191], v30 offset:33544\n"
    "v_mul_u32_u24 v46, 20, v45\n"
    "v_mfma_f32_16x16x32_bf16 v[116:119], a[180:183], a[228:231], v[116:119]\n"
    "s_add_u32 s86, s73, 0x24000\n"
    "v_sub_u32 v46, v44, v46\n"
    "v_lshrrev_b32 v47, 1, v45\n"
    "v_mfma_f32_16x16x32_bf16 v[120:123], a[180:183], a[232:235], v[120:123]\n"
    "v_and_b32 v47, 2, v47\n"
    "v_xor_b32 v46, v46, v47\n"
    "v_mfma_f32_16x16x32_bf16 v[124:127], a[180:183], a[236:239], v[124:127]\n"
    "v_mul_u32_u24 v45, 0x500, v45\n"
    "v_lshl_add_u32 v45, v46, 4, v45\n"
    "v_mfma_f32_16x16x32_bf16 v[128:131], a[180:183], a[240:243], v[128:131]\n"
    "ds_read_b64_tr_b16 a[144:145], v30 offset:33792\n"
    "v_add_u32 v26, 0, v45\n"
    "v_mfma_f32_16x16x32_bf16 v[132:135], a[180:183], a[244:247], v[132:135]\n"
    "s_waitcnt lgkmcnt(1)\n"
    "ds_read_b64_tr_b16 a[146:147], v30 offset:34048\n"
    "v_add_u32 v44, 0x100, v0\n"
    "v_mfma_f32_16x16x32_bf16 v[136:139], a[184:187], a[228:231], v[136:139]\n"
    "v_mul_u32_u24 v45, 0xccd, v44\n"
    "v_lshrrev_b32 v45, 16, v45\n"
    "v_mfma_f32_16x16x32_bf16 v[140:143], a[184:187], a[232:235], v[140:143]\n"
    "v_mul_u32_u24 v46, 20, v45\n"
    "v_sub_u32 v46, v44, v46\n"
    "v_mfma_f32_16x16x32_bf16 v[144:147], a[184:187], a[236:239], v[144:147]\n"
    "v_lshrrev_b32 v47, 1, v45\n"
    "v_and_b32 v47, 2, v47\n"
    "v_mfma_f32_16x16x32_bf16 v[148:151], a[184:187], a[240:243], v[148:151]\n"
    "ds_read_b64_tr_b16 a[148:149], v30 offset:33800\n"
    "v_xor_b32 v46, v46, v47\n"
    "v_mfma_f32_16x16x32_bf16 v[152:155], a[184:187], a[244:247], v[152:155]\n"
    "ds_read_b64_tr_b16 a[150:151], v30 offset:34056\n"
    "v_mul_u32_u24 v45, 0x500, v45\n"
    "v_mfma_f32_16x16x32_bf16 v[156:159], a[188:191], a[228:231], v[156:159]\n"
    "v_lshl_add_u32 v45, v46, 4, v45\n"
    "v_add_u32 v27, 0xc00, v45\n"
    "v_mfma_f32_16x16x32_bf16 v[160:163], a[188:191], a[232:235], v[160:163]\n"
    "buffer_load_dwordx4 v8, s[44:47], s86 offen nt lds\n"
    "v_mfma_f32_16x16x32_bf16 v[164:167], a[188:191], a[236:239], v[164:167]\n"
    "buffer_load_dwordx4 v9, s[44:47], s86 offen offset:1024 nt lds\n"
    "v_mfma_f32_16x16x32_bf16 v[168:171], a[188:191], a[240:243], v[168:171]\n"
    "ds_read_b64_tr_b16 a[152:153], v30 offset:34304\n"
    "ds_read_b64_tr_b16 a[154:155], v30 offset:34560\n"
    "v_mfma_f32_16x16x32_bf16 v[172:175], a[188:191], a[244:247], v[172:175]\n"
    "s_waitcnt lgkmcnt(2)\n"
    "v_mfma_f32_16x16x32_bf16 v[176:179], a[144:147], a[228:231], v[176:179]\n"
    "buffer_load_dwordx4 v10, s[44:47], s86 offen offset:2048 nt lds\n"
    "v_mfma_f32_16x16x32_bf16 v[180:183], a[144:147], a[232:235], v[180:183]\n"
    "buffer_load_dwordx4 v11, s[44:47], s86 offen offset:3072 nt lds\n"
    "v_mfma_f32_16x16x32_bf16 v[184:187], a[144:147], a[236:239], v[184:187]\n"
    "v_mfma_f32_16x16x32_bf16 v[188:191], a[144:147], a[240:243], v[188:191]\n"
    "ds_read_b64_tr_b16 a[156:157], v30 offset:34312\n"
    "ds_read_b64_tr_b16 a[158:159], v30 offset:34568\n"
    "v_mfma_f32_16x16x32_bf16 v[192:195], a[144:147], a[244:247], v[192:195]\n"
    "v_mfma_f32_16x16x32_bf16 v[196:199], a[148:151], a[228:231], v[196:199]\n"
    "v_mfma_f32_16x16x32_bf16 v[200:203], a[148:151], a[232:235], v[200:203]\n"
    "v_mfma_f32_16x16x32_bf16 v[204:207], a[148:151], a[236:239], v[204:207]\n"
    "v_mfma_f32_16x16x32_bf16 v[208:211], a[148:151], a[240:243], v[208:211]\n"
    "v_mfma_f32_16x16x32_bf16 v[212:215], a[148:151], a[244:247], v[212:215]\n"
    "s_waitcnt lgkmcnt(0)\n"
    "v_mfma_f32_16x16x32_bf16 v[216:219], a[152:155], a[228:231], v[216:219]\n"
    "v_mfma_f32_16x16x32_bf16 v[220:223], a[152:155], a[232:235], v[220:223]\n"
    "v_mfma_f32_16x16x32_bf16 v[224:227], a[152:155], a[236:239], v[224:227]\n"
    "v_mfma_f32_16x16x32_bf16 v[228:231], a[152:155], a[240:243], v[228:231]\n"
    "v_mfma_f32_16x16x32_bf16 v[232:235], a[152:155], a[244:247], v[232:235]\n"
    "v_mfma_f32_16x16x32_bf16 v[236:239], a[156:159], a[228:231], v[236:239]\n"
    "v_mfma_f32_16x16x32_bf16 v[240:243], a[156:159], a[232:235], v[240:243]\n"
    "v_mfma_f32_16x16x32_bf16 v[244:247], a[156:159], a[236:239], v[244:247]\n"
    "v_mfma_f32_16x16x32_bf16 v[248:251], a[156:159], a[240:243], v[248:251]\n"
    "v_mfma_f32_16x16x32_bf16 v[252:255], a[156:159], a[244:247], v[252:255]\n"
    "LJOIN_%=:\n"
    "s_barrier\n"
    "s_add_u32 m0, s73, 0x18000\n"
    "s_add_u32 s87, s73, 0x4000\n"
    "buffer_load_dwordx4 v12, s[48:51], s87 offen nt lds\n"
    "buffer_load_dwordx4 v13, s[48:51], s87 offen offset:1024 nt lds\n"
    "buffer_load_dwordx4 v12, s[48:51], s87 offen offset:2048 nt lds\n"
    "buffer_load_dwordx4 v13, s[48:51], s87 offen offset:3072 nt lds\n"
    "v_pk_mul_f32 v[96:97], v[96:97], s[82:83]\n"
    "v_pk_mul_f32 v[98:99], v[98:99], s[82:83]\n"
    "v_pk_mul_f32 v[116:117], v[116:117], s[82:83]\n"
    "v_pk_mul_f32 v[118:119], v[118:119], s[82:83]\n"
    "v_cvt_pk_bf16_f32 v56, v96, v97\n"
    "v_cvt_pk_bf16_f32 v57, v98, v99\n"
    "v_cvt_pk_bf16_f32 v58, v116, v117\n"
    "v_cvt_pk_bf16_f32 v59, v118, v119\n"
    "v_accvgpr_write_b32 a0, v56\n"
    "v_accvgpr_write_b32 a1, v57\n"
    "v_accvgpr_write_b32 a2, v58\n"
    "v_accvgpr_write_b32 a3, v59\n"
    "v_pk_mul_f32 v[100:101], v[100:101], s[82:83]\n"
    "v_pk_mul_f32 v[102:103], v[102:103], s[82:83]\n"
    "v_pk_mul_f32 v[120:121], v[120:121], s[82:83]\n"
    "v_pk_mul_f32 v[122:123], v[122:123], s[82:83]\n"
    "v_cvt_pk_bf16_f32 v60, v100, v101\n"
    "v_cvt_pk_bf16_f32 v61, v102, v103\n"
    "v_cvt_pk_bf16_f32 v62, v120, v121\n"
    "v_cvt_pk_bf16_f32 v63, v122, v123\n"
    "v_accvgpr_write_b32 a16, v60\n"
    "v_accvgpr_write_b32 a17, v61\n"
    "v_accvgpr_write_b32 a18, v62\n"
    "v_accvgpr_write_b32 a19, v63\n"
    "v_pk_mul_f32 v[104:105], v[104:105], s[82:83]\n"
    "v_pk_mul_f32 v[106:107], v[106:107], s[82:83]\n"
    "v_pk_mul_f32 v[124:125], v[124:125], s[82:83]\n"
    "v_pk_mul_f32 v[126:127], v[126:127], s[82:83]\n"
    "v_cvt_pk_bf16_f32 v56, v104, v105\n"
    "v_cvt_pk_bf16_f32 v57, v106, v107\n"
    "v_cvt_pk_bf16_f32 v58, v124, v125\n"
    "v_cvt_pk_bf16_f32 v59, v126, v127\n"
    "v_accvgpr_write_b32 a32, v56\n"
    "v_accvgpr_write_b32 a33, v57\n"
    "v_accvgpr_write_b32 a34, v58\n"
    "v_accvgpr_write_b32 a35, v59\n"
    "v_pk_mul_f32 v[108:109], v[108:109], s[82:83]\n"
    "v_pk_mul_f32 v[110:111], v[110:111], s[82:83]\n"
    "v_pk_mul_f32 v[128:129], v[128:129], s[82:83]\n"
    "v_pk_mul_f32 v[130:131], v[130:131], s[82:83]\n"
    "v_cvt_pk_bf16_f32 v60, v108, v109\n"
    "v_cvt_pk_bf16_f32 v61, v110, v111\n"
    "v_cvt_pk_bf16_f32 v62, v128, v129\n"
    "v_cvt_pk_bf16_f32 v63, v130, v131\n"
    "v_accvgpr_write_b32 a48, v60\n"
    "v_accvgpr_write_b32 a49, v61\n"
    "v_accvgpr_write_b32 a50, v62\n"
    "v_accvgpr_write_b32 a51, v63\n"
    "v_pk_mul_f32 v[112:113], v[112:113], s[82:83]\n"
    "v_pk_mul_f32 v[114:115], v[114:115], s[82:83]\n"
    "v_pk_mul_f32 v[132:133], v[132:133], s[82:83]\n"
    "v_pk_mul_f32 v[134:135], v[134:135], s[82:83]\n"
    "v_cvt_pk_bf16_f32 v56, v112, v113\n"
    "v_cvt_pk_bf16_f32 v57, v114, v115\n"
    "v_cvt_pk_bf16_f32 v58, v132, v133\n"
    "v_cvt_pk_bf16_f32 v59, v134, v135\n"
    "v_accvgpr_write_b32 a64, v56\n"
    "v_accvgpr_write_b32 a65, v57\n"
    "v_accvgpr_write_b32 a66, v58\n"
    "v_accvgpr_write_b32 a67, v59\n"
    "v_pk_mul_f32 v[136:137], v[136:137], s[82:83]\n"
    "v_pk_mul_f32 v[138:139], v[138:139], s[82:83]\n"
    "v_pk_mul_f32 v[156:157], v[156:157], s[82:83]\n"
    "v_pk_mul_f32 v[158:159], v[158:159], s[82:83]\n"
    "v_cvt_pk_bf16_f32 v60, v136, v137\n"
    "v_cvt_pk_bf16_f32 v61, v138, v139\n"
    "v_cvt_pk_bf16_f32 v62, v156, v157\n"
    "v_cvt_pk_bf16_f32 v63, v158, v159\n"
    "v_accvgpr_write_b32 a4, v60\n"
    "v_accvgpr_write_b32 a5, v61\n"
    "v_accvgpr_write_b32 a6, v62\n"
    "v_accvgpr_write_b32 a7, v63\n"
    "v_pk_mul_f32 v[140:141], v[140:141], s[82:83]\n"
    "v_pk_mul_f32 v[142:143], v[142:143], s[82:83]\n"
    "v_pk_mul_f32 v[160:161], v[160:161], s[82:83]\n"
    "v_pk_mul_f32 v[162:163], v[162:163], s[82:83]\n"
    "v_cvt_pk_bf16_f32 v56, v140, v141\n"
    "v_cvt_pk_bf16_f32 v57, v142, v143\n"
    "v_cvt_pk_bf16_f32 v58, v160, v161\n"
    "v_cvt_pk_bf16_f32 v59, v162, v163\n"
    "v_accvgpr_write_b32 a20, v56\n"
    "v_accvgpr_write_b32 a21, v57\n"
    "v_accvgpr_write_b32 a22, v58\n"
    "v_accvgpr_write_b32 a23, v59\n"
    "v_pk_mul_f32 v[144:145], v[144:145], s[82:83]\n"
    "v_pk_mul_f32 v[146:147], v[146:147], s[82:83]\n"
    "v_pk_mul_f32 v[164:165], v[164:165], s[82:83]\n"
    "v_pk_mul_f32 v[166:167], v[166:167], s[82:83]\n"
    "v_cvt_pk_bf16_f32 v60, v144, v145\n"
    "v_cvt_pk_bf16_f32 v61, v146, v147\n"
    "v_cvt_pk_bf16_f32 v62, v164, v165\n"
    "v_cvt_pk_bf16_f32 v63, v166, v167\n"
    "v_accvgpr_write_b32 a36, v60\n"
    "v_accvgpr_write_b32 a37, v61\n"
    "v_accvgpr_write_b32 a38, v62\n"
    "v_accvgpr_write_b32 a39, v63\n"
    "v_pk_mul_f32 v[148:149], v[148:149], s[82:83]\n"
    "v_pk_mul_f32 v[150:151], v[150:151], s[82:83]\n"
    "v_pk_mul_f32 v[168:169], v[168:169], s[82:83]\n"
    "v_pk_mul_f32 v[170:171], v[170:171], s[82:83]\n"
    "v_cvt_pk_bf16_f32 v56, v148, v149\n"
    "v_cvt_pk_bf16_f32 v57, v150, v151\n"
    "v_cvt_pk_bf16_f32 v58, v168, v169\n"
    "v_cvt_pk_bf16_f32 v59, v170, v171\n"
    "v_accvgpr_write_b32 a52, v56\n"
    "v_accvgpr_write_b32 a53, v57\n"
    "v_accvgpr_write_b32 a54, v58\n"
    "v_accvgpr_write_b32 a55, v59\n"
    "v_pk_mul_f32 v[152:153], v[152:153], s[82:83]\n"
    "v_pk_mul_f32 v[154:155], v[154:155], s[82:83]\n"
    "v_pk_mul_f32 v[172:173], v[172:173], s[82:83]\n"
    "v_pk_mul_f32 v[174:175], v[174:175], s[82:83]\n"
    "v_cvt_pk_bf16_f32 v60, v152, v153\n"
    "v_cvt_pk_bf16_f32 v61, v154, v155\n"
    "v_cvt_pk_bf16_f32 v62, v172, v173\n"
    "v_cvt_pk_bf16_f32 v63, v174, v175\n"
    "v_accvgpr_write_b32 a68, v60\n"
    "v_accvgpr_write_b32 a69, v61\n"
    "v_accvgpr_write_b32 a70, v62\n"
    "v_accvgpr_write_b32 a71, v63\n"
    "v_pk_mul_f32 v[176:177], v[176:177], s[82:83]\n"
    "v_pk_mul_f32 v[178:179], v[178:179], s[82:83]\n"
    "v_pk_mul_f32 v[196:197], v[196:197], s[82:83]\n"
    "v_pk_mul_f32 v[198:199], v[198:199], s[82:83]\n"
    "v_cvt_pk_bf16_f32 v56, v176, v177\n"
    "v_cvt_pk_bf16_f32 v57, v178, v179\n"
    "v_cvt_pk_bf16_f32 v58, v196, v197\n"
    "v_cvt_pk_bf16_f32 v59, v198, v199\n"
    "v_accvgpr_write_b32 a8, v56\n"
    "v_accvgpr_write_b32 a9, v57\n"
    "v_accvgpr_write_b32 a10, v58\n"
    "v_accvgpr_write_b32 a11, v59\n"
    "v_pk_mul_f32 v[180:181], v[180:181], s[82:83]\n"
    "v_pk_mul_f32 v[182:183], v[182:183], s[82:83]\n"
    "v_pk_mul_f32 v[200:201], v[200:201], s[82:83]\n"
    "v_pk_mul_f32 v[202:203], v[202:203], s[82:83]\n"
    "v_cvt_pk_bf16_f32 v60, v180, v181\n"
    "v_cvt_pk_bf16_f32 v61, v182, v183\n"
    "v_cvt_pk_bf16_f32 v62, v200, v201\n"
    "v_cvt_pk_bf16_f32 v63, v202, v203\n"
    "v_accvgpr_write_b32 a24, v60\n"
    "v_accvgpr_write_b32 a25, v61\n"
    "v_accvgpr_write_b32 a26, v62\n"
    "v_accvgpr_write_b32 a27, v63\n"
    "v_pk_mul_f32 v[184:185], v[184:185], s[82:83]\n"
    "v_pk_mul_f32 v[186:187], v[186:187], s[82:83]\n"
    "v_pk_mul_f32 v[204:205], v[204:205], s[82:83]\n"
    "v_pk_mul_f32 v[206:207], v[206:207], s[82:83]\n"
    "v_cvt_pk_bf16_f32 v56, v184, v185\n"
    "v_cvt_pk_bf16_f32 v57, v186, v187\n"
    "v_cvt_pk_bf16_f32 v58, v204, v205\n"
    "v_cvt_pk_bf16_f32 v59, v206, v207\n"
    "v_accvgpr_write_b32 a40, v56\n"
    "v_accvgpr_write_b32 a41, v57\n"
    "v_accvgpr_write_b32 a42, v58\n"
    "v_accvgpr_write_b32 a43, v59\n"
    "v_pk_mul_f32 v[188:189], v[188:189], s[82:83]\n"
    "v_pk_mul_f32 v[190:191], v[190:191], s[82:83]\n"
    "v_pk_mul_f32 v[208:209], v[208:209], s[82:83]\n"
    "v_pk_mul_f32 v[210:211], v[210:211], s[82:83]\n"
    "v_cvt_pk_bf16_f32 v60, v188, v189\n"
    "v_cvt_pk_bf16_f32 v61, v190, v191\n"
    "v_cvt_pk_bf16_f32 v62, v208, v209\n"
    "v_cvt_pk_bf16_f32 v63, v210, v211\n"
    "v_accvgpr_write_b32 a56, v60\n"
    "v_accvgpr_write_b32 a57, v61\n"
    "v_accvgpr_write_b32 a58, v62\n"
    "v_accvgpr_write_b32 a59, v63\n"
    "v_pk_mul_f32 v[192:193], v[192:193], s[82:83]\n"
    "v_pk_mul_f32 v[194:195], v[194:195], s[82:83]\n"
    "v_pk_mul_f32 v[212:213], v[212:213], s[82:83]\n"
    "v_pk_mul_f32 v[214:215], v[214:215], s[82:83]\n"
    "v_cvt_pk_bf16_f32 v56, v192, v193\n"
    "v_cvt_pk_bf16_f32 v57, v194, v195\n"
    "v_cvt_pk_bf16_f32 v58, v212, v213\n"
    "v_cvt_pk_bf16_f32 v59, v214, v215\n"
    "v_accvgpr_write_b32 a72, v56\n"
    "v_accvgpr_write_b32 a73, v57\n"
    "v_accvgpr_write_b32 a74, v58\n"
    "v_accvgpr_write_b32 a75, v59\n"
    "v_pk_mul_f32 v[216:217], v[216:217], s[82:83]\n"
    "v_pk_mul_f32 v[218:219], v[218:219], s[82:83]\n"
    "v_pk_mul_f32 v[236:237], v[236:237], s[82:83]\n"
    "v_pk_mul_f32 v[238:239], v[238:239], s[82:83]\n"
    "v_cvt_pk_bf16_f32 v60, v216, v217\n"
    "v_cvt_pk_bf16_f32 v61, v218, v219\n"
    "v_cvt_pk_bf16_f32 v62, v236, v237\n"
    "v_cvt_pk_bf16_f32 v63, v238, v239\n"
    "v_accvgpr_write_b32 a12, v60\n"
    "v_accvgpr_write_b32 a13, v61\n"
    "v_accvgpr_write_b32 a14, v62\n"
    "v_accvgpr_write_b32 a15, v63\n"
    "v_pk_mul_f32 v[220:221], v[220:221], s[82:83]\n"
    "v_pk_mul_f32 v[222:223], v[222:223], s[82:83]\n"
    "v_pk_mul_f32 v[240:241], v[240:241], s[82:83]\n"
    "v_pk_mul_f32 v[242:243], v[242:243], s[82:83]\n"
    "v_cvt_pk_bf16_f32 v56, v220, v221\n"
    "v_cvt_pk_bf16_f32 v57, v222, v223\n"
    "v_cvt_pk_bf16_f32 v58, v240, v241\n"
    "v_cvt_pk_bf16_f32 v59, v242, v243\n"
    "v_accvgpr_write_b32 a28, v56\n"
    "v_accvgpr_write_b32 a29, v57\n"
    "v_accvgpr_write_b32 a30, v58\n"
    "v_accvgpr_write_b32 a31, v59\n"
    "v_pk_mul_f32 v[224:225], v[224:225], s[82:83]\n"
    "v_pk_mul_f32 v[226:227], v[226:227], s[82:83]\n"
    "v_pk_mul_f32 v[244:245], v[244:245], s[82:83]\n"
    "v_pk_mul_f32 v[246:247], v[246:247], s[82:83]\n"
    "v_cvt_pk_bf16_f32 v60, v224, v225\n"
    "v_cvt_pk_bf16_f32 v61, v226, v227\n"
    "v_cvt_pk_bf16_f32 v62, v244, v245\n"
    "v_cvt_pk_bf16_f32 v63, v246, v247\n"
    "v_accvgpr_write_b32 a44, v60\n"
    "v_accvgpr_write_b32 a45, v61\n"
    "v_accvgpr_write_b32 a46, v62\n"
    "v_accvgpr_write_b32 a47, v63\n"
    "v_pk_mul_f32 v[228:229], v[228:229], s[82:83]\n"
    "v_pk_mul_f32 v[230:231], v[230:231], s[82:83]\n"
    "v_pk_mul_f32 v[248:249], v[248:249], s[82:83]\n"
    "v_pk_mul_f32 v[250:251], v[250:251], s[82:83]\n"
    "v_cvt_pk_bf16_f32 v56, v228, v229\n"
    "v_cvt_pk_bf16_f32 v57, v230, v231\n"
    "v_cvt_pk_bf16_f32 v58, v248, v249\n"
    "v_cvt_pk_bf16_f32 v59, v250, v251\n"
    "v_accvgpr_write_b32 a60, v56\n"
    "v_accvgpr_write_b32 a61, v57\n"
    "v_accvgpr_write_b32 a62, v58\n"
    "v_accvgpr_write_b32 a63, v59\n"
    "v_pk_mul_f32 v[232:233], v[232:233], s[82:83]\n"
    "v_pk_mul_f32 v[234:235], v[234:235], s[82:83]\n"
    "v_pk_mul_f32 v[252:253], v[252:253], s[82:83]\n"
    "v_pk_mul_f32 v[254:255], v[254:255], s[82:83]\n"
    "v_cvt_pk_bf16_f32 v60, v232, v233\n"
    "v_cvt_pk_bf16_f32 v61, v234, v235\n"
    "v_cvt_pk_bf16_f32 v62, v252, v253\n"
    "v_cvt_pk_bf16_f32 v63, v254, v255\n"
    "v_accvgpr_write_b32 a76, v60\n"
    "v_accvgpr_write_b32 a77, v61\n"
    "v_accvgpr_write_b32 a78, v62\n"
    "v_accvgpr_write_b32 a79, v63\n"
    "s_add_u32 m0, s73, 0x1c000\n"
    "s_add_u32 s87, s73, 0x24000\n"
    "buffer_load_dwordx4 v12, s[48:51], s87 offen nt lds\n"
    "buffer_load_dwordx4 v13, s[48:51], s87 offen offset:1024 nt lds\n"
    "buffer_load_dwordx4 v12, s[48:51], s87 offen offset:2048 nt lds\n"
    "buffer_load_dwordx4 v13, s[48:51], s87 offen offset:3072 nt lds\n"
    "s_waitcnt vmcnt(51) lgkmcnt(0)\n"
    "s_barrier\n"
    "ds_read_b128 a[144:147], v2\n"
    "ds_read_b128 a[148:151], v3\n"
    "ds_read_b128 a[152:155], v4\n"
    "ds_read_b128 a[156:159], v5\n"
    "s_waitcnt lgkmcnt(3)\n"
    "v_mfma_f32_16x16x32_bf16 v[108:111], a[144:147], a[0:3], 0\n"
    "v_mfma_f32_16x16x32_bf16 v[112:115], a[144:147], a[16:19], 0\n"
    "v_mfma_f32_16x16x32_bf16 v[116:119], a[144:147], a[32:35], 0\n"
    "v_mfma_f32_16x16x32_bf16 v[120:123], a[144:147], a[48:51], 0\n"
    "v_mfma_f32_16x16x32_bf16 v[124:127], a[144:147], a[64:67], 0\n"
    "s_waitcnt lgkmcnt(2)\n"
    "v_mfma_f32_16x16x32_bf16 v[108:111], a[148:151], a[4:7], v[108:111]\n"
    "v_mfma_f32_16x16x32_bf16 v[112:115], a[148:151], a[20:23], v[112:115]\n"
    "v_mfma_f32_16x16x32_bf16 v[116:119], a[148:151], a[36:39], v[116:119]\n"
    "v_mfma_f32_16x16x32_bf16 v[120:123], a[148:151], a[52:55], v[120:123]\n"
    "v_mfma_f32_16x16x32_bf16 v[124:127], a[148:151], a[68:71], v[124:127]\n"
    "s_waitcnt lgkmcnt(1)\n"
    "v_mfma_f32_16x16x32_bf16 v[108:111], a[152:155], a[8:11], v[108:111]\n"
    "v_mfma_f32_16x16x32_bf16 v[112:115], a[152:155], a[24:27], v[112:115]\n"
    "v_mfma_f32_16x16x32_bf16 v[116:119], a[152:155], a[40:43], v[116:119]\n"
    "v_mfma_f32_16x16x32_bf16 v[120:123], a[152:155], a[56:59], v[120:123]\n"
    "v_mfma_f32_16x16x32_bf16 v[124:127], a[152:155], a[72:75], v[124:127]\n"
    "s_waitcnt lgkmcnt(0)\n"
    "v_mfma_f32_16x16x32_bf16 v[108:111], a[156:159], a[12:15], v[108:111]\n"
    "v_mfma_f32_16x16x32_bf16 v[112:115], a[156:159], a[28:31], v[112:115]\n"
    "v_mfma_f32_16x16x32_bf16 v[116:119], a[156:159], a[44:47], v[116:119]\n"
    "v_mfma_f32_16x16x32_bf16 v[120:123], a[156:159], a[60:63], v[120:123]\n"
    "v_mfma_f32_16x16x32_bf16 v[124:127], a[156:159], a[76:79], v[124:127]\n"
    "s_nop 7\n"
    "v_max3_f32 v28, v108, v109, v110\n"
    "v_max3_f32 v32, v112, v113, v114\n"
    "v_max3_f32 v36, v116, v117, v118\n"
    "v_max3_f32 v40, v120, v121, v122\n"
    "v_max3_f32 v44, v124, v125, v126\n"
    "v_max_f32 v28, v28, v111\n"
    "v_max_f32 v32, v32, v115\n"
    "v_max_f32 v36, v36, v119\n"
    "v_max_f32 v40, v40, v123\n"
    "v_max_f32 v44, v44, v127\n"
    "v_mov_b32 v29, v28\n"
    "v_mov_b32 v33, v32\n"
    "v_mov_b32 v37, v36\n"
    "v_mov_b32 v41, v40\n"
    "v_mov_b32 v45, v44\n"
    "v_permlane16_swap_b32 v28, v29\n"
    "v_permlane16_swap_b32 v32, v33\n"
    "v_permlane16_swap_b32 v36, v37\n"
    "v_permlane16_swap_b32 v40, v41\n"
    "v_permlane16_swap_b32 v44, v45\n"
    "v_max_f32 v28, v28, v29\n"
    "v_max_f32 v32, v32, v33\n"
    "v_max_f32 v36, v36, v37\n"
    "v_max_f32 v40, v40, v41\n"
    "v_max_f32 v44, v44, v45\n"
    "v_mov_b32 v29, v28\n"
    "v_mov_b32 v33, v32\n"
    "v_mov_b32 v37, v36\n"
    "v_mov_b32 v41, v40\n"
    "v_mov_b32 v45, v44\n"
    "v_permlane32_swap_b32 v28, v29\n"
    "v_permlane32_swap_b32 v32, v33\n"
    "v_permlane32_swap_b32 v36, v37\n"
    "v_permlane32_swap_b32 v40, v41\n"
    "v_permlane32_swap_b32 v44, v45\n"
    "v_max_f32 v28, v28, v29\n"
    "v_max_f32 v32, v32, v33\n"
    "v_max_f32 v36, v36, v37\n"
    "v_max_f32 v40, v40, v41\n"
    "v_max_f32 v44, v44, v45\n"
    "v_xor_b32 v68, 0x80000000, v28\n"
    "v_xor_b32 v72, 0x80000000, v32\n"
    "v_xor_b32 v76, 0x80000000, v36\n"
    "v_xor_b32 v80, 0x80000000, v40\n"
    "v_xor_b32 v84, 0x80000000, v44\n"
    "v_mov_b32 v69, v68\n"
    "v_mov_b32 v73, v72\n"
    "v_mov_b32 v77, v76\n"
    "v_mov_b32 v81, v80\n"
    "v_mov_b32 v85, v84\n"
    "v_mov_b32 v70, v68\n"
    "v_mov_b32 v74, v72\n"
    "v_mov_b32 v78, v76\n"
    "v_mov_b32 v82, v80\n"
    "v_mov_b32 v86, v84\n"
    "v_mov_b32 v71, v68\n"
    "v_mov_b32 v75, v72\n"
    "v_mov_b32 v79, v76\n"
    "v_mov_b32 v83, v80\n"
    "v_mov_b32 v87, v84\n"
    "ds_read_b128 a[160:163], v2 offset:4096\n"
    "ds_read_b128 a[164:167], v3 offset:4096\n"
    "ds_read_b128 a[168:171], v4 offset:4096\n"
    "ds_read_b128 a[172:175], v5 offset:4096\n"
    "s_waitcnt lgkmcnt(3)\n"
    "v_mfma_f32_16x16x32_bf16 v[48:51], a[160:163], a[0:3], v[68:71]\n"
    "s_add_u32 m0, s73, 0x20000\n"
    "v_add_f32 v108, v108, v68\n"
    "v_mfma_f32_16x16x32_bf16 v[52:55], a[160:163], a[16:19], v[72:75]\n"
    "s_add_u32 s86, s73, 0x8000\n"
    "v_exp_f32 v108, v108\n"
    "v_mfma_f32_16x16x32_bf16 v[56:59], a[160:163], a[32:35], v[76:79]\n"
    "v_add_f32 v109, v109, v68\n"
    "v_mfma_f32_16x16x32_bf16 v[60:63], a[160:163], a[48:51], v[80:83]\n"
    "v_exp_f32 v109, v109\n"
    "v_mfma_f32_16x16x32_bf16 v[64:67], a[160:163], a[64:67], v[84:87]\n"
    "s_waitcnt lgkmcnt(0)\n"
    "v_add_f32 v110, v110, v68\n"
    "v_mfma_f32_16x16x32_bf16 v[48:51], a[164:167], a[4:7], v[48:51]\n"
    "buffer_load_dwordx4 v8, s[44:47], s86 offen nt lds\n"
    "v_mfma_f32_16x16x32_bf16 v[52:55], a[164:167], a[20:23], v[52:55]\n"
    "v_exp_f32 v110, v110\n"
    "v_mfma_f32_16x16x32_bf16 v[56:59], a[164:167], a[36:39], v[56:59]\n"
    "v_cvt_pk_bf16_f32 v88, v108, v109\n"
    "v_add_f32 v111, v111, v68\n"
    "v_mfma_f32_16x16x32_bf16 v[60:63], a[164:167], a[52:55], v[60:63]\n"
    "v_exp_f32 v111, v111\n"
    "v_mfma_f32_16x16x32_bf16 v[64:67], a[164:167], a[68:71], v[64:67]\n"
    "v_add_f32 v112, v112, v72\n"
    "v_mfma_f32_16x16x32_bf16 v[48:51], a[168:171], a[8:11], v[48:51]\n"
    "s_waitcnt vmcnt(17) lgkmcnt(0)\n"
    "s_barrier\n"
    "v_mfma_f32_16x16x32_bf16 v[52:55], a[168:171], a[24:27], v[52:55]\n"
    "ds_read_b128 a[176:179], v2 offset:8192\n"
    "v_mfma_f32_16x16x32_bf16 v[56:59], a[168:171], a[40:43], v[56:59]\n"
    "v_exp_f32 v112, v112\n"
    "v_mfma_f32_16x16x32_bf16 v[60:63], a[168:171], a[56:59], v[60:63]\n"
    "ds_read_b128 a[180:183], v3 offset:8192\n"
    "v_cvt_pk_bf16_f32 v89, v110, v111\n"
    "v_mfma_f32_16x16x32_bf16 v[64:67], a[168:171], a[72:75], v[64:67]\n"
    "buffer_load_dwordx4 v9, s[44:47], s86 offen offset:1024 nt lds\n"
    "v_mfma_f32_16x16x32_bf16 v[48:51], a[172:175], a[12:15], v[48:51]\n"
    "v_add_f32 v113, v113, v72\n"
    "v_mfma_f32_16x16x32_bf16 v[52:55], a[172:175], a[28:31], v[52:55]\n"
    "v_exp_f32 v113, v113\n"
    "v_mfma_f32_16x16x32_bf16 v[56:59], a[172:175], a[44:47], v[56:59]\n"
    "v_add_f32 v114, v114, v72\n"
    "v_mfma_f32_16x16x32_bf16 v[60:63], a[172:175], a[60:63], v[60:63]\n"
    "ds_read_b128 a[184:187], v4 offset:8192\n"
    "v_mfma_f32_16x16x32_bf16 v[64:67], a[172:175], a[76:79], v[64:67]\n"
    "s_waitcnt lgkmcnt(1)\n"
    "v_exp_f32 v114, v114\n"
    "v_mfma_f32_16x16x32_bf16 v[28:31], a[176:179], a[0:3], v[68:71]\n"
    "v_cvt_pk_bf16_f32 v92, v112, v113\n"
    "v_add_f32 v115, v115, v72\n"
    "v_mfma_f32_16x16x32_bf16 v[32:35], a[176:179], a[16:19], v[72:75]\n"
    "v_exp_f32 v115, v115\n"
    "v_mfma_f32_16x16x32_bf16 v[36:39], a[176:179], a[32:35], v[76:79]\n"
    "v_add_f32 v116, v116, v76\n"
    "v_mfma_f32_16x16x32_bf16 v[40:43], a[176:179], a[48:51], v[80:83]\n"
    "ds_read_b128 a[188:191], v5 offset:8192\n"
    "v_mfma_f32_16x16x32_bf16 v[44:47], a[176:179], a[64:67], v[84:87]\n"
    "v_exp_f32 v116, v116\n"
    "v_mfma_f32_16x16x32_bf16 v[28:31], a[180:183], a[4:7], v[28:31]\n"
    "v_cvt_pk_bf16_f32 v93, v114, v115\n"
    "v_add_f32 v117, v117, v76\n"
    "v_mfma_f32_16x16x32_bf16 v[32:35], a[180:183], a[20:23], v[32:35]\n"
    "buffer_load_dwordx4 v10, s[44:47], s86 offen offset:2048 nt lds\n"
    "v_mfma_f32_16x16x32_bf16 v[36:39], a[180:183], a[36:39], v[36:39]\n"
    "v_exp_f32 v117, v117\n"
    "v_mfma_f32_16x16x32_bf16 v[40:43], a[180:183], a[52:55], v[40:43]\n"
    "ds_read_b128 a[144:147], v2 offset:12288\n"
    "v_add_f32 v118, v118, v76\n"
    "v_add_u32 v2, 0x10000, v2\n"
    "v_mfma_f32_16x16x32_bf16 v[44:47], a[180:183], a[68:71], v[44:47]\n"
    "s_waitcnt lgkmcnt(1)\n"
    "v_exp_f32 v118, v118\n"
    "v_mfma_f32_16x16x32_bf16 v[28:31], a[184:187], a[8:11], v[28:31]\n"
    "v_cvt_pk_bf16_f32 v96, v116, v117\n"
    "v_add_f32 v119, v119, v76\n"
    "v_mfma_f32_16x16x32_bf16 v[32:35], a[184:187], a[24:27], v[32:35]\n"
    "v_exp_f32 v119, v119\n"
    "v_mfma_f32_16x16x32_bf16 v[36:39], a[184:187], a[40:43], v[36:39]\n"
    "v_cvt_pk_bf16_f32 v97, v118, v119\n"
    "v_mfma_f32_16x16x32_bf16 v[40:43], a[184:187], a[56:59], v[40:43]\n"
    "ds_read_b128 a[148:151], v3 offset:12288\n"
    "v_add_u32 v3, 0x10000, v3\n"
    "v_mfma_f32_16x16x32_bf16 v[44:47], a[184:187], a[72:75], v[44:47]\n"
    "v_exp_f32 v48, v48\n"
    "v_mfma_f32_16x16x32_bf16 v[28:31], a[188:191], a[12:15], v[28:31]\n"
    "v_exp_f32 v49, v49\n"
    "v_mfma_f32_16x16x32_bf16 v[32:35], a[188:191], a[28:31], v[32:35]\n"
    "v_exp_f32 v50, v50\n"
    "v_mfma_f32_16x16x32_bf16 v[36:39], a[188:191], a[44:47], v[36:39]\n"
    "v_cvt_pk_bf16_f32 v90, v48, v49\n"
    "v_mfma_f32_16x16x32_bf16 v[40:43], a[188:191], a[60:63], v[40:43]\n"
    "v_exp_f32 v51, v51\n"
    "v_mfma_f32_16x16x32_bf16 v[44:47], a[188:191], a[76:79], v[44:47]\n"
    "s_waitcnt lgkmcnt(0)\n"
    "v_exp_f32 v52, v52\n"
    "v_cvt_pk_bf16_f32 v91, v50, v51\n"
    "v_exp_f32 v53, v53\n"
    "v_exp_f32 v54, v54\n"
    "v_cvt_pk_bf16_f32 v94, v52, v53\n"
    "v_exp_f32 v55, v55\n"
    "v_exp_f32 v56, v56\n"
    "v_cvt_pk_bf16_f32 v95, v54, v55\n"
    "v_exp_f32 v57, v57\n"
    "v_exp_f32 v58, v58\n"
    "v_cvt_pk_bf16_f32 v98, v56, v57\n"
    "v_exp_f32 v59, v59\n"
    "v_exp_f32 v60, v60\n"
    "v_cvt_pk_bf16_f32 v99, v58, v59\n"
    "v_exp_f32 v61, v61\n"
    "v_exp_f32 v62, v62\n"
    "v_cvt_pk_bf16_f32 v102, v60, v61\n"
    "v_exp_f32 v63, v63\n"
    "v_exp_f32 v64, v64\n"
    "v_cvt_pk_bf16_f32 v103, v62, v63\n"
    "v_exp_f32 v65, v65\n"
    "v_exp_f32 v66, v66\n"
    "v_cvt_pk_bf16_f32 v106, v64, v65\n"
    "v_exp_f32 v67, v67\n"
    "s_nop 0\n"
    "v_cvt_pk_bf16_f32 v107, v66, v67\n"
    "v_mfma_f32_16x16x32_bf16 v[48:51], a[144:147], a[0:3], v[68:71]\n"
    "ds_read_b128 a[152:155], v4 offset:12288\n"
    "v_add_f32 v120, v120, v80\n"
    "v_add_u32 v4, 0x10000, v4\n"
    "v_mfma_f32_16x16x32_bf16 v[52:55], a[144:147], a[16:19], v[72:75]\n"
    "buffer_load_dwordx4 v11, s[44:47], s86 offen offset:3072 nt lds\n"
    "v_mfma_f32_16x16x32_bf16 v[56:59], a[144:147], a[32:35], v[76:79]\n"
    "s_add_u32 m0, s73, 0x24000\n"
    "v_exp_f32 v120, v120\n"
    "v_mfma_f32_16x16x32_bf16 v[60:63], a[144:147], a[48:51], v[80:83]\n"
    "s_add_u32 s86, s73, 0x28000\n"
    "ds_read_b128 a[156:159], v5 offset:12288\n"
    "v_add_f32 v121, v121, v80\n"
    "v_add_u32 v5, 0x10000, v5\n"
    "v_mfma_f32_16x16x32_bf16 v[64:67], a[144:147], a[64:67], v[84:87]\n"
    "buffer_load_dwordx4 v8, s[44:47], s86 offen nt lds\n"
    "v_mfma_f32_16x16x32_bf16 v[48:51], a[148:151], a[4:7], v[48:51]\n"
    "buffer_load_dwordx4 v9, s[44:47], s86 offen offset:1024 nt lds\n"
    "v_mfma_f32_16x16x32_bf16 v[52:55], a[148:151], a[20:23], v[52:55]\n"
    "v_exp_f32 v121, v121\n"
    "v_mfma_f32_16x16x32_bf16 v[56:59], a[148:151], a[36:39], v[56:59]\n"
    "v_add_f32 v122, v122, v80\n"
    "v_mfma_f32_16x16x32_bf16 v[60:63], a[148:151], a[52:55], v[60:63]\n"
    "v_exp_f32 v122, v122\n"
    "v_mfma_f32_16x16x32_bf16 v[64:67], a[148:151], a[68:71], v[64:67]\n"
    "s_waitcnt lgkmcnt(0)\n"
    "v_cvt_pk_bf16_f32 v100, v120, v121\n"
    "v_add_f32 v123, v123, v80\n"
    "v_mfma_f32_16x16x32_bf16 v[48:51], a[152:155], a[8:11], v[48:51]\n"
    "v_exp_f32 v123, v123\n"
    "v_mfma_f32_16x16x32_bf16 v[52:55], a[152:155], a[24:27], v[52:55]\n"
    "v_add_f32 v124, v124, v84\n"
    "v_mfma_f32_16x16x32_bf16 v[56:59], a[152:155], a[40:43], v[56:59]\n"
    "v_exp_f32 v124, v124\n"
    "v_mfma_f32_16x16x32_bf16 v[60:63], a[152:155], a[56:59], v[60:63]\n"
    "ds_read_b64_tr_b16 a[160:161], v6\n"
    "v_cvt_pk_bf16_f32 v101, v122, v123\n"
    "v_mfma_f32_16x16x32_bf16 v[64:67], a[152:155], a[72:75], v[64:67]\n"
    "ds_read_b64_tr_b16 a[162:163], v6 offset:4096\n"
    "v_add_f32 v125, v125, v84\n"
    "v_mfma_f32_16x16x32_bf16 v[48:51], a[156:159], a[12:15], v[48:51]\n"
    "v_exp_f32 v125, v125\n"
    "v_mfma_f32_16x16x32_bf16 v[52:55], a[156:159], a[28:31], v[52:55]\n"
    "v_add_f32 v126, v126, v84\n"
    "v_mfma_f32_16x16x32_bf16 v[56:59], a[156:159], a[44:47], v[56:59]\n"
    "v_exp_f32 v126, v126\n"
    "v_mfma_f32_16x16x32_bf16 v[60:63], a[156:159], a[60:63], v[60:63]\n"
    "ds_read_b64_tr_b16 a[164:165], v7\n"
    "v_cvt_pk_bf16_f32 v104, v124, v125\n"
    "v_add_f32 v127, v127, v84\n"
    "v_exp_f32 v127, v127\n"
    "s_nop 0\n"
    "v_cvt_pk_bf16_f32 v105, v126, v127\n"
    "v_mfma_f32_16x16x32_bf16 v[64:67], a[156:159], a[76:79], v[64:67]\n"
    "ds_read_b64_tr_b16 a[166:167], v7 offset:4096\n"
    "v_mfma_f32_16x16x32_bf16 a[80:83], a[132:135], v[88:91], 0\n"
    "buffer_load_dwordx4 v10, s[44:47], s86 offen offset:2048 nt lds\n"
    "v_mfma_f32_16x16x32_bf16 a[84:87], a[132:135], v[92:95], 0\n"
    "buffer_load_dwordx4 v11, s[44:47], s86 offen offset:3072 nt lds\n"
    "v_mfma_f32_16x16x32_bf16 a[88:91], a[132:135], v[96:99], 0\n"
    "v_exp_f32 v28, v28\n"
    "v_mfma_f32_16x16x32_bf16 a[92:95], a[132:135], v[100:103], 0\n"
    "ds_read_b64_tr_b16 a[168:169], v6 offset:512\n"
    "ds_read_b64_tr_b16 a[170:171], v6 offset:4608\n"
    "v_mfma_f32_16x16x32_bf16 a[96:99], a[132:135], v[104:107], 0\n"
    "s_waitcnt lgkmcnt(2)\n"
    "v_exp_f32 v29, v29\n"
    "v_mfma_f32_16x16x32_bf16 v[128:131], a[160:163], v[88:91], 0\n"
    "v_exp_f32 v30, v30\n"
    "v_mfma_f32_16x16x32_bf16 v[132:135], a[160:163], v[92:95], 0\n"
    "v_cvt_pk_bf16_f32 v108, v28, v29\n"
    "v_mfma_f32_16x16x32_bf16 v[136:139], a[160:163], v[96:99], 0\n"
    "v_exp_f32 v31, v31\n"
    "v_mfma_f32_16x16x32_bf16 v[140:143], a[160:163], v[100:103], 0\n"
    "ds_read_b64_tr_b16 a[172:173], v7 offset:512\n"
    "ds_read_b64_tr_b16 a[174:175], v7 offset:4608\n"
    "v_mfma_f32_16x16x32_bf16 v[144:147], a[160:163], v[104:107], 0\n"
    "v_exp_f32 v32, v32\n"
    "v_mfma_f32_16x16x32_bf16 v[148:151], a[164:167], v[88:91], 0\n"
    "v_cvt_pk_bf16_f32 v109, v30, v31\n"
    "v_mfma_f32_16x16x32_bf16 v[152:155], a[164:167], v[92:95], 0\n"
    "v_exp_f32 v33, v33\n"
    "v_mfma_f32_16x16x32_bf16 v[156:159], a[164:167], v[96:99], 0\n"
    "v_exp_f32 v34, v34\n"
    "v_mfma_f32_16x16x32_bf16 v[160:163], a[164:167], v[100:103], 0\n"
    "ds_read_b64_tr_b16 a[176:177], v6 offset:1024\n"
    "v_cvt_pk_bf16_f32 v112, v32, v33\n"
    "v_mfma_f32_16x16x32_bf16 v[164:167], a[164:167], v[104:107], 0\n"
    "s_waitcnt lgkmcnt(1)\n"
    "ds_read_b64_tr_b16 a[178:179], v6 offset:5120\n"
    "v_mfma_f32_16x16x32_bf16 v[168:171], a[168:171], v[88:91], 0\n"
    "v_exp_f32 v35, v35\n"
    "v_mfma_f32_16x16x32_bf16 v[172:175], a[168:171], v[92:95], 0\n"
    "v_exp_f32 v36, v36\n"
    "v_mfma_f32_16x16x32_bf16 v[176:179], a[168:171], v[96:99], 0\n"
    "v_cvt_pk_bf16_f32 v113, v34, v35\n"
    "v_mfma_f32_16x16x32_bf16 v[180:183], a[168:171], v[100:103], 0\n"
    "ds_read_b64_tr_b16 a[180:181], v7 offset:1024\n"
    "ds_read_b64_tr_b16 a[182:183], v7 offset:5120\n"
    "v_mfma_f32_16x16x32_bf16 v[184:187], a[168:171], v[104:107], 0\n"
    "v_exp_f32 v37, v37\n"
    "v_mfma_f32_16x16x32_bf16 v[188:191], a[172:175], v[88:91], 0\n"
    "v_exp_f32 v38, v38\n"
    "v_mfma_f32_16x16x32_bf16 a[100:103], a[172:175], v[92:95], 0\n"
    "v_cvt_pk_bf16_f32 v116, v36, v37\n"
    "v_mfma_f32_16x16x32_bf16 a[104:107], a[172:175], v[96:99], 0\n"
    "v_exp_f32 v39, v39\n"
    "v_mfma_f32_16x16x32_bf16 a[108:111], a[172:175], v[100:103], 0\n"
    "ds_read_b64_tr_b16 a[184:185], v6 offset:1536\n"
    "ds_read_b64_tr_b16 a[186:187], v6 offset:5632\n"
    "v_mfma_f32_16x16x32_bf16 a[112:115], a[172:175], v[104:107], 0\n"
    "s_waitcnt lgkmcnt(2)\n"
    "v_exp_f32 v40, v40\n"
    "v_mfma_f32_16x16x32_bf16 a[116:119], a[176:179], v[88:91], 0\n"
    "ds_read_b64_tr_b16 a[188:189], v7 offset:1536\n"
    "v_cvt_pk_bf16_f32 v117, v38, v39\n"
    "v_mfma_f32_16x16x32_bf16 a[120:123], a[176:179], v[92:95], 0\n"
    "ds_read_b64_tr_b16 a[190:191], v7 offset:5632\n"
    "v_mfma_f32_16x16x32_bf16 a[124:127], a[176:179], v[96:99], 0\n"
    "v_exp_f32 v41, v41\n"
    "v_mfma_f32_16x16x32_bf16 a[128:131], a[176:179], v[100:103], 0\n"
    "v_exp_f32 v42, v42\n"
    "v_mfma_f32_16x16x32_bf16 a[192:195], a[176:179], v[104:107], 0\n"
    "v_cvt_pk_bf16_f32 v120, v40, v41\n"
    "v_mfma_f32_16x16x32_bf16 a[196:199], a[180:183], v[88:91], 0\n"
    "v_exp_f32 v43, v43\n"
    "v_mfma_f32_16x16x32_bf16 a[200:203], a[180:183], v[92:95], 0\n"
    "v_exp_f32 v44, v44\n"
    "v_mfma_f32_16x16x32_bf16 a[204:207], a[180:183], v[96:99], 0\n"
    "v_cvt_pk_bf16_f32 v121, v42, v43\n"
    "v_mfma_f32_16x16x32_bf16 a[208:211], a[180:183], v[100:103], 0\n"
    "v_exp_f32 v45, v45\n"
    "v_mfma_f32_16x16x32_bf16 a[212:215], a[180:183], v[104:107], 0\n"
    "s_waitcnt vmcnt(16) lgkmcnt(0)\n"
    "s_barrier\n"
    "v_mfma_f32_16x16x32_bf16 a[216:219], a[184:187], v[88:91], 0\n"
    "s_add_u32 m0, s73, 0\n"
    "ds_read_b128 a[144:147], v2\n"
    "v_mfma_f32_16x16x32_bf16 a[220:223], a[184:187], v[92:95], 0\n"
    "s_add_u32 s87, s73, 0x8000\n"
    "v_exp_f32 v46, v46\n"
    "v_mfma_f32_16x16x32_bf16 a[224:227], a[184:187], v[96:99], 0\n"
    "s_waitcnt vmcnt(6)\n"
    "v_cvt_pk_bf16_f32 v124, v44, v45\n"
    "v_mfma_f32_16x16x32_bf16 a[228:231], a[184:187], v[100:103], 0\n"
    "ds_read_b128 a[148:151], v3\n"
    "v_mfma_f32_16x16x32_bf16 a[232:235], a[184:187], v[104:107], 0\n"
    "v_exp_f32 v47, v47\n"
    "v_mfma_f32_16x16x32_bf16 a[236:239], a[188:191], v[88:91], 0\n"
    "v_cvt_pk_bf16_f32 v125, v46, v47\n"
    "v_mfma_f32_16x16x32_bf16 a[240:243], a[188:191], v[92:95], 0\n"
    "v_exp_f32 v48, v48\n"
    "v_mfma_f32_16x16x32_bf16 a[244:247], a[188:191], v[96:99], 0\n"
    "v_exp_f32 v49, v49\n"
    "v_mfma_f32_16x16x32_bf16 a[248:251], a[188:191], v[100:103], 0\n"
    "ds_read_b128 a[152:155], v4\n"
    "v_mfma_f32_16x16x32_bf16 a[252:255], a[188:191], v[104:107], 0\n"
    "s_waitcnt lgkmcnt(1)\n"
    "v_exp_f32 v50, v50\n"
    "v_mfma_f32_16x16x32_bf16 v[28:31], a[144:147], a[0:3], v[68:71]\n"
    "v_cvt_pk_bf16_f32 v110, v48, v49\n"
    "v_mfma_f32_16x16x32_bf16 v[32:35], a[144:147], a[16:19], v[72:75]\n"
    "v_exp_f32 v51, v51\n"
    "v_mfma_f32_16x16x32_bf16 v[36:39], a[144:147], a[32:35], v[76:79]\n"
    "v_exp_f32 v52, v52\n"
    "v_mfma_f32_16x16x32_bf16 v[40:43], a[144:147], a[48:51], v[80:83]\n"
    "ds_read_b128 a[156:159], v5\n"
    "v_cvt_pk_bf16_f32 v111, v50, v51\n"
    "v_mfma_f32_16x16x32_bf16 v[44:47], a[144:147], a[64:67], v[84:87]\n"
    "buffer_load_dwordx4 v12, s[48:51], s87 offen nt lds\n"
    "v_mfma_f32_16x16x32_bf16 v[28:31], a[148:151], a[4:7], v[28:31]\n"
    "s_waitcnt vmcnt(6)\n"
    "v_exp_f32 v53, v53\n"
    "v_mfma_f32_16x16x32_bf16 v[32:35], a[148:151], a[20:23], v[32:35]\n"
    "v_exp_f32 v54, v54\n"
    "v_mfma_f32_16x16x32_bf16 v[36:39], a[148:151], a[36:39], v[36:39]\n"
    "v_cvt_pk_bf16_f32 v114, v52, v53\n"
    "v_mfma_f32_16x16x32_bf16 v[40:43], a[148:151], a[52:55], v[40:43]\n"
    "ds_read_b128 a[160:163], v2 offset:4096\n"
    "v_mfma_f32_16x16x32_bf16 v[44:47], a[148:151], a[68:71], v[44:47]\n"
    "s_waitcnt lgkmcnt(1)\n"
    "v_exp_f32 v55, v55\n"
    "v_mfma_f32_16x16x32_bf16 v[28:31], a[152:155], a[8:11], v[28:31]\n"
    "v_exp_f32 v56, v56\n"
    "v_mfma_f32_16x16x32_bf16 v[32:35], a[152:155], a[24:27], v[32:35]\n"
    "buffer_load_dwordx4 v13, s[48:51], s87 offen offset:1024 nt lds\n"
    "v_mfma_f32_16x16x32_bf16 v[36:39], a[152:155], a[40:43], v[36:39]\n"
    "s_waitcnt vmcnt(6)\n"
    "v_cvt_pk_bf16_f32 v115, v54, v55\n"
    "v_mfma_f32_16x16x32_bf16 v[40:43], a[152:155], a[56:59], v[40:43]\n"
    "ds_read_b128 a[164:167], v3 offset:4096\n"
    "v_mfma_f32_16x16x32_bf16 v[44:47], a[152:155], a[72:75], v[44:47]\n"
    "v_exp_f32 v57, v57\n"
    "v_mfma_f32_16x16x32_bf16 v[28:31], a[156:159], a[12:15], v[28:31]\n"
    "v_exp_f32 v58, v58\n"
    "v_mfma_f32_16x16x32_bf16 v[32:35], a[156:159], a[28:31], v[32:35]\n"
    "v_cvt_pk_bf16_f32 v118, v56, v57\n"
    "v_mfma_f32_16x16x32_bf16 v[36:39], a[156:159], a[44:47], v[36:39]\n"
    "v_exp_f32 v59, v59\n"
    "v_mfma_f32_16x16x32_bf16 v[40:43], a[156:159], a[60:63], v[40:43]\n"
    "v_exp_f32 v60, v60\n"
    "v_mfma_f32_16x16x32_bf16 v[44:47], a[156:159], a[76:79], v[44:47]\n"
    "s_waitcnt lgkmcnt(0)\n"
    "ds_read_b128 a[168:171], v4 offset:4096\n"
    "v_cvt_pk_bf16_f32 v119, v58, v59\n"
    "v_exp_f32 v61, v61\n"
    "v_exp_f32 v62, v62\n"
    "v_cvt_pk_bf16_f32 v122, v60, v61\n"
    "v_exp_f32 v63, v63\n"
    "v_exp_f32 v64, v64\n"
    "v_cvt_pk_bf16_f32 v123, v62, v63\n"
    "v_exp_f32 v65, v65\n"
    "v_exp_f32 v66, v66\n"
    "v_cvt_pk_bf16_f32 v126, v64, v65\n"
    "v_exp_f32 v67, v67\n"
    "s_nop 0\n"
    "v_cvt_pk_bf16_f32 v127, v66, v67\n"
    "v_mfma_f32_16x16x32_bf16 v[48:51], a[160:163], a[0:3], v[68:71]\n"
    "buffer_load_dwordx4 v12, s[48:51], s87 offen offset:2048 nt lds\n"
    "v_mfma_f32_16x16x32_bf16 v[52:55], a[160:163], a[16:19], v[72:75]\n"
    "s_waitcnt vmcnt(6)\n"
    "v_mfma_f32_16x16x32_bf16 v[56:59], a[160:163], a[32:35], v[76:79]\n"
    "buffer_load_dwordx4 v13, s[48:51], s87 offen offset:3072 nt lds\n"
    "v_mfma_f32_16x16x32_bf16 v[60:63], a[160:163], a[48:51], v[80:83]\n"
    "s_add_u32 m0, s73, 0x4000\n"
    "ds_read_b128 a[172:175], v5 offset:4096\n"
    "v_mfma_f32_16x16x32_bf16 v[64:67], a[160:163], a[64:67], v[84:87]\n"
    "s_add_u32 s87, s73, 0x28000\n"
    "v_exp_f32 v28, v28\n"
    "v_mfma_f32_16x16x32_bf16 v[48:51], a[164:167], a[4:7], v[48:51]\n"
    "s_waitcnt vmcnt(6)\n"
    "v_mfma_f32_16x16x32_bf16 v[52:55], a[164:167], a[20:23], v[52:55]\n"
    "buffer_load_dwordx4 v12, s[48:51], s87 offen nt lds\n"
    "v_mfma_f32_16x16x32_bf16 v[56:59], a[164:167], a[36:39], v[56:59]\n"
    "v_exp_f32 v29, v29\n"
    "v_mfma_f32_16x16x32_bf16 v[60:63], a[164:167], a[52:55], v[60:63]\n"
    "s_waitcnt vmcnt(6)\n"
    "v_exp_f32 v30, v30\n"
    "v_mfma_f32_16x16x32_bf16 v[64:67], a[164:167], a[68:71], v[64:67]\n"
    "s_waitcnt lgkmcnt(0)\n"
    "v_cvt_pk_bf16_f32 v88, v28, v29\n"
    "v_mfma_f32_16x16x32_bf16 v[48:51], a[168:171], a[8:11], v[48:51]\n"
    "v_exp_f32 v31, v31\n"
    "v_mfma_f32_16x16x32_bf16 v[52:55], a[168:171], a[24:27], v[52:55]\n"
    "v_mfma_f32_16x16x32_bf16 v[56:59], a[168:171], a[40:43], v[56:59]\n"
    "v_exp_f32 v32, v32\n"
    "v_mfma_f32_16x16x32_bf16 v[60:63], a[168:171], a[56:59], v[60:63]\n"
    "ds_read_b64_tr_b16 a[176:177], v6 offset:8192\n"
    "v_cvt_pk_bf16_f32 v89, v30, v31\n"
    "v_mfma_f32_16x16x32_bf16 v[64:67], a[168:171], a[72:75], v[64:67]\n"
    "ds_read_b64_tr_b16 a[178:179], v6 offset:12288\n"
    "v_mfma_f32_16x16x32_bf16 v[48:51], a[172:175], a[12:15], v[48:51]\n"
    "buffer_load_dwordx4 v13, s[48:51], s87 offen offset:1024 nt lds\n"
    "v_mfma_f32_16x16x32_bf16 v[52:55], a[172:175], a[28:31], v[52:55]\n"
    "s_waitcnt vmcnt(6)\n"
    "v_exp_f32 v33, v33\n"
    "v_mfma_f32_16x16x32_bf16 v[56:59], a[172:175], a[44:47], v[56:59]\n"
    "v_exp_f32 v34, v34\n"
    "v_mfma_f32_16x16x32_bf16 v[60:63], a[172:175], a[60:63], v[60:63]\n"
    "ds_read_b64_tr_b16 a[180:181], v7 offset:8192\n"
    "v_cvt_pk_bf16_f32 v92, v32, v33\n"
    "v_mfma_f32_16x16x32_bf16 v[64:67], a[172:175], a[76:79], v[64:67]\n"
    "ds_read_b64_tr_b16 a[182:183], v7 offset:12288\n"
    "v_mfma_f32_16x16x32_bf16 a[80:83], a[132:135], v[108:111], a[80:83]\n"
    "v_exp_f32 v35, v35\n"
    "v_mfma_f32_16x16x32_bf16 a[84:87], a[132:135], v[112:115], a[84:87]\n"
    "buffer_load_dwordx4 v12, s[48:51], s87 offen offset:2048 nt lds\n"
    "v_mfma_f32_16x16x32_bf16 a[88:91], a[132:135], v[116:119], a[88:91]\n"
    "v_exp_f32 v36, v36\n"
    "v_mfma_f32_16x16x32_bf16 a[92:95], a[132:135], v[120:123], a[92:95]\n"
    "ds_read_b64_tr_b16 a[184:185], v6 offset:8704\n"
    "v_cvt_pk_bf16_f32 v93, v34, v35\n"
    "v_mfma_f32_16x16x32_bf16 a[96:99], a[132:135], v[124:127], a[96:99]\n"
    "s_waitcnt lgkmcnt(1)\n"
    "ds_read_b64_tr_b16 a[186:187], v6 offset:12800\n"
    "v_mfma_f32_16x16x32_bf16 v[128:131], a[176:179], v[108:111], v[128:131]\n"
    "s_waitcnt vmcnt(6)\n"
    "v_exp_f32 v37, v37\n"
    "v_mfma_f32_16x16x32_bf16 v[132:135], a[176:179], v[112:115], v[132:135]\n"
    "v_exp_f32 v38, v38\n"
    "v_mfma_f32_16x16x32_bf16 v[136:139], a[176:179], v[116:119], v[136:139]\n"
    "v_cvt_pk_bf16_f32 v96, v36, v37\n"
    "v_mfma_f32_16x16x32_bf16 v[140:143], a[176:179], v[120:123], v[140:143]\n"
    "ds_read_b64_tr_b16 a[188:189], v7 offset:8704\n"
    "ds_read_b64_tr_b16 a[190:191], v7 offset:12800\n"
    "v_mfma_f32_16x16x32_bf16 v[144:147], a[176:179], v[124:127], v[144:147]\n"
    "buffer_load_dwordx4 v13, s[48:51], s87 offen offset:3072 nt lds\n"
    "v_mfma_f32_16x16x32_bf16 v[148:151], a[180:183], v[108:111], v[148:151]\n"
    "v_exp_f32 v39, v39\n"
    "v_mfma_f32_16x16x32_bf16 v[152:155], a[180:183], v[112:115], v[152:155]\n"
    "v_exp_f32 v40, v40\n"
    "v_mfma_f32_16x16x32_bf16 v[156:159], a[180:183], v[116:119], v[156:159]\n"
    "v_cvt_pk_bf16_f32 v97, v38, v39\n"
    "v_mfma_f32_16x16x32_bf16 v[160:163], a[180:183], v[120:123], v[160:163]\n"
    "ds_read_b64_tr_b16 a[144:145], v6 offset:9216\n"
    "ds_read_b64_tr_b16 a[146:147], v6 offset:13312\n"
    "v_mfma_f32_16x16x32_bf16 v[164:167], a[180:183], v[124:127], v[164:167]\n"
    "s_waitcnt lgkmcnt(2)\n"
    "v_exp_f32 v41, v41\n"
    "v_mfma_f32_16x16x32_bf16 v[168:171], a[184:187], v[108:111], v[168:171]\n"
    "v_exp_f32 v42, v42\n"
    "v_mfma_f32_16x16x32_bf16 v[172:175], a[184:187], v[112:115], v[172:175]\n"
    "v_cvt_pk_bf16_f32 v100, v40, v41\n"
    "v_mfma_f32_16x16x32_bf16 v[176:179], a[184:187], v[116:119], v[176:179]\n"
    "v_exp_f32 v43, v43\n"
    "v_mfma_f32_16x16x32_bf16 v[180:183], a[184:187], v[120:123], v[180:183]\n"
    "ds_read_b64_tr_b16 a[148:149], v7 offset:9216\n"
    "ds_read_b64_tr_b16 a[150:151], v7 offset:13312\n"
    "v_mfma_f32_16x16x32_bf16 v[184:187], a[184:187], v[124:127], v[184:187]\n"
    "v_exp_f32 v44, v44\n"
    "v_mfma_f32_16x16x32_bf16 v[188:191], a[188:191], v[108:111], v[188:191]\n"
    "v_cvt_pk_bf16_f32 v101, v42, v43\n"
    "v_mfma_f32_16x16x32_bf16 a[100:103], a[188:191], v[112:115], a[100:103]\n"
    "v_exp_f32 v45, v45\n"
    "v_mfma_f32_16x16x32_bf16 a[104:107], a[188:191], v[116:119], a[104:107]\n"
    "v_exp_f32 v46, v46\n"
    "v_mfma_f32_16x16x32_bf16 a[108:111], a[188:191], v[120:123], a[108:111]\n"
    "ds_read_b64_tr_b16 a[152:153], v6 offset:9728\n"
    "v_cvt_pk_bf16_f32 v104, v44, v45\n"
    "v_mfma_f32_16x16x32_bf16 a[112:115], a[188:191], v[124:127], a[112:115]\n"
    "s_waitcnt lgkmcnt(1)\n"
    "ds_read_b64_tr_b16 a[154:155], v6 offset:13824\n"
    "ds_read_b64_tr_b16 a[156:157], v7 offset:9728\n"
    "v_add_u32 v6, 0x10000, v6\n"
    "v_mfma_f32_16x16x32_bf16 a[116:119], a[144:147], v[108:111], a[116:119]\n"
    "ds_read_b64_tr_b16 a[158:159], v7 offset:13824\n"
    "v_add_u32 v7, 0x10000, v7\n"
    "v_mfma_f32_16x16x32_bf16 a[120:123], a[144:147], v[112:115], a[120:123]\n"
    "v_exp_f32 v47, v47\n"
    "v_mfma_f32_16x16x32_bf16 a[124:127], a[144:147], v[116:119], a[124:127]\n"
    "v_cvt_pk_bf16_f32 v105, v46, v47\n"
    "v_mfma_f32_16x16x32_bf16 a[128:131], a[144:147], v[120:123], a[128:131]\n"
    "v_exp_f32 v48, v48\n"
    "v_mfma_f32_16x16x32_bf16 a[192:195], a[144:147], v[124:127], a[192:195]\n"
    "v_exp_f32 v49, v49\n"
    "v_mfma_f32_16x16x32_bf16 a[196:199], a[148:151], v[108:111], a[196:199]\n"
    "v_exp_f32 v50, v50\n"
    "v_mfma_f32_16x16x32_bf16 a[200:203], a[148:151], v[112:115], a[200:203]\n"
    "v_cvt_pk_bf16_f32 v90, v48, v49\n"
    "v_mfma_f32_16x16x32_bf16 a[204:207], a[148:151], v[116:119], a[204:207]\n"
    "v_exp_f32 v51, v51\n"
    "v_mfma_f32_16x16x32_bf16 a[208:211], a[148:151], v[120:123], a[208:211]\n"
    "v_mfma_f32_16x16x32_bf16 a[212:215], a[148:151], v[124:127], a[212:215]\n"
    "s_waitcnt vmcnt(16) lgkmcnt(0)\n"
    "s_barrier\n"
    "v_mfma_f32_16x16x32_bf16 a[216:219], a[152:155], v[108:111], a[216:219]\n"
    "s_add_u32 m0, s73, 0x8000\n"
    "ds_read_b128 a[160:163], v2 offset:8192\n"
    "v_mfma_f32_16x16x32_bf16 a[220:223], a[152:155], v[112:115], a[220:223]\n"
    "s_add_u32 s86, s73, 0xc000\n"
    "v_exp_f32 v52, v52\n"
    "v_mfma_f32_16x16x32_bf16 a[224:227], a[152:155], v[116:119], a[224:227]\n"
    "s_waitcnt vmcnt(6)\n"
    "v_cvt_pk_bf16_f32 v91, v50, v51\n"
    "v_mfma_f32_16x16x32_bf16 a[228:231], a[152:155], v[120:123], a[228:231]\n"
    "ds_read_b128 a[164:167], v3 offset:8192\n"
    "v_mfma_f32_16x16x32_bf16 a[232:235], a[152:155], v[124:127], a[232:235]\n"
    "buffer_load_dwordx4 v8, s[44:47], s86 offen nt lds\n"
    "v_mfma_f32_16x16x32_bf16 a[236:239], a[156:159], v[108:111], a[236:239]\n"
    "v_exp_f32 v53, v53\n"
    "v_mfma_f32_16x16x32_bf16 a[240:243], a[156:159], v[112:115], a[240:243]\n"
    "s_waitcnt vmcnt(6)\n"
    "v_exp_f32 v54, v54\n"
    "v_mfma_f32_16x16x32_bf16 a[244:247], a[156:159], v[116:119], a[244:247]\n"
    "v_cvt_pk_bf16_f32 v94, v52, v53\n"
    "v_mfma_f32_16x16x32_bf16 a[248:251], a[156:159], v[120:123], a[248:251]\n"
    "ds_read_b128 a[168:171], v4 offset:8192\n"
    "v_mfma_f32_16x16x32_bf16 a[252:255], a[156:159], v[124:127], a[252:255]\n"
    "s_waitcnt lgkmcnt(1)\n"
    "v_exp_f32 v55, v55\n"
    "v_mfma_f32_16x16x32_bf16 v[28:31], a[160:163], a[0:3], v[68:71]\n"
    "v_exp_f32 v56, v56\n"
    "v_mfma_f32_16x16x32_bf16 v[32:35], a[160:163], a[16:19], v[72:75]\n"
    "buffer_load_dwordx4 v9, s[44:47], s86 offen offset:1024 nt lds\n"
    "v_mfma_f32_16x16x32_bf16 v[36:39], a[160:163], a[32:35], v[76:79]\n"
    "v_cvt_pk_bf16_f32 v95, v54, v55\n"
    "v_mfma_f32_16x16x32_bf16 v[40:43], a[160:163], a[48:51], v[80:83]\n"
    "ds_read_b128 a[172:175], v5 offset:8192\n"
    "v_mfma_f32_16x16x32_bf16 v[44:47], a[160:163], a[64:67], v[84:87]\n"
    "s_waitcnt vmcnt(6)\n"
    "v_exp_f32 v57, v57\n"
    "v_mfma_f32_16x16x32_bf16 v[28:31], a[164:167], a[4:7], v[28:31]\n"
    "v_exp_f32 v58, v58\n"
    "v_mfma_f32_16x16x32_bf16 v[32:35], a[164:167], a[20:23], v[32:35]\n"
    "v_cvt_pk_bf16_f32 v98, v56, v57\n"
    "v_mfma_f32_16x16x32_bf16 v[36:39], a[164:167], a[36:39], v[36:39]\n"
    "v_exp_f32 v59, v59\n"
    "v_mfma_f32_16x16x32_bf16 v[40:43], a[164:167], a[52:55], v[40:43]\n"
    "ds_read_b128 a[176:179], v2 offset:12288\n"
    "v_add_u32 v2, 0x10000, v2\n"
    "v_mfma_f32_16x16x32_bf16 v[44:47], a[164:167], a[68:71], v[44:47]\n"
    "s_waitcnt lgkmcnt(1)\n"
    "v_exp_f32 v60, v60\n"
    "v_mfma_f32_16x16x32_bf16 v[28:31], a[168:171], a[8:11], v[28:31]\n"
    "buffer_load_dwordx4 v10, s[44:47], s86 offen offset:2048 nt lds\n"
    "v_mfma_f32_16x16x32_bf16 v[32:35], a[168:171], a[24:27], v[32:35]\n"
    "v_cvt_pk_bf16_f32 v99, v58, v59\n"
    "v_mfma_f32_16x16x32_bf16 v[36:39], a[168:171], a[40:43], v[36:39]\n"
    "s_waitcnt vmcnt(6)\n"
    "v_exp_f32 v61, v61\n"
    "v_mfma_f32_16x16x32_bf16 v[40:43], a[168:171], a[56:59], v[40:43]\n"
    "ds_read_b128 a[180:183], v3 offset:12288\n"
    "v_add_u32 v3, 0x10000, v3\n"
    "v_mfma_f32_16x16x32_bf16 v[44:47], a[168:171], a[72:75], v[44:47]\n"
    "v_exp_f32 v62, v62\n"
    "v_mfma_f32_16x16x32_bf16 v[28:31], a[172:175], a[12:15], v[28:31]\n"
    "v_cvt_pk_bf16_f32 v102, v60, v61\n"
    "v_mfma_f32_16x16x32_bf16 v[32:35], a[172:175], a[28:31], v[32:35]\n"
    "v_exp_f32 v63, v63\n"
    "v_mfma_f32_16x16x32_bf16 v[36:39], a[172:175], a[44:47], v[36:39]\n"
    "v_exp_f32 v64, v64\n"
    "v_mfma_f32_16x16x32_bf16 v[40:43], a[172:175], a[60:63], v[40:43]\n"
    "ds_read_b128 a[184:187], v4 offset:12288\n"
    "v_cvt_pk_bf16_f32 v103, v62, v63\n"
    "v_add_u32 v4, 0x10000, v4\n"
    "v_mfma_f32_16x16x32_bf16 v[44:47], a[172:175], a[76:79], v[44:47]\n"
    "s_waitcnt lgkmcnt(1)\n"
    "v_exp_f32 v65, v65\n"
    "v_exp_f32 v66, v66\n"
    "v_cvt_pk_bf16_f32 v106, v64, v65\n"
    "v_exp_f32 v67, v67\n"
    "s_nop 0\n"
    "v_cvt_pk_bf16_f32 v107, v66, v67\n"
    "v_mfma_f32_16x16x32_bf16 v[48:51], a[176:179], a[0:3], v[68:71]\n"
    "buffer_load_dwordx4 v11, s[44:47], s86 offen offset:3072 nt lds\n"
    "v_mfma_f32_16x16x32_bf16 v[52:55], a[176:179], a[16:19], v[72:75]\n"
    "s_add_u32 m0, s73, 0xc000\n"
    "v_mfma_f32_16x16x32_bf16 v[56:59], a[176:179], a[32:35], v[76:79]\n"
    "s_add_u32 s86, s73, 0x2c000\n"
    "v_mfma_f32_16x16x32_bf16 v[60:63], a[176:179], a[48:51], v[80:83]\n"
    "s_waitcnt vmcnt(6)\n"
    "ds_read_b128 a[188:191], v5 offset:12288\n"
    "v_add_u32 v5, 0x10000, v5\n"
    "v_mfma_f32_16x16x32_bf16 v[64:67], a[176:179], a[64:67], v[84:87]\n"
    "v_exp_f32 v28, v28\n"
    "v_mfma_f32_16x16x32_bf16 v[48:51], a[180:183], a[4:7], v[48:51]\n"
    "buffer_load_dwordx4 v8, s[44:47], s86 offen nt lds\n"
    "v_mfma_f32_16x16x32_bf16 v[52:55], a[180:183], a[20:23], v[52:55]\n"
    "v_exp_f32 v29, v29\n"
    "v_mfma_f32_16x16x32_bf16 v[56:59], a[180:183], a[36:39], v[56:59]\n"
    "v_mfma_f32_16x16x32_bf16 v[60:63], a[180:183], a[52:55], v[60:63]\n"
    "s_waitcnt vmcnt(6)\n"
    "v_exp_f32 v30, v30\n"
    "v_mfma_f32_16x16x32_bf16 v[64:67], a[180:183], a[68:71], v[64:67]\n"
    "s_waitcnt lgkmcnt(0)\n"
    "v_cvt_pk_bf16_f32 v108, v28, v29\n"
    "v_mfma_f32_16x16x32_bf16 v[48:51], a[184:187], a[8:11], v[48:51]\n"
    "v_exp_f32 v31, v31\n"
    "v_mfma_f32_16x16x32_bf16 v[52:55], a[184:187], a[24:27], v[52:55]\n"
    "v_mfma_f32_16x16x32_bf16 v[56:59], a[184:187], a[40:43], v[56:59]\n"
    "v_exp_f32 v32, v32\n"
    "v_mfma_f32_16x16x32_bf16 v[60:63], a[184:187], a[56:59], v[60:63]\n"
    "ds_read_b64_tr_b16 a[144:145], v6\n"
    "v_cvt_pk_bf16_f32 v109, v30, v31\n"
    "v_mfma_f32_16x16x32_bf16 v[64:67], a[184:187], a[72:75], v[64:67]\n"
    "ds_read_b64_tr_b16 a[146:147], v6 offset:4096\n"
    "v_mfma_f32_16x16x32_bf16 v[48:51], a[188:191], a[12:15], v[48:51]\n"
    "buffer_load_dwordx4 v9, s[44:47], s86 offen offset:1024 nt lds\n"
    "v_mfma_f32_16x16x32_bf16 v[52:55], a[188:191], a[28:31], v[52:55]\n"
    "s_waitcnt vmcnt(6)\n"
    "v_exp_f32 v33, v33\n"
    "v_mfma_f32_16x16x32_bf16 v[56:59], a[188:191], a[44:47], v[56:59]\n"
    "v_exp_f32 v34, v34\n"
    "v_mfma_f32_16x16x32_bf16 v[60:63], a[188:191], a[60:63], v[60:63]\n"
    "ds_read_b64_tr_b16 a[148:149], v7\n"
    "v_cvt_pk_bf16_f32 v112, v32, v33\n"
    "v_mfma_f32_16x16x32_bf16 v[64:67], a[188:191], a[76:79], v[64:67]\n"
    "ds_read_b64_tr_b16 a[150:151], v7 offset:4096\n"
    "v_mfma_f32_16x16x32_bf16 a[80:83], a[132:135], v[88:91], a[80:83]\n"
    "v_exp_f32 v35, v35\n"
    "v_mfma_f32_16x16x32_bf16 a[84:87], a[132:135], v[92:95], a[84:87]\n"
    "buffer_load_dwordx4 v10, s[44:47], s86 offen offset:2048 nt lds\n"
    "v_mfma_f32_16x16x32_bf16 a[88:91], a[132:135], v[96:99], a[88:91]\n"
    "v_exp_f32 v36, v36\n"
    "v_mfma_f32_16x16x32_bf16 a[92:95], a[132:135], v[100:103], a[92:95]\n"
    "ds_read_b64_tr_b16 a[152:153], v6 offset:512\n"
    "v_cvt_pk_bf16_f32 v113, v34, v35\n"
    "v_mfma_f32_16x16x32_bf16 a[96:99], a[132:135], v[104:107], a[96:99]\n"
    "s_waitcnt lgkmcnt(1)\n"
    "ds_read_b64_tr_b16 a[154:155], v6 offset:4608\n"
    "v_mfma_f32_16x16x32_bf16 v[128:131], a[144:147], v[88:91], v[128:131]\n"
    "s_waitcnt vmcnt(6)\n"
    "v_exp_f32 v37, v37\n"
    "v_mfma_f32_16x16x32_bf16 v[132:135], a[144:147], v[92:95], v[132:135]\n"
    "v_exp_f32 v38, v38\n"
    "v_mfma_f32_16x16x32_bf16 v[136:139], a[144:147], v[96:99], v[136:139]\n"
    "v_cvt_pk_bf16_f32 v116, v36, v37\n"
    "v_mfma_f32_16x16x32_bf16 v[140:143], a[144:147], v[100:103], v[140:143]\n"
    "ds_read_b64_tr_b16 a[156:157], v7 offset:512\n"
    "ds_read_b64_tr_b16 a[158:159], v7 offset:4608\n"
    "v_mfma_f32_16x16x32_bf16 v[144:147], a[144:147], v[104:107], v[144:147]\n"
    "buffer_load_dwordx4 v11, s[44:47], s86 offen offset:3072 nt lds\n"
    "v_mfma_f32_16x16x32_bf16 v[148:151], a[148:151], v[88:91], v[148:151]\n"
    "v_exp_f32 v39, v39\n"
    "v_mfma_f32_16x16x32_bf16 v[152:155], a[148:151], v[92:95], v[152:155]\n"
    "v_exp_f32 v40, v40\n"
    "v_mfma_f32_16x16x32_bf16 v[156:159], a[148:151], v[96:99], v[156:159]\n"
    "v_cvt_pk_bf16_f32 v117, v38, v39\n"
    "v_mfma_f32_16x16x32_bf16 v[160:163], a[148:151], v[100:103], v[160:163]\n"
    "ds_read_b64_tr_b16 a[160:161], v6 offset:1024\n"
    "ds_read_b64_tr_b16 a[162:163], v6 offset:5120\n"
    "v_mfma_f32_16x16x32_bf16 v[164:167], a[148:151], v[104:107], v[164:167]\n"
    "s_waitcnt lgkmcnt(2)\n"
    "v_exp_f32 v41, v41\n"
    "v_mfma_f32_16x16x32_bf16 v[168:171], a[152:155], v[88:91], v[168:171]\n"
    "v_exp_f32 v42, v42\n"
    "v_mfma_f32_16x16x32_bf16 v[172:175], a[152:155], v[92:95], v[172:175]\n"
    "v_cvt_pk_bf16_f32 v120, v40, v41\n"
    "v_mfma_f32_16x16x32_bf16 v[176:179], a[152:155], v[96:99], v[176:179]\n"
    "v_exp_f32 v43, v43\n"
    "v_mfma_f32_16x16x32_bf16 v[180:183], a[152:155], v[100:103], v[180:183]\n"
    "ds_read_b64_tr_b16 a[164:165], v7 offset:1024\n"
    "ds_read_b64_tr_b16 a[166:167], v7 offset:5120\n"
    "v_mfma_f32_16x16x32_bf16 v[184:187], a[152:155], v[104:107], v[184:187]\n"
    "v_exp_f32 v44, v44\n"
    "v_mfma_f32_16x16x32_bf16 v[188:191], a[156:159], v[88:91], v[188:191]\n"
    "v_cvt_pk_bf16_f32 v121, v42, v43\n"
    "v_mfma_f32_16x16x32_bf16 a[100:103], a[156:159], v[92:95], a[100:103]\n"
    "v_exp_f32 v45, v45\n"
    "v_mfma_f32_16x16x32_bf16 a[104:107], a[156:159], v[96:99], a[104:107]\n"
    "v_exp_f32 v46, v46\n"
    "v_mfma_f32_16x16x32_bf16 a[108:111], a[156:159], v[100:103], a[108:111]\n"
    "ds_read_b64_tr_b16 a[168:169], v6 offset:1536\n"
    "v_cvt_pk_bf16_f32 v124, v44, v45\n"
    "v_mfma_f32_16x16x32_bf16 a[112:115], a[156:159], v[104:107], a[112:115]\n"
    "s_waitcnt lgkmcnt(1)\n"
    "ds_read_b64_tr_b16 a[170:171], v6 offset:5632\n"
    "ds_read_b64_tr_b16 a[172:173], v7 offset:1536\n"
    "v_mfma_f32_16x16x32_bf16 a[116:119], a[160:163], v[88:91], a[116:119]\n"
    "ds_read_b64_tr_b16 a[174:175], v7 offset:5632\n"
    "v_mfma_f32_16x16x32_bf16 a[120:123], a[160:163], v[92:95], a[120:123]\n"
    "v_exp_f32 v47, v47\n"
    "v_mfma_f32_16x16x32_bf16 a[124:127], a[160:163], v[96:99], a[124:127]\n"
    "v_cvt_pk_bf16_f32 v125, v46, v47\n"
    "v_mfma_f32_16x16x32_bf16 a[128:131], a[160:163], v[100:103], a[128:131]\n"
    "v_exp_f32 v48, v48\n"
    "v_mfma_f32_16x16x32_bf16 a[192:195], a[160:163], v[104:107], a[192:195]\n"
    "v_exp_f32 v49, v49\n"
    "v_mfma_f32_16x16x32_bf16 a[196:199], a[164:167], v[88:91], a[196:199]\n"
    "v_exp_f32 v50, v50\n"
    "v_mfma_f32_16x16x32_bf16 a[200:203], a[164:167], v[92:95], a[200:203]\n"
    "v_cvt_pk_bf16_f32 v110, v48, v49\n"
    "v_mfma_f32_16x16x32_bf16 a[204:207], a[164:167], v[96:99], a[204:207]\n"
    "v_exp_f32 v51, v51\n"
    "v_mfma_f32_16x16x32_bf16 a[208:211], a[164:167], v[100:103], a[208:211]\n"
    "v_mfma_f32_16x16x32_bf16 a[212:215], a[164:167], v[104:107], a[212:215]\n"
    "s_waitcnt vmcnt(16) lgkmcnt(0)\n"
    "s_barrier\n"
    "v_mfma_f32_16x16x32_bf16 a[216:219], a[168:171], v[88:91], a[216:219]\n"
    "ds_read_b128 a[176:179], v2\n"
    "v_mfma_f32_16x16x32_bf16 a[220:223], a[168:171], v[92:95], a[220:223]\n"
    "v_exp_f32 v52, v52\n"
    "v_mfma_f32_16x16x32_bf16 a[224:227], a[168:171], v[96:99], a[224:227]\n"
    "v_cvt_pk_bf16_f32 v111, v50, v51\n"
    "v_mfma_f32_16x16x32_bf16 a[228:231], a[168:171], v[100:103], a[228:231]\n"
    "ds_read_b128 a[180:183], v3\n"
    "v_mfma_f32_16x16x32_bf16 a[232:235], a[168:171], v[104:107], a[232:235]\n"
    "v_exp_f32 v53, v53\n"
    "v_mfma_f32_16x16x32_bf16 a[236:239], a[172:175], v[88:91], a[236:239]\n"
    "v_exp_f32 v54, v54\n"
    "v_mfma_f32_16x16x32_bf16 a[240:243], a[172:175], v[92:95], a[240:243]\n"
    "v_cvt_pk_bf16_f32 v114, v52, v53\n"
    "v_mfma_f32_16x16x32_bf16 a[244:247], a[172:175], v[96:99], a[244:247]\n"
    "v_exp_f32 v55, v55\n"
    "v_mfma_f32_16x16x32_bf16 a[248:251], a[172:175], v[100:103], a[248:251]\n"
    "ds_read_b128 a[184:187], v4\n"
    "v_mfma_f32_16x16x32_bf16 a[252:255], a[172:175], v[104:107], a[252:255]\n"
    "s_waitcnt lgkmcnt(1)\n"
    "v_exp_f32 v56, v56\n"
    "v_mfma_f32_16x16x32_bf16 v[28:31], a[176:179], a[0:3], v[68:71]\n"
    "v_cvt_pk_bf16_f32 v115, v54, v55\n"
    "v_mfma_f32_16x16x32_bf16 v[32:35], a[176:179], a[16:19], v[72:75]\n"
    "v_exp_f32 v57, v57\n"
    "v_mfma_f32_16x16x32_bf16 v[36:39], a[176:179], a[32:35], v[76:79]\n"
    "v_exp_f32 v58, v58\n"
    "v_mfma_f32_16x16x32_bf16 v[40:43], a[176:179], a[48:51], v[80:83]\n"
    "ds_read_b128 a[188:191], v5\n"
    "v_cvt_pk_bf16_f32 v118, v56, v57\n"
    "v_mfma_f32_16x16x32_bf16 v[44:47], a[176:179], a[64:67], v[84:87]\n"
    "v_exp_f32 v59, v59\n"
    "v_mfma_f32_16x16x32_bf16 v[28:31], a[180:183], a[4:7], v[28:31]\n"
    "v_mfma_f32_16x16x32_bf16 v[32:35], a[180:183], a[20:23], v[32:35]\n"
    "v_exp_f32 v60, v60\n"
    "v_mfma_f32_16x16x32_bf16 v[36:39], a[180:183], a[36:39], v[36:39]\n"
    "v_cvt_pk_bf16_f32 v119, v58, v59\n"
    "v_mfma_f32_16x16x32_bf16 v[40:43], a[180:183], a[52:55], v[40:43]\n"
    "ds_read_b128 a[144:147], v2 offset:4096\n"
    "v_mfma_f32_16x16x32_bf16 v[44:47], a[180:183], a[68:71], v[44:47]\n"
    "s_waitcnt lgkmcnt(1)\n"
    "v_exp_f32 v61, v61\n"
    "v_mfma_f32_16x16x32_bf16 v[28:31], a[184:187], a[8:11], v[28:31]\n"
    "v_exp_f32 v62, v62\n"
    "v_mfma_f32_16x16x32_bf16 v[32:35], a[184:187], a[24:27], v[32:35]\n"
    "v_cvt_pk_bf16_f32 v122, v60, v61\n"
    "v_mfma_f32_16x16x32_bf16 v[36:39], a[184:187], a[40:43], v[36:39]\n"
    "v_exp_f32 v63, v63\n"
    "v_mfma_f32_16x16x32_bf16 v[40:43], a[184:187], a[56:59], v[40:43]\n"
    "ds_read_b128 a[148:151], v3 offset:4096\n"
    "v_mfma_f32_16x16x32_bf16 v[44:47], a[184:187], a[72:75], v[44:47]\n"
    "v_exp_f32 v64, v64\n"
    "v_mfma_f32_16x16x32_bf16 v[28:31], a[188:191], a[12:15], v[28:31]\n"
    "v_cvt_pk_bf16_f32 v123, v62, v63\n"
    "v_mfma_f32_16x16x32_bf16 v[32:35], a[188:191], a[28:31], v[32:35]\n"
    "v_exp_f32 v65, v65\n"
    "v_mfma_f32_16x16x32_bf16 v[36:39], a[188:191], a[44:47], v[36:39]\n"
    "v_exp_f32 v66, v66\n"
    "v_mfma_f32_16x16x32_bf16 v[40:43], a[188:191], a[60:63], v[40:43]\n"
    "ds_read_b128 a[152:155], v4 offset:4096\n"
    "v_cvt_pk_bf16_f32 v126, v64, v65\n"
    "v_mfma_f32_16x16x32_bf16 v[44:47], a[188:191], a[76:79], v[44:47]\n"
    "s_waitcnt lgkmcnt(1)\n"
    "v_exp_f32 v67, v67\n"
    "s_nop 0\n"
    "v_cvt_pk_bf16_f32 v127, v66, v67\n"
    "v_mfma_f32_16x16x32_bf16 v[48:51], a[144:147], a[0:3], v[68:71]\n"
    "v_mfma_f32_16x16x32_bf16 v[52:55], a[144:147], a[16:19], v[72:75]\n"
    "v_mfma_f32_16x16x32_bf16 v[56:59], a[144:147], a[32:35], v[76:79]\n"
    "v_mfma_f32_16x16x32_bf16 v[60:63], a[144:147], a[48:51], v[80:83]\n"
    "ds_read_b128 a[156:159], v5 offset:4096\n"
    "v_mfma_f32_16x16x32_bf16 v[64:67], a[144:147], a[64:67], v[84:87]\n"
    "v_exp_f32 v28, v28\n"
    "v_mfma_f32_16x16x32_bf16 v[48:51], a[148:151], a[4:7], v[48:51]\n"
    "v_mfma_f32_16x16x32_bf16 v[52:55], a[148:151], a[20:23], v[52:55]\n"
    "v_exp_f32 v29, v29\n"
    "v_mfma_f32_16x16x32_bf16 v[56:59], a[148:151], a[36:39], v[56:59]\n"
    "v_mfma_f32_16x16x32_bf16 v[60:63], a[148:151], a[52:55], v[60:63]\n"
    "v_exp_f32 v30, v30\n"
    "v_mfma_f32_16x16x32_bf16 v[64:67], a[148:151], a[68:71], v[64:67]\n"
    "s_waitcnt lgkmcnt(0)\n"
    "v_cvt_pk_bf16_f32 v88, v28, v29\n"
    "v_mfma_f32_16x16x32_bf16 v[48:51], a[152:155], a[8:11], v[48:51]\n"
    "v_exp_f32 v31, v31\n"
    "v_mfma_f32_16x16x32_bf16 v[52:55], a[152:155], a[24:27], v[52:55]\n"
    "v_mfma_f32_16x16x32_bf16 v[56:59], a[152:155], a[40:43], v[56:59]\n"
    "v_exp_f32 v32, v32\n"
    "v_mfma_f32_16x16x32_bf16 v[60:63], a[152:155], a[56:59], v[60:63]\n"
    "ds_read_b64_tr_b16 a[160:161], v6 offset:8192\n"
    "v_cvt_pk_bf16_f32 v89, v30, v31\n"
    "v_mfma_f32_16x16x32_bf16 v[64:67], a[152:155], a[72:75], v[64:67]\n"
    "ds_read_b64_tr_b16 a[162:163], v6 offset:12288\n"
    "v_mfma_f32_16x16x32_bf16 v[48:51], a[156:159], a[12:15], v[48:51]\n"
    "v_exp_f32 v33, v33\n"
    "v_mfma_f32_16x16x32_bf16 v[52:55], a[156:159], a[28:31], v[52:55]\n"
    "v_exp_f32 v34, v34\n"
    "v_mfma_f32_16x16x32_bf16 v[56:59], a[156:159], a[44:47], v[56:59]\n"
    "v_cvt_pk_bf16_f32 v92, v32, v33\n"
    "v_mfma_f32_16x16x32_bf16 v[60:63], a[156:159], a[60:63], v[60:63]\n"
    "ds_read_b64_tr_b16 a[164:165], v7 offset:8192\n"
    "ds_read_b64_tr_b16 a[166:167], v7 offset:12288\n"
    "v_mfma_f32_16x16x32_bf16 v[64:67], a[156:159], a[76:79], v[64:67]\n"
    "v_exp_f32 v35, v35\n"
    "v_mfma_f32_16x16x32_bf16 a[80:83], a[132:135], v[108:111], a[80:83]\n"
    "v_exp_f32 v36, v36\n"
    "v_mfma_f32_16x16x32_bf16 a[84:87], a[132:135], v[112:115], a[84:87]\n"
    "v_cvt_pk_bf16_f32 v93, v34, v35\n"
    "v_mfma_f32_16x16x32_bf16 a[88:91], a[132:135], v[116:119], a[88:91]\n"
    "v_exp_f32 v37, v37\n"
    "v_mfma_f32_16x16x32_bf16 a[92:95], a[132:135], v[120:123], a[92:95]\n"
    "ds_read_b64_tr_b16 a[168:169], v6 offset:8704\n"
    "ds_read_b64_tr_b16 a[170:171], v6 offset:12800\n"
    "v_mfma_f32_16x16x32_bf16 a[96:99], a[132:135], v[124:127], a[96:99]\n"
    "s_waitcnt lgkmcnt(2)\n"
    "v_mfma_f32_16x16x32_bf16 v[128:131], a[160:163], v[108:111], v[128:131]\n"
    "v_exp_f32 v38, v38\n"
    "v_mfma_f32_16x16x32_bf16 v[132:135], a[160:163], v[112:115], v[132:135]\n"
    "v_cvt_pk_bf16_f32 v96, v36, v37\n"
    "v_mfma_f32_16x16x32_bf16 v[136:139], a[160:163], v[116:119], v[136:139]\n"
    "v_exp_f32 v39, v39\n"
    "v_mfma_f32_16x16x32_bf16 v[140:143], a[160:163], v[120:123], v[140:143]\n"
    "ds_read_b64_tr_b16 a[172:173], v7 offset:8704\n"
    "ds_read_b64_tr_b16 a[174:175], v7 offset:12800\n"
    "v_mfma_f32_16x16x32_bf16 v[144:147], a[160:163], v[124:127], v[144:147]\n"
    "v_exp_f32 v40, v40\n"
    "v_mfma_f32_16x16x32_bf16 v[148:151], a[164:167], v[108:111], v[148:151]\n"
    "v_cvt_pk_bf16_f32 v97, v38, v39\n"
    "v_mfma_f32_16x16x32_bf16 v[152:155], a[164:167], v[112:115], v[152:155]\n"
    "v_exp_f32 v41, v41\n"
    "v_mfma_f32_16x16x32_bf16 v[156:159], a[164:167], v[116:119], v[156:159]\n"
    "v_mfma_f32_16x16x32_bf16 v[160:163], a[164:167], v[120:123], v[160:163]\n"
    "ds_read_b64_tr_b16 a[176:177], v6 offset:9216\n"
    "ds_read_b64_tr_b16 a[178:179], v6 offset:13312\n"
    "v_mfma_f32_16x16x32_bf16 v[164:167], a[164:167], v[124:127], v[164:167]\n"
    "s_waitcnt lgkmcnt(2)\n"
    "v_exp_f32 v42, v42\n"
    "v_mfma_f32_16x16x32_bf16 v[168:171], a[168:171], v[108:111], v[168:171]\n"
    "v_cvt_pk_bf16_f32 v100, v40, v41\n"
    "v_mfma_f32_16x16x32_bf16 v[172:175], a[168:171], v[112:115], v[172:175]\n"
    "v_exp_f32 v43, v43\n"
    "v_mfma_f32_16x16x32_bf16 v[176:179], a[168:171], v[116:119], v[176:179]\n"
    "v_exp_f32 v44, v44\n"
    "v_mfma_f32_16x16x32_bf16 v[180:183], a[168:171], v[120:123], v[180:183]\n"
    "ds_read_b64_tr_b16 a[180:181], v7 offset:9216\n"
    "v_cvt_pk_bf16_f32 v101, v42, v43\n"
    "v_mfma_f32_16x16x32_bf16 v[184:187], a[168:171], v[124:127], v[184:187]\n"
    "ds_read_b64_tr_b16 a[182:183], v7 offset:13312\n"
    "v_mfma_f32_16x16x32_bf16 v[188:191], a[172:175], v[108:111], v[188:191]\n"
    "v_exp_f32 v45, v45\n"
    "v_mfma_f32_16x16x32_bf16 a[100:103], a[172:175], v[112:115], a[100:103]\n"
    "v_exp_f32 v46, v46\n"
    "v_mfma_f32_16x16x32_bf16 a[104:107], a[172:175], v[116:119], a[104:107]\n"
    "v_cvt_pk_bf16_f32 v104, v44, v45\n"
    "v_mfma_f32_16x16x32_bf16 a[108:111], a[172:175], v[120:123], a[108:111]\n"
    "ds_read_b64_tr_b16 a[184:185], v6 offset:9728\n"
    "ds_read_b64_tr_b16 a[186:187], v6 offset:13824\n"
    "v_add_u32 v6, 0xfffe8000, v6\n"
    "v_mfma_f32_16x16x32_bf16 a[112:115], a[172:175], v[124:127], a[112:115]\n"
    "s_waitcnt lgkmcnt(2)\n"
    "v_exp_f32 v47, v47\n"
    "v_mfma_f32_16x16x32_bf16 a[116:119], a[176:179], v[108:111], a[116:119]\n"
    "ds_read_b64_tr_b16 a[188:189], v7 offset:9728\n"
    "ds_read_b64_tr_b16 a[190:191], v7 offset:13824\n"
    "v_add_u32 v7, 0xfffe8000, v7\n"
    "v_mfma_f32_16x16x32_bf16 a[120:123], a[176:179], v[112:115], a[120:123]\n"
    "v_mfma_f32_16x16x32_bf16 a[124:127], a[176:179], v[116:119], a[124:127]\n"
    "v_cvt_pk_bf16_f32 v105, v46, v47\n"
    "v_mfma_f32_16x16x32_bf16 a[128:131], a[176:179], v[120:123], a[128:131]\n"
    "v_exp_f32 v48, v48\n"
    "v_mfma_f32_16x16x32_bf16 a[192:195], a[176:179], v[124:127], a[192:195]\n"
    "v_exp_f32 v49, v49\n"
    "v_mfma_f32_16x16x32_bf16 a[196:199], a[180:183], v[108:111], a[196:199]\n"
    "v_exp_f32 v50, v50\n"
    "v_mfma_f32_16x16x32_bf16 a[200:203], a[180:183], v[112:115], a[200:203]\n"
    "v_cvt_pk_bf16_f32 v90, v48, v49\n"
    "v_mfma_f32_16x16x32_bf16 a[204:207], a[180:183], v[116:119], a[204:207]\n"
    "v_exp_f32 v51, v51\n"
    "v_mfma_f32_16x16x32_bf16 a[208:211], a[180:183], v[120:123], a[208:211]\n"
    "v_mfma_f32_16x16x32_bf16 a[212:215], a[180:183], v[124:127], a[212:215]\n"
    "s_waitcnt vmcnt(8) lgkmcnt(0)\n"
    "s_barrier\n"
    "v_mfma_f32_16x16x32_bf16 a[216:219], a[184:187], v[108:111], a[216:219]\n"
    "ds_read_b128 a[144:147], v2 offset:8192\n"
    "v_mfma_f32_16x16x32_bf16 a[220:223], a[184:187], v[112:115], a[220:223]\n"
    "v_exp_f32 v52, v52\n"
    "v_mfma_f32_16x16x32_bf16 a[224:227], a[184:187], v[116:119], a[224:227]\n"
    "v_cvt_pk_bf16_f32 v91, v50, v51\n"
    "v_mfma_f32_16x16x32_bf16 a[228:231], a[184:187], v[120:123], a[228:231]\n"
    "ds_read_b128 a[148:151], v3 offset:8192\n"
    "v_mfma_f32_16x16x32_bf16 a[232:235], a[184:187], v[124:127], a[232:235]\n"
    "v_exp_f32 v53, v53\n"
    "v_mfma_f32_16x16x32_bf16 a[236:239], a[188:191], v[108:111], a[236:239]\n"
    "v_exp_f32 v54, v54\n"
    "v_mfma_f32_16x16x32_bf16 a[240:243], a[188:191], v[112:115], a[240:243]\n"
    "v_cvt_pk_bf16_f32 v94, v52, v53\n"
    "v_mfma_f32_16x16x32_bf16 a[244:247], a[188:191], v[116:119], a[244:247]\n"
    "v_exp_f32 v55, v55\n"
    "v_mfma_f32_16x16x32_bf16 a[248:251], a[188:191], v[120:123], a[248:251]\n"
    "ds_read_b128 a[152:155], v4 offset:8192\n"
    "v_mfma_f32_16x16x32_bf16 a[252:255], a[188:191], v[124:127], a[252:255]\n"
    "s_waitcnt lgkmcnt(1)\n"
    "v_exp_f32 v56, v56\n"
    "v_mfma_f32_16x16x32_bf16 v[28:31], a[144:147], a[0:3], v[68:71]\n"
    "v_cvt_pk_bf16_f32 v95, v54, v55\n"
    "v_mfma_f32_16x16x32_bf16 v[32:35], a[144:147], a[16:19], v[72:75]\n"
    "v_exp_f32 v57, v57\n"
    "v_mfma_f32_16x16x32_bf16 v[36:39], a[144:147], a[32:35], v[76:79]\n"
    "v_exp_f32 v58, v58\n"
    "v_mfma_f32_16x16x32_bf16 v[40:43], a[144:147], a[48:51], v[80:83]\n"
    "ds_read_b128 a[156:159], v5 offset:8192\n"
    "v_cvt_pk_bf16_f32 v98, v56, v57\n"
    "v_mfma_f32_16x16x32_bf16 v[44:47], a[144:147], a[64:67], v[84:87]\n"
    "v_exp_f32 v59, v59\n"
    "v_mfma_f32_16x16x32_bf16 v[28:31], a[148:151], a[4:7], v[28:31]\n"
    "v_mfma_f32_16x16x32_bf16 v[32:35], a[148:151], a[20:23], v[32:35]\n"
    "v_exp_f32 v60, v60\n"
    "v_mfma_f32_16x16x32_bf16 v[36:39], a[148:151], a[36:39], v[36:39]\n"
    "v_cvt_pk_bf16_f32 v99, v58, v59\n"
    "v_mfma_f32_16x16x32_bf16 v[40:43], a[148:151], a[52:55], v[40:43]\n"
    "ds_read_b128 a[160:163], v2 offset:12288\n"
    "v_add_u32 v2, 0xfffe8000, v2\n"
    "v_mfma_f32_16x16x32_bf16 v[44:47], a[148:151], a[68:71], v[44:47]\n"
    "s_waitcnt lgkmcnt(1)\n"
    "v_exp_f32 v61, v61\n"
    "v_mfma_f32_16x16x32_bf16 v[28:31], a[152:155], a[8:11], v[28:31]\n"
    "v_exp_f32 v62, v62\n"
    "v_mfma_f32_16x16x32_bf16 v[32:35], a[152:155], a[24:27], v[32:35]\n"
    "v_cvt_pk_bf16_f32 v102, v60, v61\n"
    "v_mfma_f32_16x16x32_bf16 v[36:39], a[152:155], a[40:43], v[36:39]\n"
    "v_exp_f32 v63, v63\n"
    "v_mfma_f32_16x16x32_bf16 v[40:43], a[152:155], a[56:59], v[40:43]\n"
    "ds_read_b128 a[164:167], v3 offset:12288\n"
    "v_add_u32 v3, 0xfffe8000, v3\n"
    "v_mfma_f32_16x16x32_bf16 v[44:47], a[152:155], a[72:75], v[44:47]\n"
    "v_exp_f32 v64, v64\n"
    "v_mfma_f32_16x16x32_bf16 v[28:31], a[156:159], a[12:15], v[28:31]\n"
    "v_cvt_pk_bf16_f32 v103, v62, v63\n"
    "v_mfma_f32_16x16x32_bf16 v[32:35], a[156:159], a[28:31], v[32:35]\n"
    "v_exp_f32 v65, v65\n"
    "v_mfma_f32_16x16x32_bf16 v[36:39], a[156:159], a[44:47], v[36:39]\n"
    "v_exp_f32 v66, v66\n"
    "v_mfma_f32_16x16x32_bf16 v[40:43], a[156:159], a[60:63], v[40:43]\n"
    "ds_read_b128 a[168:171], v4 offset:12288\n"
    "v_cvt_pk_bf16_f32 v106, v64, v65\n"
    "v_add_u32 v4, 0xfffe8000, v4\n"
    "v_mfma_f32_16x16x32_bf16 v[44:47], a[156:159], a[76:79], v[44:47]\n"
    "s_waitcnt lgkmcnt(1)\n"
    "v_exp_f32 v67, v67\n"
    "s_nop 0\n"
    "v_cvt_pk_bf16_f32 v107, v66, v67\n"
    "v_mfma_f32_16x16x32_bf16 v[48:51], a[160:163], a[0:3], v[68:71]\n"
    "v_mfma_f32_16x16x32_bf16 v[52:55], a[160:163], a[16:19], v[72:75]\n"
    "v_mfma_f32_16x16x32_bf16 v[56:59], a[160:163], a[32:35], v[76:79]\n"
    "s_add_u32 m0, s73, 0x10000\n"
    "v_mfma_f32_16x16x32_bf16 v[60:63], a[160:163], a[48:51], v[80:83]\n"
    "s_add_u32 s87, s73, 0xc000\n"
    "ds_read_b128 a[172:175], v5 offset:12288\n"
    "v_add_u32 v5, 0xfffe8000, v5\n"
    "v_mfma_f32_16x16x32_bf16 v[64:67], a[160:163], a[64:67], v[84:87]\n"
    "s_waitcnt vmcnt(6)\n"
    "v_exp_f32 v28, v28\n"
    "v_mfma_f32_16x16x32_bf16 v[48:51], a[164:167], a[4:7], v[48:51]\n"
    "v_mfma_f32_16x16x32_bf16 v[52:55], a[164:167], a[20:23], v[52:55]\n"
    "v_exp_f32 v29, v29\n"
    "v_mfma_f32_16x16x32_bf16 v[56:59], a[164:167], a[36:39], v[56:59]\n"
    "buffer_load_dwordx4 v12, s[48:51], s87 offen nt lds\n"
    "v_mfma_f32_16x16x32_bf16 v[60:63], a[164:167], a[52:55], v[60:63]\n"
    "v_exp_f32 v30, v30\n"
    "v_mfma_f32_16x16x32_bf16 v[64:67], a[164:167], a[68:71], v[64:67]\n"
    "s_waitcnt lgkmcnt(0)\n"
    "v_cvt_pk_bf16_f32 v108, v28, v29\n"
    "v_mfma_f32_16x16x32_bf16 v[48:51], a[168:171], a[8:11], v[48:51]\n"
    "v_exp_f32 v31, v31\n"
    "v_mfma_f32_16x16x32_bf16 v[52:55], a[168:171], a[24:27], v[52:55]\n"
    "v_mfma_f32_16x16x32_bf16 v[56:59], a[168:171], a[40:43], v[56:59]\n"
    "v_exp_f32 v32, v32\n"
    "v_mfma_f32_16x16x32_bf16 v[60:63], a[168:171], a[56:59], v[60:63]\n"
    "s_waitcnt vmcnt(6)\n"
    "ds_read_b64_tr_b16 a[176:177], v6\n"
    "v_cvt_pk_bf16_f32 v109, v30, v31\n"
    "v_mfma_f32_16x16x32_bf16 v[64:67], a[168:171], a[72:75], v[64:67]\n"
    "ds_read_b64_tr_b16 a[178:179], v6 offset:4096\n"
    "v_mfma_f32_16x16x32_bf16 v[48:51], a[172:175], a[12:15], v[48:51]\n"
    "v_exp_f32 v33, v33\n"
    "v_mfma_f32_16x16x32_bf16 v[52:55], a[172:175], a[28:31], v[52:55]\n"
    "v_exp_f32 v34, v34\n"
    "v_mfma_f32_16x16x32_bf16 v[56:59], a[172:175], a[44:47], v[56:59]\n"
    "v_cvt_pk_bf16_f32 v112, v32, v33\n"
    "v_mfma_f32_16x16x32_bf16 v[60:63], a[172:175], a[60:63], v[60:63]\n"
    "ds_read_b64_tr_b16 a[180:181], v7\n"
    "ds_read_b64_tr_b16 a[182:183], v7 offset:4096\n"
    "v_mfma_f32_16x16x32_bf16 v[64:67], a[172:175], a[76:79], v[64:67]\n"
    "buffer_load_dwordx4 v13, s[48:51], s87 offen offset:1024 nt lds\n"
    "v_mfma_f32_16x16x32_bf16 a[80:83], a[132:135], v[88:91], a[80:83]\n"
    "v_exp_f32 v35, v35\n"
    "v_mfma_f32_16x16x32_bf16 a[84:87], a[132:135], v[92:95], a[84:87]\n"
    "v_exp_f32 v36, v36\n"
    "v_mfma_f32_16x16x32_bf16 a[88:91], a[132:135], v[96:99], a[88:91]\n"
    "v_cvt_pk_bf16_f32 v113, v34, v35\n"
    "v_mfma_f32_16x16x32_bf16 a[92:95], a[132:135], v[100:103], a[92:95]\n"
    "ds_read_b64_tr_b16 a[184:185], v6 offset:512\n"
    "ds_read_b64_tr_b16 a[186:187], v6 offset:4608\n"
    "v_mfma_f32_16x16x32_bf16 a[96:99], a[132:135], v[104:107], a[96:99]\n"
    "s_waitcnt lgkmcnt(2)\n"
    "v_exp_f32 v37, v37\n"
    "v_mfma_f32_16x16x32_bf16 v[128:131], a[176:179], v[88:91], v[128:131]\n"
    "s_waitcnt vmcnt(6)\n"
    "v_exp_f32 v38, v38\n"
    "v_mfma_f32_16x16x32_bf16 v[132:135], a[176:179], v[92:95], v[132:135]\n"
    "v_cvt_pk_bf16_f32 v116, v36, v37\n"
    "v_mfma_f32_16x16x32_bf16 v[136:139], a[176:179], v[96:99], v[136:139]\n"
    "v_exp_f32 v39, v39\n"
    "v_mfma_f32_16x16x32_bf16 v[140:143], a[176:179], v[100:103], v[140:143]\n"
    "ds_read_b64_tr_b16 a[188:189], v7 offset:512\n"
    "ds_read_b64_tr_b16 a[190:191], v7 offset:4608\n"
    "v_mfma_f32_16x16x32_bf16 v[144:147], a[176:179], v[104:107], v[144:147]\n"
    "buffer_load_dwordx4 v12, s[48:51], s87 offen offset:2048 nt lds\n"
    "v_mfma_f32_16x16x32_bf16 v[148:151], a[180:183], v[88:91], v[148:151]\n"
    "v_exp_f32 v40, v40\n"
    "v_mfma_f32_16x16x32_bf16 v[152:155], a[180:183], v[92:95], v[152:155]\n"
    "v_cvt_pk_bf16_f32 v117, v38, v39\n"
    "v_mfma_f32_16x16x32_bf16 v[156:159], a[180:183], v[96:99], v[156:159]\n"
    "v_exp_f32 v41, v41\n"
    "v_mfma_f32_16x16x32_bf16 v[160:163], a[180:183], v[100:103], v[160:163]\n"
    "ds_read_b64_tr_b16 a[144:145], v6 offset:1024\n"
    "ds_read_b64_tr_b16 a[146:147], v6 offset:5120\n"
    "v_mfma_f32_16x16x32_bf16 v[164:167], a[180:183], v[104:107], v[164:167]\n"
    "s_waitcnt lgkmcnt(2)\n"
    "v_exp_f32 v42, v42\n"
    "v_mfma_f32_16x16x32_bf16 v[168:171], a[184:187], v[88:91], v[168:171]\n"
    "s_waitcnt vmcnt(6)\n"
    "v_cvt_pk_bf16_f32 v120, v40, v41\n"
    "v_mfma_f32_16x16x32_bf16 v[172:175], a[184:187], v[92:95], v[172:175]\n"
    "v_exp_f32 v43, v43\n"
    "v_mfma_f32_16x16x32_bf16 v[176:179], a[184:187], v[96:99], v[176:179]\n"
    "v_exp_f32 v44, v44\n"
    "v_mfma_f32_16x16x32_bf16 v[180:183], a[184:187], v[100:103], v[180:183]\n"
    "ds_read_b64_tr_b16 a[148:149], v7 offset:1024\n"
    "v_cvt_pk_bf16_f32 v121, v42, v43\n"
    "v_mfma_f32_16x16x32_bf16 v[184:187], a[184:187], v[104:107], v[184:187]\n"
    "ds_read_b64_tr_b16 a[150:151], v7 offset:5120\n"
    "v_mfma_f32_16x16x32_bf16 v[188:191], a[188:191], v[88:91], v[188:191]\n"
    "buffer_load_dwordx4 v13, s[48:51], s87 offen offset:3072 nt lds\n"
    "v_mfma_f32_16x16x32_bf16 a[100:103], a[188:191], v[92:95], a[100:103]\n"
    "v_exp_f32 v45, v45\n"
    "v_mfma_f32_16x16x32_bf16 a[104:107], a[188:191], v[96:99], a[104:107]\n"
    "v_exp_f32 v46, v46\n"
    "v_mfma_f32_16x16x32_bf16 a[108:111], a[188:191], v[100:103], a[108:111]\n"
    "ds_read_b64_tr_b16 a[152:153], v6 offset:1536\n"
    "v_cvt_pk_bf16_f32 v124, v44, v45\n"
    "v_mfma_f32_16x16x32_bf16 a[112:115], a[188:191], v[104:107], a[112:115]\n"
    "s_waitcnt lgkmcnt(1)\n"
    "ds_read_b64_tr_b16 a[154:155], v6 offset:5632\n"
    "ds_read_b64_tr_b16 a[156:157], v7 offset:1536\n"
    "v_mfma_f32_16x16x32_bf16 a[116:119], a[144:147], v[88:91], a[116:119]\n"
    "s_add_u32 m0, s73, 0x14000\n"
    "ds_read_b64_tr_b16 a[158:159], v7 offset:5632\n"
    "v_mfma_f32_16x16x32_bf16 a[120:123], a[144:147], v[92:95], a[120:123]\n"
    "s_add_u32 s87, s73, 0x2c000\n"
    "v_exp_f32 v47, v47\n"
    "v_mfma_f32_16x16x32_bf16 a[124:127], a[144:147], v[96:99], a[124:127]\n"
    "s_waitcnt vmcnt(6)\n"
    "v_cvt_pk_bf16_f32 v125, v46, v47\n"
    "v_mfma_f32_16x16x32_bf16 a[128:131], a[144:147], v[100:103], a[128:131]\n"
    "v_exp_f32 v48, v48\n"
    "v_mfma_f32_16x16x32_bf16 a[192:195], a[144:147], v[104:107], a[192:195]\n"
    "v_exp_f32 v49, v49\n"
    "v_mfma_f32_16x16x32_bf16 a[196:199], a[148:151], v[88:91], a[196:199]\n"
    "buffer_load_dwordx4 v12, s[48:51], s87 offen nt lds\n"
    "v_mfma_f32_16x16x32_bf16 a[200:203], a[148:151], v[92:95], a[200:203]\n"
    "v_exp_f32 v50, v50\n"
    "v_mfma_f32_16x16x32_bf16 a[204:207], a[148:151], v[96:99], a[204:207]\n"
    "v_cvt_pk_bf16_f32 v110, v48, v49\n"
    "v_mfma_f32_16x16x32_bf16 a[208:211], a[148:151], v[100:103], a[208:211]\n"
    "v_exp_f32 v51, v51\n"
    "v_mfma_f32_16x16x32_bf16 a[212:215], a[148:151], v[104:107], a[212:215]\n"
    "s_waitcnt vmcnt(5) lgkmcnt(0)\n"
    "s_barrier\n"
    "v_mfma_f32_16x16x32_bf16 a[216:219], a[152:155], v[88:91], a[216:219]\n"
    "ds_read_b128 a[160:163], v2\n"
    "v_mfma_f32_16x16x32_bf16 a[220:223], a[152:155], v[92:95], a[220:223]\n"
    "s_waitcnt vmcnt(6)\n"
    "v_exp_f32 v52, v52\n"
    "v_mfma_f32_16x16x32_bf16 a[224:227], a[152:155], v[96:99], a[224:227]\n"
    "v_cvt_pk_bf16_f32 v111, v50, v51\n"
    "v_mfma_f32_16x16x32_bf16 a[228:231], a[152:155], v[100:103], a[228:231]\n"
    "ds_read_b128 a[164:167], v3\n"
    "v_mfma_f32_16x16x32_bf16 a[232:235], a[152:155], v[104:107], a[232:235]\n"
    "v_exp_f32 v53, v53\n"
    "v_mfma_f32_16x16x32_bf16 a[236:239], a[156:159], v[88:91], a[236:239]\n"
    "v_exp_f32 v54, v54\n"
    "v_mfma_f32_16x16x32_bf16 a[240:243], a[156:159], v[92:95], a[240:243]\n"
    "buffer_load_dwordx4 v13, s[48:51], s87 offen offset:1024 nt lds\n"
    "v_mfma_f32_16x16x32_bf16 a[244:247], a[156:159], v[96:99], a[244:247]\n"
    "v_cvt_pk_bf16_f32 v114, v52, v53\n"
    "v_mfma_f32_16x16x32_bf16 a[248:251], a[156:159], v[100:103], a[248:251]\n"
    "ds_read_b128 a[168:171], v4\n"
    "v_mfma_f32_16x16x32_bf16 a[252:255], a[156:159], v[104:107], a[252:255]\n"
    "s_waitcnt lgkmcnt(1)\n"
    "v_exp_f32 v55, v55\n"
    "v_mfma_f32_16x16x32_bf16 v[28:31], a[160:163], a[0:3], v[68:71]\n"
    "s_and_b32 s76, s90, 3\n"
    "v_exp_f32 v56, v56\n"
    "v_add_co_u32 v16, vcc, 1, v16\n"
    "s_nop 1\n"
    "v_addc_co_u32 v17, vcc, 0, v17, vcc\n"
    "v_mfma_f32_16x16x32_bf16 v[32:35], a[160:163], a[16:19], v[72:75]\n"
    "s_waitcnt vmcnt(6)\n"
    "v_cvt_pk_bf16_f32 v115, v54, v55\n"
    "v_mfma_f32_16x16x32_bf16 v[36:39], a[160:163], a[32:35], v[76:79]\n"
    "s_and_b32 s77, s90, 12\n"
    "v_exp_f32 v57, v57\n"
    "v_mfma_f32_16x16x32_bf16 v[40:43], a[160:163], a[48:51], v[80:83]\n"
    "s_lshr_b32 s98, s72, 1\n"
    "ds_read_b128 a[172:175], v5\n"
    "v_mfma_f32_16x16x32_bf16 v[44:47], a[160:163], a[64:67], v[84:87]\n"
    "s_and_b32 s99, s72, 1\n"
    "v_exp_f32 v58, v58\n"
    "v_mfma_f32_16x16x32_bf16 v[28:31], a[164:167], a[4:7], v[28:31]\n"
    "s_mul_i32 s74, s76, 0x140\n"
    "v_cvt_pk_bf16_f32 v118, v56, v57\n"
    "v_mfma_f32_16x16x32_bf16 v[32:35], a[164:167], a[20:23], v[32:35]\n"
    "buffer_load_dwordx4 v12, s[48:51], s87 offen offset:2048 nt lds\n"
    "v_mfma_f32_16x16x32_bf16 v[36:39], a[164:167], a[36:39], v[36:39]\n"
    "s_mov_b64 s[60:61], s[28:29]\n"
    "v_exp_f32 v59, v59\n"
    "v_mfma_f32_16x16x32_bf16 v[40:43], a[164:167], a[52:55], v[40:43]\n"
    "s_add_u32 s60, s60, s74\n"
    "s_addc_u32 s61, s61, 0\n"
    "ds_read_b128 a[176:179], v2 offset:4096\n"
    "v_mfma_f32_16x16x32_bf16 v[44:47], a[164:167], a[68:71], v[44:47]\n"
    "s_waitcnt lgkmcnt(1)\n"
    "v_exp_f32 v60, v60\n"
    "v_mfma_f32_16x16x32_bf16 v[28:31], a[168:171], a[8:11], v[28:31]\n"
    "s_sub_u32 s60, s60, 0xc00\n"
    "s_subb_u32 s61, s61, 0\n"
    "v_mfma_f32_16x16x32_bf16 v[32:35], a[168:171], a[24:27], v[32:35]\n"
    "s_sub_u32 s62, 0x500c00, s74\n"
    "v_cvt_pk_bf16_f32 v119, v58, v59\n"
    "v_mfma_f32_16x16x32_bf16 v[36:39], a[168:171], a[40:43], v[36:39]\n"
    "s_waitcnt vmcnt(6)\n"
    "v_exp_f32 v61, v61\n"
    "v_mfma_f32_16x16x32_bf16 v[40:43], a[168:171], a[56:59], v[40:43]\n"
    "s_mov_b32 s63, 0x20000\n"
    "ds_read_b128 a[180:183], v3 offset:4096\n"
    "v_mfma_f32_16x16x32_bf16 v[44:47], a[168:171], a[72:75], v[44:47]\n"
    "s_xor_b32 s74, s90, 0\n"
    "v_exp_f32 v62, v62\n"
    "v_mfma_f32_16x16x32_bf16 v[28:31], a[172:175], a[12:15], v[28:31]\n"
    "s_lshl_b32 s74, s74, 1\n"
    "v_cvt_pk_bf16_f32 v122, v60, v61\n"
    "v_mfma_f32_16x16x32_bf16 v[32:35], a[172:175], a[28:31], v[32:35]\n"
    "s_add_u32 s74, s74, s98\n"
    "v_exp_f32 v63, v63\n"
    "v_mfma_f32_16x16x32_bf16 v[36:39], a[172:175], a[44:47], v[36:39]\n"
    "v_exp_f32 v64, v64\n"
    "v_mfma_f32_16x16x32_bf16 v[40:43], a[172:175], a[60:63], v[40:43]\n"
    "ds_read_b128 a[184:187], v4 offset:4096\n"
    "v_cvt_pk_bf16_f32 v123, v62, v63\n"
    "v_mfma_f32_16x16x32_bf16 v[44:47], a[172:175], a[76:79], v[44:47]\n"
    "s_waitcnt lgkmcnt(1)\n"
    "v_exp_f32 v65, v65\n"
    "v_exp_f32 v66, v66\n"
    "v_cvt_pk_bf16_f32 v126, v64, v65\n"
    "v_exp_f32 v67, v67\n"
    "s_nop 0\n"
    "v_cvt_pk_bf16_f32 v127, v66, v67\n"
    "v_mfma_f32_16x16x32_bf16 v[48:51], a[176:179], a[0:3], v[68:71]\n"
    "buffer_load_dwordx4 v13, s[48:51], s87 offen offset:3072 nt lds\n"
    "v_mfma_f32_16x16x32_bf16 v[52:55], a[176:179], a[16:19], v[72:75]\n"
    "s_mul_i32 s40, s74, 0x28000\n"
    "v_mfma_f32_16x16x32_bf16 v[56:59], a[176:179], a[32:35], v[76:79]\n"
    "s_add_u32 m0, s73, 0x18000\n"
    "v_mfma_f32_16x16x32_bf16 v[60:63], a[176:179], a[48:51], v[80:83]\n"
    "s_add_u32 s86, s73, 0x10000\n"
    "ds_read_b128 a[188:191], v5 offset:4096\n"
    "v_mfma_f32_16x16x32_bf16 v[64:67], a[176:179], a[64:67], v[84:87]\n"
    "s_waitcnt vmcnt(6)\n"
    "v_exp_f32 v28, v28\n"
    "v_mfma_f32_16x16x32_bf16 v[48:51], a[180:183], a[4:7], v[48:51]\n"
    "s_mul_i32 s75, s99, 0x5000\n"
    "v_mfma_f32_16x16x32_bf16 v[52:55], a[180:183], a[20:23], v[52:55]\n"
    "s_add_u32 s40, s40, s75\n"
    "v_exp_f32 v29, v29\n"
    "v_mfma_f32_16x16x32_bf16 v[56:59], a[180:183], a[36:39], v[56:59]\n"
    "buffer_load_dwordx4 v8, s[44:47], s86 offen nt lds\n"
    "v_mfma_f32_16x16x32_bf16 v[60:63], a[180:183], a[52:55], v[60:63]\n"
    "s_mul_i32 s58, s74, 0xa000\n"
    "v_exp_f32 v30, v30\n"
    "v_mfma_f32_16x16x32_bf16 v[64:67], a[180:183], a[68:71], v[64:67]\n"
    "s_waitcnt lgkmcnt(0)\n"
    "v_cvt_pk_bf16_f32 v88, v28, v29\n"
    "v_mfma_f32_16x16x32_bf16 v[48:51], a[184:187], a[8:11], v[48:51]\n"
    "s_mul_i32 s75, s99, 0x1400\n"
    "v_exp_f32 v31, v31\n"
    "v_mfma_f32_16x16x32_bf16 v[52:55], a[184:187], a[24:27], v[52:55]\n"
    "s_add_u32 s58, s58, s75\n"
    "v_mfma_f32_16x16x32_bf16 v[56:59], a[184:187], a[40:43], v[56:59]\n"
    "s_cmp_le_u32 s76, 0\n"
    "s_addc_u32 s74, s77, 0\n"
    "v_mfma_f32_16x16x32_bf16 v[60:63], a[184:187], a[56:59], v[60:63]\n"
    "s_waitcnt vmcnt(6)\n"
    "ds_read_b64_tr_b16 a[144:145], v6 offset:8192\n"
    "ds_read_b64_tr_b16 a[146:147], v6 offset:12288\n"
    "v_mfma_f32_16x16x32_bf16 v[64:67], a[184:187], a[72:75], v[64:67]\n"
    "s_lshl_b32 s74, s74, 1\n"
    "v_exp_f32 v32, v32\n"
    "v_mfma_f32_16x16x32_bf16 v[48:51], a[188:191], a[12:15], v[48:51]\n"
    "s_add_u32 s74, s74, s98\n"
    "v_cvt_pk_bf16_f32 v89, v30, v31\n"
    "v_mfma_f32_16x16x32_bf16 v[52:55], a[188:191], a[28:31], v[52:55]\n"
    "s_mul_i32 s41, s74, 0x28000\n"
    "v_exp_f32 v33, v33\n"
    "v_mfma_f32_16x16x32_bf16 v[56:59], a[188:191], a[44:47], v[56:59]\n"
    "s_mul_i32 s75, s99, 0x5000\n"
    "v_exp_f32 v34, v34\n"
    "v_mfma_f32_16x16x32_bf16 v[60:63], a[188:191], a[60:63], v[60:63]\n"
    "ds_read_b64_tr_b16 a[148:149], v7 offset:8192\n"
    "v_cvt_pk_bf16_f32 v92, v32, v33\n"
    "v_mfma_f32_16x16x32_bf16 v[64:67], a[188:191], a[76:79], v[64:67]\n"
    "ds_read_b64_tr_b16 a[150:151], v7 offset:12288\n"
    "v_mfma_f32_16x16x32_bf16 a[80:83], a[132:135], v[108:111], a[80:83]\n"
    "buffer_load_dwordx4 v9, s[44:47], s86 offen offset:1024 nt lds\n"
    "v_mfma_f32_16x16x32_bf16 a[84:87], a[132:135], v[112:115], a[84:87]\n"
    "s_add_u32 s41, s41, s75\n"
    "v_exp_f32 v35, v35\n"
    "v_mfma_f32_16x16x32_bf16 a[88:91], a[132:135], v[116:119], a[88:91]\n"
    "s_mul_i32 s59, s74, 0xa000\n"
    "v_exp_f32 v36, v36\n"
    "v_mfma_f32_16x16x32_bf16 a[92:95], a[132:135], v[120:123], a[92:95]\n"
    "s_mul_i32 s75, s99, 0x1400\n"
    "ds_read_b64_tr_b16 a[152:153], v6 offset:8704\n"
    "v_cvt_pk_bf16_f32 v93, v34, v35\n"
    "v_mfma_f32_16x16x32_bf16 a[96:99], a[132:135], v[124:127], a[96:99]\n"
    "s_waitcnt lgkmcnt(1)\n"
    "ds_read_b64_tr_b16 a[154:155], v6 offset:12800\n"
    "v_mfma_f32_16x16x32_bf16 v[128:131], a[144:147], v[108:111], v[128:131]\n"
    "s_waitcnt vmcnt(6)\n"
    "v_exp_f32 v37, v37\n"
    "v_mfma_f32_16x16x32_bf16 v[132:135], a[144:147], v[112:115], v[132:135]\n"
    "s_add_u32 s59, s59, s75\n"
    "v_exp_f32 v38, v38\n"
    "v_mfma_f32_16x16x32_bf16 v[136:139], a[144:147], v[116:119], v[136:139]\n"
    "s_cmp_le_u32 s76, 1\n"
    "s_addc_u32 s74, s77, 1\n"
    "v_mfma_f32_16x16x32_bf16 v[140:143], a[144:147], v[120:123], v[140:143]\n"
    "s_lshl_b32 s74, s74, 1\n"
    "ds_read_b64_tr_b16 a[156:157], v7 offset:8704\n"
    "v_cvt_pk_bf16_f32 v96, v36, v37\n"
    "v_mfma_f32_16x16x32_bf16 v[144:147], a[144:147], v[124:127], v[144:147]\n"
    "ds_read_b64_tr_b16 a[158:159], v7 offset:12800\n"
    "v_mfma_f32_16x16x32_bf16 v[148:151], a[148:151], v[108:111], v[148:151]\n"
    "buffer_load_dwordx4 v10, s[44:47], s86 offen offset:2048 nt lds\n"
    "v_mfma_f32_16x16x32_bf16 v[152:155], a[148:151], v[112:115], v[152:155]\n"
    "s_add_u32 s74, s74, s98\n"
    "v_exp_f32 v39, v39\n"
    "v_mfma_f32_16x16x32_bf16 v[156:159], a[148:151], v[116:119], v[156:159]\n"
    "s_mul_i32 s42, s74, 0x28000\n"
    "v_exp_f32 v40, v40\n"
    "v_mfma_f32_16x16x32_bf16 v[160:163], a[148:151], v[120:123], v[160:163]\n"
    "s_mul_i32 s75, s99, 0x5000\n"
    "ds_read_b64_tr_b16 a[160:161], v6 offset:9216\n"
    "v_cvt_pk_bf16_f32 v97, v38, v39\n"
    "v_mfma_f32_16x16x32_bf16 v[164:167], a[148:151], v[124:127], v[164:167]\n"
    "s_waitcnt lgkmcnt(1)\n"
    "ds_read_b64_tr_b16 a[162:163], v6 offset:13312\n"
    "v_mfma_f32_16x16x32_bf16 v[168:171], a[152:155], v[108:111], v[168:171]\n"
    "s_waitcnt vmcnt(6)\n"
    "v_exp_f32 v41, v41\n"
    "v_mfma_f32_16x16x32_bf16 v[172:175], a[152:155], v[112:115], v[172:175]\n"
    "s_add_u32 s42, s42, s75\n"
    "v_exp_f32 v42, v42\n"
    "v_mfma_f32_16x16x32_bf16 v[176:179], a[152:155], v[116:119], v[176:179]\n"
    "s_mul_i32 s78, s74, 0xa000\n"
    "v_cvt_pk_bf16_f32 v100, v40, v41\n"
    "v_mfma_f32_16x16x32_bf16 v[180:183], a[152:155], v[120:123], v[180:183]\n"
    "s_mul_i32 s75, s99, 0x1400\n"
    "ds_read_b64_tr_b16 a[164:165], v7 offset:9216\n"
    "ds_read_b64_tr_b16 a[166:167], v7 offset:13312\n"
    "v_mfma_f32_16x16x32_bf16 v[184:187], a[152:155], v[124:127], v[184:187]\n"
    "v_exp_f32 v43, v43\n"
    "v_mfma_f32_16x16x32_bf16 v[188:191], a[156:159], v[108:111], v[188:191]\n"
    "buffer_load_dwordx4 v11, s[44:47], s86 offen offset:3072 nt lds\n"
    "v_mfma_f32_16x16x32_bf16 a[100:103], a[156:159], v[112:115], a[100:103]\n"
    "s_add_u32 s78, s78, s75\n"
    "v_exp_f32 v44, v44\n"
    "v_mfma_f32_16x16x32_bf16 a[104:107], a[156:159], v[116:119], a[104:107]\n"
    "s_cmp_le_u32 s76, 2\n"
    "s_addc_u32 s74, s77, 2\n"
    "v_mfma_f32_16x16x32_bf16 a[108:111], a[156:159], v[120:123], a[108:111]\n"
    "s_lshl_b32 s74, s74, 1\n"
    "ds_read_b64_tr_b16 a[168:169], v6 offset:9728\n"
    "v_cvt_pk_bf16_f32 v101, v42, v43\n"
    "v_mfma_f32_16x16x32_bf16 a[112:115], a[156:159], v[124:127], a[112:115]\n"
    "s_waitcnt lgkmcnt(1)\n"
    "ds_read_b64_tr_b16 a[170:171], v6 offset:13824\n"
    "ds_read_b64_tr_b16 a[172:173], v7 offset:9728\n"
    "v_add_u32 v6, 0x10000, v6\n"
    "v_mfma_f32_16x16x32_bf16 a[116:119], a[160:163], v[108:111], a[116:119]\n"
    "s_add_u32 m0, s73, 0x1c000\n"
    "ds_read_b64_tr_b16 a[174:175], v7 offset:13824\n"
    "v_add_u32 v7, 0x10000, v7\n"
    "v_mfma_f32_16x16x32_bf16 a[120:123], a[160:163], v[112:115], a[120:123]\n"
    "s_add_u32 s86, s73, 0x30000\n"
    "v_exp_f32 v45, v45\n"
    "v_mfma_f32_16x16x32_bf16 a[124:127], a[160:163], v[116:119], a[124:127]\n"
    "s_waitcnt vmcnt(6)\n"
    "v_exp_f32 v46, v46\n"
    "v_mfma_f32_16x16x32_bf16 a[128:131], a[160:163], v[120:123], a[128:131]\n"
    "s_add_u32 s74, s74, s98\n"
    "v_cvt_pk_bf16_f32 v104, v44, v45\n"
    "v_mfma_f32_16x16x32_bf16 a[192:195], a[160:163], v[124:127], a[192:195]\n"
    "s_mul_i32 s43, s74, 0x28000\n"
    "v_exp_f32 v47, v47\n"
    "v_mfma_f32_16x16x32_bf16 a[196:199], a[164:167], v[108:111], a[196:199]\n"
    "buffer_load_dwordx4 v8, s[44:47], s86 offen nt lds\n"
    "v_mfma_f32_16x16x32_bf16 a[200:203], a[164:167], v[112:115], a[200:203]\n"
    "s_mul_i32 s75, s99, 0x5000\n"
    "v_cvt_pk_bf16_f32 v105, v46, v47\n"
    "v_mfma_f32_16x16x32_bf16 a[204:207], a[164:167], v[116:119], a[204:207]\n"
    "s_add_u32 s43, s43, s75\n"
    "v_exp_f32 v48, v48\n"
    "v_mfma_f32_16x16x32_bf16 a[208:211], a[164:167], v[120:123], a[208:211]\n"
    "s_mul_i32 s79, s74, 0xa000\n"
    "v_exp_f32 v49, v49\n"
    "v_mfma_f32_16x16x32_bf16 a[212:215], a[164:167], v[124:127], a[212:215]\n"
    "s_waitcnt vmcnt(5) lgkmcnt(0)\n"
    "s_barrier\n"
    "v_mfma_f32_16x16x32_bf16 a[216:219], a[168:171], v[108:111], a[216:219]\n"
    "s_mul_i32 s75, s99, 0x1400\n"
    "ds_read_b128 a[176:179], v2 offset:8192\n"
    "v_mfma_f32_16x16x32_bf16 a[220:223], a[168:171], v[112:115], a[220:223]\n"
    "s_waitcnt vmcnt(6)\n"
    "v_exp_f32 v50, v50\n"
    "v_mfma_f32_16x16x32_bf16 a[224:227], a[168:171], v[116:119], a[224:227]\n"
    "s_add_u32 s79, s79, s75\n"
    "v_cvt_pk_bf16_f32 v90, v48, v49\n"
    "v_mfma_f32_16x16x32_bf16 a[228:231], a[168:171], v[120:123], a[228:231]\n"
    "s_mul_i32 s85, s72, 0x1400\n"
    "ds_read_b128 a[180:183], v3 offset:8192\n"
    "v_mfma_f32_16x16x32_bf16 a[232:235], a[168:171], v[124:127], a[232:235]\n"
    "s_mul_i32 s74, s89, 0x32000\n"
    "v_exp_f32 v51, v51\n"
    "v_mfma_f32_16x16x32_bf16 a[236:239], a[172:175], v[108:111], a[236:239]\n"
    "s_mul_i32 s75, s99, 0x19000\n"
    "v_exp_f32 v52, v52\n"
    "v_mfma_f32_16x16x32_bf16 a[240:243], a[172:175], v[112:115], a[240:243]\n"
    "v_cvt_pk_bf16_f32 v91, v50, v51\n"
    "v_mfma_f32_16x16x32_bf16 a[244:247], a[172:175], v[116:119], a[244:247]\n"
    "v_exp_f32 v53, v53\n"
    "v_mfma_f32_16x16x32_bf16 a[248:251], a[172:175], v[120:123], a[248:251]\n"
    "ds_read_b128 a[184:187], v4 offset:8192\n"
    "v_mfma_f32_16x16x32_bf16 a[252:255], a[172:175], v[124:127], a[252:255]\n"
    "s_waitcnt lgkmcnt(1)\n"
    "v_exp_f32 v54, v54\n"
    "v_mfma_f32_16x16x32_bf16 v[28:31], a[176:179], a[0:3], v[68:71]\n"
    "buffer_load_dwordx4 v9, s[44:47], s86 offen offset:1024 nt lds\n"
    "v_mfma_f32_16x16x32_bf16 v[32:35], a[176:179], a[16:19], v[72:75]\n"
    "s_waitcnt vmcnt(6)\n"
    "v_cvt_pk_bf16_f32 v94, v52, v53\n"
    "v_mfma_f32_16x16x32_bf16 v[36:39], a[176:179], a[32:35], v[76:79]\n"
    "s_add_u32 s74, s74, s75\n"
    "v_exp_f32 v55, v55\n"
    "v_mfma_f32_16x16x32_bf16 v[40:43], a[176:179], a[48:51], v[80:83]\n"
    "s_mul_i32 s75, s76, 0x140\n"
    "ds_read_b128 a[188:191], v5 offset:8192\n"
    "v_mfma_f32_16x16x32_bf16 v[44:47], a[176:179], a[64:67], v[84:87]\n"
    "s_add_u32 s74, s74, s75\n"
    "v_exp_f32 v56, v56\n"
    "v_mfma_f32_16x16x32_bf16 v[28:31], a[180:183], a[4:7], v[28:31]\n"
    "s_mul_i32 s75, s98, 0xa0\n"
    "v_cvt_pk_bf16_f32 v95, v54, v55\n"
    "v_mfma_f32_16x16x32_bf16 v[32:35], a[180:183], a[20:23], v[32:35]\n"
    "v_exp_f32 v57, v57\n"
    "v_mfma_f32_16x16x32_bf16 v[36:39], a[180:183], a[36:39], v[36:39]\n"
    "v_exp_f32 v58, v58\n"
    "v_mfma_f32_16x16x32_bf16 v[40:43], a[180:183], a[52:55], v[40:43]\n"
    "ds_read_b128 a[144:147], v2 offset:12288\n"
    "v_cvt_pk_bf16_f32 v98, v56, v57\n"
    "v_add_u32 v2, 0x10000, v2\n"
    "v_mfma_f32_16x16x32_bf16 v[44:47], a[180:183], a[68:71], v[44:47]\n"
    "s_waitcnt lgkmcnt(1)\n"
    "v_exp_f32 v59, v59\n"
    "v_mfma_f32_16x16x32_bf16 v[28:31], a[184:187], a[8:11], v[28:31]\n"
    "buffer_load_dwordx4 v10, s[44:47], s86 offen offset:2048 nt lds\n"
    "v_mfma_f32_16x16x32_bf16 v[32:35], a[184:187], a[24:27], v[32:35]\n"
    "s_add_u32 s74, s74, s75\n"
    "v_exp_f32 v60, v60\n"
    "v_mfma_f32_16x16x32_bf16 v[36:39], a[184:187], a[40:43], v[36:39]\n"
    "s_waitcnt vmcnt(6)\n"
    "v_cvt_pk_bf16_f32 v99, v58, v59\n"
    "v_mfma_f32_16x16x32_bf16 v[40:43], a[184:187], a[56:59], v[40:43]\n"
    "s_mov_b64 s[36:37], s[32:33]\n"
    "ds_read_b128 a[148:151], v3 offset:12288\n"
    "v_add_u32 v3, 0x10000, v3\n"
    "v_mfma_f32_16x16x32_bf16 v[44:47], a[184:187], a[72:75], v[44:47]\n"
    "s_add_u32 s36, s36, s74\n"
    "s_addc_u32 s37, s37, 0\n"
    "v_mfma_f32_16x16x32_bf16 v[28:31], a[188:191], a[12:15], v[28:31]\n"
    "s_sub_u32 s38, 0x320000, s74\n"
    "v_exp_f32 v61, v61\n"
    "v_mfma_f32_16x16x32_bf16 v[32:35], a[188:191], a[28:31], v[32:35]\n"
    "s_mov_b32 s39, 0x20000\n"
    "v_exp_f32 v62, v62\n"
    "v_mfma_f32_16x16x32_bf16 v[36:39], a[188:191], a[44:47], v[36:39]\n"
    "v_cvt_pk_bf16_f32 v102, v60, v61\n"
    "v_mfma_f32_16x16x32_bf16 v[40:43], a[188:191], a[60:63], v[40:43]\n"
    "v_exp_f32 v63, v63\n"
    "v_mfma_f32_16x16x32_bf16 v[44:47], a[188:191], a[76:79], v[44:47]\n"
    "s_waitcnt lgkmcnt(0)\n"
    "v_exp_f32 v64, v64\n"
    "v_cvt_pk_bf16_f32 v103, v62, v63\n"
    "v_exp_f32 v65, v65\n"
    "v_exp_f32 v66, v66\n"
    "v_cvt_pk_bf16_f32 v106, v64, v65\n"
    "v_exp_f32 v67, v67\n"
    "s_nop 0\n"
    "v_cvt_pk_bf16_f32 v107, v66, v67\n"
    "v_mfma_f32_16x16x32_bf16 v[48:51], a[144:147], a[0:3], v[68:71]\n"
    "ds_read_b128 a[152:155], v4 offset:12288\n"
    "v_add_u32 v4, 0x10000, v4\n"
    "v_mfma_f32_16x16x32_bf16 v[52:55], a[144:147], a[16:19], v[72:75]\n"
    "buffer_load_dwordx4 v11, s[44:47], s86 offen offset:3072 nt lds\n"
    "v_mfma_f32_16x16x32_bf16 v[56:59], a[144:147], a[32:35], v[76:79]\n"
    "s_add_u32 m0, s73, 0x20000\n"
    "v_mfma_f32_16x16x32_bf16 v[60:63], a[144:147], a[48:51], v[80:83]\n"
    "s_add_u32 s87, s73, 0x10000\n"
    "ds_read_b128 a[156:159], v5 offset:12288\n"
    "v_add_u32 v5, 0x10000, v5\n"
    "v_mfma_f32_16x16x32_bf16 v[64:67], a[144:147], a[64:67], v[84:87]\n"
    "s_waitcnt vmcnt(6)\n"
    "v_exp_f32 v28, v28\n"
    "v_mfma_f32_16x16x32_bf16 v[48:51], a[148:151], a[4:7], v[48:51]\n"
    "s_lshl_b32 s74, s89, 2\n"
    "v_mfma_f32_16x16x32_bf16 v[52:55], a[148:151], a[20:23], v[52:55]\n"
    "s_add_u32 s74, s74, s76\n"
    "v_exp_f32 v29, v29\n"
    "v_mfma_f32_16x16x32_bf16 v[56:59], a[148:151], a[36:39], v[56:59]\n"
    "buffer_load_dwordx4 v12, s[48:51], s87 offen nt lds\n"
    "v_mfma_f32_16x16x32_bf16 v[60:63], a[148:151], a[52:55], v[60:63]\n"
    "s_lshl_b32 s74, s74, 18\n"
    "v_exp_f32 v30, v30\n"
    "v_mfma_f32_16x16x32_bf16 v[64:67], a[148:151], a[68:71], v[64:67]\n"
    "s_waitcnt lgkmcnt(0)\n"
    "v_cvt_pk_bf16_f32 v108, v28, v29\n"
    "v_mfma_f32_16x16x32_bf16 v[48:51], a[152:155], a[8:11], v[48:51]\n"
    "s_add_u32 s74, s74, 0x1600000\n"
    "v_exp_f32 v31, v31\n"
    "v_mfma_f32_16x16x32_bf16 v[52:55], a[152:155], a[24:27], v[52:55]\n"
    "s_mov_b64 s[64:65], s[30:31]\n"
    "v_mfma_f32_16x16x32_bf16 v[56:59], a[152:155], a[40:43], v[56:59]\n"
    "s_add_u32 s64, s64, s74\n"
    "s_addc_u32 s65, s65, 0\n"
    "v_mfma_f32_16x16x32_bf16 v[60:63], a[152:155], a[56:59], v[60:63]\n"
    "s_waitcnt vmcnt(6)\n"
    "ds_read_b64_tr_b16 a[160:161], v6\n"
    "ds_read_b64_tr_b16 a[162:163], v6 offset:4096\n"
    "v_mfma_f32_16x16x32_bf16 v[64:67], a[152:155], a[72:75], v[64:67]\n"
    "s_mov_b32 s66, 0x40000\n"
    "v_exp_f32 v32, v32\n"
    "v_mfma_f32_16x16x32_bf16 v[48:51], a[156:159], a[12:15], v[48:51]\n"
    "s_mov_b32 s67, 0x20000\n"
    "v_cvt_pk_bf16_f32 v109, v30, v31\n"
    "v_mfma_f32_16x16x32_bf16 v[52:55], a[156:159], a[28:31], v[52:55]\n"
    "s_lshl_b32 s74, 1, s90\n"
    "v_exp_f32 v33, v33\n"
    "v_mfma_f32_16x16x32_bf16 v[56:59], a[156:159], a[44:47], v[56:59]\n"
    "s_lshl_b32 s91, 15, s77\n"
    "v_exp_f32 v34, v34\n"
    "v_mfma_f32_16x16x32_bf16 v[60:63], a[156:159], a[60:63], v[60:63]\n"
    "ds_read_b64_tr_b16 a[164:165], v7\n"
    "v_cvt_pk_bf16_f32 v112, v32, v33\n"
    "v_mfma_f32_16x16x32_bf16 v[64:67], a[156:159], a[76:79], v[64:67]\n"
    "ds_read_b64_tr_b16 a[166:167], v7 offset:4096\n"
    "v_mfma_f32_16x16x32_bf16 a[80:83], a[132:135], v[88:91], a[80:83]\n"
    "buffer_load_dwordx4 v13, s[48:51], s87 offen offset:1024 nt lds\n"
    "v_mfma_f32_16x16x32_bf16 a[84:87], a[132:135], v[92:95], a[84:87]\n"
    "s_andn2_b32 s91, s91, s74\n"
    "v_exp_f32 v35, v35\n"
    "v_mfma_f32_16x16x32_bf16 a[88:91], a[132:135], v[96:99], a[88:91]\n"
    "s_lshl_b32 s92, 0x1111, s76\n"
    "v_exp_f32 v36, v36\n"
    "v_mfma_f32_16x16x32_bf16 a[92:95], a[132:135], v[100:103], a[92:95]\n"
    "s_andn2_b32 s92, s92, s74\n"
    "ds_read_b64_tr_b16 a[168:169], v6 offset:512\n"
    "v_cvt_pk_bf16_f32 v113, v34, v35\n"
    "v_mfma_f32_16x16x32_bf16 a[96:99], a[132:135], v[104:107], a[96:99]\n"
    "s_waitcnt lgkmcnt(1)\n"
    "ds_read_b64_tr_b16 a[170:171], v6 offset:4608\n"
    "v_mfma_f32_16x16x32_bf16 v[128:131], a[160:163], v[88:91], v[128:131]\n"
    "s_waitcnt vmcnt(6)\n"
    "v_exp_f32 v37, v37\n"
    "v_mfma_f32_16x16x32_bf16 v[132:135], a[160:163], v[92:95], v[132:135]\n"
    "s_lshl_b32 s96, %[blk], 10\n"
    "v_exp_f32 v38, v38\n"
    "v_mfma_f32_16x16x32_bf16 v[136:139], a[160:163], v[96:99], v[136:139]\n"
    "v_cvt_pk_bf16_f32 v116, v36, v37\n"
    "v_mfma_f32_16x16x32_bf16 v[140:143], a[160:163], v[100:103], v[140:143]\n"
    "ds_read_b64_tr_b16 a[172:173], v7 offset:512\n"
    "ds_read_b64_tr_b16 a[174:175], v7 offset:4608\n"
    "v_mfma_f32_16x16x32_bf16 v[144:147], a[160:163], v[104:107], v[144:147]\n"
    "buffer_load_dwordx4 v12, s[48:51], s87 offen offset:2048 nt lds\n"
    "v_mfma_f32_16x16x32_bf16 v[148:151], a[164:167], v[88:91], v[148:151]\n"
    "v_exp_f32 v39, v39\n"
    "v_mfma_f32_16x16x32_bf16 v[152:155], a[164:167], v[92:95], v[152:155]\n"
    "v_exp_f32 v40, v40\n"
    "v_mfma_f32_16x16x32_bf16 v[156:159], a[164:167], v[96:99], v[156:159]\n"
    "v_cvt_pk_bf16_f32 v117, v38, v39\n"
    "v_mfma_f32_16x16x32_bf16 v[160:163], a[164:167], v[100:103], v[160:163]\n"
    "ds_read_b64_tr_b16 a[176:177], v6 offset:1024\n"
    "ds_read_b64_tr_b16 a[178:179], v6 offset:5120\n"
    "v_mfma_f32_16x16x32_bf16 v[164:167], a[164:167], v[104:107], v[164:167]\n"
    "s_waitcnt lgkmcnt(2)\n"
    "v_exp_f32 v41, v41\n"
    "v_mfma_f32_16x16x32_bf16 v[168:171], a[168:171], v[88:91], v[168:171]\n"
    "s_waitcnt vmcnt(6)\n"
    "v_exp_f32 v42, v42\n"
    "v_mfma_f32_16x16x32_bf16 v[172:175], a[168:171], v[92:95], v[172:175]\n"
    "v_cvt_pk_bf16_f32 v120, v40, v41\n"
    "v_mfma_f32_16x16x32_bf16 v[176:179], a[168:171], v[96:99], v[176:179]\n"
    "v_exp_f32 v43, v43\n"
    "v_mfma_f32_16x16x32_bf16 v[180:183], a[168:171], v[100:103], v[180:183]\n"
    "ds_read_b64_tr_b16 a[180:181], v7 offset:1024\n"
    "ds_read_b64_tr_b16 a[182:183], v7 offset:5120\n"
    "v_mfma_f32_16x16x32_bf16 v[184:187], a[168:171], v[104:107], v[184:187]\n"
    "buffer_load_dwordx4 v13, s[48:51], s87 offen offset:3072 nt lds\n"
    "v_mfma_f32_16x16x32_bf16 v[188:191], a[172:175], v[88:91], v[188:191]\n"
    "v_exp_f32 v44, v44\n"
    "v_mfma_f32_16x16x32_bf16 a[100:103], a[172:175], v[92:95], a[100:103]\n"
    "v_cvt_pk_bf16_f32 v121, v42, v43\n"
    "v_mfma_f32_16x16x32_bf16 a[104:107], a[172:175], v[96:99], a[104:107]\n"
    "v_exp_f32 v45, v45\n"
    "v_mfma_f32_16x16x32_bf16 a[108:111], a[172:175], v[100:103], a[108:111]\n"
    "ds_read_b64_tr_b16 a[184:185], v6 offset:1536\n"
    "ds_read_b64_tr_b16 a[186:187], v6 offset:5632\n"
    "v_mfma_f32_16x16x32_bf16 a[112:115], a[172:175], v[104:107], a[112:115]\n"
    "s_waitcnt lgkmcnt(2)\n"
    "v_exp_f32 v46, v46\n"
    "v_mfma_f32_16x16x32_bf16 a[116:119], a[176:179], v[88:91], a[116:119]\n"
    "s_add_u32 m0, s73, 0x24000\n"
    "ds_read_b64_tr_b16 a[188:189], v7 offset:1536\n"
    "v_cvt_pk_bf16_f32 v124, v44, v45\n"
    "v_mfma_f32_16x16x32_bf16 a[120:123], a[176:179], v[92:95], a[120:123]\n"
    "s_add_u32 s87, s73, 0x30000\n"
    "ds_read_b64_tr_b16 a[190:191], v7 offset:5632\n"
    "v_mfma_f32_16x16x32_bf16 a[124:127], a[176:179], v[96:99], a[124:127]\n"
    "s_waitcnt vmcnt(6)\n"
    "v_exp_f32 v47, v47\n"
    "v_mfma_f32_16x16x32_bf16 a[128:131], a[176:179], v[100:103], a[128:131]\n"
    "v_cvt_pk_bf16_f32 v125, v46, v47\n"
    "v_mfma_f32_16x16x32_bf16 a[192:195], a[176:179], v[104:107], a[192:195]\n"
    "v_exp_f32 v48, v48\n"
    "v_mfma_f32_16x16x32_bf16 a[196:199], a[180:183], v[88:91], a[196:199]\n"
    "buffer_load_dwordx4 v12, s[48:51], s87 offen nt lds\n"
    "v_mfma_f32_16x16x32_bf16 a[200:203], a[180:183], v[92:95], a[200:203]\n"
    "v_exp_f32 v49, v49\n"
    "v_mfma_f32_16x16x32_bf16 a[204:207], a[180:183], v[96:99], a[204:207]\n"
    "v_exp_f32 v50, v50\n"
    "v_mfma_f32_16x16x32_bf16 a[208:211], a[180:183], v[100:103], a[208:211]\n"
    "v_cvt_pk_bf16_f32 v110, v48, v49\n"
    "v_mfma_f32_16x16x32_bf16 a[212:215], a[180:183], v[104:107], a[212:215]\n"
    "s_waitcnt vmcnt(5) lgkmcnt(0)\n"
    "s_barrier\n"
    "v_mfma_f32_16x16x32_bf16 a[216:219], a[184:187], v[88:91], a[216:219]\n"
    "ds_read_b128 a[144:147], v2\n"
    "v_mfma_f32_16x16x32_bf16 a[220:223], a[184:187], v[92:95], a[220:223]\n"
    "s_waitcnt vmcnt(6)\n"
    "v_exp_f32 v51, v51\n"
    "v_mfma_f32_16x16x32_bf16 a[224:227], a[184:187], v[96:99], a[224:227]\n"
    "v_exp_f32 v52, v52\n"
    "v_mfma_f32_16x16x32_bf16 a[228:231], a[184:187], v[100:103], a[228:231]\n"
    "ds_read_b128 a[148:151], v3\n"
    "v_cvt_pk_bf16_f32 v111, v50, v51\n"
    "v_mfma_f32_16x16x32_bf16 a[232:235], a[184:187], v[104:107], a[232:235]\n"
    "v_exp_f32 v53, v53\n"
    "v_mfma_f32_16x16x32_bf16 a[236:239], a[188:191], v[88:91], a[236:239]\n"
    "v_exp_f32 v54, v54\n"
    "v_mfma_f32_16x16x32_bf16 a[240:243], a[188:191], v[92:95], a[240:243]\n"
    "buffer_load_dwordx4 v13, s[48:51], s87 offen offset:1024 nt lds\n"
    "v_mfma_f32_16x16x32_bf16 a[244:247], a[188:191], v[96:99], a[244:247]\n"
    "v_cvt_pk_bf16_f32 v114, v52, v53\n"
    "v_mfma_f32_16x16x32_bf16 a[248:251], a[188:191], v[100:103], a[248:251]\n"
    "ds_read_b128 a[152:155], v4\n"
    "v_mfma_f32_16x16x32_bf16 a[252:255], a[188:191], v[104:107], a[252:255]\n"
    "s_waitcnt lgkmcnt(1)\n"
    "v_exp_f32 v55, v55\n"
    "v_mfma_f32_16x16x32_bf16 v[28:31], a[144:147], a[0:3], v[68:71]\n"
    "v_exp_f32 v56, v56\n"
    "v_mfma_f32_16x16x32_bf16 v[32:35], a[144:147], a[16:19], v[72:75]\n"
    "s_waitcnt vmcnt(6)\n"
    "v_cvt_pk_bf16_f32 v115, v54, v55\n"
    "v_mfma_f32_16x16x32_bf16 v[36:39], a[144:147], a[32:35], v[76:79]\n"
    "v_exp_f32 v57, v57\n"
    "v_mfma_f32_16x16x32_bf16 v[40:43], a[144:147], a[48:51], v[80:83]\n"
    "ds_read_b128 a[156:159], v5\n"
    "v_mfma_f32_16x16x32_bf16 v[44:47], a[144:147], a[64:67], v[84:87]\n"
    "v_exp_f32 v58, v58\n"
    "v_mfma_f32_16x16x32_bf16 v[28:31], a[148:151], a[4:7], v[28:31]\n"
    "v_cvt_pk_bf16_f32 v118, v56, v57\n"
    "v_mfma_f32_16x16x32_bf16 v[32:35], a[148:151], a[20:23], v[32:35]\n"
    "buffer_load_dwordx4 v12, s[48:51], s87 offen offset:2048 nt lds\n"
    "v_mfma_f32_16x16x32_bf16 v[36:39], a[148:151], a[36:39], v[36:39]\n"
    "v_exp_f32 v59, v59\n"
    "v_mfma_f32_16x16x32_bf16 v[40:43], a[148:151], a[52:55], v[40:43]\n"
    "ds_read_b128 a[160:163], v2 offset:4096\n"
    "v_mfma_f32_16x16x32_bf16 v[44:47], a[148:151], a[68:71], v[44:47]\n"
    "s_waitcnt lgkmcnt(1)\n"
    "v_exp_f32 v60, v60\n"
    "v_mfma_f32_16x16x32_bf16 v[28:31], a[152:155], a[8:11], v[28:31]\n"
    "v_cvt_pk_bf16_f32 v119, v58, v59\n"
    "v_mfma_f32_16x16x32_bf16 v[32:35], a[152:155], a[24:27], v[32:35]\n"
    "v_exp_f32 v61, v61\n"
    "v_mfma_f32_16x16x32_bf16 v[36:39], a[152:155], a[40:43], v[36:39]\n"
    "s_waitcnt vmcnt(6)\n"
    "v_exp_f32 v62, v62\n"
    "v_mfma_f32_16x16x32_bf16 v[40:43], a[152:155], a[56:59], v[40:43]\n"
    "ds_read_b128 a[164:167], v3 offset:4096\n"
    "v_cvt_pk_bf16_f32 v122, v60, v61\n"
    "v_mfma_f32_16x16x32_bf16 v[44:47], a[152:155], a[72:75], v[44:47]\n"
    "v_exp_f32 v63, v63\n"
    "v_mfma_f32_16x16x32_bf16 v[28:31], a[156:159], a[12:15], v[28:31]\n"
    "v_exp_f32 v64, v64\n"
    "v_mfma_f32_16x16x32_bf16 v[32:35], a[156:159], a[28:31], v[32:35]\n"
    "v_cvt_pk_bf16_f32 v123, v62, v63\n"
    "v_mfma_f32_16x16x32_bf16 v[36:39], a[156:159], a[44:47], v[36:39]\n"
    "v_exp_f32 v65, v65\n"
    "v_mfma_f32_16x16x32_bf16 v[40:43], a[156:159], a[60:63], v[40:43]\n"
    "v_exp_f32 v66, v66\n"
    "v_mfma_f32_16x16x32_bf16 v[44:47], a[156:159], a[76:79], v[44:47]\n"
    "s_waitcnt lgkmcnt(0)\n"
    "ds_read_b128 a[168:171], v4 offset:4096\n"
    "v_cvt_pk_bf16_f32 v126, v64, v65\n"
    "v_exp_f32 v67, v67\n"
    "s_nop 0\n"
    "v_cvt_pk_bf16_f32 v127, v66, v67\n"
    "v_mfma_f32_16x16x32_bf16 v[48:51], a[160:163], a[0:3], v[68:71]\n"
    "buffer_load_dwordx4 v13, s[48:51], s87 offen offset:3072 nt lds\n"
    "v_mfma_f32_16x16x32_bf16 v[52:55], a[160:163], a[16:19], v[72:75]\n"
    "v_mfma_f32_16x16x32_bf16 v[56:59], a[160:163], a[32:35], v[76:79]\n"
    "s_add_u32 m0, s73, 0\n"
    "v_mfma_f32_16x16x32_bf16 v[60:63], a[160:163], a[48:51], v[80:83]\n"
    "s_add_u32 s86, s73, 0x14000\n"
    "ds_read_b128 a[172:175], v5 offset:4096\n"
    "v_mfma_f32_16x16x32_bf16 v[64:67], a[160:163], a[64:67], v[84:87]\n"
    "s_waitcnt vmcnt(6)\n"
    "v_exp_f32 v28, v28\n"
    "v_mfma_f32_16x16x32_bf16 v[48:51], a[164:167], a[4:7], v[48:51]\n"
    "v_mfma_f32_16x16x32_bf16 v[52:55], a[164:167], a[20:23], v[52:55]\n"
    "v_exp_f32 v29, v29\n"
    "v_mfma_f32_16x16x32_bf16 v[56:59], a[164:167], a[36:39], v[56:59]\n"
    "buffer_load_dwordx4 v8, s[44:47], s86 offen nt lds\n"
    "v_mfma_f32_16x16x32_bf16 v[60:63], a[164:167], a[52:55], v[60:63]\n"
    "v_exp_f32 v30, v30\n"
    "v_mfma_f32_16x16x32_bf16 v[64:67], a[164:167], a[68:71], v[64:67]\n"
    "s_waitcnt lgkmcnt(0)\n"
    "v_cvt_pk_bf16_f32 v88, v28, v29\n"
    "v_mfma_f32_16x16x32_bf16 v[48:51], a[168:171], a[8:11], v[48:51]\n"
    "v_exp_f32 v31, v31\n"
    "v_mfma_f32_16x16x32_bf16 v[52:55], a[168:171], a[24:27], v[52:55]\n"
    "v_mfma_f32_16x16x32_bf16 v[56:59], a[168:171], a[40:43], v[56:59]\n"
    "v_exp_f32 v32, v32\n"
    "v_mfma_f32_16x16x32_bf16 v[60:63], a[168:171], a[56:59], v[60:63]\n"
    "s_waitcnt vmcnt(6)\n"
    "ds_read_b64_tr_b16 a[176:177], v6 offset:8192\n"
    "v_cvt_pk_bf16_f32 v89, v30, v31\n"
    "v_mfma_f32_16x16x32_bf16 v[64:67], a[168:171], a[72:75], v[64:67]\n"
    "ds_read_b64_tr_b16 a[178:179], v6 offset:12288\n"
    "v_mfma_f32_16x16x32_bf16 v[48:51], a[172:175], a[12:15], v[48:51]\n"
    "v_exp_f32 v33, v33\n"
    "v_mfma_f32_16x16x32_bf16 v[52:55], a[172:175], a[28:31], v[52:55]\n"
    "v_exp_f32 v34, v34\n"
    "v_mfma_f32_16x16x32_bf16 v[56:59], a[172:175], a[44:47], v[56:59]\n"
    "v_cvt_pk_bf16_f32 v92, v32, v33\n"
    "v_mfma_f32_16x16x32_bf16 v[60:63], a[172:175], a[60:63], v[60:63]\n"
    "ds_read_b64_tr_b16 a[180:181], v7 offset:8192\n"
    "ds_read_b64_tr_b16 a[182:183], v7 offset:12288\n"
    "v_mfma_f32_16x16x32_bf16 v[64:67], a[172:175], a[76:79], v[64:67]\n"
    "buffer_load_dwordx4 v9, s[44:47], s86 offen offset:1024 nt lds\n"
    "v_mfma_f32_16x16x32_bf16 a[80:83], a[132:135], v[108:111], a[80:83]\n"
    "v_exp_f32 v35, v35\n"
    "v_mfma_f32_16x16x32_bf16 a[84:87], a[132:135], v[112:115], a[84:87]\n"
    "v_exp_f32 v36, v36\n"
    "v_mfma_f32_16x16x32_bf16 a[88:91], a[132:135], v[116:119], a[88:91]\n"
    "v_cvt_pk_bf16_f32 v93, v34, v35\n"
    "v_mfma_f32_16x16x32_bf16 a[92:95], a[132:135], v[120:123], a[92:95]\n"
    "ds_read_b64_tr_b16 a[184:185], v6 offset:8704\n"
    "ds_read_b64_tr_b16 a[186:187], v6 offset:12800\n"
    "v_mfma_f32_16x16x32_bf16 a[96:99], a[132:135], v[124:127], a[96:99]\n"
    "s_waitcnt lgkmcnt(2)\n"
    "v_exp_f32 v37, v37\n"
    "v_mfma_f32_16x16x32_bf16 v[128:131], a[176:179], v[108:111], v[128:131]\n"
    "s_waitcnt vmcnt(6)\n"
    "v_exp_f32 v38, v38\n"
    "v_mfma_f32_16x16x32_bf16 v[132:135], a[176:179], v[112:115], v[132:135]\n"
    "v_cvt_pk_bf16_f32 v96, v36, v37\n"
    "v_mfma_f32_16x16x32_bf16 v[136:139], a[176:179], v[116:119], v[136:139]\n"
    "v_exp_f32 v39, v39\n"
    "v_mfma_f32_16x16x32_bf16 v[140:143], a[176:179], v[120:123], v[140:143]\n"
    "ds_read_b64_tr_b16 a[188:189], v7 offset:8704\n"
    "ds_read_b64_tr_b16 a[190:191], v7 offset:12800\n"
    "v_mfma_f32_16x16x32_bf16 v[144:147], a[176:179], v[124:127], v[144:147]\n"
    "buffer_load_dwordx4 v10, s[44:47], s86 offen offset:2048 nt lds\n"
    "v_mfma_f32_16x16x32_bf16 v[148:151], a[180:183], v[108:111], v[148:151]\n"
    "v_exp_f32 v40, v40\n"
    "v_mfma_f32_16x16x32_bf16 v[152:155], a[180:183], v[112:115], v[152:155]\n"
    "v_cvt_pk_bf16_f32 v97, v38, v39\n"
    "v_mfma_f32_16x16x32_bf16 v[156:159], a[180:183], v[116:119], v[156:159]\n"
    "v_exp_f32 v41, v41\n"
    "v_mfma_f32_16x16x32_bf16 v[160:163], a[180:183], v[120:123], v[160:163]\n"
    "ds_read_b64_tr_b16 a[144:145], v6 offset:9216\n"
    "ds_read_b64_tr_b16 a[146:147], v6 offset:13312\n"
    "v_mfma_f32_16x16x32_bf16 v[164:167], a[180:183], v[124:127], v[164:167]\n"
    "s_waitcnt lgkmcnt(2)\n"
    "v_exp_f32 v42, v42\n"
    "v_mfma_f32_16x16x32_bf16 v[168:171], a[184:187], v[108:111], v[168:171]\n"
    "s_waitcnt vmcnt(6)\n"
    "v_cvt_pk_bf16_f32 v100, v40, v41\n"
    "v_mfma_f32_16x16x32_bf16 v[172:175], a[184:187], v[112:115], v[172:175]\n"
    "v_exp_f32 v43, v43\n"
    "v_mfma_f32_16x16x32_bf16 v[176:179], a[184:187], v[116:119], v[176:179]\n"
    "v_exp_f32 v44, v44\n"
    "v_mfma_f32_16x16x32_bf16 v[180:183], a[184:187], v[120:123], v[180:183]\n"
    "ds_read_b64_tr_b16 a[148:149], v7 offset:9216\n"
    "v_cvt_pk_bf16_f32 v101, v42, v43\n"
    "v_mfma_f32_16x16x32_bf16 v[184:187], a[184:187], v[124:127], v[184:187]\n"
    "ds_read_b64_tr_b16 a[150:151], v7 offset:13312\n"
    "v_mfma_f32_16x16x32_bf16 v[188:191], a[188:191], v[108:111], v[188:191]\n"
    "buffer_load_dwordx4 v11, s[44:47], s86 offen offset:3072 nt lds\n"
    "v_mfma_f32_16x16x32_bf16 a[100:103], a[188:191], v[112:115], a[100:103]\n"
    "v_exp_f32 v45, v45\n"
    "v_mfma_f32_16x16x32_bf16 a[104:107], a[188:191], v[116:119], a[104:107]\n"
    "v_exp_f32 v46, v46\n"
    "v_mfma_f32_16x16x32_bf16 a[108:111], a[188:191], v[120:123], a[108:111]\n"
    "ds_read_b64_tr_b16 a[152:153], v6 offset:9728\n"
    "v_cvt_pk_bf16_f32 v104, v44, v45\n"
    "v_mfma_f32_16x16x32_bf16 a[112:115], a[188:191], v[124:127], a[112:115]\n"
    "s_waitcnt lgkmcnt(1)\n"
    "ds_read_b64_tr_b16 a[154:155], v6 offset:13824\n"
    "ds_read_b64_tr_b16 a[156:157], v7 offset:9728\n"
    "v_add_u32 v6, 0x10000, v6\n"
    "v_mfma_f32_16x16x32_bf16 a[116:119], a[144:147], v[108:111], a[116:119]\n"
    "s_add_u32 m0, s73, 0x4000\n"
    "ds_read_b64_tr_b16 a[158:159], v7 offset:13824\n"
    "v_add_u32 v7, 0x10000, v7\n"
    "v_mfma_f32_16x16x32_bf16 a[120:123], a[144:147], v[112:115], a[120:123]\n"
    "s_add_u32 s86, s73, 0x34000\n"
    "v_exp_f32 v47, v47\n"
    "v_mfma_f32_16x16x32_bf16 a[124:127], a[144:147], v[116:119], a[124:127]\n"
    "s_waitcnt vmcnt(6)\n"
    "v_cvt_pk_bf16_f32 v105, v46, v47\n"
    "v_mfma_f32_16x16x32_bf16 a[128:131], a[144:147], v[120:123], a[128:131]\n"
    "v_exp_f32 v48, v48\n"
    "v_mfma_f32_16x16x32_bf16 a[192:195], a[144:147], v[124:127], a[192:195]\n"
    "v_exp_f32 v49, v49\n"
    "v_mfma_f32_16x16x32_bf16 a[196:199], a[148:151], v[108:111], a[196:199]\n"
    "buffer_load_dwordx4 v8, s[44:47], s86 offen nt lds\n"
    "v_mfma_f32_16x16x32_bf16 a[200:203], a[148:151], v[112:115], a[200:203]\n"
    "v_exp_f32 v50, v50\n"
    "v_mfma_f32_16x16x32_bf16 a[204:207], a[148:151], v[116:119], a[204:207]\n"
    "v_cvt_pk_bf16_f32 v90, v48, v49\n"
    "v_mfma_f32_16x16x32_bf16 a[208:211], a[148:151], v[120:123], a[208:211]\n"
    "v_exp_f32 v51, v51\n"
    "v_mfma_f32_16x16x32_bf16 a[212:215], a[148:151], v[124:127], a[212:215]\n"
    "s_waitcnt vmcnt(5) lgkmcnt(0)\n"
    "s_barrier\n"
    "v_mfma_f32_16x16x32_bf16 a[216:219], a[152:155], v[108:111], a[216:219]\n"
    "ds_read_b128 a[160:163], v2 offset:8192\n"
    "v_mfma_f32_16x16x32_bf16 a[220:223], a[152:155], v[112:115], a[220:223]\n"
    "s_waitcnt vmcnt(6)\n"
    "v_exp_f32 v52, v52\n"
    "v_mfma_f32_16x16x32_bf16 a[224:227], a[152:155], v[116:119], a[224:227]\n"
    "v_cvt_pk_bf16_f32 v91, v50, v51\n"
    "v_mfma_f32_16x16x32_bf16 a[228:231], a[152:155], v[120:123], a[228:231]\n"
    "ds_read_b128 a[164:167], v3 offset:8192\n"
    "v_mfma_f32_16x16x32_bf16 a[232:235], a[152:155], v[124:127], a[232:235]\n"
    "v_exp_f32 v53, v53\n"
    "v_mfma_f32_16x16x32_bf16 a[236:239], a[156:159], v[108:111], a[236:239]\n"
    "v_exp_f32 v54, v54\n"
    "v_mfma_f32_16x16x32_bf16 a[240:243], a[156:159], v[112:115], a[240:243]\n"
    "buffer_load_dwordx4 v9, s[44:47], s86 offen offset:1024 nt lds\n"
    "v_mfma_f32_16x16x32_bf16 a[244:247], a[156:159], v[116:119], a[244:247]\n"
    "v_cvt_pk_bf16_f32 v94, v52, v53\n"
    "v_mfma_f32_16x16x32_bf16 a[248:251], a[156:159], v[120:123], a[248:251]\n"
    "ds_read_b128 a[168:171], v4 offset:8192\n"
    "v_mfma_f32_16x16x32_bf16 a[252:255], a[156:159], v[124:127], a[252:255]\n"
    "s_waitcnt lgkmcnt(1)\n"
    "v_exp_f32 v55, v55\n"
    "v_mfma_f32_16x16x32_bf16 v[28:31], a[160:163], a[0:3], v[68:71]\n"
    "v_exp_f32 v56, v56\n"
    "v_mfma_f32_16x16x32_bf16 v[32:35], a[160:163], a[16:19], v[72:75]\n"
    "s_waitcnt vmcnt(6)\n"
    "v_cvt_pk_bf16_f32 v95, v54, v55\n"
    "v_mfma_f32_16x16x32_bf16 v[36:39], a[160:163], a[32:35], v[76:79]\n"
    "v_exp_f32 v57, v57\n"
    "v_mfma_f32_16x16x32_bf16 v[40:43], a[160:163], a[48:51], v[80:83]\n"
    "ds_read_b128 a[172:175], v5 offset:8192\n"
    "v_mfma_f32_16x16x32_bf16 v[44:47], a[160:163], a[64:67], v[84:87]\n"
    "v_exp_f32 v58, v58\n"
    "v_mfma_f32_16x16x32_bf16 v[28:31], a[164:167], a[4:7], v[28:31]\n"
    "v_cvt_pk_bf16_f32 v98, v56, v57\n"
    "v_mfma_f32_16x16x32_bf16 v[32:35], a[164:167], a[20:23], v[32:35]\n"
    "buffer_load_dwordx4 v10, s[44:47], s86 offen offset:2048 nt lds\n"
    "v_mfma_f32_16x16x32_bf16 v[36:39], a[164:167], a[36:39], v[36:39]\n"
    "v_exp_f32 v59, v59\n"
    "v_mfma_f32_16x16x32_bf16 v[40:43], a[164:167], a[52:55], v[40:43]\n"
    "ds_read_b128 a[176:179], v2 offset:12288\n"
    "v_add_u32 v2, 0xfffe8000, v2\n"
    "v_mfma_f32_16x16x32_bf16 v[44:47], a[164:167], a[68:71], v[44:47]\n"
    "s_waitcnt lgkmcnt(1)\n"
    "v_exp_f32 v60, v60\n"
    "v_mfma_f32_16x16x32_bf16 v[28:31], a[168:171], a[8:11], v[28:31]\n"
    "v_cvt_pk_bf16_f32 v99, v58, v59\n"
    "v_mfma_f32_16x16x32_bf16 v[32:35], a[168:171], a[24:27], v[32:35]\n"
    "v_exp_f32 v61, v61\n"
    "v_mfma_f32_16x16x32_bf16 v[36:39], a[168:171], a[40:43], v[36:39]\n"
    "s_waitcnt vmcnt(6)\n"
    "v_exp_f32 v62, v62\n"
    "v_mfma_f32_16x16x32_bf16 v[40:43], a[168:171], a[56:59], v[40:43]\n"
    "ds_read_b128 a[180:183], v3 offset:12288\n"
    "v_cvt_pk_bf16_f32 v102, v60, v61\n"
    "v_add_u32 v3, 0xfffe8000, v3\n"
    "v_mfma_f32_16x16x32_bf16 v[44:47], a[168:171], a[72:75], v[44:47]\n"
    "v_exp_f32 v63, v63\n"
    "v_mfma_f32_16x16x32_bf16 v[28:31], a[172:175], a[12:15], v[28:31]\n"
    "v_exp_f32 v64, v64\n"
    "v_mfma_f32_16x16x32_bf16 v[32:35], a[172:175], a[28:31], v[32:35]\n"
    "v_cvt_pk_bf16_f32 v103, v62, v63\n"
    "v_mfma_f32_16x16x32_bf16 v[36:39], a[172:175], a[44:47], v[36:39]\n"
    "v_exp_f32 v65, v65\n"
    "v_mfma_f32_16x16x32_bf16 v[40:43], a[172:175], a[60:63], v[40:43]\n"
    "v_exp_f32 v66, v66\n"
    "v_mfma_f32_16x16x32_bf16 v[44:47], a[172:175], a[76:79], v[44:47]\n"
    "s_waitcnt lgkmcnt(0)\n"
    "ds_read_b128 a[184:187], v4 offset:12288\n"
    "v_cvt_pk_bf16_f32 v106, v64, v65\n"
    "v_exp_f32 v67, v67\n"
    "s_nop 0\n"
    "v_cvt_pk_bf16_f32 v107, v66, v67\n"
    "v_add_u32 v4, 0xfffe8000, v4\n"
    "v_mfma_f32_16x16x32_bf16 v[48:51], a[176:179], a[0:3], v[68:71]\n"
    "buffer_load_dwordx4 v11, s[44:47], s86 offen offset:3072 nt lds\n"
    "v_mfma_f32_16x16x32_bf16 v[52:55], a[176:179], a[16:19], v[72:75]\n"
    "v_mfma_f32_16x16x32_bf16 v[56:59], a[176:179], a[32:35], v[76:79]\n"
    "s_add_u32 m0, s73, 0x8000\n"
    "v_mfma_f32_16x16x32_bf16 v[60:63], a[176:179], a[48:51], v[80:83]\n"
    "s_add_u32 s87, s73, 0x14000\n"
    "ds_read_b128 a[188:191], v5 offset:12288\n"
    "v_add_u32 v5, 0xfffe8000, v5\n"
    "v_mfma_f32_16x16x32_bf16 v[64:67], a[176:179], a[64:67], v[84:87]\n"
    "s_waitcnt vmcnt(6)\n"
    "v_exp_f32 v28, v28\n"
    "v_mfma_f32_16x16x32_bf16 v[48:51], a[180:183], a[4:7], v[48:51]\n"
    "v_mfma_f32_16x16x32_bf16 v[52:55], a[180:183], a[20:23], v[52:55]\n"
    "v_exp_f32 v29, v29\n"
    "v_mfma_f32_16x16x32_bf16 v[56:59], a[180:183], a[36:39], v[56:59]\n"
    "buffer_load_dwordx4 v12, s[48:51], s87 offen nt lds\n"
    "v_mfma_f32_16x16x32_bf16 v[60:63], a[180:183], a[52:55], v[60:63]\n"
    "v_exp_f32 v30, v30\n"
    "v_mfma_f32_16x16x32_bf16 v[64:67], a[180:183], a[68:71], v[64:67]\n"
    "s_waitcnt lgkmcnt(0)\n"
    "v_cvt_pk_bf16_f32 v108, v28, v29\n"
    "v_mfma_f32_16x16x32_bf16 v[48:51], a[184:187], a[8:11], v[48:51]\n"
    "v_exp_f32 v31, v31\n"
    "v_mfma_f32_16x16x32_bf16 v[52:55], a[184:187], a[24:27], v[52:55]\n"
    "v_mfma_f32_16x16x32_bf16 v[56:59], a[184:187], a[40:43], v[56:59]\n"
    "v_exp_f32 v32, v32\n"
    "v_mfma_f32_16x16x32_bf16 v[60:63], a[184:187], a[56:59], v[60:63]\n"
    "s_waitcnt vmcnt(6)\n"
    "ds_read_b64_tr_b16 a[144:145], v6\n"
    "v_cvt_pk_bf16_f32 v109, v30, v31\n"
    "v_mfma_f32_16x16x32_bf16 v[64:67], a[184:187], a[72:75], v[64:67]\n"
    "ds_read_b64_tr_b16 a[146:147], v6 offset:4096\n"
    "v_mfma_f32_16x16x32_bf16 v[48:51], a[188:191], a[12:15], v[48:51]\n"
    "v_exp_f32 v33, v33\n"
    "v_mfma_f32_16x16x32_bf16 v[52:55], a[188:191], a[28:31], v[52:55]\n"
    "v_exp_f32 v34, v34\n"
    "v_mfma_f32_16x16x32_bf16 v[56:59], a[188:191], a[44:47], v[56:59]\n"
    "v_cvt_pk_bf16_f32 v112, v32, v33\n"
    "v_mfma_f32_16x16x32_bf16 v[60:63], a[188:191], a[60:63], v[60:63]\n"
    "ds_read_b64_tr_b16 a[148:149], v7\n"
    "ds_read_b64_tr_b16 a[150:151], v7 offset:4096\n"
    "v_mfma_f32_16x16x32_bf16 v[64:67], a[188:191], a[76:79], v[64:67]\n"
    "buffer_load_dwordx4 v13, s[48:51], s87 offen offset:1024 nt lds\n"
    "v_mfma_f32_16x16x32_bf16 a[80:83], a[132:135], v[88:91], a[80:83]\n"
    "v_exp_f32 v35, v35\n"
    "v_mfma_f32_16x16x32_bf16 a[84:87], a[132:135], v[92:95], a[84:87]\n"
    "v_exp_f32 v36, v36\n"
    "v_mfma_f32_16x16x32_bf16 a[88:91], a[132:135], v[96:99], a[88:91]\n"
    "v_cvt_pk_bf16_f32 v113, v34, v35\n"
    "v_mfma_f32_16x16x32_bf16 a[92:95], a[132:135], v[100:103], a[92:95]\n"
    "ds_read_b64_tr_b16 a[152:153], v6 offset:512\n"
    "ds_read_b64_tr_b16 a[154:155], v6 offset:4608\n"
    "v_mfma_f32_16x16x32_bf16 a[96:99], a[132:135], v[104:107], a[96:99]\n"
    "s_waitcnt lgkmcnt(2)\n"
    "v_exp_f32 v37, v37\n"
    "v_mfma_f32_16x16x32_bf16 v[128:131], a[144:147], v[88:91], v[128:131]\n"
    "s_waitcnt vmcnt(6)\n"
    "v_exp_f32 v38, v38\n"
    "v_mfma_f32_16x16x32_bf16 v[132:135], a[144:147], v[92:95], v[132:135]\n"
    "v_cvt_pk_bf16_f32 v116, v36, v37\n"
    "v_mfma_f32_16x16x32_bf16 v[136:139], a[144:147], v[96:99], v[136:139]\n"
    "v_exp_f32 v39, v39\n"
    "v_mfma_f32_16x16x32_bf16 v[140:143], a[144:147], v[100:103], v[140:143]\n"
    "ds_read_b64_tr_b16 a[156:157], v7 offset:512\n"
    "ds_read_b64_tr_b16 a[158:159], v7 offset:4608\n"
    "v_mfma_f32_16x16x32_bf16 v[144:147], a[144:147], v[104:107], v[144:147]\n"
    "buffer_load_dwordx4 v12, s[48:51], s87 offen offset:2048 nt lds\n"
    "v_mfma_f32_16x16x32_bf16 v[148:151], a[148:151], v[88:91], v[148:151]\n"
    "v_exp_f32 v40, v40\n"
    "v_mfma_f32_16x16x32_bf16 v[152:155], a[148:151], v[92:95], v[152:155]\n"
    "v_cvt_pk_bf16_f32 v117, v38, v39\n"
    "v_mfma_f32_16x16x32_bf16 v[156:159], a[148:151], v[96:99], v[156:159]\n"
    "v_exp_f32 v41, v41\n"
    "v_mfma_f32_16x16x32_bf16 v[160:163], a[148:151], v[100:103], v[160:163]\n"
    "ds_read_b64_tr_b16 a[160:161], v6 offset:1024\n"
    "ds_read_b64_tr_b16 a[162:163], v6 offset:5120\n"
    "v_mfma_f32_16x16x32_bf16 v[164:167], a[148:151], v[104:107], v[164:167]\n"
    "s_waitcnt lgkmcnt(2)\n"
    "v_exp_f32 v42, v42\n"
    "v_mfma_f32_16x16x32_bf16 v[168:171], a[152:155], v[88:91], v[168:171]\n"
    "s_waitcnt vmcnt(6)\n"
    "v_cvt_pk_bf16_f32 v120, v40, v41\n"
    "v_mfma_f32_16x16x32_bf16 v[172:175], a[152:155], v[92:95], v[172:175]\n"
    "v_exp_f32 v43, v43\n"
    "v_mfma_f32_16x16x32_bf16 v[176:179], a[152:155], v[96:99], v[176:179]\n"
    "v_exp_f32 v44, v44\n"
    "v_mfma_f32_16x16x32_bf16 v[180:183], a[152:155], v[100:103], v[180:183]\n"
    "ds_read_b64_tr_b16 a[164:165], v7 offset:1024\n"
    "v_cvt_pk_bf16_f32 v121, v42, v43\n"
    "v_mfma_f32_16x16x32_bf16 v[184:187], a[152:155], v[104:107], v[184:187]\n"
    "ds_read_b64_tr_b16 a[166:167], v7 offset:5120\n"
    "v_mfma_f32_16x16x32_bf16 v[188:191], a[156:159], v[88:91], v[188:191]\n"
    "buffer_load_dwordx4 v13, s[48:51], s87 offen offset:3072 nt lds\n"
    "v_mfma_f32_16x16x32_bf16 a[100:103], a[156:159], v[92:95], a[100:103]\n"
    "v_exp_f32 v45, v45\n"
    "v_mfma_f32_16x16x32_bf16 a[104:107], a[156:159], v[96:99], a[104:107]\n"
    "v_exp_f32 v46, v46\n"
    "v_mfma_f32_16x16x32_bf16 a[108:111], a[156:159], v[100:103], a[108:111]\n"
    "ds_read_b64_tr_b16 a[168:169], v6 offset:1536\n"
    "v_cvt_pk_bf16_f32 v124, v44, v45\n"
    "v_mfma_f32_16x16x32_bf16 a[112:115], a[156:159], v[104:107], a[112:115]\n"
    "s_waitcnt lgkmcnt(1)\n"
    "ds_read_b64_tr_b16 a[170:171], v6 offset:5632\n"
    "ds_read_b64_tr_b16 a[172:173], v7 offset:1536\n"
    "v_mfma_f32_16x16x32_bf16 a[116:119], a[160:163], v[88:91], a[116:119]\n"
    "s_add_u32 m0, s73, 0xc000\n"
    "ds_read_b64_tr_b16 a[174:175], v7 offset:5632\n"
    "v_mfma_f32_16x16x32_bf16 a[120:123], a[160:163], v[92:95], a[120:123]\n"
    "s_add_u32 s87, s73, 0x34000\n"
    "v_exp_f32 v47, v47\n"
    "v_mfma_f32_16x16x32_bf16 a[124:127], a[160:163], v[96:99], a[124:127]\n"
    "s_waitcnt vmcnt(6)\n"
    "v_cvt_pk_bf16_f32 v125, v46, v47\n"
    "v_mfma_f32_16x16x32_bf16 a[128:131], a[160:163], v[100:103], a[128:131]\n"
    "v_exp_f32 v48, v48\n"
    "v_mfma_f32_16x16x32_bf16 a[192:195], a[160:163], v[104:107], a[192:195]\n"
    "v_exp_f32 v49, v49\n"
    "v_mfma_f32_16x16x32_bf16 a[196:199], a[164:167], v[88:91], a[196:199]\n"
    "buffer_load_dwordx4 v12, s[48:51], s87 offen nt lds\n"
    "v_mfma_f32_16x16x32_bf16 a[200:203], a[164:167], v[92:95], a[200:203]\n"
    "v_exp_f32 v50, v50\n"
    "v_mfma_f32_16x16x32_bf16 a[204:207], a[164:167], v[96:99], a[204:207]\n"
    "v_cvt_pk_bf16_f32 v110, v48, v49\n"
    "v_mfma_f32_16x16x32_bf16 a[208:211], a[164:167], v[100:103], a[208:211]\n"
    "v_exp_f32 v51, v51\n"
    "v_mfma_f32_16x16x32_bf16 a[212:215], a[164:167], v[104:107], a[212:215]\n"
    "s_waitcnt vmcnt(5) lgkmcnt(0)\n"
    "s_barrier\n"
    "v_mfma_f32_16x16x32_bf16 a[216:219], a[168:171], v[88:91], a[216:219]\n"
    "ds_read_b128 a[176:179], v2\n"
    "v_mfma_f32_16x16x32_bf16 a[220:223], a[168:171], v[92:95], a[220:223]\n"
    "s_waitcnt vmcnt(6)\n"
    "v_exp_f32 v52, v52\n"
    "v_mfma_f32_16x16x32_bf16 a[224:227], a[168:171], v[96:99], a[224:227]\n"
    "v_cvt_pk_bf16_f32 v111, v50, v51\n"
    "v_mfma_f32_16x16x32_bf16 a[228:231], a[168:171], v[100:103], a[228:231]\n"
    "ds_read_b128 a[180:183], v3\n"
    "v_mfma_f32_16x16x32_bf16 a[232:235], a[168:171], v[104:107], a[232:235]\n"
    "v_exp_f32 v53, v53\n"
    "v_mfma_f32_16x16x32_bf16 a[236:239], a[172:175], v[88:91], a[236:239]\n"
    "v_exp_f32 v54, v54\n"
    "v_mfma_f32_16x16x32_bf16 a[240:243], a[172:175], v[92:95], a[240:243]\n"
    "buffer_load_dwordx4 v13, s[48:51], s87 offen offset:1024 nt lds\n"
    "v_mfma_f32_16x16x32_bf16 a[244:247], a[172:175], v[96:99], a[244:247]\n"
    "v_cvt_pk_bf16_f32 v114, v52, v53\n"
    "v_mfma_f32_16x16x32_bf16 a[248:251], a[172:175], v[100:103], a[248:251]\n"
    "ds_read_b128 a[184:187], v4\n"
    "v_mfma_f32_16x16x32_bf16 a[252:255], a[172:175], v[104:107], a[252:255]\n"
    "s_waitcnt lgkmcnt(1)\n"
    "v_exp_f32 v55, v55\n"
    "v_mfma_f32_16x16x32_bf16 v[28:31], a[176:179], a[0:3], v[68:71]\n"
    "v_exp_f32 v56, v56\n"
    "v_mfma_f32_16x16x32_bf16 v[32:35], a[176:179], a[16:19], v[72:75]\n"
    "s_waitcnt vmcnt(6)\n"
    "v_cvt_pk_bf16_f32 v115, v54, v55\n"
    "v_mfma_f32_16x16x32_bf16 v[36:39], a[176:179], a[32:35], v[76:79]\n"
    "v_exp_f32 v57, v57\n"
    "v_mfma_f32_16x16x32_bf16 v[40:43], a[176:179], a[48:51], v[80:83]\n"
    "ds_read_b128 a[188:191], v5\n"
    "v_mfma_f32_16x16x32_bf16 v[44:47], a[176:179], a[64:67], v[84:87]\n"
    "v_exp_f32 v58, v58\n"
    "v_mfma_f32_16x16x32_bf16 v[28:31], a[180:183], a[4:7], v[28:31]\n"
    "v_cvt_pk_bf16_f32 v118, v56, v57\n"
    "v_mfma_f32_16x16x32_bf16 v[32:35], a[180:183], a[20:23], v[32:35]\n"
    "buffer_load_dwordx4 v12, s[48:51], s87 offen offset:2048 nt lds\n"
    "v_mfma_f32_16x16x32_bf16 v[36:39], a[180:183], a[36:39], v[36:39]\n"
    "v_exp_f32 v59, v59\n"
    "v_mfma_f32_16x16x32_bf16 v[40:43], a[180:183], a[52:55], v[40:43]\n"
    "ds_read_b128 a[144:147], v2 offset:4096\n"
    "v_mfma_f32_16x16x32_bf16 v[44:47], a[180:183], a[68:71], v[44:47]\n"
    "s_waitcnt lgkmcnt(1)\n"
    "v_exp_f32 v60, v60\n"
    "v_mfma_f32_16x16x32_bf16 v[28:31], a[184:187], a[8:11], v[28:31]\n"
    "v_cvt_pk_bf16_f32 v119, v58, v59\n"
    "v_mfma_f32_16x16x32_bf16 v[32:35], a[184:187], a[24:27], v[32:35]\n"
    "v_exp_f32 v61, v61\n"
    "v_mfma_f32_16x16x32_bf16 v[36:39], a[184:187], a[40:43], v[36:39]\n"
    "s_waitcnt vmcnt(6)\n"
    "v_exp_f32 v62, v62\n"
    "v_mfma_f32_16x16x32_bf16 v[40:43], a[184:187], a[56:59], v[40:43]\n"
    "ds_read_b128 a[148:151], v3 offset:4096\n"
    "v_cvt_pk_bf16_f32 v122, v60, v61\n"
    "v_mfma_f32_16x16x32_bf16 v[44:47], a[184:187], a[72:75], v[44:47]\n"
    "v_exp_f32 v63, v63\n"
    "v_mfma_f32_16x16x32_bf16 v[28:31], a[188:191], a[12:15], v[28:31]\n"
    "v_exp_f32 v64, v64\n"
    "v_mfma_f32_16x16x32_bf16 v[32:35], a[188:191], a[28:31], v[32:35]\n"
    "v_cvt_pk_bf16_f32 v123, v62, v63\n"
    "v_mfma_f32_16x16x32_bf16 v[36:39], a[188:191], a[44:47], v[36:39]\n"
    "v_exp_f32 v65, v65\n"
    "v_mfma_f32_16x16x32_bf16 v[40:43], a[188:191], a[60:63], v[40:43]\n"
    "v_exp_f32 v66, v66\n"
    "v_mfma_f32_16x16x32_bf16 v[44:47], a[188:191], a[76:79], v[44:47]\n"
    "s_waitcnt lgkmcnt(0)\n"
    "ds_read_b128 a[152:155], v4 offset:4096\n"
    "v_cvt_pk_bf16_f32 v126, v64, v65\n"
    "v_exp_f32 v67, v67\n"
    "s_nop 0\n"
    "v_cvt_pk_bf16_f32 v127, v66, v67\n"
    "v_mfma_f32_16x16x32_bf16 v[48:51], a[144:147], a[0:3], v[68:71]\n"
    "buffer_load_dwordx4 v13, s[48:51], s87 offen offset:3072 nt lds\n"
    "v_mfma_f32_16x16x32_bf16 v[52:55], a[144:147], a[16:19], v[72:75]\n"
    "v_mfma_f32_16x16x32_bf16 v[56:59], a[144:147], a[32:35], v[76:79]\n"
    "s_add_u32 m0, s73, 0x10000\n"
    "v_mfma_f32_16x16x32_bf16 v[60:63], a[144:147], a[48:51], v[80:83]\n"
    "s_add_u32 s86, s73, 0x18000\n"
    "ds_read_b128 a[156:159], v5 offset:4096\n"
    "v_mfma_f32_16x16x32_bf16 v[64:67], a[144:147], a[64:67], v[84:87]\n"
    "s_waitcnt vmcnt(6)\n"
    "v_exp_f32 v28, v28\n"
    "v_mfma_f32_16x16x32_bf16 v[48:51], a[148:151], a[4:7], v[48:51]\n"
    "v_mfma_f32_16x16x32_bf16 v[52:55], a[148:151], a[20:23], v[52:55]\n"
    "v_exp_f32 v29, v29\n"
    "v_mfma_f32_16x16x32_bf16 v[56:59], a[148:151], a[36:39], v[56:59]\n"
    "buffer_load_dwordx4 v8, s[44:47], s86 offen nt lds\n"
    "v_mfma_f32_16x16x32_bf16 v[60:63], a[148:151], a[52:55], v[60:63]\n"
    "v_exp_f32 v30, v30\n"
    "v_mfma_f32_16x16x32_bf16 v[64:67], a[148:151], a[68:71], v[64:67]\n"
    "s_waitcnt lgkmcnt(0)\n"
    "v_cvt_pk_bf16_f32 v88, v28, v29\n"
    "v_mfma_f32_16x16x32_bf16 v[48:51], a[152:155], a[8:11], v[48:51]\n"
    "v_exp_f32 v31, v31\n"
    "v_mfma_f32_16x16x32_bf16 v[52:55], a[152:155], a[24:27], v[52:55]\n"
    "v_mfma_f32_16x16x32_bf16 v[56:59], a[152:155], a[40:43], v[56:59]\n"
    "v_exp_f32 v32, v32\n"
    "v_mfma_f32_16x16x32_bf16 v[60:63], a[152:155], a[56:59], v[60:63]\n"
    "s_waitcnt vmcnt(6)\n"
    "ds_read_b64_tr_b16 a[160:161], v6 offset:8192\n"
    "v_cvt_pk_bf16_f32 v89, v30, v31\n"
    "v_mfma_f32_16x16x32_bf16 v[64:67], a[152:155], a[72:75], v[64:67]\n"
    "ds_read_b64_tr_b16 a[162:163], v6 offset:12288\n"
    "v_mfma_f32_16x16x32_bf16 v[48:51], a[156:159], a[12:15], v[48:51]\n"
    "v_exp_f32 v33, v33\n"
    "v_mfma_f32_16x16x32_bf16 v[52:55], a[156:159], a[28:31], v[52:55]\n"
    "v_exp_f32 v34, v34\n"
    "v_mfma_f32_16x16x32_bf16 v[56:59], a[156:159], a[44:47], v[56:59]\n"
    "v_cvt_pk_bf16_f32 v92, v32, v33\n"
    "v_mfma_f32_16x16x32_bf16 v[60:63], a[156:159], a[60:63], v[60:63]\n"
    "ds_read_b64_tr_b16 a[164:165], v7 offset:8192\n"
    "ds_read_b64_tr_b16 a[166:167], v7 offset:12288\n"
    "v_mfma_f32_16x16x32_bf16 v[64:67], a[156:159], a[76:79], v[64:67]\n"
    "buffer_load_dwordx4 v9, s[44:47], s86 offen offset:1024 nt lds\n"
    "v_mfma_f32_16x16x32_bf16 a[80:83], a[132:135], v[108:111], a[80:83]\n"
    "v_exp_f32 v35, v35\n"
    "v_mfma_f32_16x16x32_bf16 a[84:87], a[132:135], v[112:115], a[84:87]\n"
    "v_exp_f32 v36, v36\n"
    "v_mfma_f32_16x16x32_bf16 a[88:91], a[132:135], v[116:119], a[88:91]\n"
    "v_cvt_pk_bf16_f32 v93, v34, v35\n"
    "v_mfma_f32_16x16x32_bf16 a[92:95], a[132:135], v[120:123], a[92:95]\n"
    "ds_read_b64_tr_b16 a[168:169], v6 offset:8704\n"
    "ds_read_b64_tr_b16 a[170:171], v6 offset:12800\n"
    "v_mfma_f32_16x16x32_bf16 a[96:99], a[132:135], v[124:127], a[96:99]\n"
    "s_waitcnt lgkmcnt(2)\n"
    "v_exp_f32 v37, v37\n"
    "v_mfma_f32_16x16x32_bf16 v[128:131], a[160:163], v[108:111], v[128:131]\n"
    "s_waitcnt vmcnt(6)\n"
    "v_exp_f32 v38, v38\n"
    "v_mfma_f32_16x16x32_bf16 v[132:135], a[160:163], v[112:115], v[132:135]\n"
    "v_cvt_pk_bf16_f32 v96, v36, v37\n"
    "v_mfma_f32_16x16x32_bf16 v[136:139], a[160:163], v[116:119], v[136:139]\n"
    "v_exp_f32 v39, v39\n"
    "v_mfma_f32_16x16x32_bf16 v[140:143], a[160:163], v[120:123], v[140:143]\n"
    "ds_read_b64_tr_b16 a[172:173], v7 offset:8704\n"
    "ds_read_b64_tr_b16 a[174:175], v7 offset:12800\n"
    "v_mfma_f32_16x16x32_bf16 v[144:147], a[160:163], v[124:127], v[144:147]\n"
    "buffer_load_dwordx4 v10, s[44:47], s86 offen offset:2048 nt lds\n"
    "v_mfma_f32_16x16x32_bf16 v[148:151], a[164:167], v[108:111], v[148:151]\n"
    "v_exp_f32 v40, v40\n"
    "v_mfma_f32_16x16x32_bf16 v[152:155], a[164:167], v[112:115], v[152:155]\n"
    "v_cvt_pk_bf16_f32 v97, v38, v39\n"
    "v_mfma_f32_16x16x32_bf16 v[156:159], a[164:167], v[116:119], v[156:159]\n"
    "v_exp_f32 v41, v41\n"
    "v_mfma_f32_16x16x32_bf16 v[160:163], a[164:167], v[120:123], v[160:163]\n"
    "ds_read_b64_tr_b16 a[176:177], v6 offset:9216\n"
    "ds_read_b64_tr_b16 a[178:179], v6 offset:13312\n"
    "v_mfma_f32_16x16x32_bf16 v[164:167], a[164:167], v[124:127], v[164:167]\n"
    "s_waitcnt lgkmcnt(2)\n"
    "v_exp_f32 v42, v42\n"
    "v_mfma_f32_16x16x32_bf16 v[168:171], a[168:171], v[108:111], v[168:171]\n"
    "s_waitcnt vmcnt(6)\n"
    "v_cvt_pk_bf16_f32 v100, v40, v41\n"
    "v_mfma_f32_16x16x32_bf16 v[172:175], a[168:171], v[112:115], v[172:175]\n"
    "v_exp_f32 v43, v43\n"
    "v_mfma_f32_16x16x32_bf16 v[176:179], a[168:171], v[116:119], v[176:179]\n"
    "v_exp_f32 v44, v44\n"
    "v_mfma_f32_16x16x32_bf16 v[180:183], a[168:171], v[120:123], v[180:183]\n"
    "ds_read_b64_tr_b16 a[180:181], v7 offset:9216\n"
    "v_cvt_pk_bf16_f32 v101, v42, v43\n"
    "v_mfma_f32_16x16x32_bf16 v[184:187], a[168:171], v[124:127], v[184:187]\n"
    "ds_read_b64_tr_b16 a[182:183], v7 offset:13312\n"
    "v_mfma_f32_16x16x32_bf16 v[188:191], a[172:175], v[108:111], v[188:191]\n"
    "buffer_load_dwordx4 v11, s[44:47], s86 offen offset:3072 nt lds\n"
    "v_mfma_f32_16x16x32_bf16 a[100:103], a[172:175], v[112:115], a[100:103]\n"
    "v_exp_f32 v45, v45\n"
    "v_mfma_f32_16x16x32_bf16 a[104:107], a[172:175], v[116:119], a[104:107]\n"
    "v_exp_f32 v46, v46\n"
    "v_mfma_f32_16x16x32_bf16 a[108:111], a[172:175], v[120:123], a[108:111]\n"
    "ds_read_b64_tr_b16 a[184:185], v6 offset:9728\n"
    "v_cvt_pk_bf16_f32 v104, v44, v45\n"
    "v_mfma_f32_16x16x32_bf16 a[112:115], a[172:175], v[124:127], a[112:115]\n"
    "s_waitcnt lgkmcnt(1)\n"
    "ds_read_b64_tr_b16 a[186:187], v6 offset:13824\n"
    "ds_read_b64_tr_b16 a[188:189], v7 offset:9728\n"
    "v_add_u32 v6, 0xfffe8000, v6\n"
    "v_mfma_f32_16x16x32_bf16 a[116:119], a[176:179], v[108:111], a[116:119]\n"
    "s_add_u32 m0, s73, 0x14000\n"
    "ds_read_b64_tr_b16 a[190:191], v7 offset:13824\n"
    "v_add_u32 v7, 0xfffe8000, v7\n"
    "v_mfma_f32_16x16x32_bf16 a[120:123], a[176:179], v[112:115], a[120:123]\n"
    "s_add_u32 s86, s73, 0x38000\n"
    "v_exp_f32 v47, v47\n"
    "v_mfma_f32_16x16x32_bf16 a[124:127], a[176:179], v[116:119], a[124:127]\n"
    "s_waitcnt vmcnt(6)\n"
    "v_cvt_pk_bf16_f32 v105, v46, v47\n"
    "v_mfma_f32_16x16x32_bf16 a[128:131], a[176:179], v[120:123], a[128:131]\n"
    "v_exp_f32 v48, v48\n"
    "v_mfma_f32_16x16x32_bf16 a[192:195], a[176:179], v[124:127], a[192:195]\n"
    "v_exp_f32 v49, v49\n"
    "v_mfma_f32_16x16x32_bf16 a[196:199], a[180:183], v[108:111], a[196:199]\n"
    "buffer_load_dwordx4 v8, s[44:47], s86 offen nt lds\n"
    "v_mfma_f32_16x16x32_bf16 a[200:203], a[180:183], v[112:115], a[200:203]\n"
    "v_exp_f32 v50, v50\n"
    "v_mfma_f32_16x16x32_bf16 a[204:207], a[180:183], v[116:119], a[204:207]\n"
    "v_cvt_pk_bf16_f32 v90, v48, v49\n"
    "v_mfma_f32_16x16x32_bf16 a[208:211], a[180:183], v[120:123], a[208:211]\n"
    "v_exp_f32 v51, v51\n"
    "v_mfma_f32_16x16x32_bf16 a[212:215], a[180:183], v[124:127], a[212:215]\n"
    "s_waitcnt vmcnt(5) lgkmcnt(0)\n"
    "s_barrier\n"
    "v_mfma_f32_16x16x32_bf16 a[216:219], a[184:187], v[108:111], a[216:219]\n"
    "ds_read_b128 a[144:147], v2 offset:8192\n"
    "v_mfma_f32_16x16x32_bf16 a[220:223], a[184:187], v[112:115], a[220:223]\n"
    "s_waitcnt vmcnt(6)\n"
    "v_exp_f32 v52, v52\n"
    "v_mfma_f32_16x16x32_bf16 a[224:227], a[184:187], v[116:119], a[224:227]\n"
    "v_cvt_pk_bf16_f32 v91, v50, v51\n"
    "v_mfma_f32_16x16x32_bf16 a[228:231], a[184:187], v[120:123], a[228:231]\n"
    "ds_read_b128 a[148:151], v3 offset:8192\n"
    "v_mfma_f32_16x16x32_bf16 a[232:235], a[184:187], v[124:127], a[232:235]\n"
    "v_exp_f32 v53, v53\n"
    "v_mfma_f32_16x16x32_bf16 a[236:239], a[188:191], v[108:111], a[236:239]\n"
    "v_exp_f32 v54, v54\n"
    "v_mfma_f32_16x16x32_bf16 a[240:243], a[188:191], v[112:115], a[240:243]\n"
    "buffer_load_dwordx4 v9, s[44:47], s86 offen offset:1024 nt lds\n"
    "v_mfma_f32_16x16x32_bf16 a[244:247], a[188:191], v[116:119], a[244:247]\n"
    "v_cvt_pk_bf16_f32 v94, v52, v53\n"
    "v_mfma_f32_16x16x32_bf16 a[248:251], a[188:191], v[120:123], a[248:251]\n"
    "ds_read_b128 a[152:155], v4 offset:8192\n"
    "v_mfma_f32_16x16x32_bf16 a[252:255], a[188:191], v[124:127], a[252:255]\n"
    "s_waitcnt lgkmcnt(1)\n"
    "v_exp_f32 v55, v55\n"
    "v_mfma_f32_16x16x32_bf16 v[28:31], a[144:147], a[0:3], v[68:71]\n"
    "v_exp_f32 v56, v56\n"
    "v_mfma_f32_16x16x32_bf16 v[32:35], a[144:147], a[16:19], v[72:75]\n"
    "s_waitcnt vmcnt(6)\n"
    "v_cvt_pk_bf16_f32 v95, v54, v55\n"
    "v_mfma_f32_16x16x32_bf16 v[36:39], a[144:147], a[32:35], v[76:79]\n"
    "v_exp_f32 v57, v57\n"
    "v_mfma_f32_16x16x32_bf16 v[40:43], a[144:147], a[48:51], v[80:83]\n"
    "ds_read_b128 a[156:159], v5 offset:8192\n"
    "v_mfma_f32_16x16x32_bf16 v[44:47], a[144:147], a[64:67], v[84:87]\n"
    "v_exp_f32 v58, v58\n"
    "v_mfma_f32_16x16x32_bf16 v[28:31], a[148:151], a[4:7], v[28:31]\n"
    "v_cvt_pk_bf16_f32 v98, v56, v57\n"
    "v_mfma_f32_16x16x32_bf16 v[32:35], a[148:151], a[20:23], v[32:35]\n"
    "buffer_load_dwordx4 v10, s[44:47], s86 offen offset:2048 nt lds\n"
    "v_mfma_f32_16x16x32_bf16 v[36:39], a[148:151], a[36:39], v[36:39]\n"
    "v_exp_f32 v59, v59\n"
    "v_mfma_f32_16x16x32_bf16 v[40:43], a[148:151], a[52:55], v[40:43]\n"
    "ds_read_b128 a[160:163], v2 offset:12288\n"
    "v_add_u32 v2, 0x10000, v2\n"
    "v_mfma_f32_16x16x32_bf16 v[44:47], a[148:151], a[68:71], v[44:47]\n"
    "s_waitcnt lgkmcnt(1)\n"
    "v_exp_f32 v60, v60\n"
    "v_mfma_f32_16x16x32_bf16 v[28:31], a[152:155], a[8:11], v[28:31]\n"
    "v_cvt_pk_bf16_f32 v99, v58, v59\n"
    "v_mfma_f32_16x16x32_bf16 v[32:35], a[152:155], a[24:27], v[32:35]\n"
    "v_exp_f32 v61, v61\n"
    "v_mfma_f32_16x16x32_bf16 v[36:39], a[152:155], a[40:43], v[36:39]\n"
    "s_waitcnt vmcnt(6)\n"
    "v_exp_f32 v62, v62\n"
    "v_mfma_f32_16x16x32_bf16 v[40:43], a[152:155], a[56:59], v[40:43]\n"
    "ds_read_b128 a[164:167], v3 offset:12288\n"
    "v_cvt_pk_bf16_f32 v102, v60, v61\n"
    "v_add_u32 v3, 0x10000, v3\n"
    "v_mfma_f32_16x16x32_bf16 v[44:47], a[152:155], a[72:75], v[44:47]\n"
    "v_exp_f32 v63, v63\n"
    "v_mfma_f32_16x16x32_bf16 v[28:31], a[156:159], a[12:15], v[28:31]\n"
    "v_exp_f32 v64, v64\n"
    "v_mfma_f32_16x16x32_bf16 v[32:35], a[156:159], a[28:31], v[32:35]\n"
    "v_cvt_pk_bf16_f32 v103, v62, v63\n"
    "v_mfma_f32_16x16x32_bf16 v[36:39], a[156:159], a[44:47], v[36:39]\n"
    "v_exp_f32 v65, v65\n"
    "v_mfma_f32_16x16x32_bf16 v[40:43], a[156:159], a[60:63], v[40:43]\n"
    "v_exp_f32 v66, v66\n"
    "v_mfma_f32_16x16x32_bf16 v[44:47], a[156:159], a[76:79], v[44:47]\n"
    "s_waitcnt lgkmcnt(0)\n"
    "ds_read_b128 a[168:171], v4 offset:12288\n"
    "v_cvt_pk_bf16_f32 v106, v64, v65\n"
    "v_exp_f32 v67, v67\n"
    "s_nop 0\n"
    "v_cvt_pk_bf16_f32 v107, v66, v67\n"
    "v_add_u32 v4, 0x10000, v4\n"
    "v_mfma_f32_16x16x32_bf16 v[48:51], a[160:163], a[0:3], v[68:71]\n"
    "buffer_load_dwordx4 v11, s[44:47], s86 offen offset:3072 nt lds\n"
    "v_mfma_f32_16x16x32_bf16 v[52:55], a[160:163], a[16:19], v[72:75]\n"
    "v_mfma_f32_16x16x32_bf16 v[56:59], a[160:163], a[32:35], v[76:79]\n"
    "s_add_u32 m0, s73, 0x18000\n"
    "v_mfma_f32_16x16x32_bf16 v[60:63], a[160:163], a[48:51], v[80:83]\n"
    "s_add_u32 s87, s73, 0x18000\n"
    "ds_read_b128 a[172:175], v5 offset:12288\n"
    "v_add_u32 v5, 0x10000, v5\n"
    "v_mfma_f32_16x16x32_bf16 v[64:67], a[160:163], a[64:67], v[84:87]\n"
    "s_waitcnt vmcnt(6)\n"
    "v_exp_f32 v28, v28\n"
    "v_mfma_f32_16x16x32_bf16 v[48:51], a[164:167], a[4:7], v[48:51]\n"
    "v_mfma_f32_16x16x32_bf16 v[52:55], a[164:167], a[20:23], v[52:55]\n"
    "v_exp_f32 v29, v29\n"
    "v_mfma_f32_16x16x32_bf16 v[56:59], a[164:167], a[36:39], v[56:59]\n"
    "buffer_load_dwordx4 v12, s[48:51], s87 offen nt lds\n"
    "v_mfma_f32_16x16x32_bf16 v[60:63], a[164:167], a[52:55], v[60:63]\n"
    "v_exp_f32 v30, v30\n"
    "v_mfma_f32_16x16x32_bf16 v[64:67], a[164:167], a[68:71], v[64:67]\n"
    "s_waitcnt lgkmcnt(0)\n"
    "v_cvt_pk_bf16_f32 v108, v28, v29\n"
    "v_mfma_f32_16x16x32_bf16 v[48:51], a[168:171], a[8:11], v[48:51]\n"
    "v_exp_f32 v31, v31\n"
    "v_mfma_f32_16x16x32_bf16 v[52:55], a[168:171], a[24:27], v[52:55]\n"
    "v_mfma_f32_16x16x32_bf16 v[56:59], a[168:171], a[40:43], v[56:59]\n"
    "v_exp_f32 v32, v32\n"
    "v_mfma_f32_16x16x32_bf16 v[60:63], a[168:171], a[56:59], v[60:63]\n"
    "s_waitcnt vmcnt(6)\n"
    "ds_read_b64_tr_b16 a[176:177], v6\n"
    "v_cvt_pk_bf16_f32 v109, v30, v31\n"
    "v_mfma_f32_16x16x32_bf16 v[64:67], a[168:171], a[72:75], v[64:67]\n"
    "ds_read_b64_tr_b16 a[178:179], v6 offset:4096\n"
    "v_mfma_f32_16x16x32_bf16 v[48:51], a[172:175], a[12:15], v[48:51]\n"
    "v_exp_f32 v33, v33\n"
    "v_mfma_f32_16x16x32_bf16 v[52:55], a[172:175], a[28:31], v[52:55]\n"
    "v_exp_f32 v34, v34\n"
    "v_mfma_f32_16x16x32_bf16 v[56:59], a[172:175], a[44:47], v[56:59]\n"
    "v_cvt_pk_bf16_f32 v112, v32, v33\n"
    "v_mfma_f32_16x16x32_bf16 v[60:63], a[172:175], a[60:63], v[60:63]\n"
    "ds_read_b64_tr_b16 a[180:181], v7\n"
    "ds_read_b64_tr_b16 a[182:183], v7 offset:4096\n"
    "v_mfma_f32_16x16x32_bf16 v[64:67], a[172:175], a[76:79], v[64:67]\n"
    "buffer_load_dwordx4 v13, s[48:51], s87 offen offset:1024 nt lds\n"
    "v_mfma_f32_16x16x32_bf16 a[80:83], a[132:135], v[88:91], a[80:83]\n"
    "v_exp_f32 v35, v35\n"
    "v_mfma_f32_16x16x32_bf16 a[84:87], a[132:135], v[92:95], a[84:87]\n"
    "v_exp_f32 v36, v36\n"
    "v_mfma_f32_16x16x32_bf16 a[88:91], a[132:135], v[96:99], a[88:91]\n"
    "v_cvt_pk_bf16_f32 v113, v34, v35\n"
    "v_mfma_f32_16x16x32_bf16 a[92:95], a[132:135], v[100:103], a[92:95]\n"
    "ds_read_b64_tr_b16 a[184:185], v6 offset:512\n"
    "ds_read_b64_tr_b16 a[186:187], v6 offset:4608\n"
    "v_mfma_f32_16x16x32_bf16 a[96:99], a[132:135], v[104:107], a[96:99]\n"
    "s_waitcnt lgkmcnt(2)\n"
    "v_exp_f32 v37, v37\n"
    "v_mfma_f32_16x16x32_bf16 v[128:131], a[176:179], v[88:91], v[128:131]\n"
    "s_waitcnt vmcnt(6)\n"
    "v_exp_f32 v38, v38\n"
    "v_mfma_f32_16x16x32_bf16 v[132:135], a[176:179], v[92:95], v[132:135]\n"
    "v_cvt_pk_bf16_f32 v116, v36, v37\n"
    "v_mfma_f32_16x16x32_bf16 v[136:139], a[176:179], v[96:99], v[136:139]\n"
    "v_exp_f32 v39, v39\n"
    "v_mfma_f32_16x16x32_bf16 v[140:143], a[176:179], v[100:103], v[140:143]\n"
    "ds_read_b64_tr_b16 a[188:189], v7 offset:512\n"
    "ds_read_b64_tr_b16 a[190:191], v7 offset:4608\n"
    "v_mfma_f32_16x16x32_bf16 v[144:147], a[176:179], v[104:107], v[144:147]\n"
    "buffer_load_dwordx4 v12, s[48:51], s87 offen offset:2048 nt lds\n"
    "v_mfma_f32_16x16x32_bf16 v[148:151], a[180:183], v[88:91], v[148:151]\n"
    "v_exp_f32 v40, v40\n"
    "v_mfma_f32_16x16x32_bf16 v[152:155], a[180:183], v[92:95], v[152:155]\n"
    "v_cvt_pk_bf16_f32 v117, v38, v39\n"
    "v_mfma_f32_16x16x32_bf16 v[156:159], a[180:183], v[96:99], v[156:159]\n"
    "v_exp_f32 v41, v41\n"
    "v_mfma_f32_16x16x32_bf16 v[160:163], a[180:183], v[100:103], v[160:163]\n"
    "ds_read_b64_tr_b16 a[144:145], v6 offset:1024\n"
    "ds_read_b64_tr_b16 a[146:147], v6 offset:5120\n"
    "v_mfma_f32_16x16x32_bf16 v[164:167], a[180:183], v[104:107], v[164:167]\n"
    "s_waitcnt lgkmcnt(2)\n"
    "v_exp_f32 v42, v42\n"
    "v_mfma_f32_16x16x32_bf16 v[168:171], a[184:187], v[88:91], v[168:171]\n"
    "s_waitcnt vmcnt(6)\n"
    "v_cvt_pk_bf16_f32 v120, v40, v41\n"
    "v_mfma_f32_16x16x32_bf16 v[172:175], a[184:187], v[92:95], v[172:175]\n"
    "v_exp_f32 v43, v43\n"
    "v_mfma_f32_16x16x32_bf16 v[176:179], a[184:187], v[96:99], v[176:179]\n"
    "v_exp_f32 v44, v44\n"
    "v_mfma_f32_16x16x32_bf16 v[180:183], a[184:187], v[100:103], v[180:183]\n"
    "ds_read_b64_tr_b16 a[148:149], v7 offset:1024\n"
    "v_cvt_pk_bf16_f32 v121, v42, v43\n"
    "v_mfma_f32_16x16x32_bf16 v[184:187], a[184:187], v[104:107], v[184:187]\n"
    "ds_read_b64_tr_b16 a[150:151], v7 offset:5120\n"
    "v_mfma_f32_16x16x32_bf16 v[188:191], a[188:191], v[88:91], v[188:191]\n"
    "buffer_load_dwordx4 v13, s[48:51], s87 offen offset:3072 nt lds\n"
    "v_mfma_f32_16x16x32_bf16 a[100:103], a[188:191], v[92:95], a[100:103]\n"
    "v_exp_f32 v45, v45\n"
    "v_mfma_f32_16x16x32_bf16 a[104:107], a[188:191], v[96:99], a[104:107]\n"
    "v_exp_f32 v46, v46\n"
    "v_mfma_f32_16x16x32_bf16 a[108:111], a[188:191], v[100:103], a[108:111]\n"
    "ds_read_b64_tr_b16 a[152:153], v6 offset:1536\n"
    "v_cvt_pk_bf16_f32 v124, v44, v45\n"
    "v_mfma_f32_16x16x32_bf16 a[112:115], a[188:191], v[104:107], a[112:115]\n"
    "s_waitcnt lgkmcnt(1)\n"
    "ds_read_b64_tr_b16 a[154:155], v6 offset:5632\n"
    "ds_read_b64_tr_b16 a[156:157], v7 offset:1536\n"
    "v_mfma_f32_16x16x32_bf16 a[116:119], a[144:147], v[88:91], a[116:119]\n"
    "s_add_u32 m0, s73, 0x1c000\n"
    "ds_read_b64_tr_b16 a[158:159], v7 offset:5632\n"
    "v_mfma_f32_16x16x32_bf16 a[120:123], a[144:147], v[92:95], a[120:123]\n"
    "s_add_u32 s87, s73, 0x38000\n"
    "v_exp_f32 v47, v47\n"
    "v_mfma_f32_16x16x32_bf16 a[124:127], a[144:147], v[96:99], a[124:127]\n"
    "s_waitcnt vmcnt(6)\n"
    "v_cvt_pk_bf16_f32 v125, v46, v47\n"
    "v_mfma_f32_16x16x32_bf16 a[128:131], a[144:147], v[100:103], a[128:131]\n"
    "v_exp_f32 v48, v48\n"
    "v_mfma_f32_16x16x32_bf16 a[192:195], a[144:147], v[104:107], a[192:195]\n"
    "v_exp_f32 v49, v49\n"
    "v_mfma_f32_16x16x32_bf16 a[196:199], a[148:151], v[88:91], a[196:199]\n"
    "buffer_load_dwordx4 v12, s[48:51], s87 offen nt lds\n"
    "v_mfma_f32_16x16x32_bf16 a[200:203], a[148:151], v[92:95], a[200:203]\n"
    "v_exp_f32 v50, v50\n"
    "v_mfma_f32_16x16x32_bf16 a[204:207], a[148:151], v[96:99], a[204:207]\n"
    "v_cvt_pk_bf16_f32 v110, v48, v49\n"
    "v_mfma_f32_16x16x32_bf16 a[208:211], a[148:151], v[100:103], a[208:211]\n"
    "v_exp_f32 v51, v51\n"
    "v_mfma_f32_16x16x32_bf16 a[212:215], a[148:151], v[104:107], a[212:215]\n"
    "s_waitcnt vmcnt(5) lgkmcnt(0)\n"
    "s_barrier\n"
    "v_mfma_f32_16x16x32_bf16 a[216:219], a[152:155], v[88:91], a[216:219]\n"
    "ds_read_b128 a[160:163], v2\n"
    "v_mfma_f32_16x16x32_bf16 a[220:223], a[152:155], v[92:95], a[220:223]\n"
    "s_waitcnt vmcnt(6)\n"
    "v_exp_f32 v52, v52\n"
    "v_mfma_f32_16x16x32_bf16 a[224:227], a[152:155], v[96:99], a[224:227]\n"
    "v_cvt_pk_bf16_f32 v111, v50, v51\n"
    "v_mfma_f32_16x16x32_bf16 a[228:231], a[152:155], v[100:103], a[228:231]\n"
    "ds_read_b128 a[164:167], v3\n"
    "v_mfma_f32_16x16x32_bf16 a[232:235], a[152:155], v[104:107], a[232:235]\n"
    "v_exp_f32 v53, v53\n"
    "v_mfma_f32_16x16x32_bf16 a[236:239], a[156:159], v[88:91], a[236:239]\n"
    "v_exp_f32 v54, v54\n"
    "v_mfma_f32_16x16x32_bf16 a[240:243], a[156:159], v[92:95], a[240:243]\n"
    "buffer_load_dwordx4 v13, s[48:51], s87 offen offset:1024 nt lds\n"
    "v_mfma_f32_16x16x32_bf16 a[244:247], a[156:159], v[96:99], a[244:247]\n"
    "v_cvt_pk_bf16_f32 v114, v52, v53\n"
    "v_mfma_f32_16x16x32_bf16 a[248:251], a[156:159], v[100:103], a[248:251]\n"
    "ds_read_b128 a[168:171], v4\n"
    "v_mfma_f32_16x16x32_bf16 a[252:255], a[156:159], v[104:107], a[252:255]\n"
    "s_waitcnt lgkmcnt(1)\n"
    "v_exp_f32 v55, v55\n"
    "v_mfma_f32_16x16x32_bf16 v[28:31], a[160:163], a[0:3], v[68:71]\n"
    "v_exp_f32 v56, v56\n"
    "v_mfma_f32_16x16x32_bf16 v[32:35], a[160:163], a[16:19], v[72:75]\n"
    "s_waitcnt vmcnt(6)\n"
    "v_cvt_pk_bf16_f32 v115, v54, v55\n"
    "v_mfma_f32_16x16x32_bf16 v[36:39], a[160:163], a[32:35], v[76:79]\n"
    "v_exp_f32 v57, v57\n"
    "v_mfma_f32_16x16x32_bf16 v[40:43], a[160:163], a[48:51], v[80:83]\n"
    "ds_read_b128 a[172:175], v5\n"
    "v_mfma_f32_16x16x32_bf16 v[44:47], a[160:163], a[64:67], v[84:87]\n"
    "v_exp_f32 v58, v58\n"
    "v_mfma_f32_16x16x32_bf16 v[28:31], a[164:167], a[4:7], v[28:31]\n"
    "v_cvt_pk_bf16_f32 v118, v56, v57\n"
    "v_mfma_f32_16x16x32_bf16 v[32:35], a[164:167], a[20:23], v[32:35]\n"
    "buffer_load_dwordx4 v12, s[48:51], s87 offen offset:2048 nt lds\n"
    "v_mfma_f32_16x16x32_bf16 v[36:39], a[164:167], a[36:39], v[36:39]\n"
    "v_exp_f32 v59, v59\n"
    "v_mfma_f32_16x16x32_bf16 v[40:43], a[164:167], a[52:55], v[40:43]\n"
    "ds_read_b128 a[176:179], v2 offset:4096\n"
    "v_mfma_f32_16x16x32_bf16 v[44:47], a[164:167], a[68:71], v[44:47]\n"
    "s_waitcnt lgkmcnt(1)\n"
    "v_exp_f32 v60, v60\n"
    "v_mfma_f32_16x16x32_bf16 v[28:31], a[168:171], a[8:11], v[28:31]\n"
    "v_cvt_pk_bf16_f32 v119, v58, v59\n"
    "v_mfma_f32_16x16x32_bf16 v[32:35], a[168:171], a[24:27], v[32:35]\n"
    "v_exp_f32 v61, v61\n"
    "v_mfma_f32_16x16x32_bf16 v[36:39], a[168:171], a[40:43], v[36:39]\n"
    "s_waitcnt vmcnt(6)\n"
    "v_exp_f32 v62, v62\n"
    "v_mfma_f32_16x16x32_bf16 v[40:43], a[168:171], a[56:59], v[40:43]\n"
    "ds_read_b128 a[180:183], v3 offset:4096\n"
    "v_cvt_pk_bf16_f32 v122, v60, v61\n"
    "v_mfma_f32_16x16x32_bf16 v[44:47], a[168:171], a[72:75], v[44:47]\n"
    "v_exp_f32 v63, v63\n"
    "v_mfma_f32_16x16x32_bf16 v[28:31], a[172:175], a[12:15], v[28:31]\n"
    "v_exp_f32 v64, v64\n"
    "v_mfma_f32_16x16x32_bf16 v[32:35], a[172:175], a[28:31], v[32:35]\n"
    "v_cvt_pk_bf16_f32 v123, v62, v63\n"
    "v_mfma_f32_16x16x32_bf16 v[36:39], a[172:175], a[44:47], v[36:39]\n"
    "v_exp_f32 v65, v65\n"
    "v_mfma_f32_16x16x32_bf16 v[40:43], a[172:175], a[60:63], v[40:43]\n"
    "v_exp_f32 v66, v66\n"
    "v_mfma_f32_16x16x32_bf16 v[44:47], a[172:175], a[76:79], v[44:47]\n"
    "s_waitcnt lgkmcnt(0)\n"
    "ds_read_b128 a[184:187], v4 offset:4096\n"
    "v_cvt_pk_bf16_f32 v126, v64, v65\n"
    "v_exp_f32 v67, v67\n"
    "s_nop 0\n"
    "v_cvt_pk_bf16_f32 v127, v66, v67\n"
    "v_mfma_f32_16x16x32_bf16 v[48:51], a[176:179], a[0:3], v[68:71]\n"
    "buffer_load_dwordx4 v13, s[48:51], s87 offen offset:3072 nt lds\n"
    "v_mfma_f32_16x16x32_bf16 v[52:55], a[176:179], a[16:19], v[72:75]\n"
    "v_mfma_f32_16x16x32_bf16 v[56:59], a[176:179], a[32:35], v[76:79]\n"
    "s_add_u32 m0, s73, 0x20000\n"
    "v_mfma_f32_16x16x32_bf16 v[60:63], a[176:179], a[48:51], v[80:83]\n"
    "s_add_u32 s86, s73, 0x1c000\n"
    "ds_read_b128 a[188:191], v5 offset:4096\n"
    "v_mfma_f32_16x16x32_bf16 v[64:67], a[176:179], a[64:67], v[84:87]\n"
    "s_waitcnt vmcnt(6)\n"
    "v_exp_f32 v28, v28\n"
    "v_mfma_f32_16x16x32_bf16 v[48:51], a[180:183], a[4:7], v[48:51]\n"
    "v_mfma_f32_16x16x32_bf16 v[52:55], a[180:183], a[20:23], v[52:55]\n"
    "v_exp_f32 v29, v29\n"
    "v_mfma_f32_16x16x32_bf16 v[56:59], a[180:183], a[36:39], v[56:59]\n"
    "buffer_load_dwordx4 v8, s[44:47], s86 offen nt lds\n"
    "v_mfma_f32_16x16x32_bf16 v[60:63], a[180:183], a[52:55], v[60:63]\n"
    "v_exp_f32 v30, v30\n"
    "v_mfma_f32_16x16x32_bf16 v[64:67], a[180:183], a[68:71], v[64:67]\n"
    "s_waitcnt lgkmcnt(0)\n"
    "v_cvt_pk_bf16_f32 v88, v28, v29\n"
    "v_mfma_f32_16x16x32_bf16 v[48:51], a[184:187], a[8:11], v[48:51]\n"
    "v_exp_f32 v31, v31\n"
    "v_mfma_f32_16x16x32_bf16 v[52:55], a[184:187], a[24:27], v[52:55]\n"
    "v_mfma_f32_16x16x32_bf16 v[56:59], a[184:187], a[40:43], v[56:59]\n"
    "v_exp_f32 v32, v32\n"
    "v_mfma_f32_16x16x32_bf16 v[60:63], a[184:187], a[56:59], v[60:63]\n"
    "s_waitcnt vmcnt(6)\n"
    "ds_read_b64_tr_b16 a[144:145], v6 offset:8192\n"
    "v_cvt_pk_bf16_f32 v89, v30, v31\n"
    "v_mfma_f32_16x16x32_bf16 v[64:67], a[184:187], a[72:75], v[64:67]\n"
    "ds_read_b64_tr_b16 a[146:147], v6 offset:12288\n"
    "v_mfma_f32_16x16x32_bf16 v[48:51], a[188:191], a[12:15], v[48:51]\n"
    "v_exp_f32 v33, v33\n"
    "v_mfma_f32_16x16x32_bf16 v[52:55], a[188:191], a[28:31], v[52:55]\n"
    "v_exp_f32 v34, v34\n"
    "v_mfma_f32_16x16x32_bf16 v[56:59], a[188:191], a[44:47], v[56:59]\n"
    "v_cvt_pk_bf16_f32 v92, v32, v33\n"
    "v_mfma_f32_16x16x32_bf16 v[60:63], a[188:191], a[60:63], v[60:63]\n"
    "ds_read_b64_tr_b16 a[148:149], v7 offset:8192\n"
    "ds_read_b64_tr_b16 a[150:151], v7 offset:12288\n"
    "v_mfma_f32_16x16x32_bf16 v[64:67], a[188:191], a[76:79], v[64:67]\n"
    "buffer_load_dwordx4 v9, s[44:47], s86 offen offset:1024 nt lds\n"
    "v_mfma_f32_16x16x32_bf16 a[80:83], a[132:135], v[108:111], a[80:83]\n"
    "v_exp_f32 v35, v35\n"
    "v_mfma_f32_16x16x32_bf16 a[84:87], a[132:135], v[112:115], a[84:87]\n"
    "v_exp_f32 v36, v36\n"
    "v_mfma_f32_16x16x32_bf16 a[88:91], a[132:135], v[116:119], a[88:91]\n"
    "v_cvt_pk_bf16_f32 v93, v34, v35\n"
    "v_mfma_f32_16x16x32_bf16 a[92:95], a[132:135], v[120:123], a[92:95]\n"
    "ds_read_b64_tr_b16 a[152:153], v6 offset:8704\n"
    "ds_read_b64_tr_b16 a[154:155], v6 offset:12800\n"
    "v_mfma_f32_16x16x32_bf16 a[96:99], a[132:135], v[124:127], a[96:99]\n"
    "s_waitcnt lgkmcnt(2)\n"
    "v_exp_f32 v37, v37\n"
    "v_mfma_f32_16x16x32_bf16 v[128:131], a[144:147], v[108:111], v[128:131]\n"
    "s_waitcnt vmcnt(6)\n"
    "v_exp_f32 v38, v38\n"
    "v_mfma_f32_16x16x32_bf16 v[132:135], a[144:147], v[112:115], v[132:135]\n"
    "v_cvt_pk_bf16_f32 v96, v36, v37\n"
    "v_mfma_f32_16x16x32_bf16 v[136:139], a[144:147], v[116:119], v[136:139]\n"
    "v_exp_f32 v39, v39\n"
    "v_mfma_f32_16x16x32_bf16 v[140:143], a[144:147], v[120:123], v[140:143]\n"
    "ds_read_b64_tr_b16 a[156:157], v7 offset:8704\n"
    "ds_read_b64_tr_b16 a[158:159], v7 offset:12800\n"
    "v_mfma_f32_16x16x32_bf16 v[144:147], a[144:147], v[124:127], v[144:147]\n"
    "buffer_load_dwordx4 v10, s[44:47], s86 offen offset:2048 nt lds\n"
    "v_mfma_f32_16x16x32_bf16 v[148:151], a[148:151], v[108:111], v[148:151]\n"
    "v_exp_f32 v40, v40\n"
    "v_mfma_f32_16x16x32_bf16 v[152:155], a[148:151], v[112:115], v[152:155]\n"
    "v_cvt_pk_bf16_f32 v97, v38, v39\n"
    "v_mfma_f32_16x16x32_bf16 v[156:159], a[148:151], v[116:119], v[156:159]\n"
    "v_exp_f32 v41, v41\n"
    "v_mfma_f32_16x16x32_bf16 v[160:163], a[148:151], v[120:123], v[160:163]\n"
    "ds_read_b64_tr_b16 a[160:161], v6 offset:9216\n"
    "ds_read_b64_tr_b16 a[162:163], v6 offset:13312\n"
    "v_mfma_f32_16x16x32_bf16 v[164:167], a[148:151], v[124:127], v[164:167]\n"
    "s_waitcnt lgkmcnt(2)\n"
    "v_exp_f32 v42, v42\n"
    "v_mfma_f32_16x16x32_bf16 v[168:171], a[152:155], v[108:111], v[168:171]\n"
    "s_waitcnt vmcnt(6)\n"
    "v_cvt_pk_bf16_f32 v100, v40, v41\n"
    "v_mfma_f32_16x16x32_bf16 v[172:175], a[152:155], v[112:115], v[172:175]\n"
    "v_exp_f32 v43, v43\n"
    "v_mfma_f32_16x16x32_bf16 v[176:179], a[152:155], v[116:119], v[176:179]\n"
    "v_exp_f32 v44, v44\n"
    "v_mfma_f32_16x16x32_bf16 v[180:183], a[152:155], v[120:123], v[180:183]\n"
    "ds_read_b64_tr_b16 a[164:165], v7 offset:9216\n"
    "v_cvt_pk_bf16_f32 v101, v42, v43\n"
    "v_mfma_f32_16x16x32_bf16 v[184:187], a[152:155], v[124:127], v[184:187]\n"
    "ds_read_b64_tr_b16 a[166:167], v7 offset:13312\n"
    "v_mfma_f32_16x16x32_bf16 v[188:191], a[156:159], v[108:111], v[188:191]\n"
    "buffer_load_dwordx4 v11, s[44:47], s86 offen offset:3072 nt lds\n"
    "v_mfma_f32_16x16x32_bf16 a[100:103], a[156:159], v[112:115], a[100:103]\n"
    "v_exp_f32 v45, v45\n"
    "v_mfma_f32_16x16x32_bf16 a[104:107], a[156:159], v[116:119], a[104:107]\n"
    "v_exp_f32 v46, v46\n"
    "v_mfma_f32_16x16x32_bf16 a[108:111], a[156:159], v[120:123], a[108:111]\n"
    "ds_read_b64_tr_b16 a[168:169], v6 offset:9728\n"
    "v_cvt_pk_bf16_f32 v104, v44, v45\n"
    "v_mfma_f32_16x16x32_bf16 a[112:115], a[156:159], v[124:127], a[112:115]\n"
    "s_waitcnt lgkmcnt(1)\n"
    "ds_read_b64_tr_b16 a[170:171], v6 offset:13824\n"
    "ds_read_b64_tr_b16 a[172:173], v7 offset:9728\n"
    "v_add_u32 v6, 0x10000, v6\n"
    "v_mfma_f32_16x16x32_bf16 a[116:119], a[160:163], v[108:111], a[116:119]\n"
    "s_add_u32 m0, s73, 0x24000\n"
    "ds_read_b64_tr_b16 a[174:175], v7 offset:13824\n"
    "v_add_u32 v7, 0x10000, v7\n"
    "v_mfma_f32_16x16x32_bf16 a[120:123], a[160:163], v[112:115], a[120:123]\n"
    "s_add_u32 s86, s73, 0x3c000\n"
    "v_exp_f32 v47, v47\n"
    "v_mfma_f32_16x16x32_bf16 a[124:127], a[160:163], v[116:119], a[124:127]\n"
    "s_waitcnt vmcnt(6)\n"
    "v_cvt_pk_bf16_f32 v105, v46, v47\n"
    "v_mfma_f32_16x16x32_bf16 a[128:131], a[160:163], v[120:123], a[128:131]\n"
    "v_exp_f32 v48, v48\n"
    "v_mfma_f32_16x16x32_bf16 a[192:195], a[160:163], v[124:127], a[192:195]\n"
    "v_exp_f32 v49, v49\n"
    "v_mfma_f32_16x16x32_bf16 a[196:199], a[164:167], v[108:111], a[196:199]\n"
    "buffer_load_dwordx4 v8, s[44:47], s86 offen nt lds\n"
    "v_mfma_f32_16x16x32_bf16 a[200:203], a[164:167], v[112:115], a[200:203]\n"
    "v_exp_f32 v50, v50\n"
    "v_mfma_f32_16x16x32_bf16 a[204:207], a[164:167], v[116:119], a[204:207]\n"
    "v_cvt_pk_bf16_f32 v90, v48, v49\n"
    "v_mfma_f32_16x16x32_bf16 a[208:211], a[164:167], v[120:123], a[208:211]\n"
    "v_exp_f32 v51, v51\n"
    "v_mfma_f32_16x16x32_bf16 a[212:215], a[164:167], v[124:127], a[212:215]\n"
    "s_waitcnt vmcnt(5) lgkmcnt(0)\n"
    "s_barrier\n"
    "v_mfma_f32_16x16x32_bf16 a[216:219], a[168:171], v[108:111], a[216:219]\n"
    "s_waitcnt vmcnt(6)\n"
    "ds_read_b128 a[176:179], v2 offset:8192\n"
    "v_mfma_f32_16x16x32_bf16 a[220:223], a[168:171], v[112:115], a[220:223]\n"
    "v_exp_f32 v52, v52\n"
    "v_mfma_f32_16x16x32_bf16 a[224:227], a[168:171], v[116:119], a[224:227]\n"
    "v_cvt_pk_bf16_f32 v91, v50, v51\n"
    "v_mfma_f32_16x16x32_bf16 a[228:231], a[168:171], v[120:123], a[228:231]\n"
    "ds_read_b128 a[180:183], v3 offset:8192\n"
    "v_mfma_f32_16x16x32_bf16 a[232:235], a[168:171], v[124:127], a[232:235]\n"
    "v_exp_f32 v53, v53\n"
    "v_mfma_f32_16x16x32_bf16 a[236:239], a[172:175], v[108:111], a[236:239]\n"
    "buffer_load_dwordx4 v9, s[44:47], s86 offen offset:1024 nt lds\n"
    "v_mfma_f32_16x16x32_bf16 a[240:243], a[172:175], v[112:115], a[240:243]\n"
    "v_exp_f32 v54, v54\n"
    "v_mfma_f32_16x16x32_bf16 a[244:247], a[172:175], v[116:119], a[244:247]\n"
    "v_cvt_pk_bf16_f32 v94, v52, v53\n"
    "v_mfma_f32_16x16x32_bf16 a[248:251], a[172:175], v[120:123], a[248:251]\n"
    "ds_read_b128 a[184:187], v4 offset:8192\n"
    "v_mfma_f32_16x16x32_bf16 a[252:255], a[172:175], v[124:127], a[252:255]\n"
    "s_waitcnt lgkmcnt(1)\n"
    "v_exp_f32 v55, v55\n"
    "v_mfma_f32_16x16x32_bf16 v[28:31], a[176:179], a[0:3], v[68:71]\n"
    "v_exp_f32 v56, v56\n"
    "v_mfma_f32_16x16x32_bf16 v[32:35], a[176:179], a[16:19], v[72:75]\n"
    "s_waitcnt vmcnt(6)\n"
    "v_cvt_pk_bf16_f32 v95, v54, v55\n"
    "v_mfma_f32_16x16x32_bf16 v[36:39], a[176:179], a[32:35], v[76:79]\n"
    "v_exp_f32 v57, v57\n"
    "v_mfma_f32_16x16x32_bf16 v[40:43], a[176:179], a[48:51], v[80:83]\n"
    "ds_read_b128 a[188:191], v5 offset:8192\n"
    "v_mfma_f32_16x16x32_bf16 v[44:47], a[176:179], a[64:67], v[84:87]\n"
    "v_exp_f32 v58, v58\n"
    "v_mfma_f32_16x16x32_bf16 v[28:31], a[180:183], a[4:7], v[28:31]\n"
    "v_cvt_pk_bf16_f32 v98, v56, v57\n"
    "v_mfma_f32_16x16x32_bf16 v[32:35], a[180:183], a[20:23], v[32:35]\n"
    "buffer_load_dwordx4 v10, s[44:47], s86 offen offset:2048 nt lds\n"
    "v_mfma_f32_16x16x32_bf16 v[36:39], a[180:183], a[36:39], v[36:39]\n"
    "v_exp_f32 v59, v59\n"
    "v_mfma_f32_16x16x32_bf16 v[40:43], a[180:183], a[52:55], v[40:43]\n"
    "ds_read_b128 a[144:147], v2 offset:12288\n"
    "v_add_u32 v2, 0x10000, v2\n"
    "v_mfma_f32_16x16x32_bf16 v[44:47], a[180:183], a[68:71], v[44:47]\n"
    "s_waitcnt lgkmcnt(1)\n"
    "v_exp_f32 v60, v60\n"
    "v_mfma_f32_16x16x32_bf16 v[28:31], a[184:187], a[8:11], v[28:31]\n"
    "v_cvt_pk_bf16_f32 v99, v58, v59\n"
    "v_mfma_f32_16x16x32_bf16 v[32:35], a[184:187], a[24:27], v[32:35]\n"
    "v_exp_f32 v61, v61\n"
    "v_mfma_f32_16x16x32_bf16 v[36:39], a[184:187], a[40:43], v[36:39]\n"
    "s_waitcnt vmcnt(6)\n"
    "v_exp_f32 v62, v62\n"
    "v_mfma_f32_16x16x32_bf16 v[40:43], a[184:187], a[56:59], v[40:43]\n"
    "ds_read_b128 a[148:151], v3 offset:12288\n"
    "v_cvt_pk_bf16_f32 v102, v60, v61\n"
    "v_add_u32 v3, 0x10000, v3\n"
    "v_mfma_f32_16x16x32_bf16 v[44:47], a[184:187], a[72:75], v[44:47]\n"
    "v_exp_f32 v63, v63\n"
    "v_mfma_f32_16x16x32_bf16 v[28:31], a[188:191], a[12:15], v[28:31]\n"
    "v_exp_f32 v64, v64\n"
    "v_mfma_f32_16x16x32_bf16 v[32:35], a[188:191], a[28:31], v[32:35]\n"
    "v_cvt_pk_bf16_f32 v103, v62, v63\n"
    "v_mfma_f32_16x16x32_bf16 v[36:39], a[188:191], a[44:47], v[36:39]\n"
    "v_exp_f32 v65, v65\n"
    "v_mfma_f32_16x16x32_bf16 v[40:43], a[188:191], a[60:63], v[40:43]\n"
    "v_exp_f32 v66, v66\n"
    "v_mfma_f32_16x16x32_bf16 v[44:47], a[188:191], a[76:79], v[44:47]\n"
    "s_waitcnt lgkmcnt(0)\n"
    "ds_read_b128 a[152:155], v4 offset:12288\n"
    "v_cvt_pk_bf16_f32 v106, v64, v65\n"
    "v_exp_f32 v67, v67\n"
    "s_nop 0\n"
    "v_cvt_pk_bf16_f32 v107, v66, v67\n"
    "v_add_u32 v4, 0x10000, v4\n"
    "v_mfma_f32_16x16x32_bf16 v[48:51], a[144:147], a[0:3], v[68:71]\n"
    "buffer_load_dwordx4 v11, s[44:47], s86 offen offset:3072 nt lds\n"
    "v_mfma_f32_16x16x32_bf16 v[52:55], a[144:147], a[16:19], v[72:75]\n"
    "v_mfma_f32_16x16x32_bf16 v[56:59], a[144:147], a[32:35], v[76:79]\n"
    "s_add_u32 m0, s73, 0\n"
    "v_mfma_f32_16x16x32_bf16 v[60:63], a[144:147], a[48:51], v[80:83]\n"
    "s_add_u32 s87, s73, 0x1c000\n"
    "ds_read_b128 a[156:159], v5 offset:12288\n"
    "v_add_u32 v5, 0x10000, v5\n"
    "v_mfma_f32_16x16x32_bf16 v[64:67], a[144:147], a[64:67], v[84:87]\n"
    "s_waitcnt vmcnt(6)\n"
    "v_exp_f32 v28, v28\n"
    "v_mfma_f32_16x16x32_bf16 v[48:51], a[148:151], a[4:7], v[48:51]\n"
    "v_mfma_f32_16x16x32_bf16 v[52:55], a[148:151], a[20:23], v[52:55]\n"
    "v_exp_f32 v29, v29\n"
    "v_mfma_f32_16x16x32_bf16 v[56:59], a[148:151], a[36:39], v[56:59]\n"
    "buffer_load_dwordx4 v12, s[48:51], s87 offen nt lds\n"
    "v_mfma_f32_16x16x32_bf16 v[60:63], a[148:151], a[52:55], v[60:63]\n"
    "v_exp_f32 v30, v30\n"
    "v_mfma_f32_16x16x32_bf16 v[64:67], a[148:151], a[68:71], v[64:67]\n"
    "s_waitcnt lgkmcnt(0)\n"
    "v_cvt_pk_bf16_f32 v108, v28, v29\n"
    "v_mfma_f32_16x16x32_bf16 v[48:51], a[152:155], a[8:11], v[48:51]\n"
    "v_exp_f32 v31, v31\n"
    "v_mfma_f32_16x16x32_bf16 v[52:55], a[152:155], a[24:27], v[52:55]\n"
    "v_mfma_f32_16x16x32_bf16 v[56:59], a[152:155], a[40:43], v[56:59]\n"
    "v_exp_f32 v32, v32\n"
    "v_mfma_f32_16x16x32_bf16 v[60:63], a[152:155], a[56:59], v[60:63]\n"
    "s_waitcnt vmcnt(6)\n"
    "ds_read_b64_tr_b16 a[160:161], v6\n"
    "v_cvt_pk_bf16_f32 v109, v30, v31\n"
    "v_mfma_f32_16x16x32_bf16 v[64:67], a[152:155], a[72:75], v[64:67]\n"
    "ds_read_b64_tr_b16 a[162:163], v6 offset:4096\n"
    "v_mfma_f32_16x16x32_bf16 v[48:51], a[156:159], a[12:15], v[48:51]\n"
    "v_exp_f32 v33, v33\n"
    "v_mfma_f32_16x16x32_bf16 v[52:55], a[156:159], a[28:31], v[52:55]\n"
    "v_exp_f32 v34, v34\n"
    "v_mfma_f32_16x16x32_bf16 v[56:59], a[156:159], a[44:47], v[56:59]\n"
    "v_cvt_pk_bf16_f32 v112, v32, v33\n"
    "v_mfma_f32_16x16x32_bf16 v[60:63], a[156:159], a[60:63], v[60:63]\n"
    "ds_read_b64_tr_b16 a[164:165], v7\n"
    "ds_read_b64_tr_b16 a[166:167], v7 offset:4096\n"
    "v_mfma_f32_16x16x32_bf16 v[64:67], a[156:159], a[76:79], v[64:67]\n"
    "buffer_load_dwordx4 v13, s[48:51], s87 offen offset:1024 nt lds\n"
    "v_mfma_f32_16x16x32_bf16 a[80:83], a[132:135], v[88:91], a[80:83]\n"
    "v_exp_f32 v35, v35\n"
    "v_mfma_f32_16x16x32_bf16 a[84:87], a[132:135], v[92:95], a[84:87]\n"
    "v_exp_f32 v36, v36\n"
    "v_mfma_f32_16x16x32_bf16 a[88:91], a[132:135], v[96:99], a[88:91]\n"
    "v_cvt_pk_bf16_f32 v113, v34, v35\n"
    "v_mfma_f32_16x16x32_bf16 a[92:95], a[132:135], v[100:103], a[92:95]\n"
    "ds_read_b64_tr_b16 a[168:169], v6 offset:512\n"
    "ds_read_b64_tr_b16 a[170:171], v6 offset:4608\n"
    "v_mfma_f32_16x16x32_bf16 a[96:99], a[132:135], v[104:107], a[96:99]\n"
    "s_waitcnt lgkmcnt(2)\n"
    "v_exp_f32 v37, v37\n"
    "v_mfma_f32_16x16x32_bf16 v[128:131], a[160:163], v[88:91], v[128:131]\n"
    "s_waitcnt vmcnt(6)\n"
    "v_exp_f32 v38, v38\n"
    "v_mfma_f32_16x16x32_bf16 v[132:135], a[160:163], v[92:95], v[132:135]\n"
    "v_cvt_pk_bf16_f32 v116, v36, v37\n"
    "v_mfma_f32_16x16x32_bf16 v[136:139], a[160:163], v[96:99], v[136:139]\n"
    "v_exp_f32 v39, v39\n"
    "v_mfma_f32_16x16x32_bf16 v[140:143], a[160:163], v[100:103], v[140:143]\n"
    "ds_read_b64_tr_b16 a[172:173], v7 offset:512\n"
    "ds_read_b64_tr_b16 a[174:175], v7 offset:4608\n"
    "v_mfma_f32_16x16x32_bf16 v[144:147], a[160:163], v[104:107], v[144:147]\n"
    "buffer_load_dwordx4 v12, s[48:51], s87 offen offset:2048 nt lds\n"
    "v_mfma_f32_16x16x32_bf16 v[148:151], a[164:167], v[88:91], v[148:151]\n"
    "v_exp_f32 v40, v40\n"
    "v_mfma_f32_16x16x32_bf16 v[152:155], a[164:167], v[92:95], v[152:155]\n"
    "v_cvt_pk_bf16_f32 v117, v38, v39\n"
    "v_mfma_f32_16x16x32_bf16 v[156:159], a[164:167], v[96:99], v[156:159]\n"
    "v_exp_f32 v41, v41\n"
    "v_mfma_f32_16x16x32_bf16 v[160:163], a[164:167], v[100:103], v[160:163]\n"
    "ds_read_b64_tr_b16 a[176:177], v6 offset:1024\n"
    "ds_read_b64_tr_b16 a[178:179], v6 offset:5120\n"
    "v_mfma_f32_16x16x32_bf16 v[164:167], a[164:167], v[104:107], v[164:167]\n"
    "s_waitcnt lgkmcnt(2)\n"
    "v_exp_f32 v42, v42\n"
    "v_mfma_f32_16x16x32_bf16 v[168:171], a[168:171], v[88:91], v[168:171]\n"
    "s_waitcnt vmcnt(6)\n"
    "v_cvt_pk_bf16_f32 v120, v40, v41\n"
    "v_mfma_f32_16x16x32_bf16 v[172:175], a[168:171], v[92:95], v[172:175]\n"
    "v_exp_f32 v43, v43\n"
    "v_mfma_f32_16x16x32_bf16 v[176:179], a[168:171], v[96:99], v[176:179]\n"
    "v_exp_f32 v44, v44\n"
    "v_mfma_f32_16x16x32_bf16 v[180:183], a[168:171], v[100:103], v[180:183]\n"
    "ds_read_b64_tr_b16 a[180:181], v7 offset:1024\n"
    "v_cvt_pk_bf16_f32 v121, v42, v43\n"
    "v_mfma_f32_16x16x32_bf16 v[184:187], a[168:171], v[104:107], v[184:187]\n"
    "ds_read_b64_tr_b16 a[182:183], v7 offset:5120\n"
    "v_mfma_f32_16x16x32_bf16 v[188:191], a[172:175], v[88:91], v[188:191]\n"
    "buffer_load_dwordx4 v13, s[48:51], s87 offen offset:3072 nt lds\n"
    "v_mfma_f32_16x16x32_bf16 a[100:103], a[172:175], v[92:95], a[100:103]\n"
    "v_exp_f32 v45, v45\n"
    "v_mfma_f32_16x16x32_bf16 a[104:107], a[172:175], v[96:99], a[104:107]\n"
    "v_exp_f32 v46, v46\n"
    "v_mfma_f32_16x16x32_bf16 a[108:111], a[172:175], v[100:103], a[108:111]\n"
    "ds_read_b64_tr_b16 a[184:185], v6 offset:1536\n"
    "v_cvt_pk_bf16_f32 v124, v44, v45\n"
    "v_mfma_f32_16x16x32_bf16 a[112:115], a[172:175], v[104:107], a[112:115]\n"
    "s_waitcnt lgkmcnt(1)\n"
    "ds_read_b64_tr_b16 a[186:187], v6 offset:5632\n"
    "ds_read_b64_tr_b16 a[188:189], v7 offset:1536\n"
    "v_mfma_f32_16x16x32_bf16 a[116:119], a[176:179], v[88:91], a[116:119]\n"
    "s_add_u32 m0, s73, 0x4000\n"
    "ds_read_b64_tr_b16 a[190:191], v7 offset:5632\n"
    "v_mfma_f32_16x16x32_bf16 a[120:123], a[176:179], v[92:95], a[120:123]\n"
    "s_add_u32 s87, s73, 0x3c000\n"
    "v_exp_f32 v47, v47\n"
    "v_mfma_f32_16x16x32_bf16 a[124:127], a[176:179], v[96:99], a[124:127]\n"
    "s_waitcnt vmcnt(6)\n"
    "v_cvt_pk_bf16_f32 v125, v46, v47\n"
    "v_mfma_f32_16x16x32_bf16 a[128:131], a[176:179], v[100:103], a[128:131]\n"
    "v_exp_f32 v48, v48\n"
    "v_mfma_f32_16x16x32_bf16 a[192:195], a[176:179], v[104:107], a[192:195]\n"
    "v_exp_f32 v49, v49\n"
    "v_mfma_f32_16x16x32_bf16 a[196:199], a[180:183], v[88:91], a[196:199]\n"
    "buffer_load_dwordx4 v12, s[48:51], s87 offen nt lds\n"
    "v_mfma_f32_16x16x32_bf16 a[200:203], a[180:183], v[92:95], a[200:203]\n"
    "v_exp_f32 v50, v50\n"
    "v_mfma_f32_16x16x32_bf16 a[204:207], a[180:183], v[96:99], a[204:207]\n"
    "v_cvt_pk_bf16_f32 v110, v48, v49\n"
    "v_mfma_f32_16x16x32_bf16 a[208:211], a[180:183], v[100:103], a[208:211]\n"
    "v_exp_f32 v51, v51\n"
    "v_mfma_f32_16x16x32_bf16 a[212:215], a[180:183], v[104:107], a[212:215]\n"
    "s_waitcnt vmcnt(5) lgkmcnt(0)\n"
    "s_barrier\n"
    "v_mfma_f32_16x16x32_bf16 a[216:219], a[184:187], v[88:91], a[216:219]\n"
    "s_waitcnt vmcnt(6)\n"
    "ds_read_b128 a[144:147], v2\n"
    "v_mfma_f32_16x16x32_bf16 a[220:223], a[184:187], v[92:95], a[220:223]\n"
    "v_exp_f32 v52, v52\n"
    "v_mfma_f32_16x16x32_bf16 a[224:227], a[184:187], v[96:99], a[224:227]\n"
    "v_cvt_pk_bf16_f32 v111, v50, v51\n"
    "v_mfma_f32_16x16x32_bf16 a[228:231], a[184:187], v[100:103], a[228:231]\n"
    "ds_read_b128 a[148:151], v3\n"
    "v_mfma_f32_16x16x32_bf16 a[232:235], a[184:187], v[104:107], a[232:235]\n"
    "v_exp_f32 v53, v53\n"
    "v_mfma_f32_16x16x32_bf16 a[236:239], a[188:191], v[88:91], a[236:239]\n"
    "buffer_load_dwordx4 v13, s[48:51], s87 offen offset:1024 nt lds\n"
    "v_mfma_f32_16x16x32_bf16 a[240:243], a[188:191], v[92:95], a[240:243]\n"
    "v_exp_f32 v54, v54\n"
    "v_mfma_f32_16x16x32_bf16 a[244:247], a[188:191], v[96:99], a[244:247]\n"
    "v_cvt_pk_bf16_f32 v114, v52, v53\n"
    "v_mfma_f32_16x16x32_bf16 a[248:251], a[188:191], v[100:103], a[248:251]\n"
    "ds_read_b128 a[152:155], v4\n"
    "v_mfma_f32_16x16x32_bf16 a[252:255], a[188:191], v[104:107], a[252:255]\n"
    "s_waitcnt lgkmcnt(1)\n"
    "v_exp_f32 v55, v55\n"
    "v_mfma_f32_16x16x32_bf16 v[28:31], a[144:147], a[0:3], v[68:71]\n"
    "v_exp_f32 v56, v56\n"
    "v_mfma_f32_16x16x32_bf16 v[32:35], a[144:147], a[16:19], v[72:75]\n"
    "s_waitcnt vmcnt(6)\n"
    "v_cvt_pk_bf16_f32 v115, v54, v55\n"
    "v_mfma_f32_16x16x32_bf16 v[36:39], a[144:147], a[32:35], v[76:79]\n"
    "v_exp_f32 v57, v57\n"
    "v_mfma_f32_16x16x32_bf16 v[40:43], a[144:147], a[48:51], v[80:83]\n"
    "ds_read_b128 a[156:159], v5\n"
    "v_mfma_f32_16x16x32_bf16 v[44:47], a[144:147], a[64:67], v[84:87]\n"
    "v_exp_f32 v58, v58\n"
    "v_mfma_f32_16x16x32_bf16 v[28:31], a[148:151], a[4:7], v[28:31]\n"
    "v_cvt_pk_bf16_f32 v118, v56, v57\n"
    "v_mfma_f32_16x16x32_bf16 v[32:35], a[148:151], a[20:23], v[32:35]\n"
    "buffer_load_dwordx4 v12, s[48:51], s87 offen offset:2048 nt lds\n"
    "v_mfma_f32_16x16x32_bf16 v[36:39], a[148:151], a[36:39], v[36:39]\n"
    "v_exp_f32 v59, v59\n"
    "v_mfma_f32_16x16x32_bf16 v[40:43], a[148:151], a[52:55], v[40:43]\n"
    "ds_read_b128 a[160:163], v2 offset:4096\n"
    "v_mfma_f32_16x16x32_bf16 v[44:47], a[148:151], a[68:71], v[44:47]\n"
    "s_waitcnt lgkmcnt(1)\n"
    "v_exp_f32 v60, v60\n"
    "v_mfma_f32_16x16x32_bf16 v[28:31], a[152:155], a[8:11], v[28:31]\n"
    "v_cvt_pk_bf16_f32 v119, v58, v59\n"
    "v_mfma_f32_16x16x32_bf16 v[32:35], a[152:155], a[24:27], v[32:35]\n"
    "v_exp_f32 v61, v61\n"
    "v_mfma_f32_16x16x32_bf16 v[36:39], a[152:155], a[40:43], v[36:39]\n"
    "s_waitcnt vmcnt(6)\n"
    "v_exp_f32 v62, v62\n"
    "v_mfma_f32_16x16x32_bf16 v[40:43], a[152:155], a[56:59], v[40:43]\n"
    "ds_read_b128 a[164:167], v3 offset:4096\n"
    "v_cvt_pk_bf16_f32 v122, v60, v61\n"
    "v_mfma_f32_16x16x32_bf16 v[44:47], a[152:155], a[72:75], v[44:47]\n"
    "v_exp_f32 v63, v63\n"
    "v_mfma_f32_16x16x32_bf16 v[28:31], a[156:159], a[12:15], v[28:31]\n"
    "v_exp_f32 v64, v64\n"
    "v_mfma_f32_16x16x32_bf16 v[32:35], a[156:159], a[28:31], v[32:35]\n"
    "v_cvt_pk_bf16_f32 v123, v62, v63\n"
    "v_mfma_f32_16x16x32_bf16 v[36:39], a[156:159], a[44:47], v[36:39]\n"
    "v_exp_f32 v65, v65\n"
    "v_mfma_f32_16x16x32_bf16 v[40:43], a[156:159], a[60:63], v[40:43]\n"
    "v_exp_f32 v66, v66\n"
    "v_mfma_f32_16x16x32_bf16 v[44:47], a[156:159], a[76:79], v[44:47]\n"
    "s_waitcnt lgkmcnt(0)\n"
    "ds_read_b128 a[168:171], v4 offset:4096\n"
    "v_cvt_pk_bf16_f32 v126, v64, v65\n"
    "v_exp_f32 v67, v67\n"
    "s_nop 0\n"
    "v_cvt_pk_bf16_f32 v127, v66, v67\n"
    "v_mfma_f32_16x16x32_bf16 v[48:51], a[160:163], a[0:3], v[68:71]\n"
    "buffer_load_dwordx4 v13, s[48:51], s87 offen offset:3072 nt lds\n"
    "v_mfma_f32_16x16x32_bf16 v[52:55], a[160:163], a[16:19], v[72:75]\n"
    "v_mfma_f32_16x16x32_bf16 v[56:59], a[160:163], a[32:35], v[76:79]\n"
    "s_add_u32 m0, s85, 0x8000\n"
    "v_mfma_f32_16x16x32_bf16 v[60:63], a[160:163], a[48:51], v[80:83]\n"
    "s_add_u32 s98, s40, 0\n"
    "ds_read_b128 a[172:175], v5 offset:4096\n"
    "v_mfma_f32_16x16x32_bf16 v[64:67], a[160:163], a[64:67], v[84:87]\n"
    "v_exp_f32 v28, v28\n"
    "v_mfma_f32_16x16x32_bf16 v[48:51], a[164:167], a[4:7], v[48:51]\n"
    "v_exp_f32 v29, v29\n"
    "v_mfma_f32_16x16x32_bf16 v[52:55], a[164:167], a[20:23], v[52:55]\n"
    "v_exp_f32 v30, v30\n"
    "v_mfma_f32_16x16x32_bf16 v[56:59], a[164:167], a[36:39], v[56:59]\n"
    "buffer_load_dwordx4 v23, s[60:63], s98 offen lds\n"
    "v_mfma_f32_16x16x32_bf16 v[60:63], a[164:167], a[52:55], v[60:63]\n"
    "ds_read_b128 a[176:179], v2 offset:8192\n"
    "v_cvt_pk_bf16_f32 v88, v28, v29\n"
    "v_mfma_f32_16x16x32_bf16 v[64:67], a[164:167], a[68:71], v[64:67]\n"
    "s_waitcnt lgkmcnt(1)\n"
    "v_exp_f32 v31, v31\n"
    "v_mfma_f32_16x16x32_bf16 v[48:51], a[168:171], a[8:11], v[48:51]\n"
    "v_exp_f32 v32, v32\n"
    "v_mfma_f32_16x16x32_bf16 v[52:55], a[168:171], a[24:27], v[52:55]\n"
    "v_cvt_pk_bf16_f32 v89, v30, v31\n"
    "v_mfma_f32_16x16x32_bf16 v[56:59], a[168:171], a[40:43], v[56:59]\n"
    "v_exp_f32 v33, v33\n"
    "v_mfma_f32_16x16x32_bf16 v[60:63], a[168:171], a[56:59], v[60:63]\n"
    "ds_read_b128 a[180:183], v3 offset:8192\n"
    "v_mfma_f32_16x16x32_bf16 v[64:67], a[168:171], a[72:75], v[64:67]\n"
    "buffer_load_dwordx4 v24, s[60:63], s98 offen offset:1024 lds\n"
    "v_mfma_f32_16x16x32_bf16 v[48:51], a[172:175], a[12:15], v[48:51]\n"
    "v_exp_f32 v34, v34\n"
    "v_mfma_f32_16x16x32_bf16 v[52:55], a[172:175], a[28:31], v[52:55]\n"
    "v_cvt_pk_bf16_f32 v92, v32, v33\n"
    "v_mfma_f32_16x16x32_bf16 v[56:59], a[172:175], a[44:47], v[56:59]\n"
    "v_exp_f32 v35, v35\n"
    "v_mfma_f32_16x16x32_bf16 v[60:63], a[172:175], a[60:63], v[60:63]\n"
    "ds_read_b128 a[184:187], v4 offset:8192\n"
    "v_mfma_f32_16x16x32_bf16 v[64:67], a[172:175], a[76:79], v[64:67]\n"
    "s_waitcnt lgkmcnt(1)\n"
    "v_exp_f32 v36, v36\n"
    "v_mfma_f32_16x16x32_bf16 v[192:195], a[176:179], a[0:3], v[68:71]\n"
    "v_cvt_pk_bf16_f32 v93, v34, v35\n"
    "v_mfma_f32_16x16x32_bf16 v[196:199], a[176:179], a[16:19], v[72:75]\n"
    "v_exp_f32 v37, v37\n"
    "v_mfma_f32_16x16x32_bf16 v[200:203], a[176:179], a[32:35], v[76:79]\n"
    "v_exp_f32 v38, v38\n"
    "v_mfma_f32_16x16x32_bf16 v[204:207], a[176:179], a[48:51], v[80:83]\n"
    "ds_read_b128 a[188:191], v5 offset:8192\n"
    "v_cvt_pk_bf16_f32 v96, v36, v37\n"
    "v_mfma_f32_16x16x32_bf16 v[208:211], a[176:179], a[64:67], v[84:87]\n"
    "buffer_load_dwordx4 v25, s[60:63], s98 offen offset:2048 lds\n"
    "v_mfma_f32_16x16x32_bf16 v[192:195], a[180:183], a[4:7], v[192:195]\n"
    "v_exp_f32 v39, v39\n"
    "v_mfma_f32_16x16x32_bf16 v[196:199], a[180:183], a[20:23], v[196:199]\n"
    "v_exp_f32 v40, v40\n"
    "v_mfma_f32_16x16x32_bf16 v[200:203], a[180:183], a[36:39], v[200:203]\n"
    "v_cvt_pk_bf16_f32 v97, v38, v39\n"
    "v_mfma_f32_16x16x32_bf16 v[204:207], a[180:183], a[52:55], v[204:207]\n"
    "ds_read_b128 a[144:147], v2 offset:12288\n"
    "v_mfma_f32_16x16x32_bf16 v[208:211], a[180:183], a[68:71], v[208:211]\n"
    "s_waitcnt lgkmcnt(1)\n"
    "v_exp_f32 v41, v41\n"
    "v_mfma_f32_16x16x32_bf16 v[192:195], a[184:187], a[8:11], v[192:195]\n"
    "v_exp_f32 v42, v42\n"
    "v_mfma_f32_16x16x32_bf16 v[196:199], a[184:187], a[24:27], v[196:199]\n"
    "v_cvt_pk_bf16_f32 v100, v40, v41\n"
    "v_mfma_f32_16x16x32_bf16 v[200:203], a[184:187], a[40:43], v[200:203]\n"
    "v_exp_f32 v43, v43\n"
    "v_mfma_f32_16x16x32_bf16 v[204:207], a[184:187], a[56:59], v[204:207]\n"
    "ds_read_b128 a[148:151], v3 offset:12288\n"
    "v_mfma_f32_16x16x32_bf16 v[208:211], a[184:187], a[72:75], v[208:211]\n"
    "buffer_load_dwordx4 v26, s[60:63], s98 offen offset:3072 lds\n"
    "v_mfma_f32_16x16x32_bf16 v[192:195], a[188:191], a[12:15], v[192:195]\n"
    "s_add_u32 m0, s85, 0x9000\n"
    "v_exp_f32 v44, v44\n"
    "v_mfma_f32_16x16x32_bf16 v[196:199], a[188:191], a[28:31], v[196:199]\n"
    "v_cvt_pk_bf16_f32 v101, v42, v43\n"
    "v_mfma_f32_16x16x32_bf16 v[200:203], a[188:191], a[44:47], v[200:203]\n"
    "v_exp_f32 v45, v45\n"
    "v_mfma_f32_16x16x32_bf16 v[204:207], a[188:191], a[60:63], v[204:207]\n"
    "ds_read_b128 a[152:155], v4 offset:12288\n"
    "v_mfma_f32_16x16x32_bf16 v[208:211], a[188:191], a[76:79], v[208:211]\n"
    "s_waitcnt lgkmcnt(1)\n"
    "v_exp_f32 v46, v46\n"
    "v_mfma_f32_16x16x32_bf16 v[212:215], a[144:147], a[0:3], v[68:71]\n"
    "v_cvt_pk_bf16_f32 v104, v44, v45\n"
    "v_mfma_f32_16x16x32_bf16 v[216:219], a[144:147], a[16:19], v[72:75]\n"
    "v_exp_f32 v47, v47\n"
    "v_mfma_f32_16x16x32_bf16 v[220:223], a[144:147], a[32:35], v[76:79]\n"
    "buffer_load_dwordx4 v27, s[60:63], s98 offen lds\n"
    "v_mfma_f32_16x16x32_bf16 v[224:227], a[144:147], a[48:51], v[80:83]\n"
    "s_add_u32 m0, s85, 0xd000\n"
    "ds_read_b128 a[156:159], v5 offset:12288\n"
    "v_cvt_pk_bf16_f32 v105, v46, v47\n"
    "v_mfma_f32_16x16x32_bf16 v[228:231], a[144:147], a[64:67], v[84:87]\n"
    "s_add_u32 s98, s40, 0xa000\n"
    "v_exp_f32 v48, v48\n"
    "v_mfma_f32_16x16x32_bf16 v[212:215], a[148:151], a[4:7], v[212:215]\n"
    "buffer_load_dwordx4 v23, s[60:63], s98 offen lds\n"
    "v_mfma_f32_16x16x32_bf16 v[216:219], a[148:151], a[20:23], v[216:219]\n"
    "buffer_load_dwordx4 v24, s[60:63], s98 offen offset:1024 lds\n"
    "v_mfma_f32_16x16x32_bf16 v[220:223], a[148:151], a[36:39], v[220:223]\n"
    "buffer_load_dwordx4 v25, s[60:63], s98 offen offset:2048 lds\n"
    "v_mfma_f32_16x16x32_bf16 v[224:227], a[148:151], a[52:55], v[224:227]\n"
    "buffer_load_dwordx4 v26, s[60:63], s98 offen offset:3072 lds\n"
    "v_mfma_f32_16x16x32_bf16 v[228:231], a[148:151], a[68:71], v[228:231]\n"
    "s_waitcnt lgkmcnt(0)\n"
    "v_exp_f32 v49, v49\n"
    "v_mfma_f32_16x16x32_bf16 v[212:215], a[152:155], a[8:11], v[212:215]\n"
    "s_add_u32 m0, s85, 0xe000\n"
    "v_exp_f32 v50, v50\n"
    "v_mfma_f32_16x16x32_bf16 v[216:219], a[152:155], a[24:27], v[216:219]\n"
    "buffer_load_dwordx4 v27, s[60:63], s98 offen lds\n"
    "v_mfma_f32_16x16x32_bf16 v[220:223], a[152:155], a[40:43], v[220:223]\n"
    "s_add_u32 m0, s85, 0x12000\n"
    "v_cvt_pk_bf16_f32 v90, v48, v49\n"
    "v_mfma_f32_16x16x32_bf16 v[224:227], a[152:155], a[56:59], v[224:227]\n"
    "s_add_u32 s98, s40, 0x14000\n"
    "ds_read_b64_tr_b16 a[160:161], v6 offset:8192\n"
    "ds_read_b64_tr_b16 a[162:163], v6 offset:12288\n"
    "v_mfma_f32_16x16x32_bf16 v[228:231], a[152:155], a[72:75], v[228:231]\n"
    "buffer_load_dwordx4 v23, s[60:63], s98 offen lds\n"
    "v_mfma_f32_16x16x32_bf16 v[212:215], a[156:159], a[12:15], v[212:215]\n"
    "buffer_load_dwordx4 v24, s[60:63], s98 offen offset:1024 lds\n"
    "v_mfma_f32_16x16x32_bf16 v[216:219], a[156:159], a[28:31], v[216:219]\n"
    "buffer_load_dwordx4 v25, s[60:63], s98 offen offset:2048 lds\n"
    "v_mfma_f32_16x16x32_bf16 v[220:223], a[156:159], a[44:47], v[220:223]\n"
    "buffer_load_dwordx4 v26, s[60:63], s98 offen offset:3072 lds\n"
    "v_mfma_f32_16x16x32_bf16 v[224:227], a[156:159], a[60:63], v[224:227]\n"
    "s_add_u32 m0, s85, 0x13000\n"
    "ds_read_b64_tr_b16 a[164:165], v7 offset:8192\n"
    "ds_read_b64_tr_b16 a[166:167], v7 offset:12288\n"
    "v_mfma_f32_16x16x32_bf16 v[228:231], a[156:159], a[76:79], v[228:231]\n"
    "buffer_load_dwordx4 v27, s[60:63], s98 offen lds\n"
    "v_mfma_f32_16x16x32_bf16 a[80:83], a[132:135], v[108:111], a[80:83]\n"
    "v_exp_f32 v51, v51\n"
    "v_mfma_f32_16x16x32_bf16 a[84:87], a[132:135], v[112:115], a[84:87]\n"
    "v_exp_f32 v52, v52\n"
    "v_mfma_f32_16x16x32_bf16 a[88:91], a[132:135], v[116:119], a[88:91]\n"
    "v_cvt_pk_bf16_f32 v91, v50, v51\n"
    "v_mfma_f32_16x16x32_bf16 a[92:95], a[132:135], v[120:123], a[92:95]\n"
    "ds_read_b64_tr_b16 a[168:169], v6 offset:8704\n"
    "ds_read_b64_tr_b16 a[170:171], v6 offset:12800\n"
    "v_mfma_f32_16x16x32_bf16 a[96:99], a[132:135], v[124:127], a[96:99]\n"
    "s_waitcnt lgkmcnt(2)\n"
    "v_exp_f32 v53, v53\n"
    "v_mfma_f32_16x16x32_bf16 v[128:131], a[160:163], v[108:111], v[128:131]\n"
    "v_exp_f32 v54, v54\n"
    "v_mfma_f32_16x16x32_bf16 v[132:135], a[160:163], v[112:115], v[132:135]\n"
    "v_cvt_pk_bf16_f32 v94, v52, v53\n"
    "v_mfma_f32_16x16x32_bf16 v[136:139], a[160:163], v[116:119], v[136:139]\n"
    "v_exp_f32 v55, v55\n"
    "v_mfma_f32_16x16x32_bf16 v[140:143], a[160:163], v[120:123], v[140:143]\n"
    "ds_read_b64_tr_b16 a[172:173], v7 offset:8704\n"
    "ds_read_b64_tr_b16 a[174:175], v7 offset:12800\n"
    "v_mfma_f32_16x16x32_bf16 v[144:147], a[160:163], v[124:127], v[144:147]\n"
    "v_exp_f32 v56, v56\n"
    "v_mfma_f32_16x16x32_bf16 v[148:151], a[164:167], v[108:111], v[148:151]\n"
    "v_cvt_pk_bf16_f32 v95, v54, v55\n"
    "v_mfma_f32_16x16x32_bf16 v[152:155], a[164:167], v[112:115], v[152:155]\n"
    "v_exp_f32 v57, v57\n"
    "v_mfma_f32_16x16x32_bf16 v[156:159], a[164:167], v[116:119], v[156:159]\n"
    "v_exp_f32 v58, v58\n"
    "v_mfma_f32_16x16x32_bf16 v[160:163], a[164:167], v[120:123], v[160:163]\n"
    "ds_read_b64_tr_b16 a[176:177], v6 offset:9216\n"
    "v_cvt_pk_bf16_f32 v98, v56, v57\n"
    "v_mfma_f32_16x16x32_bf16 v[164:167], a[164:167], v[124:127], v[164:167]\n"
    "s_waitcnt lgkmcnt(1)\n"
    "ds_read_b64_tr_b16 a[178:179], v6 offset:13312\n"
    "v_mfma_f32_16x16x32_bf16 v[168:171], a[168:171], v[108:111], v[168:171]\n"
    "v_exp_f32 v59, v59\n"
    "v_mfma_f32_16x16x32_bf16 v[172:175], a[168:171], v[112:115], v[172:175]\n"
    "v_exp_f32 v60, v60\n"
    "v_mfma_f32_16x16x32_bf16 v[176:179], a[168:171], v[116:119], v[176:179]\n"
    "v_cvt_pk_bf16_f32 v99, v58, v59\n"
    "v_mfma_f32_16x16x32_bf16 v[180:183], a[168:171], v[120:123], v[180:183]\n"
    "ds_read_b64_tr_b16 a[180:181], v7 offset:9216\n"
    "ds_read_b64_tr_b16 a[182:183], v7 offset:13312\n"
    "v_mfma_f32_16x16x32_bf16 v[184:187], a[168:171], v[124:127], v[184:187]\n"
    "v_exp_f32 v61, v61\n"
    "v_mfma_f32_16x16x32_bf16 v[188:191], a[172:175], v[108:111], v[188:191]\n"
    "v_exp_f32 v62, v62\n"
    "v_mfma_f32_16x16x32_bf16 a[100:103], a[172:175], v[112:115], a[100:103]\n"
    "v_cvt_pk_bf16_f32 v102, v60, v61\n"
    "v_mfma_f32_16x16x32_bf16 a[104:107], a[172:175], v[116:119], a[104:107]\n"
    "v_exp_f32 v63, v63\n"
    "v_mfma_f32_16x16x32_bf16 a[108:111], a[172:175], v[120:123], a[108:111]\n"
    "v_exp_f32 v64, v64\n"
    "v_mfma_f32_16x16x32_bf16 a[112:115], a[172:175], v[124:127], a[112:115]\n"
    "s_waitcnt lgkmcnt(0)\n"
    "v_cvt_pk_bf16_f32 v103, v62, v63\n"
    "v_mfma_f32_16x16x32_bf16 a[116:119], a[176:179], v[108:111], a[116:119]\n"
    "v_exp_f32 v65, v65\n"
    "v_mfma_f32_16x16x32_bf16 a[120:123], a[176:179], v[112:115], a[120:123]\n"
    "v_exp_f32 v66, v66\n"
    "v_mfma_f32_16x16x32_bf16 a[124:127], a[176:179], v[116:119], a[124:127]\n"
    "v_cvt_pk_bf16_f32 v106, v64, v65\n"
    "v_mfma_f32_16x16x32_bf16 a[128:131], a[176:179], v[120:123], a[128:131]\n"
    "v_exp_f32 v67, v67\n"
    "v_mfma_f32_16x16x32_bf16 a[192:195], a[176:179], v[124:127], a[192:195]\n"
    "v_cvt_pk_bf16_f32 v107, v66, v67\n"
    "v_mfma_f32_16x16x32_bf16 a[196:199], a[180:183], v[108:111], a[196:199]\n"
    "v_exp_f32 v192, v192\n"
    "v_mfma_f32_16x16x32_bf16 a[200:203], a[180:183], v[112:115], a[200:203]\n"
    "v_exp_f32 v193, v193\n"
    "v_mfma_f32_16x16x32_bf16 a[204:207], a[180:183], v[116:119], a[204:207]\n"
    "v_exp_f32 v194, v194\n"
    "v_mfma_f32_16x16x32_bf16 a[208:211], a[180:183], v[120:123], a[208:211]\n"
    "ds_read_b64_tr_b16 a[184:185], v6 offset:9728\n"
    "v_cvt_pk_bf16_f32 v232, v192, v193\n"
    "v_mfma_f32_16x16x32_bf16 a[212:215], a[180:183], v[124:127], a[212:215]\n"
    "ds_read_b64_tr_b16 a[186:187], v6 offset:13824\n"
    "ds_read_b64_tr_b16 a[188:189], v7 offset:9728\n"
    "v_add_u32 v6, 0xfffe8000, v6\n"
    "v_mfma_f32_16x16x32_bf16 a[80:83], a[132:135], v[88:91], a[80:83]\n"
    "v_exp_f32 v195, v195\n"
    "v_mfma_f32_16x16x32_bf16 a[84:87], a[132:135], v[92:95], a[84:87]\n"
    "v_exp_f32 v196, v196\n"
    "v_mfma_f32_16x16x32_bf16 a[88:91], a[132:135], v[96:99], a[88:91]\n"
    "ds_read_b64_tr_b16 a[190:191], v7 offset:13824\n"
    "v_cvt_pk_bf16_f32 v233, v194, v195\n"
    "v_add_u32 v7, 0xfffe8000, v7\n"
    "v_mfma_f32_16x16x32_bf16 a[92:95], a[132:135], v[100:103], a[92:95]\n"
    "v_exp_f32 v197, v197\n"
    "v_exp_f32 v198, v198\n"
    "v_cvt_pk_bf16_f32 v236, v196, v197\n"
    "v_exp_f32 v199, v199\n"
    "v_exp_f32 v200, v200\n"
    "v_cvt_pk_bf16_f32 v237, v198, v199\n"
    "v_exp_f32 v201, v201\n"
    "v_exp_f32 v202, v202\n"
    "v_cvt_pk_bf16_f32 v240, v200, v201\n"
    "v_exp_f32 v203, v203\n"
    "v_exp_f32 v204, v204\n"
    "v_cvt_pk_bf16_f32 v241, v202, v203\n"
    "v_exp_f32 v205, v205\n"
    "v_exp_f32 v206, v206\n"
    "v_cvt_pk_bf16_f32 v244, v204, v205\n"
    "v_exp_f32 v207, v207\n"
    "v_exp_f32 v208, v208\n"
    "v_cvt_pk_bf16_f32 v245, v206, v207\n"
    "v_exp_f32 v209, v209\n"
    "v_exp_f32 v210, v210\n"
    "v_cvt_pk_bf16_f32 v248, v208, v209\n"
    "v_exp_f32 v211, v211\n"
    "s_nop 0\n"
    "v_cvt_pk_bf16_f32 v249, v210, v211\n"
    "v_exp_f32 v212, v212\n"
    "v_exp_f32 v213, v213\n"
    "v_exp_f32 v214, v214\n"
    "v_cvt_pk_bf16_f32 v234, v212, v213\n"
    "v_exp_f32 v215, v215\n"
    "v_exp_f32 v216, v216\n"
    "v_cvt_pk_bf16_f32 v235, v214, v215\n"
    "v_exp_f32 v217, v217\n"
    "v_exp_f32 v218, v218\n"
    "v_cvt_pk_bf16_f32 v238, v216, v217\n"
    "v_exp_f32 v219, v219\n"
    "v_exp_f32 v220, v220\n"
    "v_cvt_pk_bf16_f32 v239, v218, v219\n"
    "v_exp_f32 v221, v221\n"
    "v_exp_f32 v222, v222\n"
    "v_cvt_pk_bf16_f32 v242, v220, v221\n"
    "v_exp_f32 v223, v223\n"
    "v_exp_f32 v224, v224\n"
    "v_cvt_pk_bf16_f32 v243, v222, v223\n"
    "v_exp_f32 v225, v225\n"
    "v_exp_f32 v226, v226\n"
    "v_cvt_pk_bf16_f32 v246, v224, v225\n"
    "v_exp_f32 v227, v227\n"
    "v_exp_f32 v228, v228\n"
    "v_cvt_pk_bf16_f32 v247, v226, v227\n"
    "v_exp_f32 v229, v229\n"
    "v_exp_f32 v230, v230\n"
    "v_cvt_pk_bf16_f32 v250, v228, v229\n"
    "v_exp_f32 v231, v231\n"
    "s_nop 0\n"
    "v_cvt_pk_bf16_f32 v251, v230, v231\n"
    "v_mfma_f32_16x16x32_bf16 a[96:99], a[132:135], v[104:107], a[96:99]\n"
    "v_mfma_f32_16x16x32_bf16 a[80:83], a[132:135], v[232:235], a[80:83]\n"
    "v_mfma_f32_16x16x32_bf16 a[84:87], a[132:135], v[236:239], a[84:87]\n"
    "v_mfma_f32_16x16x32_bf16 a[88:91], a[132:135], v[240:243], a[88:91]\n"
    "v_mfma_f32_16x16x32_bf16 a[92:95], a[132:135], v[244:247], a[92:95]\n"
    "v_mfma_f32_16x16x32_bf16 a[96:99], a[132:135], v[248:251], a[96:99]\n"
    "s_waitcnt vmcnt(15) lgkmcnt(0)\n"
    "s_barrier\n"
    "v_mfma_f32_16x16x32_bf16 a[216:219], a[184:187], v[108:111], a[216:219]\n"
    "s_add_u32 m0, s85, 0x17000\n"
    "ds_read_b64_tr_b16 a[144:145], v6\n"
    "ds_read_b64_tr_b16 a[146:147], v6 offset:4096\n"
    "v_mfma_f32_16x16x32_bf16 a[220:223], a[184:187], v[112:115], a[220:223]\n"
    "s_add_u32 s98, s40, 0x1e000\n"
    "v_mfma_f32_16x16x32_bf16 a[224:227], a[184:187], v[116:119], a[224:227]\n"
    "v_mfma_f32_16x16x32_bf16 a[228:231], a[184:187], v[120:123], a[228:231]\n"
    "ds_read_b64_tr_b16 a[148:149], v6 offset:8192\n"
    "ds_read_b64_tr_b16 a[150:151], v6 offset:12288\n"
    "v_mfma_f32_16x16x32_bf16 a[232:235], a[184:187], v[124:127], a[232:235]\n"
    "buffer_load_dwordx4 v23, s[60:63], s98 offen lds\n"
    "v_mfma_f32_16x16x32_bf16 a[236:239], a[188:191], v[108:111], a[236:239]\n"
    "v_accvgpr_read_b32 v2, a80\n"
    "v_accvgpr_read_b32 v4, a84\n"
    "v_mfma_f32_16x16x32_bf16 a[240:243], a[188:191], v[112:115], a[240:243]\n"
    "v_accvgpr_read_b32 v8, a88\n"
    "v_accvgpr_read_b32 v10, a92\n"
    "v_mfma_f32_16x16x32_bf16 a[244:247], a[188:191], v[116:119], a[244:247]\n"
    "buffer_load_dwordx4 v24, s[60:63], s98 offen offset:1024 lds\n"
    "v_mfma_f32_16x16x32_bf16 a[248:251], a[188:191], v[120:123], a[248:251]\n"
    "ds_read_b64_tr_b16 a[152:153], v7\n"
    "v_accvgpr_read_b32 v12, a96\n"
    "v_mfma_f32_16x16x32_bf16 a[252:255], a[188:191], v[124:127], a[252:255]\n"
    "s_waitcnt lgkmcnt(1)\n"
    "ds_read_b64_tr_b16 a[154:155], v7 offset:4096\n"
    "v_add_f32 v22, v2, v4\n"
    "v_mfma_f32_16x16x32_bf16 v[128:131], a[144:147], v[88:91], v[128:131]\n"
    "v_add_f32 v22, v22, v8\n"
    "v_add_f32 v22, v22, v10\n"
    "v_mfma_f32_16x16x32_bf16 v[132:135], a[144:147], v[92:95], v[132:135]\n"
    "v_add_f32 v22, v22, v12\n"
    "v_mfma_f32_16x16x32_bf16 v[136:139], a[144:147], v[96:99], v[136:139]\n"
    "v_rcp_f32 v2, v2\n"
    "v_mfma_f32_16x16x32_bf16 v[140:143], a[144:147], v[100:103], v[140:143]\n"
    "ds_read_b64_tr_b16 a[156:157], v7 offset:8192\n"
    "ds_read_b64_tr_b16 a[158:159], v7 offset:12288\n"
    "v_mfma_f32_16x16x32_bf16 v[144:147], a[144:147], v[104:107], v[144:147]\n"
    "v_rcp_f32 v4, v4\n"
    "v_mfma_f32_16x16x32_bf16 v[128:131], a[148:151], v[232:235], v[128:131]\n"
    "v_rcp_f32 v8, v8\n"
    "v_mfma_f32_16x16x32_bf16 v[132:135], a[148:151], v[236:239], v[132:135]\n"
    "v_rcp_f32 v10, v10\n"
    "v_mfma_f32_16x16x32_bf16 v[136:139], a[148:151], v[240:243], v[136:139]\n"
    "v_rcp_f32 v12, v12\n"
    "v_mfma_f32_16x16x32_bf16 v[140:143], a[148:151], v[244:247], v[140:143]\n"
    "ds_read_b64_tr_b16 a[160:161], v6 offset:512\n"
    "v_cmp_nge_f32 vcc, s84, v22\n"
    "v_mfma_f32_16x16x32_bf16 v[144:147], a[148:151], v[248:251], v[144:147]\n"
    "s_waitcnt lgkmcnt(1)\n"
    "ds_read_b64_tr_b16 a[162:163], v6 offset:4608\n"
    "v_mfma_f32_16x16x32_bf16 v[148:151], a[152:155], v[88:91], v[148:151]\n"
    "buffer_load_dwordx4 v25, s[60:63], s98 offen offset:2048 lds\n"
    "v_mfma_f32_16x16x32_bf16 v[152:155], a[152:155], v[92:95], v[152:155]\n"
    "buffer_load_dwordx4 v26, s[60:63], s98 offen offset:3072 lds\n"
    "v_mfma_f32_16x16x32_bf16 v[156:159], a[152:155], v[96:99], v[156:159]\n"
    "s_add_u32 m0, s85, 0x18000\n"
    "v_mfma_f32_16x16x32_bf16 v[160:163], a[152:155], v[100:103], v[160:163]\n"
    "ds_read_b64_tr_b16 a[164:165], v6 offset:8704\n"
    "ds_read_b64_tr_b16 a[166:167], v6 offset:12800\n"
    "v_mfma_f32_16x16x32_bf16 v[164:167], a[152:155], v[104:107], v[164:167]\n"
    "buffer_load_dwordx4 v27, s[60:63], s98 offen lds\n"
    "v_mfma_f32_16x16x32_bf16 v[148:151], a[156:159], v[232:235], v[148:151]\n"
    "s_add_u32 s74, s93, 0\n"
    "v_mfma_f32_16x16x32_bf16 v[152:155], a[156:159], v[236:239], v[152:155]\n"
    "s_add_u32 s75, s93, 0x1000\n"
    "v_mfma_f32_16x16x32_bf16 v[156:159], a[156:159], v[240:243], v[156:159]\n"
    "v_mfma_f32_16x16x32_bf16 v[160:163], a[156:159], v[244:247], v[160:163]\n"
    "ds_read_b64_tr_b16 a[168:169], v7 offset:512\n"
    "ds_read_b64_tr_b16 a[170:171], v7 offset:4608\n"
    "v_mfma_f32_16x16x32_bf16 v[164:167], a[156:159], v[248:251], v[164:167]\n"
    "s_waitcnt lgkmcnt(2)\n"
    "v_mfma_f32_16x16x32_bf16 v[168:171], a[160:163], v[88:91], v[168:171]\n"
    "v_mfma_f32_16x16x32_bf16 v[172:175], a[160:163], v[92:95], v[172:175]\n"
    "v_mfma_f32_16x16x32_bf16 v[176:179], a[160:163], v[96:99], v[176:179]\n"
    "v_mfma_f32_16x16x32_bf16 v[180:183], a[160:163], v[100:103], v[180:183]\n"
    "ds_read_b64_tr_b16 a[172:173], v7 offset:8704\n"
    "ds_read_b64_tr_b16 a[174:175], v7 offset:12800\n"
    "v_mfma_f32_16x16x32_bf16 v[184:187], a[160:163], v[104:107], v[184:187]\n"
    "v_mul_f32 v128, v128, v2\n"
    "v_mul_f32 v129, v129, v2\n"
    "v_mfma_f32_16x16x32_bf16 v[168:171], a[164:167], v[232:235], v[168:171]\n"
    "v_mul_f32 v130, v130, v2\n"
    "v_mul_f32 v131, v131, v2\n"
    "v_mfma_f32_16x16x32_bf16 v[172:175], a[164:167], v[236:239], v[172:175]\n"
    "v_mul_f32 v148, v148, v2\n"
    "v_mul_f32 v149, v149, v2\n"
    "v_mfma_f32_16x16x32_bf16 v[176:179], a[164:167], v[240:243], v[176:179]\n"
    "v_mul_f32 v150, v150, v2\n"
    "v_mul_f32 v151, v151, v2\n"
    "v_mfma_f32_16x16x32_bf16 v[180:183], a[164:167], v[244:247], v[180:183]\n"
    "ds_read_b64_tr_b16 a[176:177], v6 offset:1024\n"
    "v_cvt_pk_bf16_f32 v128, v128, v129\n"
    "v_mfma_f32_16x16x32_bf16 v[184:187], a[164:167], v[248:251], v[184:187]\n"
    "s_waitcnt lgkmcnt(1)\n"
    "ds_read_b64_tr_b16 a[178:179], v6 offset:5120\n"
    "v_cvt_pk_bf16_f32 v129, v130, v131\n"
    "v_mfma_f32_16x16x32_bf16 v[188:191], a[168:171], v[88:91], v[188:191]\n"
    "v_cvt_pk_bf16_f32 v130, v148, v149\n"
    "v_cvt_pk_bf16_f32 v131, v150, v151\n"
    "v_mfma_f32_16x16x32_bf16 a[100:103], a[168:171], v[92:95], a[100:103]\n"
    "buffer_store_dwordx4 v[128:131], v1, s[52:55], s74 offen sc1\n"
    "v_mfma_f32_16x16x32_bf16 a[104:107], a[168:171], v[96:99], a[104:107]\n"
    "v_mul_f32 v132, v132, v4\n"
    "v_mul_f32 v133, v133, v4\n"
    "v_mfma_f32_16x16x32_bf16 a[108:111], a[168:171], v[100:103], a[108:111]\n"
    "ds_read_b64_tr_b16 a[180:181], v6 offset:9216\n"
    "v_mul_f32 v134, v134, v4\n"
    "v_mfma_f32_16x16x32_bf16 a[112:115], a[168:171], v[104:107], a[112:115]\n"
    "ds_read_b64_tr_b16 a[182:183], v6 offset:13312\n"
    "v_mul_f32 v135, v135, v4\n"
    "v_mfma_f32_16x16x32_bf16 v[188:191], a[172:175], v[232:235], v[188:191]\n"
    "v_mul_f32 v152, v152, v4\n"
    "v_mul_f32 v153, v153, v4\n"
    "v_mfma_f32_16x16x32_bf16 a[100:103], a[172:175], v[236:239], a[100:103]\n"
    "v_mul_f32 v154, v154, v4\n"
    "v_mul_f32 v155, v155, v4\n"
    "v_mfma_f32_16x16x32_bf16 a[104:107], a[172:175], v[240:243], a[104:107]\n"
    "v_cvt_pk_bf16_f32 v132, v132, v133\n"
    "v_cvt_pk_bf16_f32 v133, v134, v135\n"
    "v_mfma_f32_16x16x32_bf16 a[108:111], a[172:175], v[244:247], a[108:111]\n"
    "ds_read_b64_tr_b16 a[184:185], v7 offset:1024\n"
    "v_cvt_pk_bf16_f32 v134, v152, v153\n"
    "v_mfma_f32_16x16x32_bf16 a[112:115], a[172:175], v[248:251], a[112:115]\n"
    "s_waitcnt lgkmcnt(1)\n"
    "ds_read_b64_tr_b16 a[186:187], v7 offset:5120\n"
    "v_cvt_pk_bf16_f32 v135, v154, v155\n"
    "v_mfma_f32_16x16x32_bf16 a[116:119], a[176:179], v[88:91], a[116:119]\n"
    "buffer_store_dwordx4 v[132:135], v1, s[52:55], s74 offen offset:1024 sc1\n"
    "v_mfma_f32_16x16x32_bf16 a[120:123], a[176:179], v[92:95], a[120:123]\n"
    "v_mul_f32 v136, v136, v8\n"
    "v_mul_f32 v137, v137, v8\n"
    "v_mfma_f32_16x16x32_bf16 a[124:127], a[176:179], v[96:99], a[124:127]\n"
    "v_mul_f32 v138, v138, v8\n"
    "v_mul_f32 v139, v139, v8\n"
    "v_mfma_f32_16x16x32_bf16 a[128:131], a[176:179], v[100:103], a[128:131]\n"
    "ds_read_b64_tr_b16 a[188:189], v7 offset:9216\n"
    "v_mul_f32 v156, v156, v8\n"
    "v_mfma_f32_16x16x32_bf16 a[192:195], a[176:179], v[104:107], a[192:195]\n"
    "ds_read_b64_tr_b16 a[190:191], v7 offset:13312\n"
    "v_mul_f32 v157, v157, v8\n"
    "v_mfma_f32_16x16x32_bf16 a[116:119], a[180:183], v[232:235], a[116:119]\n"
    "v_mul_f32 v158, v158, v8\n"
    "v_mul_f32 v159, v159, v8\n"
    "v_mfma_f32_16x16x32_bf16 a[120:123], a[180:183], v[236:239], a[120:123]\n"
    "v_cvt_pk_bf16_f32 v136, v136, v137\n"
    "v_cvt_pk_bf16_f32 v137, v138, v139\n"
    "v_mfma_f32_16x16x32_bf16 a[124:127], a[180:183], v[240:243], a[124:127]\n"
    "v_cvt_pk_bf16_f32 v138, v156, v157\n"
    "v_cvt_pk_bf16_f32 v139, v158, v159\n"
    "v_mfma_f32_16x16x32_bf16 a[128:131], a[180:183], v[244:247], a[128:131]\n"
    "ds_read_b64_tr_b16 a[144:145], v6 offset:1536\n"
    "v_mul_f32 v140, v140, v10\n"
    "v_mfma_f32_16x16x32_bf16 a[192:195], a[180:183], v[248:251], a[192:195]\n"
    "s_waitcnt lgkmcnt(1)\n"
    "ds_read_b64_tr_b16 a[146:147], v6 offset:5632\n"
    "v_mul_f32 v141, v141, v10\n"
    "v_mfma_f32_16x16x32_bf16 a[196:199], a[184:187], v[88:91], a[196:199]\n"
    "buffer_store_dwordx4 v[136:139], v1, s[52:55], s74 offen offset:2048 sc1\n"
    "v_mfma_f32_16x16x32_bf16 a[200:203], a[184:187], v[92:95], a[200:203]\n"
    "v_mul_f32 v142, v142, v10\n"
    "v_mul_f32 v143, v143, v10\n"
    "v_mfma_f32_16x16x32_bf16 a[204:207], a[184:187], v[96:99], a[204:207]\n"
    "v_mul_f32 v160, v160, v10\n"
    "v_mul_f32 v161, v161, v10\n"
    "v_mfma_f32_16x16x32_bf16 a[208:211], a[184:187], v[100:103], a[208:211]\n"
    "ds_read_b64_tr_b16 a[148:149], v6 offset:9728\n"
    "v_mul_f32 v162, v162, v10\n"
    "v_mfma_f32_16x16x32_bf16 a[212:215], a[184:187], v[104:107], a[212:215]\n"
    "ds_read_b64_tr_b16 a[150:151], v6 offset:13824\n"
    "v_mul_f32 v163, v163, v10\n"
    "v_mfma_f32_16x16x32_bf16 a[196:199], a[188:191], v[232:235], a[196:199]\n"
    "v_cvt_pk_bf16_f32 v140, v140, v141\n"
    "v_cvt_pk_bf16_f32 v141, v142, v143\n"
    "v_mfma_f32_16x16x32_bf16 a[200:203], a[188:191], v[236:239], a[200:203]\n"
    "v_cvt_pk_bf16_f32 v142, v160, v161\n"
    "v_cvt_pk_bf16_f32 v143, v162, v163\n"
    "v_mfma_f32_16x16x32_bf16 a[204:207], a[188:191], v[240:243], a[204:207]\n"
    "buffer_store_dwordx4 v[140:143], v1, s[52:55], s74 offen offset:3072 sc1\n"
    "v_mfma_f32_16x16x32_bf16 a[208:211], a[188:191], v[244:247], a[208:211]\n"
    "ds_read_b64_tr_b16 a[152:153], v7 offset:1536\n"
    "v_mul_f32 v144, v144, v12\n"
    "v_mfma_f32_16x16x32_bf16 a[212:215], a[188:191], v[248:251], a[212:215]\n"
    "s_waitcnt lgkmcnt(1)\n"
    "ds_read_b64_tr_b16 a[154:155], v7 offset:5632\n"
    "v_mul_f32 v145, v145, v12\n"
    "v_mfma_f32_16x16x32_bf16 a[216:219], a[144:147], v[88:91], a[216:219]\n"
    "v_mul_f32 v146, v146, v12\n"
    "v_mul_f32 v147, v147, v12\n"
    "v_mfma_f32_16x16x32_bf16 a[220:223], a[144:147], v[92:95], a[220:223]\n"
    "v_mul_f32 v164, v164, v12\n"
    "v_mul_f32 v165, v165, v12\n"
    "v_mfma_f32_16x16x32_bf16 a[224:227], a[144:147], v[96:99], a[224:227]\n"
    "v_mul_f32 v166, v166, v12\n"
    "v_mul_f32 v167, v167, v12\n"
    "v_mfma_f32_16x16x32_bf16 a[228:231], a[144:147], v[100:103], a[228:231]\n"
    "ds_read_b64_tr_b16 a[156:157], v7 offset:9728\n"
    "v_cvt_pk_bf16_f32 v144, v144, v145\n"
    "v_mfma_f32_16x16x32_bf16 a[232:235], a[144:147], v[104:107], a[232:235]\n"
    "ds_read_b64_tr_b16 a[158:159], v7 offset:13824\n"
    "v_cvt_pk_bf16_f32 v145, v146, v147\n"
    "v_mfma_f32_16x16x32_bf16 a[216:219], a[148:151], v[232:235], a[216:219]\n"
    "v_cvt_pk_bf16_f32 v146, v164, v165\n"
    "v_cvt_pk_bf16_f32 v147, v166, v167\n"
    "v_mfma_f32_16x16x32_bf16 a[220:223], a[148:151], v[236:239], a[220:223]\n"
    "buffer_store_dwordx4 v[144:147], v1, s[52:55], s75 offen sc1\n"
    "v_mfma_f32_16x16x32_bf16 a[224:227], a[148:151], v[240:243], a[224:227]\n"
    "s_add_u32 s74, s93, 0x2800\n"
    "v_mfma_f32_16x16x32_bf16 a[228:231], a[148:151], v[244:247], a[228:231]\n"
    "s_add_u32 s75, s93, 0x3800\n"
    "v_mfma_f32_16x16x32_bf16 a[232:235], a[148:151], v[248:251], a[232:235]\n"
    "s_waitcnt lgkmcnt(0)\n"
    "v_mfma_f32_16x16x32_bf16 a[236:239], a[152:155], v[88:91], a[236:239]\n"
    "v_mfma_f32_16x16x32_bf16 a[240:243], a[152:155], v[92:95], a[240:243]\n"
    "v_mfma_f32_16x16x32_bf16 a[244:247], a[152:155], v[96:99], a[244:247]\n"
    "v_mfma_f32_16x16x32_bf16 a[248:251], a[152:155], v[100:103], a[248:251]\n"
    "v_mfma_f32_16x16x32_bf16 a[252:255], a[152:155], v[104:107], a[252:255]\n"
    "v_mfma_f32_16x16x32_bf16 a[236:239], a[156:159], v[232:235], a[236:239]\n"
    "v_mfma_f32_16x16x32_bf16 a[240:243], a[156:159], v[236:239], a[240:243]\n"
    "v_mfma_f32_16x16x32_bf16 a[244:247], a[156:159], v[240:243], a[244:247]\n"
    "v_mfma_f32_16x16x32_bf16 a[248:251], a[156:159], v[244:247], a[248:251]\n"
    "v_mfma_f32_16x16x32_bf16 a[252:255], a[156:159], v[248:251], a[252:255]\n"
    "v_pk_mul_f32 v[168:169], v[168:169], v[2:3] op_sel_hi:[1,0]\n"
    "v_pk_mul_f32 v[170:171], v[170:171], v[2:3] op_sel_hi:[1,0]\n"
    "v_pk_mul_f32 v[188:189], v[188:189], v[2:3] op_sel_hi:[1,0]\n"
    "v_pk_mul_f32 v[190:191], v[190:191], v[2:3] op_sel_hi:[1,0]\n"
    "v_cvt_pk_bf16_f32 v168, v168, v169\n"
    "v_cvt_pk_bf16_f32 v169, v170, v171\n"
    "v_cvt_pk_bf16_f32 v170, v188, v189\n"
    "v_cvt_pk_bf16_f32 v171, v190, v191\n"
    "buffer_store_dwordx4 v[168:171], v1, s[52:55], s74 offen sc1\n"
    "v_pk_mul_f32 v[172:173], v[172:173], v[4:5] op_sel_hi:[1,0]\n"
    "v_pk_mul_f32 v[174:175], v[174:175], v[4:5] op_sel_hi:[1,0]\n"
    "v_accvgpr_read_b32 v216, a100\n"
    "v_accvgpr_read_b32 v217, a101\n"
    "v_accvgpr_read_b32 v218, a102\n"
    "v_accvgpr_read_b32 v219, a103\n"
    "v_pk_mul_f32 v[216:217], v[216:217], v[4:5] op_sel_hi:[1,0]\n"
    "v_pk_mul_f32 v[218:219], v[218:219], v[4:5] op_sel_hi:[1,0]\n"
    "v_cvt_pk_bf16_f32 v172, v172, v173\n"
    "v_cvt_pk_bf16_f32 v173, v174, v175\n"
    "v_cvt_pk_bf16_f32 v174, v216, v217\n"
    "v_cvt_pk_bf16_f32 v175, v218, v219\n"
    "buffer_store_dwordx4 v[172:175], v1, s[52:55], s74 offen offset:1024 sc1\n"
    "v_pk_mul_f32 v[176:177], v[176:177], v[8:9] op_sel_hi:[1,0]\n"
    "v_pk_mul_f32 v[178:179], v[178:179], v[8:9] op_sel_hi:[1,0]\n"
    "v_accvgpr_read_b32 v220, a104\n"
    "v_accvgpr_read_b32 v221, a105\n"
    "v_accvgpr_read_b32 v222, a106\n"
    "v_accvgpr_read_b32 v223, a107\n"
    "v_pk_mul_f32 v[220:221], v[220:221], v[8:9] op_sel_hi:[1,0]\n"
    "v_pk_mul_f32 v[222:223], v[222:223], v[8:9] op_sel_hi:[1,0]\n"
    "v_cvt_pk_bf16_f32 v176, v176, v177\n"
    "v_cvt_pk_bf16_f32 v177, v178, v179\n"
    "v_cvt_pk_bf16_f32 v178, v220, v221\n"
    "v_cvt_pk_bf16_f32 v179, v222, v223\n"
    "buffer_store_dwordx4 v[176:179], v1, s[52:55], s74 offen offset:2048 sc1\n"
    "v_pk_mul_f32 v[180:181], v[180:181], v[10:11] op_sel_hi:[1,0]\n"
    "v_pk_mul_f32 v[182:183], v[182:183], v[10:11] op_sel_hi:[1,0]\n"
    "v_accvgpr_read_b32 v224, a108\n"
    "v_accvgpr_read_b32 v225, a109\n"
    "v_accvgpr_read_b32 v226, a110\n"
    "v_accvgpr_read_b32 v227, a111\n"
    "v_pk_mul_f32 v[224:225], v[224:225], v[10:11] op_sel_hi:[1,0]\n"
    "v_pk_mul_f32 v[226:227], v[226:227], v[10:11] op_sel_hi:[1,0]\n"
    "v_cvt_pk_bf16_f32 v180, v180, v181\n"
    "v_cvt_pk_bf16_f32 v181, v182, v183\n"
    "v_cvt_pk_bf16_f32 v182, v224, v225\n"
    "v_cvt_pk_bf16_f32 v183, v226, v227\n"
    "buffer_store_dwordx4 v[180:183], v1, s[52:55], s74 offen offset:3072 sc1\n"
    "v_pk_mul_f32 v[184:185], v[184:185], v[12:13] op_sel_hi:[1,0]\n"
    "v_pk_mul_f32 v[186:187], v[186:187], v[12:13] op_sel_hi:[1,0]\n"
    "v_accvgpr_read_b32 v228, a112\n"
    "v_accvgpr_read_b32 v229, a113\n"
    "v_accvgpr_read_b32 v230, a114\n"
    "v_accvgpr_read_b32 v231, a115\n"
    "v_pk_mul_f32 v[228:229], v[228:229], v[12:13] op_sel_hi:[1,0]\n"
    "v_pk_mul_f32 v[230:231], v[230:231], v[12:13] op_sel_hi:[1,0]\n"
    "v_cvt_pk_bf16_f32 v184, v184, v185\n"
    "v_cvt_pk_bf16_f32 v185, v186, v187\n"
    "v_cvt_pk_bf16_f32 v186, v228, v229\n"
    "v_cvt_pk_bf16_f32 v187, v230, v231\n"
    "buffer_store_dwordx4 v[184:187], v1, s[52:55], s75 offen sc1\n"
    "s_add_u32 s74, s93, 0x5000\n"
    "s_add_u32 s75, s93, 0x6000\n"
    "v_accvgpr_read_b32 v148, a116\n"
    "v_accvgpr_read_b32 v149, a117\n"
    "v_accvgpr_read_b32 v150, a118\n"
    "v_accvgpr_read_b32 v151, a119\n"
    "v_pk_mul_f32 v[148:149], v[148:149], v[2:3] op_sel_hi:[1,0]\n"
    "v_pk_mul_f32 v[150:151], v[150:151], v[2:3] op_sel_hi:[1,0]\n"
    "v_accvgpr_read_b32 v212, a196\n"
    "v_accvgpr_read_b32 v213, a197\n"
    "v_accvgpr_read_b32 v214, a198\n"
    "v_accvgpr_read_b32 v215, a199\n"
    "v_pk_mul_f32 v[212:213], v[212:213], v[2:3] op_sel_hi:[1,0]\n"
    "v_pk_mul_f32 v[214:215], v[214:215], v[2:3] op_sel_hi:[1,0]\n"
    "v_cvt_pk_bf16_f32 v148, v148, v149\n"
    "v_cvt_pk_bf16_f32 v149, v150, v151\n"
    "v_cvt_pk_bf16_f32 v150, v212, v213\n"
    "v_cvt_pk_bf16_f32 v151, v214, v215\n"
    "buffer_store_dwordx4 v[148:151], v1, s[52:55], s74 offen sc1\n"
    "v_accvgpr_read_b32 v152, a120\n"
    "v_accvgpr_read_b32 v153, a121\n"
    "v_accvgpr_read_b32 v154, a122\n"
    "v_accvgpr_read_b32 v155, a123\n"
    "v_pk_mul_f32 v[152:153], v[152:153], v[4:5] op_sel_hi:[1,0]\n"
    "v_pk_mul_f32 v[154:155], v[154:155], v[4:5] op_sel_hi:[1,0]\n"
    "v_accvgpr_read_b32 v216, a200\n"
    "v_accvgpr_read_b32 v217, a201\n"
    "v_accvgpr_read_b32 v218, a202\n"
    "v_accvgpr_read_b32 v219, a203\n"
    "v_pk_mul_f32 v[216:217], v[216:217], v[4:5] op_sel_hi:[1,0]\n"
    "v_pk_mul_f32 v[218:219], v[218:219], v[4:5] op_sel_hi:[1,0]\n"
    "v_cvt_pk_bf16_f32 v152, v152, v153\n"
    "v_cvt_pk_bf16_f32 v153, v154, v155\n"
    "v_cvt_pk_bf16_f32 v154, v216, v217\n"
    "v_cvt_pk_bf16_f32 v155, v218, v219\n"
    "buffer_store_dwordx4 v[152:155], v1, s[52:55], s74 offen offset:1024 sc1\n"
    "v_accvgpr_read_b32 v156, a124\n"
    "v_accvgpr_read_b32 v157, a125\n"
    "v_accvgpr_read_b32 v158, a126\n"
    "v_accvgpr_read_b32 v159, a127\n"
    "v_pk_mul_f32 v[156:157], v[156:157], v[8:9] op_sel_hi:[1,0]\n"
    "v_pk_mul_f32 v[158:159], v[158:159], v[8:9] op_sel_hi:[1,0]\n"
    "v_accvgpr_read_b32 v220, a204\n"
    "v_accvgpr_read_b32 v221, a205\n"
    "v_accvgpr_read_b32 v222, a206\n"
    "v_accvgpr_read_b32 v223, a207\n"
    "v_pk_mul_f32 v[220:221], v[220:221], v[8:9] op_sel_hi:[1,0]\n"
    "v_pk_mul_f32 v[222:223], v[222:223], v[8:9] op_sel_hi:[1,0]\n"
    "v_cvt_pk_bf16_f32 v156, v156, v157\n"
    "v_cvt_pk_bf16_f32 v157, v158, v159\n"
    "v_cvt_pk_bf16_f32 v158, v220, v221\n"
    "v_cvt_pk_bf16_f32 v159, v222, v223\n"
    "buffer_store_dwordx4 v[156:159], v1, s[52:55], s74 offen offset:2048 sc1\n"
    "v_accvgpr_read_b32 v160, a128\n"
    "v_accvgpr_read_b32 v161, a129\n"
    "v_accvgpr_read_b32 v162, a130\n"
    "v_accvgpr_read_b32 v163, a131\n"
    "v_pk_mul_f32 v[160:161], v[160:161], v[10:11] op_sel_hi:[1,0]\n"
    "v_pk_mul_f32 v[162:163], v[162:163], v[10:11] op_sel_hi:[1,0]\n"
    "v_accvgpr_read_b32 v224, a208\n"
    "v_accvgpr_read_b32 v225, a209\n"
    "v_accvgpr_read_b32 v226, a210\n"
    "v_accvgpr_read_b32 v227, a211\n"
    "v_pk_mul_f32 v[224:225], v[224:225], v[10:11] op_sel_hi:[1,0]\n"
    "v_pk_mul_f32 v[226:227], v[226:227], v[10:11] op_sel_hi:[1,0]\n"
    "v_cvt_pk_bf16_f32 v160, v160, v161\n"
    "v_cvt_pk_bf16_f32 v161, v162, v163\n"
    "v_cvt_pk_bf16_f32 v162, v224, v225\n"
    "v_cvt_pk_bf16_f32 v163, v226, v227\n"
    "buffer_store_dwordx4 v[160:163], v1, s[52:55], s74 offen offset:3072 sc1\n"
    "v_accvgpr_read_b32 v164, a192\n"
    "v_accvgpr_read_b32 v165, a193\n"
    "v_accvgpr_read_b32 v166, a194\n"
    "v_accvgpr_read_b32 v167, a195\n"
    "v_pk_mul_f32 v[164:165], v[164:165], v[12:13] op_sel_hi:[1,0]\n"
    "v_pk_mul_f32 v[166:167], v[166:167], v[12:13] op_sel_hi:[1,0]\n"
    "v_accvgpr_read_b32 v228, a212\n"
    "v_accvgpr_read_b32 v229, a213\n"
    "v_accvgpr_read_b32 v230, a214\n"
    "v_accvgpr_read_b32 v231, a215\n"
    "v_pk_mul_f32 v[228:229], v[228:229], v[12:13] op_sel_hi:[1,0]\n"
    "v_pk_mul_f32 v[230:231], v[230:231], v[12:13] op_sel_hi:[1,0]\n"
    "v_cvt_pk_bf16_f32 v164, v164, v165\n"
    "v_cvt_pk_bf16_f32 v165, v166, v167\n"
    "v_cvt_pk_bf16_f32 v166, v228, v229\n"
    "v_cvt_pk_bf16_f32 v167, v230, v231\n"
    "buffer_store_dwordx4 v[164:167], v1, s[52:55], s75 offen sc1\n"
    "s_add_u32 s74, s93, 0x7800\n"
    "s_add_u32 s75, s93, 0x8800\n"
    "v_accvgpr_read_b32 v192, a216\n"
    "v_accvgpr_read_b32 v193, a217\n"
    "v_accvgpr_read_b32 v194, a218\n"
    "v_accvgpr_read_b32 v195, a219\n"
    "v_pk_mul_f32 v[192:193], v[192:193], v[2:3] op_sel_hi:[1,0]\n"
    "v_pk_mul_f32 v[194:195], v[194:195], v[2:3] op_sel_hi:[1,0]\n"
    "v_accvgpr_read_b32 v212, a236\n"
    "v_accvgpr_read_b32 v213, a237\n"
    "v_accvgpr_read_b32 v214, a238\n"
    "v_accvgpr_read_b32 v215, a239\n"
    "v_pk_mul_f32 v[212:213], v[212:213], v[2:3] op_sel_hi:[1,0]\n"
    "v_pk_mul_f32 v[214:215], v[214:215], v[2:3] op_sel_hi:[1,0]\n"
    "v_cvt_pk_bf16_f32 v192, v192, v193\n"
    "v_cvt_pk_bf16_f32 v193, v194, v195\n"
    "v_cvt_pk_bf16_f32 v194, v212, v213\n"
    "v_cvt_pk_bf16_f32 v195, v214, v215\n"
    "buffer_store_dwordx4 v[192:195], v1, s[52:55], s74 offen sc1\n"
    "v_accvgpr_read_b32 v196, a220\n"
    "v_accvgpr_read_b32 v197, a221\n"
    "v_accvgpr_read_b32 v198, a222\n"
    "v_accvgpr_read_b32 v199, a223\n"
    "v_pk_mul_f32 v[196:197], v[196:197], v[4:5] op_sel_hi:[1,0]\n"
    "v_pk_mul_f32 v[198:199], v[198:199], v[4:5] op_sel_hi:[1,0]\n"
    "v_accvgpr_read_b32 v216, a240\n"
    "v_accvgpr_read_b32 v217, a241\n"
    "v_accvgpr_read_b32 v218, a242\n"
    "v_accvgpr_read_b32 v219, a243\n"
    "v_pk_mul_f32 v[216:217], v[216:217], v[4:5] op_sel_hi:[1,0]\n"
    "v_pk_mul_f32 v[218:219], v[218:219], v[4:5] op_sel_hi:[1,0]\n"
    "v_cvt_pk_bf16_f32 v196, v196, v197\n"
    "v_cvt_pk_bf16_f32 v197, v198, v199\n"
    "v_cvt_pk_bf16_f32 v198, v216, v217\n"
    "v_cvt_pk_bf16_f32 v199, v218, v219\n"
    "buffer_store_dwordx4 v[196:199], v1, s[52:55], s74 offen offset:1024 sc1\n"
    "v_accvgpr_read_b32 v200, a224\n"
    "v_accvgpr_read_b32 v201, a225\n"
    "v_accvgpr_read_b32 v202, a226\n"
    "v_accvgpr_read_b32 v203, a227\n"
    "v_pk_mul_f32 v[200:201], v[200:201], v[8:9] op_sel_hi:[1,0]\n"
    "v_pk_mul_f32 v[202:203], v[202:203], v[8:9] op_sel_hi:[1,0]\n"
    "v_accvgpr_read_b32 v220, a244\n"
    "v_accvgpr_read_b32 v221, a245\n"
    "v_accvgpr_read_b32 v222, a246\n"
    "v_accvgpr_read_b32 v223, a247\n"
    "v_pk_mul_f32 v[220:221], v[220:221], v[8:9] op_sel_hi:[1,0]\n"
    "v_pk_mul_f32 v[222:223], v[222:223], v[8:9] op_sel_hi:[1,0]\n"
    "v_cvt_pk_bf16_f32 v200, v200, v201\n"
    "v_cvt_pk_bf16_f32 v201, v202, v203\n"
    "v_cvt_pk_bf16_f32 v202, v220, v221\n"
    "v_cvt_pk_bf16_f32 v203, v222, v223\n"
    "buffer_store_dwordx4 v[200:203], v1, s[52:55], s74 offen offset:2048 sc1\n"
    "v_accvgpr_read_b32 v204, a228\n"
    "v_accvgpr_read_b32 v205, a229\n"
    "v_accvgpr_read_b32 v206, a230\n"
    "v_accvgpr_read_b32 v207, a231\n"
    "v_pk_mul_f32 v[204:205], v[204:205], v[10:11] op_sel_hi:[1,0]\n"
    "v_pk_mul_f32 v[206:207], v[206:207], v[10:11] op_sel_hi:[1,0]\n"
    "v_accvgpr_read_b32 v224, a248\n"
    "v_accvgpr_read_b32 v225, a249\n"
    "v_accvgpr_read_b32 v226, a250\n"
    "v_accvgpr_read_b32 v227, a251\n"
    "v_pk_mul_f32 v[224:225], v[224:225], v[10:11] op_sel_hi:[1,0]\n"
    "v_pk_mul_f32 v[226:227], v[226:227], v[10:11] op_sel_hi:[1,0]\n"
    "v_cvt_pk_bf16_f32 v204, v204, v205\n"
    "v_cvt_pk_bf16_f32 v205, v206, v207\n"
    "v_cvt_pk_bf16_f32 v206, v224, v225\n"
    "v_cvt_pk_bf16_f32 v207, v226, v227\n"
    "buffer_store_dwordx4 v[204:207], v1, s[52:55], s74 offen offset:3072 sc1\n"
    "v_accvgpr_read_b32 v208, a232\n"
    "v_accvgpr_read_b32 v209, a233\n"
    "v_accvgpr_read_b32 v210, a234\n"
    "v_accvgpr_read_b32 v211, a235\n"
    "v_pk_mul_f32 v[208:209], v[208:209], v[12:13] op_sel_hi:[1,0]\n"
    "v_pk_mul_f32 v[210:211], v[210:211], v[12:13] op_sel_hi:[1,0]\n"
    "v_accvgpr_read_b32 v228, a252\n"
    "v_accvgpr_read_b32 v229, a253\n"
    "v_accvgpr_read_b32 v230, a254\n"
    "v_accvgpr_read_b32 v231, a255\n"
    "v_pk_mul_f32 v[228:229], v[228:229], v[12:13] op_sel_hi:[1,0]\n"
    "v_pk_mul_f32 v[230:231], v[230:231], v[12:13] op_sel_hi:[1,0]\n"
    "v_cvt_pk_bf16_f32 v208, v208, v209\n"
    "v_cvt_pk_bf16_f32 v209, v210, v211\n"
    "v_cvt_pk_bf16_f32 v210, v228, v229\n"
    "v_cvt_pk_bf16_f32 v211, v230, v231\n"
    "buffer_store_dwordx4 v[208:211], v1, s[52:55], s75 offen sc1\n"
    "s_cmp_lg_u64 vcc, 0\n"
    "s_cselect_b32 s88, 1, 0\n"
    "s_lshl_b32 s74, s72, 2\n"
    "s_add_u32 s74, s74, 0x1c000\n"
    "v_mov_b32 v11, s74\n"
    "v_mov_b32 v22, s88\n"
    "ds_write_b32 v11, v22\n"
    "s_lshr_b32 s76, s72, 1\n"
    "v_lshrrev_b32 v9, 2, v0\n"
    "v_mul_u32_u24 v9, 0x140, v9\n"
    "v_and_b32 v13, 3, v0\n"
    "s_mul_i32 s74, s76, 0x2800\n"
    "s_add_u32 s74, s74, 0x8000\n"
    "v_lshl_add_u32 v9, v13, 3, v9\n"
    "v_add_u32 v9, s74, v9\n"
    "s_lshl_b32 s75, s76, 6\n"
    "s_sub_u32 s75, 32, s75\n"
    "v_bfe_u32 v13, v0, 4, 1\n"
    "v_mul_lo_u32 v13, v13, s75\n"
    "s_mul_i32 s74, s76, 0xa0\n"
    "s_sub_u32 s75, 0xa0, s74\n"
    "v_add_u32 v11, s74, v9\n"
    "v_add_u32 v3, v11, v13\n"
    "v_sub_u32 v5, v11, v13\n"
    "v_add_u32 v11, s75, v9\n"
    "v_add_u32 v6, v11, v13\n"
    "v_sub_u32 v7, v11, v13\n"
    "v_mov_b32 v9, 0x1c000\n"
    "s_waitcnt vmcnt(35) lgkmcnt(0)\n"
    "s_barrier\n"
    "ds_read_b64_tr_b16 a[144:145], v3\n"
    "ds_read_b64_tr_b16 a[146:147], v3 offset:5120\n"
    "ds_read_b64_tr_b16 a[148:149], v5 offset:32\n"
    "ds_read_b64_tr_b16 a[150:151], v5 offset:5152\n"
    "s_waitcnt lgkmcnt(2)\n"
    "v_mfma_f32_16x16x32_bf16 v[28:31], a[144:147], v[128:131], 0\n"
    "ds_read_b64_tr_b16 a[152:153], v3 offset:64\n"
    "ds_read_b64_tr_b16 a[154:155], v3 offset:5184\n"
    "v_mfma_f32_16x16x32_bf16 v[32:35], a[144:147], v[132:135], 0\n"
    "ds_read_b128 v[18:21], v9\n"
    "v_mfma_f32_16x16x32_bf16 v[36:39], a[144:147], v[136:139], 0\n"
    "v_mfma_f32_16x16x32_bf16 v[40:43], a[144:147], v[140:143], 0\n"
    "ds_read_b64_tr_b16 a[156:157], v5 offset:96\n"
    "ds_read_b64_tr_b16 a[158:159], v5 offset:5216\n"
    "v_add_u32 v5, 0x5000, v5\n"
    "v_mfma_f32_16x16x32_bf16 v[44:47], a[144:147], v[144:147], 0\n"
    "s_waitcnt lgkmcnt(4)\n"
    "v_mfma_f32_16x16x32_bf16 v[48:51], a[148:151], v[128:131], 0\n"
    "v_mfma_f32_16x16x32_bf16 v[52:55], a[148:151], v[132:135], 0\n"
    "v_mfma_f32_16x16x32_bf16 v[56:59], a[148:151], v[136:139], 0\n"
    "v_mfma_f32_16x16x32_bf16 v[60:63], a[148:151], v[140:143], 0\n"
    "ds_read_b64_tr_b16 a[160:161], v3 offset:128\n"
    "ds_read_b64_tr_b16 a[162:163], v3 offset:5248\n"
    "v_add_u32 v3, 0x5000, v3\n"
    "v_mfma_f32_16x16x32_bf16 v[64:67], a[148:151], v[144:147], 0\n"
    "s_waitcnt lgkmcnt(2)\n"
    "v_mfma_f32_16x16x32_bf16 v[68:71], a[152:155], v[128:131], 0\n"
    "v_mfma_f32_16x16x32_bf16 v[72:75], a[152:155], v[132:135], 0\n"
    "v_mfma_f32_16x16x32_bf16 v[76:79], a[152:155], v[136:139], 0\n"
    "v_mfma_f32_16x16x32_bf16 v[80:83], a[152:155], v[140:143], 0\n"
    "ds_read_b64_tr_b16 a[164:165], v7\n"
    "ds_read_b64_tr_b16 a[166:167], v7 offset:5120\n"
    "v_mfma_f32_16x16x32_bf16 v[84:87], a[152:155], v[144:147], 0\n"
    "v_mfma_f32_16x16x32_bf16 v[88:91], a[156:159], v[128:131], 0\n"
    "v_mfma_f32_16x16x32_bf16 v[92:95], a[156:159], v[132:135], 0\n"
    "v_mfma_f32_16x16x32_bf16 v[96:99], a[156:159], v[136:139], 0\n"
    "v_mfma_f32_16x16x32_bf16 v[100:103], a[156:159], v[140:143], 0\n"
    "ds_read_b64_tr_b16 a[168:169], v6 offset:32\n"
    "ds_read_b64_tr_b16 a[170:171], v6 offset:5152\n"
    "v_mfma_f32_16x16x32_bf16 v[104:107], a[156:159], v[144:147], 0\n"
    "s_waitcnt lgkmcnt(2)\n"
    "v_mfma_f32_16x16x32_bf16 v[108:111], a[160:163], v[128:131], 0\n"
    "v_mfma_f32_16x16x32_bf16 v[112:115], a[160:163], v[132:135], 0\n"
    "v_mfma_f32_16x16x32_bf16 v[116:119], a[160:163], v[136:139], 0\n"
    "v_mfma_f32_16x16x32_bf16 v[120:123], a[160:163], v[140:143], 0\n"
    "ds_read_b64_tr_b16 a[172:173], v7 offset:64\n"
    "ds_read_b64_tr_b16 a[174:175], v7 offset:5184\n"
    "v_mfma_f32_16x16x32_bf16 v[124:127], a[160:163], v[144:147], 0\n"
    "v_mfma_f32_16x16x32_bf16 a[0:3], a[164:167], v[128:131], 0\n"
    "v_mfma_f32_16x16x32_bf16 a[4:7], a[164:167], v[132:135], 0\n"
    "v_mfma_f32_16x16x32_bf16 a[8:11], a[164:167], v[136:139], 0\n"
    "v_mfma_f32_16x16x32_bf16 a[12:15], a[164:167], v[140:143], 0\n"
    "ds_read_b64_tr_b16 a[176:177], v6 offset:96\n"
    "ds_read_b64_tr_b16 a[178:179], v6 offset:5216\n"
    "v_add_u32 v6, 0x5000, v6\n"
    "v_mfma_f32_16x16x32_bf16 a[16:19], a[164:167], v[144:147], 0\n"
    "s_waitcnt lgkmcnt(2)\n"
    "v_mfma_f32_16x16x32_bf16 a[20:23], a[168:171], v[128:131], 0\n"
    "ds_read_b64_tr_b16 a[180:181], v7 offset:128\n"
    "ds_read_b64_tr_b16 a[182:183], v7 offset:5248\n"
    "v_add_u32 v7, 0x5000, v7\n"
    "v_mfma_f32_16x16x32_bf16 a[24:27], a[168:171], v[132:135], 0\n"
    "v_mfma_f32_16x16x32_bf16 a[28:31], a[168:171], v[136:139], 0\n"
    "v_mfma_f32_16x16x32_bf16 a[32:35], a[168:171], v[140:143], 0\n"
    "v_mfma_f32_16x16x32_bf16 a[36:39], a[168:171], v[144:147], 0\n"
    "v_mfma_f32_16x16x32_bf16 a[40:43], a[172:175], v[128:131], 0\n"
    "v_mfma_f32_16x16x32_bf16 a[44:47], a[172:175], v[132:135], 0\n"
    "v_mfma_f32_16x16x32_bf16 a[48:51], a[172:175], v[136:139], 0\n"
    "v_mfma_f32_16x16x32_bf16 a[52:55], a[172:175], v[140:143], 0\n"
    "v_mfma_f32_16x16x32_bf16 a[56:59], a[172:175], v[144:147], 0\n"
    "s_nop 0\n"
    "s_waitcnt vmcnt(0) lgkmcnt(0)\n"
    "s_barrier\n"
    "v_or3_b32 v18, v18, v19, v20\n"
    "v_or_b32 v18, v18, v21\n"
    "s_nop 0\n"
    "v_readfirstlane_b32 %[flag], v18\n"
    "s_nop 1\n"
    "s_cmp_lg_u32 %[flag], 0\n"
    "s_cbranch_scc1 LSLOW_%=\n"
    "s_cmp_lg_u32 s72, 0\n"
    "s_cbranch_scc1 LNPFO_%=\n"
    "s_lshl_b64 exec, 1, s90\n"
    "buffer_store_dwordx2 v[16:17], v15, s[68:71], s96 offen offset:128 sc1\n"
    "s_mov_b64 exec, -1\n"
    "LNPFO_%=:\n"
    "v_mfma_f32_16x16x32_bf16 a[60:63], a[176:179], v[128:131], 0\n"
    "s_add_u32 m0, s85, 0x1d000\n"
    "ds_read_b64_tr_b16 a[184:185], v3\n"
    "ds_read_b64_tr_b16 a[186:187], v3 offset:5120\n"
    "v_mfma_f32_16x16x32_bf16 a[64:67], a[176:179], v[132:135], 0\n"
    "s_add_u32 s98, s41, 0\n"
    "v_mfma_f32_16x16x32_bf16 a[68:71], a[176:179], v[136:139], 0\n"
    "buffer_load_dwordx4 v23, s[60:63], s98 offen lds\n"
    "v_mfma_f32_16x16x32_bf16 a[72:75], a[176:179], v[140:143], 0\n"
    "ds_read_b64_tr_b16 a[188:189], v5 offset:32\n"
    "ds_read_b64_tr_b16 a[190:191], v5 offset:5152\n"
    "v_mfma_f32_16x16x32_bf16 a[76:79], a[176:179], v[144:147], 0\n"
    "v_mfma_f32_16x16x32_bf16 a[80:83], a[180:183], v[128:131], 0\n"
    "buffer_load_dwordx4 v24, s[60:63], s98 offen offset:1024 lds\n"
    "v_mfma_f32_16x16x32_bf16 a[84:87], a[180:183], v[132:135], 0\n"
    "v_mfma_f32_16x16x32_bf16 a[88:91], a[180:183], v[136:139], 0\n"
    "v_mfma_f32_16x16x32_bf16 a[92:95], a[180:183], v[140:143], 0\n"
    "ds_read_b64_tr_b16 a[144:145], v3 offset:64\n"
    "ds_read_b64_tr_b16 a[146:147], v3 offset:5184\n"
    "v_mfma_f32_16x16x32_bf16 a[96:99], a[180:183], v[144:147], 0\n"
    "s_waitcnt lgkmcnt(2)\n"
    "v_mfma_f32_16x16x32_bf16 v[28:31], a[184:187], v[168:171], v[28:31]\n"
    "buffer_load_dwordx4 v25, s[60:63], s98 offen offset:2048 lds\n"
    "v_mfma_f32_16x16x32_bf16 v[32:35], a[184:187], v[172:175], v[32:35]\n"
    "buffer_load_dwordx4 v26, s[60:63], s98 offen offset:3072 lds\n"
    "v_mfma_f32_16x16x32_bf16 v[36:39], a[184:187], v[176:179], v[36:39]\n"
    "s_add_u32 m0, s85, 0x1e000\n"
    "v_mfma_f32_16x16x32_bf16 v[40:43], a[184:187], v[180:183], v[40:43]\n"
    "ds_read_b64_tr_b16 a[148:149], v5 offset:96\n"
    "ds_read_b64_tr_b16 a[150:151], v5 offset:5216\n"
    "v_add_u32 v5, 0x5000, v5\n"
    "v_mfma_f32_16x16x32_bf16 v[44:47], a[184:187], v[184:187], v[44:47]\n"
    "buffer_load_dwordx4 v27, s[60:63], s98 offen lds\n"
    "v_mfma_f32_16x16x32_bf16 v[48:51], a[188:191], v[168:171], v[48:51]\n"
    "s_add_u32 m0, s85, 0x22000\n"
    "v_mfma_f32_16x16x32_bf16 v[52:55], a[188:191], v[172:175], v[52:55]\n"
    "s_add_u32 s98, s41, 0xa000\n"
    "v_mfma_f32_16x16x32_bf16 v[56:59], a[188:191], v[176:179], v[56:59]\n"
    "buffer_load_dwordx4 v23, s[60:63], s98 offen lds\n"
    "v_mfma_f32_16x16x32_bf16 v[60:63], a[188:191], v[180:183], v[60:63]\n"
    "ds_read_b64_tr_b16 a[152:153], v3 offset:128\n"
    "ds_read_b64_tr_b16 a[154:155], v3 offset:5248\n"
    "v_add_u32 v3, 0x5000, v3\n"
    "v_mfma_f32_16x16x32_bf16 v[64:67], a[188:191], v[184:187], v[64:67]\n"
    "s_waitcnt lgkmcnt(2)\n"
    "v_mfma_f32_16x16x32_bf16 v[68:71], a[144:147], v[168:171], v[68:71]\n"
    "buffer_load_dwordx4 v24, s[60:63], s98 offen offset:1024 lds\n"
    "v_mfma_f32_16x16x32_bf16 v[72:75], a[144:147], v[172:175], v[72:75]\n"
    "s_mov_b32 s44, 0x5000\n"
    "v_mfma_f32_16x16x32_bf16 v[76:79], a[144:147], v[176:179], v[76:79]\n"
    "s_mov_b32 s45, 0xa000\n"
    "v_mfma_f32_16x16x32_bf16 v[80:83], a[144:147], v[180:183], v[80:83]\n"
    "ds_read_b64_tr_b16 a[156:157], v7\n"
    "ds_read_b64_tr_b16 a[158:159], v7 offset:5120\n"
    "v_mfma_f32_16x16x32_bf16 v[84:87], a[144:147], v[184:187], v[84:87]\n"
    "buffer_load_dwordx4 v25, s[60:63], s98 offen offset:2048 lds\n"
    "v_mfma_f32_16x16x32_bf16 v[88:91], a[148:151], v[168:171], v[88:91]\n"
    "s_mov_b32 s46, 0xf000\n"
    "v_mfma_f32_16x16x32_bf16 v[92:95], a[148:151], v[172:175], v[92:95]\n"
    "buffer_load_dwordx4 v26, s[60:63], s98 offen offset:3072 lds\n"
    "v_mfma_f32_16x16x32_bf16 v[96:99], a[148:151], v[176:179], v[96:99]\n"
    "s_add_u32 m0, s85, 0x23000\n"
    "v_mfma_f32_16x16x32_bf16 v[100:103], a[148:151], v[180:183], v[100:103]\n"
    "s_mov_b32 s47, 0x14000\n"
    "ds_read_b64_tr_b16 a[160:161], v6 offset:32\n"
    "ds_read_b64_tr_b16 a[162:163], v6 offset:5152\n"
    "v_mfma_f32_16x16x32_bf16 v[104:107], a[148:151], v[184:187], v[104:107]\n"
    "s_waitcnt lgkmcnt(2)\n"
    "v_mfma_f32_16x16x32_bf16 v[108:111], a[152:155], v[168:171], v[108:111]\n"
    "buffer_load_dwordx4 v27, s[60:63], s98 offen lds\n"
    "v_mfma_f32_16x16x32_bf16 v[112:115], a[152:155], v[172:175], v[112:115]\n"
    "v_mfma_f32_16x16x32_bf16 v[116:119], a[152:155], v[176:179], v[116:119]\n"
    "v_mfma_f32_16x16x32_bf16 v[120:123], a[152:155], v[180:183], v[120:123]\n"
    "ds_read_b64_tr_b16 a[164:165], v7 offset:64\n"
    "ds_read_b64_tr_b16 a[166:167], v7 offset:5184\n"
    "v_mfma_f32_16x16x32_bf16 v[124:127], a[152:155], v[184:187], v[124:127]\n"
    "v_mfma_f32_16x16x32_bf16 a[0:3], a[156:159], v[168:171], a[0:3]\n"
    "v_mfma_f32_16x16x32_bf16 a[4:7], a[156:159], v[172:175], a[4:7]\n"
    "v_mfma_f32_16x16x32_bf16 a[8:11], a[156:159], v[176:179], a[8:11]\n"
    "v_mfma_f32_16x16x32_bf16 a[12:15], a[156:159], v[180:183], a[12:15]\n"
    "ds_read_b64_tr_b16 a[168:169], v6 offset:96\n"
    "ds_read_b64_tr_b16 a[170:171], v6 offset:5216\n"
    "v_add_u32 v6, 0x5000, v6\n"
    "v_mfma_f32_16x16x32_bf16 a[16:19], a[156:159], v[184:187], a[16:19]\n"
    "s_waitcnt lgkmcnt(2)\n"
    "v_mfma_f32_16x16x32_bf16 a[20:23], a[160:163], v[168:171], a[20:23]\n"
    "ds_read_b64_tr_b16 a[172:173], v7 offset:128\n"
    "ds_read_b64_tr_b16 a[174:175], v7 offset:5248\n"
    "v_add_u32 v7, 0x5000, v7\n"
    "v_mfma_f32_16x16x32_bf16 a[24:27], a[160:163], v[172:175], a[24:27]\n"
    "v_mfma_f32_16x16x32_bf16 a[28:31], a[160:163], v[176:179], a[28:31]\n"
    "v_mfma_f32_16x16x32_bf16 a[32:35], a[160:163], v[180:183], a[32:35]\n"
    "v_mfma_f32_16x16x32_bf16 a[36:39], a[160:163], v[184:187], a[36:39]\n"
    "v_mfma_f32_16x16x32_bf16 a[40:43], a[164:167], v[168:171], a[40:43]\n"
    "v_mfma_f32_16x16x32_bf16 a[44:47], a[164:167], v[172:175], a[44:47]\n"
    "v_mfma_f32_16x16x32_bf16 a[48:51], a[164:167], v[176:179], a[48:51]\n"
    "v_mfma_f32_16x16x32_bf16 a[52:55], a[164:167], v[180:183], a[52:55]\n"
    "v_mfma_f32_16x16x32_bf16 a[56:59], a[164:167], v[184:187], a[56:59]\n"
    "s_waitcnt vmcnt(36) lgkmcnt(0)\n"
    "s_barrier\n"
    "v_mfma_f32_16x16x32_bf16 a[60:63], a[168:171], v[168:171], a[60:63]\n"
    "s_add_u32 m0, s85, 0\n"
    "ds_read_b64_tr_b16 a[176:177], v3\n"
    "ds_read_b64_tr_b16 a[178:179], v3 offset:5120\n"
    "v_mfma_f32_16x16x32_bf16 a[64:67], a[168:171], v[172:175], a[64:67]\n"
    "s_add_u32 s98, s41, 0x14000\n"
    "v_mfma_f32_16x16x32_bf16 a[68:71], a[168:171], v[176:179], a[68:71]\n"
    "buffer_load_dwordx4 v23, s[60:63], s98 offen lds\n"
    "v_mfma_f32_16x16x32_bf16 a[72:75], a[168:171], v[180:183], a[72:75]\n"
    "ds_read_b64_tr_b16 a[180:181], v5 offset:32\n"
    "ds_read_b64_tr_b16 a[182:183], v5 offset:5152\n"
    "v_mfma_f32_16x16x32_bf16 a[76:79], a[168:171], v[184:187], a[76:79]\n"
    "v_mfma_f32_16x16x32_bf16 a[80:83], a[172:175], v[168:171], a[80:83]\n"
    "buffer_load_dwordx4 v24, s[60:63], s98 offen offset:1024 lds\n"
    "v_mfma_f32_16x16x32_bf16 a[84:87], a[172:175], v[172:175], a[84:87]\n"
    "v_mfma_f32_16x16x32_bf16 a[88:91], a[172:175], v[176:179], a[88:91]\n"
    "v_mfma_f32_16x16x32_bf16 a[92:95], a[172:175], v[180:183], a[92:95]\n"
    "ds_read_b64_tr_b16 a[184:185], v3 offset:64\n"
    "ds_read_b64_tr_b16 a[186:187], v3 offset:5184\n"
    "v_mfma_f32_16x16x32_bf16 a[96:99], a[172:175], v[184:187], a[96:99]\n"
    "s_waitcnt lgkmcnt(2)\n"
    "v_mfma_f32_16x16x32_bf16 v[28:31], a[176:179], v[148:151], v[28:31]\n"
    "buffer_load_dwordx4 v25, s[60:63], s98 offen offset:2048 lds\n"
    "v_mfma_f32_16x16x32_bf16 v[32:35], a[176:179], v[152:155], v[32:35]\n"
    "buffer_load_dwordx4 v26, s[60:63], s98 offen offset:3072 lds\n"
    "v_mfma_f32_16x16x32_bf16 v[36:39], a[176:179], v[156:159], v[36:39]\n"
    "s_add_u32 m0, s85, 0x1000\n"
    "v_mfma_f32_16x16x32_bf16 v[40:43], a[176:179], v[160:163], v[40:43]\n"
    "ds_read_b64_tr_b16 a[188:189], v5 offset:96\n"
    "ds_read_b64_tr_b16 a[190:191], v5 offset:5216\n"
    "v_add_u32 v5, 0x5000, v5\n"
    "v_mfma_f32_16x16x32_bf16 v[44:47], a[176:179], v[164:167], v[44:47]\n"
    "buffer_load_dwordx4 v27, s[60:63], s98 offen lds\n"
    "v_mfma_f32_16x16x32_bf16 v[48:51], a[180:183], v[148:151], v[48:51]\n"
    "v_mfma_f32_16x16x32_bf16 v[52:55], a[180:183], v[152:155], v[52:55]\n"
    "v_mfma_f32_16x16x32_bf16 v[56:59], a[180:183], v[156:159], v[56:59]\n"
    "v_mfma_f32_16x16x32_bf16 v[60:63], a[180:183], v[160:163], v[60:63]\n"
    "ds_read_b64_tr_b16 a[144:145], v3 offset:128\n"
    "ds_read_b64_tr_b16 a[146:147], v3 offset:5248\n"
    "v_add_u32 v3, 0x5000, v3\n"
    "v_mfma_f32_16x16x32_bf16 v[64:67], a[180:183], v[164:167], v[64:67]\n"
    "s_waitcnt lgkmcnt(2)\n"
    "v_mfma_f32_16x16x32_bf16 v[68:71], a[184:187], v[148:151], v[68:71]\n"
    "v_mfma_f32_16x16x32_bf16 v[72:75], a[184:187], v[152:155], v[72:75]\n"
    "v_mfma_f32_16x16x32_bf16 v[76:79], a[184:187], v[156:159], v[76:79]\n"
    "v_mfma_f32_16x16x32_bf16 v[80:83], a[184:187], v[160:163], v[80:83]\n"
    "ds_read_b64_tr_b16 a[148:149], v7\n"
    "ds_read_b64_tr_b16 a[150:151], v7 offset:5120\n"
    "v_mfma_f32_16x16x32_bf16 v[84:87], a[184:187], v[164:167], v[84:87]\n"
    "v_mfma_f32_16x16x32_bf16 v[88:91], a[188:191], v[148:151], v[88:91]\n"
    "v_mfma_f32_16x16x32_bf16 v[92:95], a[188:191], v[152:155], v[92:95]\n"
    "v_mfma_f32_16x16x32_bf16 v[96:99], a[188:191], v[156:159], v[96:99]\n"
    "v_mfma_f32_16x16x32_bf16 v[100:103], a[188:191], v[160:163], v[100:103]\n"
    "ds_read_b64_tr_b16 a[152:153], v6 offset:32\n"
    "ds_read_b64_tr_b16 a[154:155], v6 offset:5152\n"
    "v_mfma_f32_16x16x32_bf16 v[104:107], a[188:191], v[164:167], v[104:107]\n"
    "s_waitcnt lgkmcnt(2)\n"
    "v_mfma_f32_16x16x32_bf16 v[108:111], a[144:147], v[148:151], v[108:111]\n"
    "v_mfma_f32_16x16x32_bf16 v[112:115], a[144:147], v[152:155], v[112:115]\n"
    "v_mfma_f32_16x16x32_bf16 v[116:119], a[144:147], v[156:159], v[116:119]\n"
    "v_mfma_f32_16x16x32_bf16 v[120:123], a[144:147], v[160:163], v[120:123]\n"
    "ds_read_b64_tr_b16 a[156:157], v7 offset:64\n"
    "ds_read_b64_tr_b16 a[158:159], v7 offset:5184\n"
    "v_mfma_f32_16x16x32_bf16 v[124:127], a[144:147], v[164:167], v[124:127]\n"
    "v_mfma_f32_16x16x32_bf16 a[0:3], a[148:151], v[148:151], a[0:3]\n"
    "v_mfma_f32_16x16x32_bf16 a[4:7], a[148:151], v[152:155], a[4:7]\n"
    "v_mfma_f32_16x16x32_bf16 a[8:11], a[148:151], v[156:159], a[8:11]\n"
    "v_mfma_f32_16x16x32_bf16 a[12:15], a[148:151], v[160:163], a[12:15]\n"
    "ds_read_b64_tr_b16 a[160:161], v6 offset:96\n"
    "ds_read_b64_tr_b16 a[162:163], v6 offset:5216\n"
    "v_add_u32 v6, 0x5000, v6\n"
    "v_mfma_f32_16x16x32_bf16 a[16:19], a[148:151], v[164:167], a[16:19]\n"
    "s_waitcnt lgkmcnt(2)\n"
    "v_mfma_f32_16x16x32_bf16 a[20:23], a[152:155], v[148:151], a[20:23]\n"
    "ds_read_b64_tr_b16 a[164:165], v7 offset:128\n"
    "ds_read_b64_tr_b16 a[166:167], v7 offset:5248\n"
    "v_add_u32 v7, 0x5000, v7\n"
    "v_mfma_f32_16x16x32_bf16 a[24:27], a[152:155], v[152:155], a[24:27]\n"
    "v_mfma_f32_16x16x32_bf16 a[28:31], a[152:155], v[156:159], a[28:31]\n"
    "v_mfma_f32_16x16x32_bf16 a[32:35], a[152:155], v[160:163], a[32:35]\n"
    "v_mfma_f32_16x16x32_bf16 a[36:39], a[152:155], v[164:167], a[36:39]\n"
    "v_mfma_f32_16x16x32_bf16 a[40:43], a[156:159], v[148:151], a[40:43]\n"
    "v_mfma_f32_16x16x32_bf16 a[44:47], a[156:159], v[152:155], a[44:47]\n"
    "v_mfma_f32_16x16x32_bf16 a[48:51], a[156:159], v[156:159], a[48:51]\n"
    "v_mfma_f32_16x16x32_bf16 a[52:55], a[156:159], v[160:163], a[52:55]\n"
    "v_mfma_f32_16x16x32_bf16 a[56:59], a[156:159], v[164:167], a[56:59]\n"
    "s_waitcnt vmcnt(36) lgkmcnt(0)\n"
    "s_barrier\n"
    "v_mfma_f32_16x16x32_bf16 a[60:63], a[160:163], v[148:151], a[60:63]\n"
    "ds_read_b64_tr_b16 a[168:169], v3\n"
    "ds_read_b64_tr_b16 a[170:171], v3 offset:5120\n"
    "v_mfma_f32_16x16x32_bf16 a[64:67], a[160:163], v[152:155], a[64:67]\n"
    "v_mfma_f32_16x16x32_bf16 a[68:71], a[160:163], v[156:159], a[68:71]\n"
    "s_mov_b32 exec_lo, s91\n"
    "s_mov_b32 exec_hi, 0\n"
    "buffer_load_dwordx2 v[18:19], v14, s[68:71], 0 offen offset:128 sc1\n"
    "s_mov_b64 exec, -1\n"
    "v_mfma_f32_16x16x32_bf16 a[72:75], a[160:163], v[160:163], a[72:75]\n"
    "ds_read_b64_tr_b16 a[172:173], v5 offset:32\n"
    "ds_read_b64_tr_b16 a[174:175], v5 offset:5152\n"
    "v_mfma_f32_16x16x32_bf16 a[76:79], a[160:163], v[164:167], a[76:79]\n"
    "v_mfma_f32_16x16x32_bf16 a[80:83], a[164:167], v[148:151], a[80:83]\n"
    "v_mfma_f32_16x16x32_bf16 a[84:87], a[164:167], v[152:155], a[84:87]\n"
    "v_mfma_f32_16x16x32_bf16 a[88:91], a[164:167], v[156:159], a[88:91]\n"
    "v_mfma_f32_16x16x32_bf16 a[92:95], a[164:167], v[160:163], a[92:95]\n"
    "ds_read_b64_tr_b16 a[176:177], v3 offset:64\n"
    "ds_read_b64_tr_b16 a[178:179], v3 offset:5184\n"
    "v_mfma_f32_16x16x32_bf16 a[96:99], a[164:167], v[164:167], a[96:99]\n"
    "s_waitcnt lgkmcnt(2)\n"
    "v_mfma_f32_16x16x32_bf16 v[28:31], a[168:171], v[192:195], v[28:31]\n"
    "v_mfma_f32_16x16x32_bf16 v[32:35], a[168:171], v[196:199], v[32:35]\n"
    "v_mfma_f32_16x16x32_bf16 v[36:39], a[168:171], v[200:203], v[36:39]\n"
    "v_mfma_f32_16x16x32_bf16 v[40:43], a[168:171], v[204:207], v[40:43]\n"
    "ds_read_b64_tr_b16 a[180:181], v5 offset:96\n"
    "ds_read_b64_tr_b16 a[182:183], v5 offset:5216\n"
    "v_add_u32 v5, 0x6000, v5\n"
    "v_mfma_f32_16x16x32_bf16 v[44:47], a[168:171], v[208:211], v[44:47]\n"
    "v_mfma_f32_16x16x32_bf16 v[48:51], a[172:175], v[192:195], v[48:51]\n"
    "v_mfma_f32_16x16x32_bf16 v[52:55], a[172:175], v[196:199], v[52:55]\n"
    "v_mfma_f32_16x16x32_bf16 v[56:59], a[172:175], v[200:203], v[56:59]\n"
    "v_mfma_f32_16x16x32_bf16 v[60:63], a[172:175], v[204:207], v[60:63]\n"
    "ds_read_b64_tr_b16 a[184:185], v3 offset:128\n"
    "ds_read_b64_tr_b16 a[186:187], v3 offset:5248\n"
    "v_add_u32 v3, 0x6000, v3\n"
    "v_mfma_f32_16x16x32_bf16 v[64:67], a[172:175], v[208:211], v[64:67]\n"
    "s_waitcnt lgkmcnt(2)\n"
    "v_mfma_f32_16x16x32_bf16 v[68:71], a[176:179], v[192:195], v[68:71]\n"
    "v_mfma_f32_16x16x32_bf16 v[72:75], a[176:179], v[196:199], v[72:75]\n"
    "v_mfma_f32_16x16x32_bf16 v[76:79], a[176:179], v[200:203], v[76:79]\n"
    "v_mfma_f32_16x16x32_bf16 v[80:83], a[176:179], v[204:207], v[80:83]\n"
    "ds_read_b64_tr_b16 a[188:189], v7\n"
    "ds_read_b64_tr_b16 a[190:191], v7 offset:5120\n"
    "v_mfma_f32_16x16x32_bf16 v[84:87], a[176:179], v[208:211], v[84:87]\n"
    "v_mfma_f32_16x16x32_bf16 v[88:91], a[180:183], v[192:195], v[88:91]\n"
    "s_waitcnt vmcnt(0)\n"
    "s_mov_b32 s80, 0\n"
    "LPFO_%=:\n"
    "v_cmp_ne_u64 vcc, v[18:19], v[16:17]\n"
    "s_nop 0\n"
    "s_and_b32 s99, vcc_lo, s91\n"
    "s_cbranch_scc0 LPDFO_%=\n"
    "s_sleep 1\n"
    "s_mov_b32 exec_lo, s91\n"
    "s_mov_b32 exec_hi, 0\n"
    "buffer_load_dwordx2 v[18:19], v14, s[68:71], 0 offen offset:128 sc1\n"
    "s_mov_b64 exec, -1\n"
    "s_add_u32 s80, s80, 1\n"
    "s_waitcnt vmcnt(0)\n"
    "s_cmp_lt_u32 s80, 0x40000\n"
    "s_cbranch_scc1 LPFO_%=\n"
    "LPDFO_%=:\n"
    "v_mfma_f32_16x16x32_bf16 v[92:95], a[180:183], v[196:199], v[92:95]\n"
    "s_add_u32 s76, s59, 0\n"
    "v_mfma_f32_16x16x32_bf16 v[96:99], a[180:183], v[200:203], v[96:99]\n"
    "s_add_u32 s77, s59, 0x1000\n"
    "v_mfma_f32_16x16x32_bf16 v[100:103], a[180:183], v[204:207], v[100:103]\n"
    "ds_read_b64_tr_b16 a[144:145], v6 offset:32\n"
    "ds_read_b64_tr_b16 a[146:147], v6 offset:5152\n"
    "v_mfma_f32_16x16x32_bf16 v[104:107], a[180:183], v[208:211], v[104:107]\n"
    "s_waitcnt lgkmcnt(2)\n"
    "v_mfma_f32_16x16x32_bf16 v[108:111], a[184:187], v[192:195], v[108:111]\n"
    "buffer_load_dwordx4 v[212:215], v1, s[52:55], s76 offen sc1\n"
    "v_mfma_f32_16x16x32_bf16 v[112:115], a[184:187], v[196:199], v[112:115]\n"
    "buffer_load_dwordx4 v[216:219], v1, s[52:55], s76 offen offset:1024 sc1\n"
    "v_mfma_f32_16x16x32_bf16 v[116:119], a[184:187], v[200:203], v[116:119]\n"
    "buffer_load_dwordx4 v[220:223], v1, s[52:55], s76 offen offset:2048 sc1\n"
    "v_mfma_f32_16x16x32_bf16 v[120:123], a[184:187], v[204:207], v[120:123]\n"
    "ds_read_b64_tr_b16 a[148:149], v7 offset:64\n"
    "ds_read_b64_tr_b16 a[150:151], v7 offset:5184\n"
    "v_mfma_f32_16x16x32_bf16 v[124:127], a[184:187], v[208:211], v[124:127]\n"
    "buffer_load_dwordx4 v[224:227], v1, s[52:55], s76 offen offset:3072 sc1\n"
    "v_mfma_f32_16x16x32_bf16 a[0:3], a[188:191], v[192:195], a[0:3]\n"
    "buffer_load_dwordx4 v[228:231], v1, s[52:55], s77 offen sc1\n"
    "v_mfma_f32_16x16x32_bf16 a[4:7], a[188:191], v[196:199], a[4:7]\n"
    "s_add_u32 s76, s59, 0x2800\n"
    "v_mfma_f32_16x16x32_bf16 a[8:11], a[188:191], v[200:203], a[8:11]\n"
    "s_add_u32 s77, s59, 0x3800\n"
    "v_mfma_f32_16x16x32_bf16 a[12:15], a[188:191], v[204:207], a[12:15]\n"
    "ds_read_b64_tr_b16 a[152:153], v6 offset:96\n"
    "ds_read_b64_tr_b16 a[154:155], v6 offset:5216\n"
    "v_add_u32 v6, 0x6000, v6\n"
    "v_mfma_f32_16x16x32_bf16 a[16:19], a[188:191], v[208:211], a[16:19]\n"
    "s_waitcnt lgkmcnt(2)\n"
    "v_mfma_f32_16x16x32_bf16 a[20:23], a[144:147], v[192:195], a[20:23]\n"
    "ds_read_b64_tr_b16 a[156:157], v7 offset:128\n"
    "ds_read_b64_tr_b16 a[158:159], v7 offset:5248\n"
    "v_add_u32 v7, 0x6000, v7\n"
    "v_mfma_f32_16x16x32_bf16 a[24:27], a[144:147], v[196:199], a[24:27]\n"
    "buffer_load_dwordx4 v[232:235], v1, s[52:55], s76 offen sc1\n"
    "v_mfma_f32_16x16x32_bf16 a[28:31], a[144:147], v[200:203], a[28:31]\n"
    "buffer_load_dwordx4 v[236:239], v1, s[52:55], s76 offen offset:1024 sc1\n"
    "v_mfma_f32_16x16x32_bf16 a[32:35], a[144:147], v[204:207], a[32:35]\n"
    "buffer_load_dwordx4 v[240:243], v1, s[52:55], s76 offen offset:2048 sc1\n"
    "v_mfma_f32_16x16x32_bf16 a[36:39], a[144:147], v[208:211], a[36:39]\n"
    "buffer_load_dwordx4 v[244:247], v1, s[52:55], s76 offen offset:3072 sc1\n"
    "v_mfma_f32_16x16x32_bf16 a[40:43], a[148:151], v[192:195], a[40:43]\n"
    "buffer_load_dwordx4 v[248:251], v1, s[52:55], s77 offen sc1\n"
    "v_mfma_f32_16x16x32_bf16 a[44:47], a[148:151], v[196:199], a[44:47]\n"
    "s_add_u32 s76, s59, 0x5000\n"
    "v_mfma_f32_16x16x32_bf16 a[48:51], a[148:151], v[200:203], a[48:51]\n"
    "s_add_u32 s77, s59, 0x6000\n"
    "v_mfma_f32_16x16x32_bf16 a[52:55], a[148:151], v[204:207], a[52:55]\n"
    "buffer_load_dwordx4 v[128:131], v1, s[52:55], s76 offen sc1\n"
    "v_mfma_f32_16x16x32_bf16 a[56:59], a[148:151], v[208:211], a[56:59]\n"
    "s_waitcnt vmcnt(22) lgkmcnt(0)\n"
    "s_barrier\n"
    "v_mfma_f32_16x16x32_bf16 a[60:63], a[152:155], v[192:195], a[60:63]\n"
    "ds_read_b64_tr_b16 a[160:161], v3\n"
    "ds_read_b64_tr_b16 a[162:163], v3 offset:5120\n"
    "v_mfma_f32_16x16x32_bf16 a[64:67], a[152:155], v[196:199], a[64:67]\n"
    "buffer_load_dwordx4 v[132:135], v1, s[52:55], s76 offen offset:1024 sc1\n"
    "v_mfma_f32_16x16x32_bf16 a[68:71], a[152:155], v[200:203], a[68:71]\n"
    "buffer_load_dwordx4 v[136:139], v1, s[52:55], s76 offen offset:2048 sc1\n"
    "v_mfma_f32_16x16x32_bf16 a[72:75], a[152:155], v[204:207], a[72:75]\n"
    "ds_read_b64_tr_b16 a[164:165], v5 offset:32\n"
    "ds_read_b64_tr_b16 a[166:167], v5 offset:5152\n"
    "v_mfma_f32_16x16x32_bf16 a[76:79], a[152:155], v[208:211], a[76:79]\n"
    "buffer_load_dwordx4 v[140:143], v1, s[52:55], s76 offen offset:3072 sc1\n"
    "v_mfma_f32_16x16x32_bf16 a[80:83], a[156:159], v[192:195], a[80:83]\n"
    "buffer_load_dwordx4 v[144:147], v1, s[52:55], s77 offen sc1\n"
    "v_mfma_f32_16x16x32_bf16 a[84:87], a[156:159], v[196:199], a[84:87]\n"
    "v_mfma_f32_16x16x32_bf16 a[88:91], a[156:159], v[200:203], a[88:91]\n"
    "v_mfma_f32_16x16x32_bf16 a[92:95], a[156:159], v[204:207], a[92:95]\n"
    "ds_read_b64_tr_b16 a[168:169], v3 offset:64\n"
    "ds_read_b64_tr_b16 a[170:171], v3 offset:5184\n"
    "v_mfma_f32_16x16x32_bf16 a[96:99], a[156:159], v[208:211], a[96:99]\n"
    "s_waitcnt lgkmcnt(2)\n"
    "s_waitcnt vmcnt(10)\n"
    "v_mfma_f32_16x16x32_bf16 v[28:31], a[160:163], v[212:215], v[28:31]\n"
    "s_add_u32 s76, s59, 0x7800\n"
    "v_mfma_f32_16x16x32_bf16 v[32:35], a[160:163], v[216:219], v[32:35]\n"
    "s_add_u32 s77, s59, 0x8800\n"
    "v_mfma_f32_16x16x32_bf16 v[36:39], a[160:163], v[220:223], v[36:39]\n"
    "buffer_load_dwordx4 v[168:171], v1, s[52:55], s76 offen sc1\n"
    "v_mfma_f32_16x16x32_bf16 v[40:43], a[160:163], v[224:227], v[40:43]\n"
    "ds_read_b64_tr_b16 a[172:173], v5 offset:96\n"
    "ds_read_b64_tr_b16 a[174:175], v5 offset:5216\n"
    "v_add_u32 v5, 0x5000, v5\n"
    "v_mfma_f32_16x16x32_bf16 v[44:47], a[160:163], v[228:231], v[44:47]\n"
    "v_mfma_f32_16x16x32_bf16 v[48:51], a[164:167], v[212:215], v[48:51]\n"
    "buffer_load_dwordx4 v[172:175], v1, s[52:55], s76 offen offset:1024 sc1\n"
    "v_mfma_f32_16x16x32_bf16 v[52:55], a[164:167], v[216:219], v[52:55]\n"
    "v_mfma_f32_16x16x32_bf16 v[56:59], a[164:167], v[220:223], v[56:59]\n"
    "v_mfma_f32_16x16x32_bf16 v[60:63], a[164:167], v[224:227], v[60:63]\n"
    "ds_read_b64_tr_b16 a[176:177], v3 offset:128\n"
    "ds_read_b64_tr_b16 a[178:179], v3 offset:5248\n"
    "v_add_u32 v3, 0x5000, v3\n"
    "v_mfma_f32_16x16x32_bf16 v[64:67], a[164:167], v[228:231], v[64:67]\n"
    "s_waitcnt lgkmcnt(2)\n"
    "v_mfma_f32_16x16x32_bf16 v[68:71], a[168:171], v[212:215], v[68:71]\n"
    "buffer_load_dwordx4 v[176:179], v1, s[52:55], s76 offen offset:2048 sc1\n"
    "v_mfma_f32_16x16x32_bf16 v[72:75], a[168:171], v[216:219], v[72:75]\n"
    "buffer_load_dwordx4 v[180:183], v1, s[52:55], s76 offen offset:3072 sc1\n"
    "v_mfma_f32_16x16x32_bf16 v[76:79], a[168:171], v[220:223], v[76:79]\n"
    "v_mfma_f32_16x16x32_bf16 v[80:83], a[168:171], v[224:227], v[80:83]\n"
    "ds_read_b64_tr_b16 a[180:181], v7\n"
    "ds_read_b64_tr_b16 a[182:183], v7 offset:5120\n"
    "v_mfma_f32_16x16x32_bf16 v[84:87], a[168:171], v[228:231], v[84:87]\n"
    "buffer_load_dwordx4 v[184:187], v1, s[52:55], s77 offen sc1\n"
    "v_mfma_f32_16x16x32_bf16 v[88:91], a[172:175], v[212:215], v[88:91]\n"
    "s_add_u32 m0, s85, 0x8000\n"
    "v_mfma_f32_16x16x32_bf16 v[92:95], a[172:175], v[216:219], v[92:95]\n"
    "s_add_u32 s98, s41, 0x1e000\n"
    "v_mfma_f32_16x16x32_bf16 v[96:99], a[172:175], v[220:223], v[96:99]\n"
    "buffer_load_dwordx4 v23, s[60:63], s98 offen lds\n"
    "v_mfma_f32_16x16x32_bf16 v[100:103], a[172:175], v[224:227], v[100:103]\n"
    "ds_read_b64_tr_b16 a[184:185], v6 offset:32\n"
    "ds_read_b64_tr_b16 a[186:187], v6 offset:5152\n"
    "v_mfma_f32_16x16x32_bf16 v[104:107], a[172:175], v[228:231], v[104:107]\n"
    "s_waitcnt lgkmcnt(2)\n"
    "v_mfma_f32_16x16x32_bf16 v[108:111], a[176:179], v[212:215], v[108:111]\n"
    "buffer_load_dwordx4 v24, s[60:63], s98 offen offset:1024 lds\n"
    "v_mfma_f32_16x16x32_bf16 v[112:115], a[176:179], v[216:219], v[112:115]\n"
    "v_mfma_f32_16x16x32_bf16 v[116:119], a[176:179], v[220:223], v[116:119]\n"
    "v_mfma_f32_16x16x32_bf16 v[120:123], a[176:179], v[224:227], v[120:123]\n"
    "ds_read_b64_tr_b16 a[188:189], v7 offset:64\n"
    "ds_read_b64_tr_b16 a[190:191], v7 offset:5184\n"
    "v_mfma_f32_16x16x32_bf16 v[124:127], a[176:179], v[228:231], v[124:127]\n"
    "buffer_load_dwordx4 v25, s[60:63], s98 offen offset:2048 lds\n"
    "v_mfma_f32_16x16x32_bf16 a[0:3], a[180:183], v[212:215], a[0:3]\n"
    "v_mfma_f32_16x16x32_bf16 a[4:7], a[180:183], v[216:219], a[4:7]\n"
    "buffer_load_dwordx4 v26, s[60:63], s98 offen offset:3072 lds\n"
    "v_mfma_f32_16x16x32_bf16 a[8:11], a[180:183], v[220:223], a[8:11]\n"
    "s_add_u32 m0, s85, 0x9000\n"
    "v_mfma_f32_16x16x32_bf16 a[12:15], a[180:183], v[224:227], a[12:15]\n"
    "ds_read_b64_tr_b16 a[144:145], v6 offset:96\n"
    "ds_read_b64_tr_b16 a[146:147], v6 offset:5216\n"
    "v_add_u32 v6, 0x5000, v6\n"
    "v_mfma_f32_16x16x32_bf16 a[16:19], a[180:183], v[228:231], a[16:19]\n"
    "s_waitcnt lgkmcnt(2)\n"
    "v_mfma_f32_16x16x32_bf16 a[20:23], a[184:187], v[212:215], a[20:23]\n"
    "ds_read_b64_tr_b16 a[148:149], v7 offset:128\n"
    "ds_read_b64_tr_b16 a[150:151], v7 offset:5248\n"
    "v_add_u32 v7, 0x5000, v7\n"
    "v_mfma_f32_16x16x32_bf16 a[24:27], a[184:187], v[216:219], a[24:27]\n"
    "buffer_load_dwordx4 v27, s[60:63], s98 offen lds\n"
    "v_mfma_f32_16x16x32_bf16 a[28:31], a[184:187], v[220:223], a[28:31]\n"
    "v_mfma_f32_16x16x32_bf16 a[32:35], a[184:187], v[224:227], a[32:35]\n"
    "v_mfma_f32_16x16x32_bf16 a[36:39], a[184:187], v[228:231], a[36:39]\n"
    "v_mfma_f32_16x16x32_bf16 a[40:43], a[188:191], v[212:215], a[40:43]\n"
    "v_mfma_f32_16x16x32_bf16 a[44:47], a[188:191], v[216:219], a[44:47]\n"
    "v_mfma_f32_16x16x32_bf16 a[48:51], a[188:191], v[220:223], a[48:51]\n"
    "v_mfma_f32_16x16x32_bf16 a[52:55], a[188:191], v[224:227], a[52:55]\n"
    "v_mfma_f32_16x16x32_bf16 a[56:59], a[188:191], v[228:231], a[56:59]\n"
    "s_waitcnt vmcnt(15) lgkmcnt(0)\n"
    "s_barrier\n"
    "v_mfma_f32_16x16x32_bf16 a[60:63], a[144:147], v[212:215], a[60:63]\n"
    "ds_read_b64_tr_b16 a[152:153], v3\n"
    "ds_read_b64_tr_b16 a[154:155], v3 offset:5120\n"
    "v_mfma_f32_16x16x32_bf16 a[64:67], a[144:147], v[216:219], a[64:67]\n"
    "v_mfma_f32_16x16x32_bf16 a[68:71], a[144:147], v[220:223], a[68:71]\n"
    "v_mfma_f32_16x16x32_bf16 a[72:75], a[144:147], v[224:227], a[72:75]\n"
    "ds_read_b64_tr_b16 a[156:157], v5 offset:32\n"
    "ds_read_b64_tr_b16 a[158:159], v5 offset:5152\n"
    "v_mfma_f32_16x16x32_bf16 a[76:79], a[144:147], v[228:231], a[76:79]\n"
    "v_mfma_f32_16x16x32_bf16 a[80:83], a[148:151], v[212:215], a[80:83]\n"
    "v_mfma_f32_16x16x32_bf16 a[84:87], a[148:151], v[216:219], a[84:87]\n"
    "v_mfma_f32_16x16x32_bf16 a[88:91], a[148:151], v[220:223], a[88:91]\n"
    "v_mfma_f32_16x16x32_bf16 a[92:95], a[148:151], v[224:227], a[92:95]\n"
    "ds_read_b64_tr_b16 a[160:161], v3 offset:64\n"
    "ds_read_b64_tr_b16 a[162:163], v3 offset:5184\n"
    "v_mfma_f32_16x16x32_bf16 a[96:99], a[148:151], v[228:231], a[96:99]\n"
    "s_waitcnt lgkmcnt(2)\n"
    "v_mfma_f32_16x16x32_bf16 v[28:31], a[152:155], v[232:235], v[28:31]\n"
    "s_add_u32 s76, s78, 0\n"
    "v_mfma_f32_16x16x32_bf16 v[32:35], a[152:155], v[236:239], v[32:35]\n"
    "s_add_u32 s77, s78, 0x1000\n"
    "v_mfma_f32_16x16x32_bf16 v[36:39], a[152:155], v[240:243], v[36:39]\n"
    "buffer_load_dwordx4 v[148:151], v1, s[52:55], s76 offen sc1\n"
    "v_mfma_f32_16x16x32_bf16 v[40:43], a[152:155], v[244:247], v[40:43]\n"
    "ds_read_b64_tr_b16 a[164:165], v5 offset:96\n"
    "ds_read_b64_tr_b16 a[166:167], v5 offset:5216\n"
    "v_add_u32 v5, 0xfffde000, v5\n"
    "v_mfma_f32_16x16x32_bf16 v[44:47], a[152:155], v[248:251], v[44:47]\n"
    "v_mfma_f32_16x16x32_bf16 v[48:51], a[156:159], v[232:235], v[48:51]\n"
    "buffer_load_dwordx4 v[152:155], v1, s[52:55], s76 offen offset:1024 sc1\n"
    "v_mfma_f32_16x16x32_bf16 v[52:55], a[156:159], v[236:239], v[52:55]\n"
    "v_mfma_f32_16x16x32_bf16 v[56:59], a[156:159], v[240:243], v[56:59]\n"
    "v_mfma_f32_16x16x32_bf16 v[60:63], a[156:159], v[244:247], v[60:63]\n"
    "ds_read_b64_tr_b16 a[168:169], v3 offset:128\n"
    "ds_read_b64_tr_b16 a[170:171], v3 offset:5248\n"
    "v_add_u32 v3, 0xfffde000, v3\n"
    "v_mfma_f32_16x16x32_bf16 v[64:67], a[156:159], v[248:251], v[64:67]\n"
    "s_waitcnt lgkmcnt(2)\n"
    "v_mfma_f32_16x16x32_bf16 v[68:71], a[160:163], v[232:235], v[68:71]\n"
    "buffer_load_dwordx4 v[156:159], v1, s[52:55], s76 offen offset:2048 sc1\n"
    "v_mfma_f32_16x16x32_bf16 v[72:75], a[160:163], v[236:239], v[72:75]\n"
    "buffer_load_dwordx4 v[160:163], v1, s[52:55], s76 offen offset:3072 sc1\n"
    "v_mfma_f32_16x16x32_bf16 v[76:79], a[160:163], v[240:243], v[76:79]\n"
    "v_mfma_f32_16x16x32_bf16 v[80:83], a[160:163], v[244:247], v[80:83]\n"
    "ds_read_b64_tr_b16 a[172:173], v7\n"
    "ds_read_b64_tr_b16 a[174:175], v7 offset:5120\n"
    "v_mfma_f32_16x16x32_bf16 v[84:87], a[160:163], v[248:251], v[84:87]\n"
    "buffer_load_dwordx4 v[164:167], v1, s[52:55], s77 offen sc1\n"
    "v_mfma_f32_16x16x32_bf16 v[88:91], a[164:167], v[232:235], v[88:91]\n"
    "s_add_u32 m0, s85, 0xd000\n"
    "v_mfma_f32_16x16x32_bf16 v[92:95], a[164:167], v[236:239], v[92:95]\n"
    "s_add_u32 s98, s42, 0\n"
    "v_mfma_f32_16x16x32_bf16 v[96:99], a[164:167], v[240:243], v[96:99]\n"
    "buffer_load_dwordx4 v23, s[60:63], s98 offen lds\n"
    "v_mfma_f32_16x16x32_bf16 v[100:103], a[164:167], v[244:247], v[100:103]\n"
    "ds_read_b64_tr_b16 a[176:177], v6 offset:32\n"
    "ds_read_b64_tr_b16 a[178:179], v6 offset:5152\n"
    "v_mfma_f32_16x16x32_bf16 v[104:107], a[164:167], v[248:251], v[104:107]\n"
    "s_waitcnt lgkmcnt(2)\n"
    "v_mfma_f32_16x16x32_bf16 v[108:111], a[168:171], v[232:235], v[108:111]\n"
    "buffer_load_dwordx4 v24, s[60:63], s98 offen offset:1024 lds\n"
    "v_mfma_f32_16x16x32_bf16 v[112:115], a[168:171], v[236:239], v[112:115]\n"
    "v_mfma_f32_16x16x32_bf16 v[116:119], a[168:171], v[240:243], v[116:119]\n"
    "v_mfma_f32_16x16x32_bf16 v[120:123], a[168:171], v[244:247], v[120:123]\n"
    "ds_read_b64_tr_b16 a[180:181], v7 offset:64\n"
    "ds_read_b64_tr_b16 a[182:183], v7 offset:5184\n"
    "v_mfma_f32_16x16x32_bf16 v[124:127], a[168:171], v[248:251], v[124:127]\n"
    "buffer_load_dwordx4 v25, s[60:63], s98 offen offset:2048 lds\n"
    "v_mfma_f32_16x16x32_bf16 a[0:3], a[172:175], v[232:235], a[0:3]\n"
    "v_mfma_f32_16x16x32_bf16 a[4:7], a[172:175], v[236:239], a[4:7]\n"
    "buffer_load_dwordx4 v26, s[60:63], s98 offen offset:3072 lds\n"
    "v_mfma_f32_16x16x32_bf16 a[8:11], a[172:175], v[240:243], a[8:11]\n"
    "s_add_u32 m0, s85, 0xe000\n"
    "v_mfma_f32_16x16x32_bf16 a[12:15], a[172:175], v[244:247], a[12:15]\n"
    "ds_read_b64_tr_b16 a[184:185], v6 offset:96\n"
    "ds_read_b64_tr_b16 a[186:187], v6 offset:5216\n"
    "v_add_u32 v6, 0xfffde000, v6\n"
    "v_mfma_f32_16x16x32_bf16 a[16:19], a[172:175], v[248:251], a[16:19]\n"
    "s_waitcnt lgkmcnt(2)\n"
    "v_mfma_f32_16x16x32_bf16 a[20:23], a[176:179], v[232:235], a[20:23]\n"
    "ds_read_b64_tr_b16 a[188:189], v7 offset:128\n"
    "ds_read_b64_tr_b16 a[190:191], v7 offset:5248\n"
    "v_add_u32 v7, 0xfffde000, v7\n"
    "v_mfma_f32_16x16x32_bf16 a[24:27], a[176:179], v[236:239], a[24:27]\n"
    "buffer_load_dwordx4 v27, s[60:63], s98 offen lds\n"
    "v_mfma_f32_16x16x32_bf16 a[28:31], a[176:179], v[240:243], a[28:31]\n"
    "v_mfma_f32_16x16x32_bf16 a[32:35], a[176:179], v[244:247], a[32:35]\n"
    "v_mfma_f32_16x16x32_bf16 a[36:39], a[176:179], v[248:251], a[36:39]\n"
    "v_mfma_f32_16x16x32_bf16 a[40:43], a[180:183], v[232:235], a[40:43]\n"
    "v_mfma_f32_16x16x32_bf16 a[44:47], a[180:183], v[236:239], a[44:47]\n"
    "v_mfma_f32_16x16x32_bf16 a[48:51], a[180:183], v[240:243], a[48:51]\n"
    "v_mfma_f32_16x16x32_bf16 a[52:55], a[180:183], v[244:247], a[52:55]\n"
    "v_mfma_f32_16x16x32_bf16 a[56:59], a[180:183], v[248:251], a[56:59]\n"
    "s_waitcnt vmcnt(20) lgkmcnt(0)\n"
    "s_barrier\n"
    "v_mfma_f32_16x16x32_bf16 a[60:63], a[184:187], v[232:235], a[60:63]\n"
    "ds_read_b64_tr_b16 a[144:145], v3\n"
    "ds_read_b64_tr_b16 a[146:147], v3 offset:5120\n"
    "v_mfma_f32_16x16x32_bf16 a[64:67], a[184:187], v[236:239], a[64:67]\n"
    "v_mfma_f32_16x16x32_bf16 a[68:71], a[184:187], v[240:243], a[68:71]\n"
    "v_mfma_f32_16x16x32_bf16 a[72:75], a[184:187], v[244:247], a[72:75]\n"
    "ds_read_b64_tr_b16 a[148:149], v5 offset:32\n"
    "ds_read_b64_tr_b16 a[150:151], v5 offset:5152\n"
    "v_mfma_f32_16x16x32_bf16 a[76:79], a[184:187], v[248:251], a[76:79]\n"
    "v_mfma_f32_16x16x32_bf16 a[80:83], a[188:191], v[232:235], a[80:83]\n"
    "v_mfma_f32_16x16x32_bf16 a[84:87], a[188:191], v[236:239], a[84:87]\n"
    "v_mfma_f32_16x16x32_bf16 a[88:91], a[188:191], v[240:243], a[88:91]\n"
    "v_mfma_f32_16x16x32_bf16 a[92:95], a[188:191], v[244:247], a[92:95]\n"
    "ds_read_b64_tr_b16 a[152:153], v3 offset:64\n"
    "ds_read_b64_tr_b16 a[154:155], v3 offset:5184\n"
    "v_mfma_f32_16x16x32_bf16 a[96:99], a[188:191], v[248:251], a[96:99]\n"
    "s_waitcnt lgkmcnt(2)\n"
    "v_mfma_f32_16x16x32_bf16 v[28:31], a[144:147], v[128:131], v[28:31]\n"
    "s_add_u32 s76, s78, 0x2800\n"
    "v_mfma_f32_16x16x32_bf16 v[32:35], a[144:147], v[132:135], v[32:35]\n"
    "s_add_u32 s77, s78, 0x3800\n"
    "v_mfma_f32_16x16x32_bf16 v[36:39], a[144:147], v[136:139], v[36:39]\n"
    "buffer_load_dwordx4 v[192:195], v1, s[52:55], s76 offen sc1\n"
    "v_mfma_f32_16x16x32_bf16 v[40:43], a[144:147], v[140:143], v[40:43]\n"
    "s_lshr_b32 s74, s90, 2\n"
    "ds_read_b64_tr_b16 a[156:157], v5 offset:96\n"
    "ds_read_b64_tr_b16 a[158:159], v5 offset:5216\n"
    "v_add_u32 v5, 0x8000, v5\n"
    "v_mfma_f32_16x16x32_bf16 v[44:47], a[144:147], v[144:147], v[44:47]\n"
    "s_lshl_b32 s74, s74, 16\n"
    "v_mfma_f32_16x16x32_bf16 v[48:51], a[148:151], v[128:131], v[48:51]\n"
    "buffer_load_dwordx4 v[196:199], v1, s[52:55], s76 offen offset:1024 sc1\n"
    "v_mfma_f32_16x16x32_bf16 v[52:55], a[148:151], v[132:135], v[52:55]\n"
    "s_lshl_b32 s81, s72, 14\n"
    "v_mfma_f32_16x16x32_bf16 v[56:59], a[148:151], v[136:139], v[56:59]\n"
    "s_add_u32 s48, s74, s81\n"
    "v_mfma_f32_16x16x32_bf16 v[60:63], a[148:151], v[140:143], v[60:63]\n"
    "ds_read_b64_tr_b16 a[160:161], v3 offset:128\n"
    "ds_read_b64_tr_b16 a[162:163], v3 offset:5248\n"
    "v_add_u32 v3, 0x8000, v3\n"
    "v_mfma_f32_16x16x32_bf16 v[64:67], a[148:151], v[144:147], v[64:67]\n"
    "s_waitcnt lgkmcnt(2)\n"
    "v_mfma_f32_16x16x32_bf16 v[68:71], a[152:155], v[128:131], v[68:71]\n"
    "buffer_load_dwordx4 v[200:203], v1, s[52:55], s76 offen offset:2048 sc1\n"
    "v_mfma_f32_16x16x32_bf16 v[72:75], a[152:155], v[132:135], v[72:75]\n"
    "buffer_load_dwordx4 v[204:207], v1, s[52:55], s76 offen offset:3072 sc1\n"
    "v_mfma_f32_16x16x32_bf16 v[76:79], a[152:155], v[136:139], v[76:79]\n"
    "s_add_u32 s49, s48, 0x1000\n"
    "v_mfma_f32_16x16x32_bf16 v[80:83], a[152:155], v[140:143], v[80:83]\n"
    "s_add_u32 s50, s48, 0x2000\n"
    "ds_read_b64_tr_b16 a[164:165], v7\n"
    "ds_read_b64_tr_b16 a[166:167], v7 offset:5120\n"
    "v_mfma_f32_16x16x32_bf16 v[84:87], a[152:155], v[144:147], v[84:87]\n"
    "buffer_load_dwordx4 v[208:211], v1, s[52:55], s77 offen sc1\n"
    "v_mfma_f32_16x16x32_bf16 v[88:91], a[156:159], v[128:131], v[88:91]\n"
    "s_add_u32 m0, s85, 0x12000\n"
    "v_mfma_f32_16x16x32_bf16 v[92:95], a[156:159], v[132:135], v[92:95]\n"
    "s_add_u32 s98, s42, 0xa000\n"
    "v_mfma_f32_16x16x32_bf16 v[96:99], a[156:159], v[136:139], v[96:99]\n"
    "buffer_load_dwordx4 v23, s[60:63], s98 offen lds\n"
    "v_mfma_f32_16x16x32_bf16 v[100:103], a[156:159], v[140:143], v[100:103]\n"
    "s_add_u32 s51, s48, 0x3000\n"
    "ds_read_b64_tr_b16 a[168:169], v6 offset:32\n"
    "ds_read_b64_tr_b16 a[170:171], v6 offset:5152\n"
    "v_mfma_f32_16x16x32_bf16 v[104:107], a[156:159], v[144:147], v[104:107]\n"
    "s_waitcnt lgkmcnt(2)\n"
    "v_mfma_f32_16x16x32_bf16 v[108:111], a[160:163], v[128:131], v[108:111]\n"
    "buffer_load_dwordx4 v24, s[60:63], s98 offen offset:1024 lds\n"
    "v_mfma_f32_16x16x32_bf16 v[112:115], a[160:163], v[132:135], v[112:115]\n"
    "s_lshr_b32 s74, s90, 2\n"
    "v_mfma_f32_16x16x32_bf16 v[116:119], a[160:163], v[136:139], v[116:119]\n"
    "s_cmp_eq_u32 s74, 0\n"
    "s_cselect_b32 s48, 0x40000, s48\n"
    "v_mfma_f32_16x16x32_bf16 v[120:123], a[160:163], v[140:143], v[120:123]\n"
    "ds_read_b64_tr_b16 a[172:173], v7 offset:64\n"
    "ds_read_b64_tr_b16 a[174:175], v7 offset:5184\n"
    "v_mfma_f32_16x16x32_bf16 v[124:127], a[160:163], v[144:147], v[124:127]\n"
    "buffer_load_dwordx4 v25, s[60:63], s98 offen offset:2048 lds\n"
    "v_mfma_f32_16x16x32_bf16 a[0:3], a[164:167], v[128:131], a[0:3]\n"
    "s_cmp_eq_u32 s74, 1\n"
    "s_cselect_b32 s49, 0x40000, s49\n"
    "v_mfma_f32_16x16x32_bf16 a[4:7], a[164:167], v[132:135], a[4:7]\n"
    "buffer_load_dwordx4 v26, s[60:63], s98 offen offset:3072 lds\n"
    "v_mfma_f32_16x16x32_bf16 a[8:11], a[164:167], v[136:139], a[8:11]\n"
    "s_add_u32 m0, s85, 0x13000\n"
    "v_mfma_f32_16x16x32_bf16 a[12:15], a[164:167], v[140:143], a[12:15]\n"
    "s_cmp_eq_u32 s74, 2\n"
    "s_cselect_b32 s50, 0x40000, s50\n"
    "ds_read_b64_tr_b16 a[176:177], v6 offset:96\n"
    "v_mfma_f32_16x16x32_bf16 a[16:19], a[164:167], v[144:147], a[16:19]\n"
    "s_waitcnt lgkmcnt(1)\n"
    "ds_read_b64_tr_b16 a[178:179], v6 offset:5216\n"
    "ds_read_b64_tr_b16 a[180:181], v7 offset:128\n"
    "v_add_u32 v6, 0x8000, v6\n"
    "v_mfma_f32_16x16x32_bf16 a[20:23], a[168:171], v[128:131], a[20:23]\n"
    "ds_read_b64_tr_b16 a[182:183], v7 offset:5248\n"
    "v_add_u32 v7, 0x8000, v7\n"
    "v_mfma_f32_16x16x32_bf16 a[24:27], a[168:171], v[132:135], a[24:27]\n"
    "buffer_load_dwordx4 v27, s[60:63], s98 offen lds\n"
    "v_mfma_f32_16x16x32_bf16 a[28:31], a[168:171], v[136:139], a[28:31]\n"
    "s_cmp_eq_u32 s74, 3\n"
    "s_cselect_b32 s51, 0x40000, s51\n"
    "v_mfma_f32_16x16x32_bf16 a[32:35], a[168:171], v[140:143], a[32:35]\n"
    "v_mfma_f32_16x16x32_bf16 a[36:39], a[168:171], v[144:147], a[36:39]\n"
    "v_mfma_f32_16x16x32_bf16 a[40:43], a[172:175], v[128:131], a[40:43]\n"
    "v_mfma_f32_16x16x32_bf16 a[44:47], a[172:175], v[132:135], a[44:47]\n"
    "v_mfma_f32_16x16x32_bf16 a[48:51], a[172:175], v[136:139], a[48:51]\n"
    "v_mfma_f32_16x16x32_bf16 a[52:55], a[172:175], v[140:143], a[52:55]\n"
    "v_mfma_f32_16x16x32_bf16 a[56:59], a[172:175], v[144:147], a[56:59]\n"
    "s_waitcnt vmcnt(20) lgkmcnt(0)\n"
    "s_barrier\n"
    "v_mfma_f32_16x16x32_bf16 a[60:63], a[176:179], v[128:131], a[60:63]\n"
    "ds_read_b64_tr_b16 a[184:185], v3\n"
    "ds_read_b64_tr_b16 a[186:187], v3 offset:5120\n"
    "v_mfma_f32_16x16x32_bf16 a[64:67], a[176:179], v[132:135], a[64:67]\n"
    "v_mfma_f32_16x16x32_bf16 a[68:71], a[176:179], v[136:139], a[68:71]\n"
    "v_mfma_f32_16x16x32_bf16 a[72:75], a[176:179], v[140:143], a[72:75]\n"
    "ds_read_b64_tr_b16 a[188:189], v5 offset:32\n"
    "ds_read_b64_tr_b16 a[190:191], v5 offset:5152\n"
    "v_mfma_f32_16x16x32_bf16 a[76:79], a[176:179], v[144:147], a[76:79]\n"
    "v_mfma_f32_16x16x32_bf16 a[80:83], a[180:183], v[128:131], a[80:83]\n"
    "v_mfma_f32_16x16x32_bf16 a[84:87], a[180:183], v[132:135], a[84:87]\n"
    "v_mfma_f32_16x16x32_bf16 a[88:91], a[180:183], v[136:139], a[88:91]\n"
    "v_mfma_f32_16x16x32_bf16 a[92:95], a[180:183], v[140:143], a[92:95]\n"
    "ds_read_b64_tr_b16 a[144:145], v3 offset:64\n"
    "ds_read_b64_tr_b16 a[146:147], v3 offset:5184\n"
    "v_mfma_f32_16x16x32_bf16 a[96:99], a[180:183], v[144:147], a[96:99]\n"
    "s_waitcnt lgkmcnt(2)\n"
    "v_mfma_f32_16x16x32_bf16 v[28:31], a[184:187], v[168:171], v[28:31]\n"
    "s_add_u32 s76, s78, 0x5000\n"
    "v_mfma_f32_16x16x32_bf16 v[32:35], a[184:187], v[172:175], v[32:35]\n"
    "s_add_u32 s77, s78, 0x6000\n"
    "v_mfma_f32_16x16x32_bf16 v[36:39], a[184:187], v[176:179], v[36:39]\n"
    "buffer_load_dwordx4 v[212:215], v1, s[52:55], s76 offen sc1\n"
    "v_mfma_f32_16x16x32_bf16 v[40:43], a[184:187], v[180:183], v[40:43]\n"
    "ds_read_b64_tr_b16 a[148:149], v5 offset:96\n"
    "ds_read_b64_tr_b16 a[150:151], v5 offset:5216\n"
    "v_add_u32 v5, 0x5000, v5\n"
    "v_mfma_f32_16x16x32_bf16 v[44:47], a[184:187], v[184:187], v[44:47]\n"
    "v_mfma_f32_16x16x32_bf16 v[48:51], a[188:191], v[168:171], v[48:51]\n"
    "buffer_load_dwordx4 v[216:219], v1, s[52:55], s76 offen offset:1024 sc1\n"
    "v_mfma_f32_16x16x32_bf16 v[52:55], a[188:191], v[172:175], v[52:55]\n"
    "v_mfma_f32_16x16x32_bf16 v[56:59], a[188:191], v[176:179], v[56:59]\n"
    "v_mfma_f32_16x16x32_bf16 v[60:63], a[188:191], v[180:183], v[60:63]\n"
    "ds_read_b64_tr_b16 a[152:153], v3 offset:128\n"
    "ds_read_b64_tr_b16 a[154:155], v3 offset:5248\n"
    "v_add_u32 v3, 0x5000, v3\n"
    "v_mfma_f32_16x16x32_bf16 v[64:67], a[188:191], v[184:187], v[64:67]\n"
    "s_waitcnt lgkmcnt(2)\n"
    "v_mfma_f32_16x16x32_bf16 v[68:71], a[144:147], v[168:171], v[68:71]\n"
    "buffer_load_dwordx4 v[220:223], v1, s[52:55], s76 offen offset:2048 sc1\n"
    "v_mfma_f32_16x16x32_bf16 v[72:75], a[144:147], v[172:175], v[72:75]\n"
    "buffer_load_dwordx4 v[224:227], v1, s[52:55], s76 offen offset:3072 sc1\n"
    "v_mfma_f32_16x16x32_bf16 v[76:79], a[144:147], v[176:179], v[76:79]\n"
    "v_mfma_f32_16x16x32_bf16 v[80:83], a[144:147], v[180:183], v[80:83]\n"
    "ds_read_b64_tr_b16 a[156:157], v7\n"
    "ds_read_b64_tr_b16 a[158:159], v7 offset:5120\n"
    "v_mfma_f32_16x16x32_bf16 v[84:87], a[144:147], v[184:187], v[84:87]\n"
    "buffer_load_dwordx4 v[228:231], v1, s[52:55], s77 offen sc1\n"
    "v_mfma_f32_16x16x32_bf16 v[88:91], a[148:151], v[168:171], v[88:91]\n"
    "s_add_u32 m0, s85, 0x17000\n"
    "v_mfma_f32_16x16x32_bf16 v[92:95], a[148:151], v[172:175], v[92:95]\n"
    "s_add_u32 s98, s42, 0x14000\n"
    "v_mfma_f32_16x16x32_bf16 v[96:99], a[148:151], v[176:179], v[96:99]\n"
    "buffer_load_dwordx4 v23, s[60:63], s98 offen lds\n"
    "v_mfma_f32_16x16x32_bf16 v[100:103], a[148:151], v[180:183], v[100:103]\n"
    "ds_read_b64_tr_b16 a[160:161], v6 offset:32\n"
    "ds_read_b64_tr_b16 a[162:163], v6 offset:5152\n"
    "v_mfma_f32_16x16x32_bf16 v[104:107], a[148:151], v[184:187], v[104:107]\n"
    "s_waitcnt lgkmcnt(2)\n"
    "v_mfma_f32_16x16x32_bf16 v[108:111], a[152:155], v[168:171], v[108:111]\n"
    "buffer_load_dwordx4 v24, s[60:63], s98 offen offset:1024 lds\n"
    "v_mfma_f32_16x16x32_bf16 v[112:115], a[152:155], v[172:175], v[112:115]\n"
    "v_mfma_f32_16x16x32_bf16 v[116:119], a[152:155], v[176:179], v[116:119]\n"
    "v_mfma_f32_16x16x32_bf16 v[120:123], a[152:155], v[180:183], v[120:123]\n"
    "ds_read_b64_tr_b16 a[164:165], v7 offset:64\n"
    "ds_read_b64_tr_b16 a[166:167], v7 offset:5184\n"
    "v_mfma_f32_16x16x32_bf16 v[124:127], a[152:155], v[184:187], v[124:127]\n"
    "buffer_load_dwordx4 v25, s[60:63], s98 offen offset:2048 lds\n"
    "v_mfma_f32_16x16x32_bf16 a[0:3], a[156:159], v[168:171], a[0:3]\n"
    "v_mfma_f32_16x16x32_bf16 a[4:7], a[156:159], v[172:175], a[4:7]\n"
    "buffer_load_dwordx4 v26, s[60:63], s98 offen offset:3072 lds\n"
    "v_mfma_f32_16x16x32_bf16 a[8:11], a[156:159], v[176:179], a[8:11]\n"
    "s_add_u32 m0, s85, 0x18000\n"
    "v_mfma_f32_16x16x32_bf16 a[12:15], a[156:159], v[180:183], a[12:15]\n"
    "ds_read_b64_tr_b16 a[168:169], v6 offset:96\n"
    "ds_read_b64_tr_b16 a[170:171], v6 offset:5216\n"
    "v_add_u32 v6, 0x5000, v6\n"
    "v_mfma_f32_16x16x32_bf16 a[16:19], a[156:159], v[184:187], a[16:19]\n"
    "s_waitcnt lgkmcnt(2)\n"
    "v_mfma_f32_16x16x32_bf16 a[20:23], a[160:163], v[168:171], a[20:23]\n"
    "ds_read_b64_tr_b16 a[172:173], v7 offset:128\n"
    "ds_read_b64_tr_b16 a[174:175], v7 offset:5248\n"
    "v_add_u32 v7, 0x5000, v7\n"
    "v_mfma_f32_16x16x32_bf16 a[24:27], a[160:163], v[172:175], a[24:27]\n"
    "buffer_load_dwordx4 v27, s[60:63], s98 offen lds\n"
    "v_mfma_f32_16x16x32_bf16 a[28:31], a[160:163], v[176:179], a[28:31]\n"
    "v_mfma_f32_16x16x32_bf16 a[32:35], a[160:163], v[180:183], a[32:35]\n"
    "v_mfma_f32_16x16x32_bf16 a[36:39], a[160:163], v[184:187], a[36:39]\n"
    "v_mfma_f32_16x16x32_bf16 a[40:43], a[164:167], v[168:171], a[40:43]\n"
    "v_mfma_f32_16x16x32_bf16 a[44:47], a[164:167], v[172:175], a[44:47]\n"
    "v_mfma_f32_16x16x32_bf16 a[48:51], a[164:167], v[176:179], a[48:51]\n"
    "v_mfma_f32_16x16x32_bf16 a[52:55], a[164:167], v[180:183], a[52:55]\n"
    "v_mfma_f32_16x16x32_bf16 a[56:59], a[164:167], v[184:187], a[56:59]\n"
    "s_waitcnt vmcnt(20) lgkmcnt(0)\n"
    "s_barrier\n"
    "v_mfma_f32_16x16x32_bf16 a[60:63], a[168:171], v[168:171], a[60:63]\n"
    "ds_read_b64_tr_b16 a[176:177], v3\n"
    "ds_read_b64_tr_b16 a[178:179], v3 offset:5120\n"
    "v_mfma_f32_16x16x32_bf16 a[64:67], a[168:171], v[172:175], a[64:67]\n"
    "v_mfma_f32_16x16x32_bf16 a[68:71], a[168:171], v[176:179], a[68:71]\n"
    "v_mfma_f32_16x16x32_bf16 a[72:75], a[168:171], v[180:183], a[72:75]\n"
    "ds_read_b64_tr_b16 a[180:181], v5 offset:32\n"
    "ds_read_b64_tr_b16 a[182:183], v5 offset:5152\n"
    "v_mfma_f32_16x16x32_bf16 a[76:79], a[168:171], v[184:187], a[76:79]\n"
    "v_mfma_f32_16x16x32_bf16 a[80:83], a[172:175], v[168:171], a[80:83]\n"
    "v_mfma_f32_16x16x32_bf16 a[84:87], a[172:175], v[172:175], a[84:87]\n"
    "v_mfma_f32_16x16x32_bf16 a[88:91], a[172:175], v[176:179], a[88:91]\n"
    "v_mfma_f32_16x16x32_bf16 a[92:95], a[172:175], v[180:183], a[92:95]\n"
    "ds_read_b64_tr_b16 a[184:185], v3 offset:64\n"
    "ds_read_b64_tr_b16 a[186:187], v3 offset:5184\n"
    "v_mfma_f32_16x16x32_bf16 a[96:99], a[172:175], v[184:187], a[96:99]\n"
    "s_waitcnt lgkmcnt(2)\n"
    "v_mfma_f32_16x16x32_bf16 v[28:31], a[176:179], v[148:151], v[28:31]\n"
    "s_add_u32 s76, s78, 0x7800\n"
    "v_mfma_f32_16x16x32_bf16 v[32:35], a[176:179], v[152:155], v[32:35]\n"
    "s_add_u32 s77, s78, 0x8800\n"
    "v_mfma_f32_16x16x32_bf16 v[36:39], a[176:179], v[156:159], v[36:39]\n"
    "buffer_load_dwordx4 v[232:235], v1, s[52:55], s76 offen sc1\n"
    "v_mfma_f32_16x16x32_bf16 v[40:43], a[176:179], v[160:163], v[40:43]\n"
    "ds_read_b64_tr_b16 a[188:189], v5 offset:96\n"
    "ds_read_b64_tr_b16 a[190:191], v5 offset:5216\n"
    "v_add_u32 v5, 0x5000, v5\n"
    "v_mfma_f32_16x16x32_bf16 v[44:47], a[176:179], v[164:167], v[44:47]\n"
    "v_mfma_f32_16x16x32_bf16 v[48:51], a[180:183], v[148:151], v[48:51]\n"
    "buffer_load_dwordx4 v[236:239], v1, s[52:55], s76 offen offset:1024 sc1\n"
    "v_mfma_f32_16x16x32_bf16 v[52:55], a[180:183], v[152:155], v[52:55]\n"
    "v_mfma_f32_16x16x32_bf16 v[56:59], a[180:183], v[156:159], v[56:59]\n"
    "v_mfma_f32_16x16x32_bf16 v[60:63], a[180:183], v[160:163], v[60:63]\n"
    "ds_read_b64_tr_b16 a[144:145], v3 offset:128\n"
    "ds_read_b64_tr_b16 a[146:147], v3 offset:5248\n"
    "v_add_u32 v3, 0x5000, v3\n"
    "v_mfma_f32_16x16x32_bf16 v[64:67], a[180:183], v[164:167], v[64:67]\n"
    "s_waitcnt lgkmcnt(2)\n"
    "v_mfma_f32_16x16x32_bf16 v[68:71], a[184:187], v[148:151], v[68:71]\n"
    "buffer_load_dwordx4 v[240:243], v1, s[52:55], s76 offen offset:2048 sc1\n"
    "v_mfma_f32_16x16x32_bf16 v[72:75], a[184:187], v[152:155], v[72:75]\n"
    "buffer_load_dwordx4 v[244:247], v1, s[52:55], s76 offen offset:3072 sc1\n"
    "v_mfma_f32_16x16x32_bf16 v[76:79], a[184:187], v[156:159], v[76:79]\n"
    "v_mfma_f32_16x16x32_bf16 v[80:83], a[184:187], v[160:163], v[80:83]\n"
    "ds_read_b64_tr_b16 a[148:149], v7\n"
    "ds_read_b64_tr_b16 a[150:151], v7 offset:5120\n"
    "v_mfma_f32_16x16x32_bf16 v[84:87], a[184:187], v[164:167], v[84:87]\n"
    "buffer_load_dwordx4 v[248:251], v1, s[52:55], s77 offen sc1\n"
    "v_mfma_f32_16x16x32_bf16 v[88:91], a[188:191], v[148:151], v[88:91]\n"
    "s_add_u32 m0, s85, 0x1d000\n"
    "v_mfma_f32_16x16x32_bf16 v[92:95], a[188:191], v[152:155], v[92:95]\n"
    "s_add_u32 s98, s42, 0x1e000\n"
    "v_mfma_f32_16x16x32_bf16 v[96:99], a[188:191], v[156:159], v[96:99]\n"
    "buffer_load_dwordx4 v23, s[60:63], s98 offen lds\n"
    "v_mfma_f32_16x16x32_bf16 v[100:103], a[188:191], v[160:163], v[100:103]\n"
    "ds_read_b64_tr_b16 a[152:153], v6 offset:32\n"
    "ds_read_b64_tr_b16 a[154:155], v6 offset:5152\n"
    "v_mfma_f32_16x16x32_bf16 v[104:107], a[188:191], v[164:167], v[104:107]\n"
    "s_waitcnt lgkmcnt(2)\n"
    "v_mfma_f32_16x16x32_bf16 v[108:111], a[144:147], v[148:151], v[108:111]\n"
    "buffer_load_dwordx4 v24, s[60:63], s98 offen offset:1024 lds\n"
    "v_mfma_f32_16x16x32_bf16 v[112:115], a[144:147], v[152:155], v[112:115]\n"
    "v_mfma_f32_16x16x32_bf16 v[116:119], a[144:147], v[156:159], v[116:119]\n"
    "v_mfma_f32_16x16x32_bf16 v[120:123], a[144:147], v[160:163], v[120:123]\n"
    "ds_read_b64_tr_b16 a[156:157], v7 offset:64\n"
    "ds_read_b64_tr_b16 a[158:159], v7 offset:5184\n"
    "v_mfma_f32_16x16x32_bf16 v[124:127], a[144:147], v[164:167], v[124:127]\n"
    "buffer_load_dwordx4 v25, s[60:63], s98 offen offset:2048 lds\n"
    "v_mfma_f32_16x16x32_bf16 a[0:3], a[148:151], v[148:151], a[0:3]\n"
    "v_mfma_f32_16x16x32_bf16 a[4:7], a[148:151], v[152:155], a[4:7]\n"
    "buffer_load_dwordx4 v26, s[60:63], s98 offen offset:3072 lds\n"
    "v_mfma_f32_16x16x32_bf16 a[8:11], a[148:151], v[156:159], a[8:11]\n"
    "s_add_u32 m0, s85, 0x1e000\n"
    "v_mfma_f32_16x16x32_bf16 a[12:15], a[148:151], v[160:163], a[12:15]\n"
    "ds_read_b64_tr_b16 a[160:161], v6 offset:96\n"
    "ds_read_b64_tr_b16 a[162:163], v6 offset:5216\n"
    "v_add_u32 v6, 0x5000, v6\n"
    "v_mfma_f32_16x16x32_bf16 a[16:19], a[148:151], v[164:167], a[16:19]\n"
    "s_waitcnt lgkmcnt(2)\n"
    "v_mfma_f32_16x16x32_bf16 a[20:23], a[152:155], v[148:151], a[20:23]\n"
    "ds_read_b64_tr_b16 a[164:165], v7 offset:128\n"
    "ds_read_b64_tr_b16 a[166:167], v7 offset:5248\n"
    "v_add_u32 v7, 0x5000, v7\n"
    "v_mfma_f32_16x16x32_bf16 a[24:27], a[152:155], v[152:155], a[24:27]\n"
    "buffer_load_dwordx4 v27, s[60:63], s98 offen lds\n"
    "v_mfma_f32_16x16x32_bf16 a[28:31], a[152:155], v[156:159], a[28:31]\n"
    "v_mfma_f32_16x16x32_bf16 a[32:35], a[152:155], v[160:163], a[32:35]\n"
    "v_mfma_f32_16x16x32_bf16 a[36:39], a[152:155], v[164:167], a[36:39]\n"
    "v_mfma_f32_16x16x32_bf16 a[40:43], a[156:159], v[148:151], a[40:43]\n"
    "v_mfma_f32_16x16x32_bf16 a[44:47], a[156:159], v[152:155], a[44:47]\n"
    "v_mfma_f32_16x16x32_bf16 a[48:51], a[156:159], v[156:159], a[48:51]\n"
    "v_mfma_f32_16x16x32_bf16 a[52:55], a[156:159], v[160:163], a[52:55]\n"
    "v_mfma_f32_16x16x32_bf16 a[56:59], a[156:159], v[164:167], a[56:59]\n"
    "s_waitcnt vmcnt(20) lgkmcnt(0)\n"
    "s_barrier\n"
    "v_mfma_f32_16x16x32_bf16 a[60:63], a[160:163], v[148:151], a[60:63]\n"
    "ds_read_b64_tr_b16 a[168:169], v3\n"
    "ds_read_b64_tr_b16 a[170:171], v3 offset:5120\n"
    "v_mfma_f32_16x16x32_bf16 a[64:67], a[160:163], v[152:155], a[64:67]\n"
    "v_mfma_f32_16x16x32_bf16 a[68:71], a[160:163], v[156:159], a[68:71]\n"
    "v_mfma_f32_16x16x32_bf16 a[72:75], a[160:163], v[160:163], a[72:75]\n"
    "ds_read_b64_tr_b16 a[172:173], v5 offset:32\n"
    "ds_read_b64_tr_b16 a[174:175], v5 offset:5152\n"
    "v_mfma_f32_16x16x32_bf16 a[76:79], a[160:163], v[164:167], a[76:79]\n"
    "v_mfma_f32_16x16x32_bf16 a[80:83], a[164:167], v[148:151], a[80:83]\n"
    "v_mfma_f32_16x16x32_bf16 a[84:87], a[164:167], v[152:155], a[84:87]\n"
    "v_mfma_f32_16x16x32_bf16 a[88:91], a[164:167], v[156:159], a[88:91]\n"
    "v_mfma_f32_16x16x32_bf16 a[92:95], a[164:167], v[160:163], a[92:95]\n"
    "ds_read_b64_tr_b16 a[176:177], v3 offset:64\n"
    "ds_read_b64_tr_b16 a[178:179], v3 offset:5184\n"
    "v_mfma_f32_16x16x32_bf16 a[96:99], a[164:167], v[164:167], a[96:99]\n"
    "s_waitcnt lgkmcnt(2)\n"
    "v_mfma_f32_16x16x32_bf16 v[28:31], a[168:171], v[192:195], v[28:31]\n"
    "s_add_u32 s76, s79, 0\n"
    "v_mfma_f32_16x16x32_bf16 v[32:35], a[168:171], v[196:199], v[32:35]\n"
    "s_add_u32 s77, s79, 0x1000\n"
    "v_mfma_f32_16x16x32_bf16 v[36:39], a[168:171], v[200:203], v[36:39]\n"
    "buffer_load_dwordx4 v[128:131], v1, s[52:55], s76 offen sc1\n"
    "v_mfma_f32_16x16x32_bf16 v[40:43], a[168:171], v[204:207], v[40:43]\n"
    "ds_read_b64_tr_b16 a[180:181], v5 offset:96\n"
    "ds_read_b64_tr_b16 a[182:183], v5 offset:5216\n"
    "v_add_u32 v5, 0x5000, v5\n"
    "v_mfma_f32_16x16x32_bf16 v[44:47], a[168:171], v[208:211], v[44:47]\n"
    "v_mfma_f32_16x16x32_bf16 v[48:51], a[172:175], v[192:195], v[48:51]\n"
    "buffer_load_dwordx4 v[132:135], v1, s[52:55], s76 offen offset:1024 sc1\n"
    "v_mfma_f32_16x16x32_bf16 v[52:55], a[172:175], v[196:199], v[52:55]\n"
    "v_mfma_f32_16x16x32_bf16 v[56:59], a[172:175], v[200:203], v[56:59]\n"
    "v_mfma_f32_16x16x32_bf16 v[60:63], a[172:175], v[204:207], v[60:63]\n"
    "ds_read_b64_tr_b16 a[184:185], v3 offset:128\n"
    "ds_read_b64_tr_b16 a[186:187], v3 offset:5248\n"
    "v_add_u32 v3, 0x5000, v3\n"
    "v_mfma_f32_16x16x32_bf16 v[64:67], a[172:175], v[208:211], v[64:67]\n"
    "s_waitcnt lgkmcnt(2)\n"
    "v_mfma_f32_16x16x32_bf16 v[68:71], a[176:179], v[192:195], v[68:71]\n"
    "buffer_load_dwordx4 v[136:139], v1, s[52:55], s76 offen offset:2048 sc1\n"
    "v_mfma_f32_16x16x32_bf16 v[72:75], a[176:179], v[196:199], v[72:75]\n"
    "buffer_load_dwordx4 v[140:143], v1, s[52:55], s76 offen offset:3072 sc1\n"
    "v_mfma_f32_16x16x32_bf16 v[76:79], a[176:179], v[200:203], v[76:79]\n"
    "v_mfma_f32_16x16x32_bf16 v[80:83], a[176:179], v[204:207], v[80:83]\n"
    "ds_read_b64_tr_b16 a[188:189], v7\n"
    "ds_read_b64_tr_b16 a[190:191], v7 offset:5120\n"
    "v_mfma_f32_16x16x32_bf16 v[84:87], a[176:179], v[208:211], v[84:87]\n"
    "buffer_load_dwordx4 v[144:147], v1, s[52:55], s77 offen sc1\n"
    "v_mfma_f32_16x16x32_bf16 v[88:91], a[180:183], v[192:195], v[88:91]\n"
    "s_add_u32 m0, s85, 0x22000\n"
    "v_mfma_f32_16x16x32_bf16 v[92:95], a[180:183], v[196:199], v[92:95]\n"
    "s_add_u32 s98, s43, 0\n"
    "v_mfma_f32_16x16x32_bf16 v[96:99], a[180:183], v[200:203], v[96:99]\n"
    "buffer_load_dwordx4 v23, s[60:63], s98 offen lds\n"
    "v_mfma_f32_16x16x32_bf16 v[100:103], a[180:183], v[204:207], v[100:103]\n"
    "ds_read_b64_tr_b16 a[144:145], v6 offset:32\n"
    "ds_read_b64_tr_b16 a[146:147], v6 offset:5152\n"
    "v_mfma_f32_16x16x32_bf16 v[104:107], a[180:183], v[208:211], v[104:107]\n"
    "s_waitcnt lgkmcnt(2)\n"
    "v_mfma_f32_16x16x32_bf16 v[108:111], a[184:187], v[192:195], v[108:111]\n"
    "buffer_load_dwordx4 v24, s[60:63], s98 offen offset:1024 lds\n"
    "s_xor_b32 s74, s72, 0\n"
    "s_mul_i32 s75, s74, 0x6400\n"
    "s_add_u32 s75, s75, 0x12000\n"
    "v_add_u32 v4, s75, v1\n"
    "s_add_u32 s75, s75, 0x3400\n"
    "s_cmp_eq_u32 s74, 3\n"
    "s_cselect_b32 s75, 20480, s75\n"
    "v_add_u32 v8, s75, v1\n"
    "s_xor_b32 s74, s72, 2\n"
    "s_mul_i32 s75, s74, 0x6400\n"
    "s_add_u32 s75, s75, 0x12000\n"
    "v_add_u32 v10, s75, v1\n"
    "s_add_u32 s75, s75, 0x3400\n"
    "s_cmp_eq_u32 s74, 3\n"
    "s_cselect_b32 s75, 20480, s75\n"
    "v_add_u32 v12, s75, v1\n"
    "v_and_b32 v9, 15, v0\n"
    "v_lshrrev_b32 v11, 4, v0\n"
    "v_mul_u32_u24 v2, 0x500, v9\n"
    "v_lshl_add_u32 v2, v11, 3, v2\n"
    "v_mov_b32 v13, 0xffff0000\n"
    "v_mfma_f32_16x16x32_bf16 v[112:115], a[184:187], v[196:199], v[112:115]\n"
    "v_mfma_f32_16x16x32_bf16 v[116:119], a[184:187], v[200:203], v[116:119]\n"
    "v_mfma_f32_16x16x32_bf16 v[120:123], a[184:187], v[204:207], v[120:123]\n"
    "ds_read_b64_tr_b16 a[148:149], v7 offset:64\n"
    "ds_read_b64_tr_b16 a[150:151], v7 offset:5184\n"
    "v_mfma_f32_16x16x32_bf16 v[124:127], a[184:187], v[208:211], v[124:127]\n"
    "buffer_load_dwordx4 v25, s[60:63], s98 offen offset:2048 lds\n"
    "v_mfma_f32_16x16x32_bf16 a[0:3], a[188:191], v[192:195], a[0:3]\n"
    "v_mfma_f32_16x16x32_bf16 a[4:7], a[188:191], v[196:199], a[4:7]\n"
    "buffer_load_dwordx4 v26, s[60:63], s98 offen offset:3072 lds\n"
    "v_mfma_f32_16x16x32_bf16 a[8:11], a[188:191], v[200:203], a[8:11]\n"
    "s_add_u32 m0, s85, 0x23000\n"
    "v_mfma_f32_16x16x32_bf16 a[12:15], a[188:191], v[204:207], a[12:15]\n"
    "ds_read_b64_tr_b16 a[152:153], v6 offset:96\n"
    "ds_read_b64_tr_b16 a[154:155], v6 offset:5216\n"
    "v_add_u32 v6, 0x5000, v6\n"
    "v_mfma_f32_16x16x32_bf16 a[16:19], a[188:191], v[208:211], a[16:19]\n"
    "s_waitcnt lgkmcnt(2)\n"
    "v_mfma_f32_16x16x32_bf16 a[20:23], a[144:147], v[192:195], a[20:23]\n"
    "ds_read_b64_tr_b16 a[156:157], v7 offset:128\n"
    "ds_read_b64_tr_b16 a[158:159], v7 offset:5248\n"
    "v_add_u32 v7, 0x5000, v7\n"
    "v_mfma_f32_16x16x32_bf16 a[24:27], a[144:147], v[196:199], a[24:27]\n"
    "buffer_load_dwordx4 v27, s[60:63], s98 offen lds\n"
    "v_mfma_f32_16x16x32_bf16 a[28:31], a[144:147], v[200:203], a[28:31]\n"
    "v_mfma_f32_16x16x32_bf16 a[32:35], a[144:147], v[204:207], a[32:35]\n"
    "v_mfma_f32_16x16x32_bf16 a[36:39], a[144:147], v[208:211], a[36:39]\n"
    "v_mfma_f32_16x16x32_bf16 a[40:43], a[148:151], v[192:195], a[40:43]\n"
    "v_mfma_f32_16x16x32_bf16 a[44:47], a[148:151], v[196:199], a[44:47]\n"
    "v_mfma_f32_16x16x32_bf16 a[48:51], a[148:151], v[200:203], a[48:51]\n"
    "v_mfma_f32_16x16x32_bf16 a[52:55], a[148:151], v[204:207], a[52:55]\n"
    "v_mfma_f32_16x16x32_bf16 a[56:59], a[148:151], v[208:211], a[56:59]\n"
    "s_waitcnt vmcnt(20) lgkmcnt(0)\n"
    "s_barrier\n"
    "v_mfma_f32_16x16x32_bf16 a[60:63], a[152:155], v[192:195], a[60:63]\n"
    "ds_read_b64_tr_b16 a[160:161], v3\n"
    "ds_read_b64_tr_b16 a[162:163], v3 offset:5120\n"
    "v_mfma_f32_16x16x32_bf16 a[64:67], a[152:155], v[196:199], a[64:67]\n"
    "v_mfma_f32_16x16x32_bf16 a[68:71], a[152:155], v[200:203], a[68:71]\n"
    "v_mfma_f32_16x16x32_bf16 a[72:75], a[152:155], v[204:207], a[72:75]\n"
    "ds_read_b64_tr_b16 a[164:165], v5 offset:32\n"
    "ds_read_b64_tr_b16 a[166:167], v5 offset:5152\n"
    "v_mfma_f32_16x16x32_bf16 a[76:79], a[152:155], v[208:211], a[76:79]\n"
    "v_mfma_f32_16x16x32_bf16 a[80:83], a[156:159], v[192:195], a[80:83]\n"
    "v_mfma_f32_16x16x32_bf16 a[84:87], a[156:159], v[196:199], a[84:87]\n"
    "v_mfma_f32_16x16x32_bf16 a[88:91], a[156:159], v[200:203], a[88:91]\n"
    "v_mfma_f32_16x16x32_bf16 a[92:95], a[156:159], v[204:207], a[92:95]\n"
    "ds_read_b64_tr_b16 a[168:169], v3 offset:64\n"
    "ds_read_b64_tr_b16 a[170:171], v3 offset:5184\n"
    "v_mfma_f32_16x16x32_bf16 a[96:99], a[156:159], v[208:211], a[96:99]\n"
    "s_waitcnt lgkmcnt(2)\n"
    "v_mfma_f32_16x16x32_bf16 v[28:31], a[160:163], v[212:215], v[28:31]\n"
    "s_add_u32 s76, s79, 0x2800\n"
    "v_mfma_f32_16x16x32_bf16 v[32:35], a[160:163], v[216:219], v[32:35]\n"
    "s_add_u32 s77, s79, 0x3800\n"
    "v_mfma_f32_16x16x32_bf16 v[36:39], a[160:163], v[220:223], v[36:39]\n"
    "buffer_load_dwordx4 v[168:171], v1, s[52:55], s76 offen sc1\n"
    "v_mfma_f32_16x16x32_bf16 v[40:43], a[160:163], v[224:227], v[40:43]\n"
    "ds_read_b64_tr_b16 a[172:173], v5 offset:96\n"
    "ds_read_b64_tr_b16 a[174:175], v5 offset:5216\n"
    "v_add_u32 v5, 0x6000, v5\n"
    "v_mfma_f32_16x16x32_bf16 v[44:47], a[160:163], v[228:231], v[44:47]\n"
    "v_mfma_f32_16x16x32_bf16 v[48:51], a[164:167], v[212:215], v[48:51]\n"
    "buffer_load_dwordx4 v[172:175], v1, s[52:55], s76 offen offset:1024 sc1\n"
    "v_mfma_f32_16x16x32_bf16 v[52:55], a[164:167], v[216:219], v[52:55]\n"
    "v_mfma_f32_16x16x32_bf16 v[56:59], a[164:167], v[220:223], v[56:59]\n"
    "v_mfma_f32_16x16x32_bf16 v[60:63], a[164:167], v[224:227], v[60:63]\n"
    "ds_read_b64_tr_b16 a[176:177], v3 offset:128\n"
    "ds_read_b64_tr_b16 a[178:179], v3 offset:5248\n"
    "v_add_u32 v3, 0x6000, v3\n"
    "v_mfma_f32_16x16x32_bf16 v[64:67], a[164:167], v[228:231], v[64:67]\n"
    "s_waitcnt lgkmcnt(2)\n"
    "v_mfma_f32_16x16x32_bf16 v[68:71], a[168:171], v[212:215], v[68:71]\n"
    "buffer_load_dwordx4 v[176:179], v1, s[52:55], s76 offen offset:2048 sc1\n"
    "v_mfma_f32_16x16x32_bf16 v[72:75], a[168:171], v[216:219], v[72:75]\n"
    "buffer_load_dwordx4 v[180:183], v1, s[52:55], s76 offen offset:3072 sc1\n"
    "v_mfma_f32_16x16x32_bf16 v[76:79], a[168:171], v[220:223], v[76:79]\n"
    "v_mfma_f32_16x16x32_bf16 v[80:83], a[168:171], v[224:227], v[80:83]\n"
    "ds_read_b64_tr_b16 a[180:181], v7\n"
    "ds_read_b64_tr_b16 a[182:183], v7 offset:5120\n"
    "v_mfma_f32_16x16x32_bf16 v[84:87], a[168:171], v[228:231], v[84:87]\n"
    "buffer_load_dwordx4 v[184:187], v1, s[52:55], s77 offen sc1\n"
    "v_mfma_f32_16x16x32_bf16 v[88:91], a[172:175], v[212:215], v[88:91]\n"
    "s_add_u32 s76, s79, 0x5000\n"
    "v_mfma_f32_16x16x32_bf16 v[92:95], a[172:175], v[216:219], v[92:95]\n"
    "s_add_u32 s77, s79, 0x6000\n"
    "v_mfma_f32_16x16x32_bf16 v[96:99], a[172:175], v[220:223], v[96:99]\n"
    "buffer_load_dwordx4 v[148:151], v1, s[52:55], s76 offen sc1\n"
    "v_mfma_f32_16x16x32_bf16 v[100:103], a[172:175], v[224:227], v[100:103]\n"
    "ds_read_b64_tr_b16 a[184:185], v6 offset:32\n"
    "ds_read_b64_tr_b16 a[186:187], v6 offset:5152\n"
    "v_mfma_f32_16x16x32_bf16 v[104:107], a[172:175], v[228:231], v[104:107]\n"
    "s_waitcnt lgkmcnt(2)\n"
    "v_mfma_f32_16x16x32_bf16 v[108:111], a[176:179], v[212:215], v[108:111]\n"
    "buffer_load_dwordx4 v[152:155], v1, s[52:55], s76 offen offset:1024 sc1\n"
    "v_mfma_f32_16x16x32_bf16 v[112:115], a[176:179], v[216:219], v[112:115]\n"
    "v_mfma_f32_16x16x32_bf16 v[116:119], a[176:179], v[220:223], v[116:119]\n"
    "v_mfma_f32_16x16x32_bf16 v[120:123], a[176:179], v[224:227], v[120:123]\n"
    "ds_read_b64_tr_b16 a[188:189], v7 offset:64\n"
    "ds_read_b64_tr_b16 a[190:191], v7 offset:5184\n"
    "v_mfma_f32_16x16x32_bf16 v[124:127], a[176:179], v[228:231], v[124:127]\n"
    "buffer_load_dwordx4 v[156:159], v1, s[52:55], s76 offen offset:2048 sc1\n"
    "v_mfma_f32_16x16x32_bf16 a[0:3], a[180:183], v[212:215], a[0:3]\n"
    "v_mfma_f32_16x16x32_bf16 a[4:7], a[180:183], v[216:219], a[4:7]\n"
    "buffer_load_dwordx4 v[160:163], v1, s[52:55], s76 offen offset:3072 sc1\n"
    "v_mfma_f32_16x16x32_bf16 a[8:11], a[180:183], v[220:223], a[8:11]\n"
    "v_mfma_f32_16x16x32_bf16 a[12:15], a[180:183], v[224:227], a[12:15]\n"
    "ds_read_b64_tr_b16 a[144:145], v6 offset:96\n"
    "ds_read_b64_tr_b16 a[146:147], v6 offset:5216\n"
    "v_add_u32 v6, 0x6000, v6\n"
    "v_mfma_f32_16x16x32_bf16 a[16:19], a[180:183], v[228:231], a[16:19]\n"
    "s_waitcnt lgkmcnt(2)\n"
    "v_mfma_f32_16x16x32_bf16 a[20:23], a[184:187], v[212:215], a[20:23]\n"
    "ds_read_b64_tr_b16 a[148:149], v7 offset:128\n"
    "ds_read_b64_tr_b16 a[150:151], v7 offset:5248\n"
    "v_add_u32 v7, 0x6000, v7\n"
    "v_mfma_f32_16x16x32_bf16 a[24:27], a[184:187], v[216:219], a[24:27]\n"
    "buffer_load_dwordx4 v[164:167], v1, s[52:55], s77 offen sc1\n"
    "v_mfma_f32_16x16x32_bf16 a[28:31], a[184:187], v[220:223], a[28:31]\n"
    "s_add_u32 s76, s79, 0x7800\n"
    "v_mfma_f32_16x16x32_bf16 a[32:35], a[184:187], v[224:227], a[32:35]\n"
    "s_add_u32 s77, s79, 0x8800\n"
    "v_mfma_f32_16x16x32_bf16 a[36:39], a[184:187], v[228:231], a[36:39]\n"
    "buffer_load_dwordx4 v[192:195], v1, s[52:55], s76 offen sc1\n"
    "v_mfma_f32_16x16x32_bf16 a[40:43], a[188:191], v[212:215], a[40:43]\n"
    "buffer_load_dwordx4 v[196:199], v1, s[52:55], s76 offen offset:1024 sc1\n"
    "v_mfma_f32_16x16x32_bf16 a[44:47], a[188:191], v[216:219], a[44:47]\n"
    "v_mfma_f32_16x16x32_bf16 a[48:51], a[188:191], v[220:223], a[48:51]\n"
    "v_mfma_f32_16x16x32_bf16 a[52:55], a[188:191], v[224:227], a[52:55]\n"
    "buffer_load_dwordx4 v[200:203], v1, s[52:55], s76 offen offset:2048 sc1\n"
    "v_mfma_f32_16x16x32_bf16 a[56:59], a[188:191], v[228:231], a[56:59]\n"
    "s_waitcnt vmcnt(23) lgkmcnt(0)\n"
    "s_barrier\n"
    "v_mfma_f32_16x16x32_bf16 a[60:63], a[144:147], v[212:215], a[60:63]\n"
    "ds_read_b64_tr_b16 a[152:153], v3\n"
    "ds_read_b64_tr_b16 a[154:155], v3 offset:5120\n"
    "v_mfma_f32_16x16x32_bf16 a[64:67], a[144:147], v[216:219], a[64:67]\n"
    "buffer_load_dwordx4 v[204:207], v1, s[52:55], s76 offen offset:3072 sc1\n"
    "v_mfma_f32_16x16x32_bf16 a[68:71], a[144:147], v[220:223], a[68:71]\n"
    "v_mfma_f32_16x16x32_bf16 a[72:75], a[144:147], v[224:227], a[72:75]\n"
    "ds_read_b64_tr_b16 a[156:157], v5 offset:32\n"
    "ds_read_b64_tr_b16 a[158:159], v5 offset:5152\n"
    "v_mfma_f32_16x16x32_bf16 a[76:79], a[144:147], v[228:231], a[76:79]\n"
    "buffer_load_dwordx4 v[208:211], v1, s[52:55], s77 offen sc1\n"
    "v_mfma_f32_16x16x32_bf16 a[80:83], a[148:151], v[212:215], a[80:83]\n"
    "s_add_u32 m0, s85, 0\n"
    "v_mfma_f32_16x16x32_bf16 a[84:87], a[148:151], v[216:219], a[84:87]\n"
    "s_add_u32 s98, s43, 0xa000\n"
    "v_mfma_f32_16x16x32_bf16 a[88:91], a[148:151], v[220:223], a[88:91]\n"
    "buffer_load_dwordx4 v23, s[60:63], s98 offen lds\n"
    "v_mfma_f32_16x16x32_bf16 a[92:95], a[148:151], v[224:227], a[92:95]\n"
    "ds_read_b64_tr_b16 a[160:161], v3 offset:64\n"
    "ds_read_b64_tr_b16 a[162:163], v3 offset:5184\n"
    "v_mfma_f32_16x16x32_bf16 a[96:99], a[148:151], v[228:231], a[96:99]\n"
    "s_waitcnt lgkmcnt(2)\n"
    "v_mfma_f32_16x16x32_bf16 v[28:31], a[152:155], v[232:235], v[28:31]\n"
    "buffer_load_dwordx4 v24, s[60:63], s98 offen offset:1024 lds\n"
    "v_mfma_f32_16x16x32_bf16 v[32:35], a[152:155], v[236:239], v[32:35]\n"
    "v_mfma_f32_16x16x32_bf16 v[36:39], a[152:155], v[240:243], v[36:39]\n"
    "v_mfma_f32_16x16x32_bf16 v[40:43], a[152:155], v[244:247], v[40:43]\n"
    "ds_read_b64_tr_b16 a[164:165], v5 offset:96\n"
    "ds_read_b64_tr_b16 a[166:167], v5 offset:5216\n"
    "v_add_u32 v5, 0x5000, v5\n"
    "v_mfma_f32_16x16x32_bf16 v[44:47], a[152:155], v[248:251], v[44:47]\n"
    "buffer_load_dwordx4 v25, s[60:63], s98 offen offset:2048 lds\n"
    "v_mfma_f32_16x16x32_bf16 v[48:51], a[156:159], v[232:235], v[48:51]\n"
    "v_mfma_f32_16x16x32_bf16 v[52:55], a[156:159], v[236:239], v[52:55]\n"
    "buffer_load_dwordx4 v26, s[60:63], s98 offen offset:3072 lds\n"
    "v_mfma_f32_16x16x32_bf16 v[56:59], a[156:159], v[240:243], v[56:59]\n"
    "s_add_u32 m0, s85, 0x1000\n"
    "v_mfma_f32_16x16x32_bf16 v[60:63], a[156:159], v[244:247], v[60:63]\n"
    "ds_read_b64_tr_b16 a[168:169], v3 offset:128\n"
    "ds_read_b64_tr_b16 a[170:171], v3 offset:5248\n"
    "v_add_u32 v3, 0x5000, v3\n"
    "v_mfma_f32_16x16x32_bf16 v[64:67], a[156:159], v[248:251], v[64:67]\n"
    "s_waitcnt lgkmcnt(2)\n"
    "v_mfma_f32_16x16x32_bf16 v[68:71], a[160:163], v[232:235], v[68:71]\n"
    "buffer_load_dwordx4 v27, s[60:63], s98 offen lds\n"
    "v_mfma_f32_16x16x32_bf16 v[72:75], a[160:163], v[236:239], v[72:75]\n"
    "s_add_u32 m0, s85, 0x8000\n"
    "v_mfma_f32_16x16x32_bf16 v[76:79], a[160:163], v[240:243], v[76:79]\n"
    "s_add_u32 s98, s43, 0x14000\n"
    "v_mfma_f32_16x16x32_bf16 v[80:83], a[160:163], v[244:247], v[80:83]\n"
    "ds_read_b64_tr_b16 a[172:173], v7\n"
    "ds_read_b64_tr_b16 a[174:175], v7 offset:5120\n"
    "v_mfma_f32_16x16x32_bf16 v[84:87], a[160:163], v[248:251], v[84:87]\n"
    "buffer_load_dwordx4 v23, s[60:63], s98 offen lds\n"
    "v_mfma_f32_16x16x32_bf16 v[88:91], a[164:167], v[232:235], v[88:91]\n"
    "buffer_load_dwordx4 v24, s[60:63], s98 offen offset:1024 lds\n"
    "v_mfma_f32_16x16x32_bf16 v[92:95], a[164:167], v[236:239], v[92:95]\n"
    "v_mfma_f32_16x16x32_bf16 v[96:99], a[164:167], v[240:243], v[96:99]\n"
    "v_mfma_f32_16x16x32_bf16 v[100:103], a[164:167], v[244:247], v[100:103]\n"
    "ds_read_b64_tr_b16 a[176:177], v6 offset:32\n"
    "ds_read_b64_tr_b16 a[178:179], v6 offset:5152\n"
    "v_mfma_f32_16x16x32_bf16 v[104:107], a[164:167], v[248:251], v[104:107]\n"
    "s_waitcnt lgkmcnt(2)\n"
    "v_mfma_f32_16x16x32_bf16 v[108:111], a[168:171], v[232:235], v[108:111]\n"
    "buffer_load_dwordx4 v25, s[60:63], s98 offen offset:2048 lds\n"
    "v_mfma_f32_16x16x32_bf16 v[112:115], a[168:171], v[236:239], v[112:115]\n"
    "buffer_load_dwordx4 v26, s[60:63], s98 offen offset:3072 lds\n"
    "v_mfma_f32_16x16x32_bf16 v[116:119], a[168:171], v[240:243], v[116:119]\n"
    "s_add_u32 m0, s85, 0x9000\n"
    "v_mfma_f32_16x16x32_bf16 v[120:123], a[168:171], v[244:247], v[120:123]\n"
    "ds_read_b64_tr_b16 a[180:181], v7 offset:64\n"
    "ds_read_b64_tr_b16 a[182:183], v7 offset:5184\n"
    "v_mfma_f32_16x16x32_bf16 v[124:127], a[168:171], v[248:251], v[124:127]\n"
    "buffer_load_dwordx4 v27, s[60:63], s98 offen lds\n"
    "v_mfma_f32_16x16x32_bf16 a[0:3], a[172:175], v[232:235], a[0:3]\n"
    "s_add_u32 m0, s85, 0xd000\n"
    "v_mfma_f32_16x16x32_bf16 a[4:7], a[172:175], v[236:239], a[4:7]\n"
    "s_add_u32 s98, s43, 0x1e000\n"
    "v_mfma_f32_16x16x32_bf16 a[8:11], a[172:175], v[240:243], a[8:11]\n"
    "buffer_load_dwordx4 v23, s[60:63], s98 offen lds\n"
    "v_mfma_f32_16x16x32_bf16 a[12:15], a[172:175], v[244:247], a[12:15]\n"
    "ds_read_b64_tr_b16 a[184:185], v6 offset:96\n"
    "ds_read_b64_tr_b16 a[186:187], v6 offset:5216\n"
    "v_add_u32 v6, 0x5000, v6\n"
    "v_mfma_f32_16x16x32_bf16 a[16:19], a[172:175], v[248:251], a[16:19]\n"
    "s_waitcnt lgkmcnt(2)\n"
    "v_mfma_f32_16x16x32_bf16 a[20:23], a[176:179], v[232:235], a[20:23]\n"
    "ds_read_b64_tr_b16 a[188:189], v7 offset:128\n"
    "ds_read_b64_tr_b16 a[190:191], v7 offset:5248\n"
    "v_add_u32 v7, 0x5000, v7\n"
    "v_mfma_f32_16x16x32_bf16 a[24:27], a[176:179], v[236:239], a[24:27]\n"
    "buffer_load_dwordx4 v24, s[60:63], s98 offen offset:1024 lds\n"
    "v_mfma_f32_16x16x32_bf16 a[28:31], a[176:179], v[240:243], a[28:31]\n"
    "buffer_load_dwordx4 v25, s[60:63], s98 offen offset:2048 lds\n"
    "v_mfma_f32_16x16x32_bf16 a[32:35], a[176:179], v[244:247], a[32:35]\n"
    "buffer_load_dwordx4 v26, s[60:63], s98 offen offset:3072 lds\n"
    "v_mfma_f32_16x16x32_bf16 a[36:39], a[176:179], v[248:251], a[36:39]\n"
    "s_add_u32 m0, s85, 0xe000\n"
    "v_mfma_f32_16x16x32_bf16 a[40:43], a[180:183], v[232:235], a[40:43]\n"
    "buffer_load_dwordx4 v27, s[60:63], s98 offen lds\n"
    "v_mfma_f32_16x16x32_bf16 a[44:47], a[180:183], v[236:239], a[44:47]\n"
    "v_mfma_f32_16x16x32_bf16 a[48:51], a[180:183], v[240:243], a[48:51]\n"
    "v_mfma_f32_16x16x32_bf16 a[52:55], a[180:183], v[244:247], a[52:55]\n"
    "v_mfma_f32_16x16x32_bf16 a[56:59], a[180:183], v[248:251], a[56:59]\n"
    "s_waitcnt vmcnt(30) lgkmcnt(0)\n"
    "s_barrier\n"
    "v_mfma_f32_16x16x32_bf16 a[60:63], a[184:187], v[232:235], a[60:63]\n"
    "ds_read_b64_tr_b16 a[144:145], v3\n"
    "ds_read_b64_tr_b16 a[146:147], v3 offset:5120\n"
    "v_mfma_f32_16x16x32_bf16 a[64:67], a[184:187], v[236:239], a[64:67]\n"
    "v_mfma_f32_16x16x32_bf16 a[68:71], a[184:187], v[240:243], a[68:71]\n"
    "v_mfma_f32_16x16x32_bf16 a[72:75], a[184:187], v[244:247], a[72:75]\n"
    "ds_read_b64_tr_b16 a[148:149], v5 offset:32\n"
    "ds_read_b64_tr_b16 a[150:151], v5 offset:5152\n"
    "v_mfma_f32_16x16x32_bf16 a[76:79], a[184:187], v[248:251], a[76:79]\n"
    "v_mfma_f32_16x16x32_bf16 a[80:83], a[188:191], v[232:235], a[80:83]\n"
    "v_mfma_f32_16x16x32_bf16 a[84:87], a[188:191], v[236:239], a[84:87]\n"
    "v_mfma_f32_16x16x32_bf16 a[88:91], a[188:191], v[240:243], a[88:91]\n"
    "v_mfma_f32_16x16x32_bf16 a[92:95], a[188:191], v[244:247], a[92:95]\n"
    "ds_read_b64_tr_b16 a[152:153], v3 offset:64\n"
    "ds_read_b64_tr_b16 a[154:155], v3 offset:5184\n"
    "v_mfma_f32_16x16x32_bf16 a[96:99], a[188:191], v[248:251], a[96:99]\n"
    "s_waitcnt lgkmcnt(2)\n"
    "v_mfma_f32_16x16x32_bf16 v[28:31], a[144:147], v[128:131], v[28:31]\n"
    "v_mfma_f32_16x16x32_bf16 v[32:35], a[144:147], v[132:135], v[32:35]\n"
    "v_mfma_f32_16x16x32_bf16 v[36:39], a[144:147], v[136:139], v[36:39]\n"
    "v_mfma_f32_16x16x32_bf16 v[40:43], a[144:147], v[140:143], v[40:43]\n"
    "ds_read_b64_tr_b16 a[156:157], v5 offset:96\n"
    "ds_read_b64_tr_b16 a[158:159], v5 offset:5216\n"
    "v_add_u32 v5, 0xfffde000, v5\n"
    "v_mfma_f32_16x16x32_bf16 v[44:47], a[144:147], v[144:147], v[44:47]\n"
    "v_mfma_f32_16x16x32_bf16 v[48:51], a[148:151], v[128:131], v[48:51]\n"
    "v_mfma_f32_16x16x32_bf16 v[52:55], a[148:151], v[132:135], v[52:55]\n"
    "v_mfma_f32_16x16x32_bf16 v[56:59], a[148:151], v[136:139], v[56:59]\n"
    "v_mfma_f32_16x16x32_bf16 v[60:63], a[148:151], v[140:143], v[60:63]\n"
    "ds_read_b64_tr_b16 a[160:161], v3 offset:128\n"
    "ds_read_b64_tr_b16 a[162:163], v3 offset:5248\n"
    "v_add_u32 v3, 0xfffde000, v3\n"
    "v_mfma_f32_16x16x32_bf16 v[64:67], a[148:151], v[144:147], v[64:67]\n"
    "s_waitcnt lgkmcnt(2)\n"
    "v_mfma_f32_16x16x32_bf16 v[68:71], a[152:155], v[128:131], v[68:71]\n"
    "v_mfma_f32_16x16x32_bf16 v[72:75], a[152:155], v[132:135], v[72:75]\n"
    "v_mfma_f32_16x16x32_bf16 v[76:79], a[152:155], v[136:139], v[76:79]\n"
    "v_mfma_f32_16x16x32_bf16 v[80:83], a[152:155], v[140:143], v[80:83]\n"
    "ds_read_b64_tr_b16 a[164:165], v7\n"
    "ds_read_b64_tr_b16 a[166:167], v7 offset:5120\n"
    "v_mfma_f32_16x16x32_bf16 v[84:87], a[152:155], v[144:147], v[84:87]\n"
    "v_mfma_f32_16x16x32_bf16 v[88:91], a[156:159], v[128:131], v[88:91]\n"
    "v_mfma_f32_16x16x32_bf16 v[92:95], a[156:159], v[132:135], v[92:95]\n"
    "v_mfma_f32_16x16x32_bf16 v[96:99], a[156:159], v[136:139], v[96:99]\n"
    "v_mfma_f32_16x16x32_bf16 v[100:103], a[156:159], v[140:143], v[100:103]\n"
    "ds_read_b64_tr_b16 a[168:169], v6 offset:32\n"
    "ds_read_b64_tr_b16 a[170:171], v6 offset:5152\n"
    "v_mfma_f32_16x16x32_bf16 v[104:107], a[156:159], v[144:147], v[104:107]\n"
    "s_waitcnt lgkmcnt(2)\n"
    "v_mfma_f32_16x16x32_bf16 v[108:111], a[160:163], v[128:131], v[108:111]\n"
    "v_mfma_f32_16x16x32_bf16 v[112:115], a[160:163], v[132:135], v[112:115]\n"
    "v_mfma_f32_16x16x32_bf16 v[116:119], a[160:163], v[136:139], v[116:119]\n"
    "v_mfma_f32_16x16x32_bf16 v[120:123], a[160:163], v[140:143], v[120:123]\n"
    "ds_read_b64_tr_b16 a[172:173], v7 offset:64\n"
    "ds_read_b64_tr_b16 a[174:175], v7 offset:5184\n"
    "v_mfma_f32_16x16x32_bf16 v[124:127], a[160:163], v[144:147], v[124:127]\n"
    "v_mfma_f32_16x16x32_bf16 a[0:3], a[164:167], v[128:131], a[0:3]\n"
    "v_mfma_f32_16x16x32_bf16 a[4:7], a[164:167], v[132:135], a[4:7]\n"
    "v_mfma_f32_16x16x32_bf16 a[8:11], a[164:167], v[136:139], a[8:11]\n"
    "v_mfma_f32_16x16x32_bf16 a[12:15], a[164:167], v[140:143], a[12:15]\n"
    "ds_read_b64_tr_b16 a[176:177], v6 offset:96\n"
    "ds_read_b64_tr_b16 a[178:179], v6 offset:5216\n"
    "v_add_u32 v6, 0xfffde000, v6\n"
    "v_mfma_f32_16x16x32_bf16 a[16:19], a[164:167], v[144:147], a[16:19]\n"
    "s_waitcnt lgkmcnt(2)\n"
    "v_mfma_f32_16x16x32_bf16 a[20:23], a[168:171], v[128:131], a[20:23]\n"
    "ds_read_b64_tr_b16 a[180:181], v7 offset:128\n"
    "ds_read_b64_tr_b16 a[182:183], v7 offset:5248\n"
    "v_add_u32 v7, 0xfffde000, v7\n"
    "v_mfma_f32_16x16x32_bf16 a[24:27], a[168:171], v[132:135], a[24:27]\n"
    "v_mfma_f32_16x16x32_bf16 a[28:31], a[168:171], v[136:139], a[28:31]\n"
    "v_mfma_f32_16x16x32_bf16 a[32:35], a[168:171], v[140:143], a[32:35]\n"
    "v_mfma_f32_16x16x32_bf16 a[36:39], a[168:171], v[144:147], a[36:39]\n"
    "v_mfma_f32_16x16x32_bf16 a[40:43], a[172:175], v[128:131], a[40:43]\n"
    "v_mfma_f32_16x16x32_bf16 a[44:47], a[172:175], v[132:135], a[44:47]\n"
    "v_mfma_f32_16x16x32_bf16 a[48:51], a[172:175], v[136:139], a[48:51]\n"
    "v_mfma_f32_16x16x32_bf16 a[52:55], a[172:175], v[140:143], a[52:55]\n"
    "v_mfma_f32_16x16x32_bf16 a[56:59], a[172:175], v[144:147], a[56:59]\n"
    "s_waitcnt vmcnt(0) lgkmcnt(0)\n"
    "s_barrier\n"
    "v_mfma_f32_16x16x32_bf16 a[60:63], a[176:179], v[128:131], a[60:63]\n"
    "ds_read_b64_tr_b16 a[184:185], v7\n"
    "ds_read_b64_tr_b16 a[186:187], v7 offset:5120\n"
    "v_mfma_f32_16x16x32_bf16 a[64:67], a[176:179], v[132:135], a[64:67]\n"
    "v_mfma_f32_16x16x32_bf16 a[68:71], a[176:179], v[136:139], a[68:71]\n"
    "v_mfma_f32_16x16x32_bf16 a[72:75], a[176:179], v[140:143], a[72:75]\n"
    "ds_read_b64_tr_b16 a[188:189], v7 offset:32768\n"
    "ds_read_b64_tr_b16 a[190:191], v7 offset:37888\n"
    "v_mfma_f32_16x16x32_bf16 a[76:79], a[176:179], v[144:147], a[76:79]\n"
    "v_mfma_f32_16x16x32_bf16 a[80:83], a[180:183], v[128:131], a[80:83]\n"
    "v_mfma_f32_16x16x32_bf16 a[84:87], a[180:183], v[132:135], a[84:87]\n"
    "v_mfma_f32_16x16x32_bf16 a[88:91], a[180:183], v[136:139], a[88:91]\n"
    "v_mfma_f32_16x16x32_bf16 a[92:95], a[180:183], v[140:143], a[92:95]\n"
    "ds_read_b64_tr_b16 a[144:145], v7 offset:53248\n"
    "ds_read_b64_tr_b16 a[146:147], v7 offset:58368\n"
    "v_mfma_f32_16x16x32_bf16 a[96:99], a[180:183], v[144:147], a[96:99]\n"
    "s_waitcnt lgkmcnt(2)\n"
    "v_mfma_f32_16x16x32_bf16 a[0:3], a[184:187], v[168:171], a[0:3]\n"
    "v_mfma_f32_16x16x32_bf16 a[4:7], a[184:187], v[172:175], a[4:7]\n"
    "v_mfma_f32_16x16x32_bf16 a[8:11], a[184:187], v[176:179], a[8:11]\n"
    "v_mfma_f32_16x16x32_bf16 a[12:15], a[184:187], v[180:183], a[12:15]\n"
    "ds_read_b64_tr_b16 a[148:149], v6 offset:32\n"
    "ds_read_b64_tr_b16 a[150:151], v6 offset:5152\n"
    "v_mfma_f32_16x16x32_bf16 a[16:19], a[184:187], v[184:187], a[16:19]\n"
    "v_mfma_f32_16x16x32_bf16 a[0:3], a[188:191], v[148:151], a[0:3]\n"
    "v_mfma_f32_16x16x32_bf16 a[4:7], a[188:191], v[152:155], a[4:7]\n"
    "v_mfma_f32_16x16x32_bf16 a[8:11], a[188:191], v[156:159], a[8:11]\n"
    "v_mfma_f32_16x16x32_bf16 a[12:15], a[188:191], v[160:163], a[12:15]\n"
    "ds_read_b64_tr_b16 a[152:153], v6 offset:32800\n"
    "ds_read_b64_tr_b16 a[154:155], v6 offset:37920\n"
    "v_mfma_f32_16x16x32_bf16 a[16:19], a[188:191], v[164:167], a[16:19]\n"
    "s_waitcnt lgkmcnt(2)\n"
    "v_mfma_f32_16x16x32_bf16 a[0:3], a[144:147], v[192:195], a[0:3]\n"
    "v_mfma_f32_16x16x32_bf16 a[4:7], a[144:147], v[196:199], a[4:7]\n"
    "v_mfma_f32_16x16x32_bf16 a[8:11], a[144:147], v[200:203], a[8:11]\n"
    "v_mfma_f32_16x16x32_bf16 a[12:15], a[144:147], v[204:207], a[12:15]\n"
    "ds_read_b64_tr_b16 a[156:157], v6 offset:53280\n"
    "ds_read_b64_tr_b16 a[158:159], v6 offset:58400\n"
    "v_mfma_f32_16x16x32_bf16 a[16:19], a[144:147], v[208:211], a[16:19]\n"
    "v_mfma_f32_16x16x32_bf16 a[20:23], a[148:151], v[168:171], a[20:23]\n"
    "v_mfma_f32_16x16x32_bf16 a[24:27], a[148:151], v[172:175], a[24:27]\n"
    "v_mfma_f32_16x16x32_bf16 a[28:31], a[148:151], v[176:179], a[28:31]\n"
    "v_mfma_f32_16x16x32_bf16 a[32:35], a[148:151], v[180:183], a[32:35]\n"
    "ds_read_b64_tr_b16 a[160:161], v7 offset:64\n"
    "ds_read_b64_tr_b16 a[162:163], v7 offset:5184\n"
    "v_mfma_f32_16x16x32_bf16 a[36:39], a[148:151], v[184:187], a[36:39]\n"
    "s_waitcnt lgkmcnt(2)\n"
    "v_mfma_f32_16x16x32_bf16 a[20:23], a[152:155], v[148:151], a[20:23]\n"
    "ds_write_b128 v4, a[0:3]\n"
    "v_mfma_f32_16x16x32_bf16 a[24:27], a[152:155], v[152:155], a[24:27]\n"
    "v_mfma_f32_16x16x32_bf16 a[28:31], a[152:155], v[156:159], a[28:31]\n"
    "v_mfma_f32_16x16x32_bf16 a[32:35], a[152:155], v[160:163], a[32:35]\n"
    "ds_read_b64_tr_b16 a[164:165], v7 offset:32832\n"
    "ds_read_b64_tr_b16 a[166:167], v7 offset:37952\n"
    "v_mfma_f32_16x16x32_bf16 a[36:39], a[152:155], v[164:167], a[36:39]\n"
    "ds_write_b128 v4, a[4:7] offset:1024\n"
    "v_mfma_f32_16x16x32_bf16 a[20:23], a[156:159], v[192:195], a[20:23]\n"
    "v_mfma_f32_16x16x32_bf16 a[24:27], a[156:159], v[196:199], a[24:27]\n"
    "ds_write_b128 v4, a[8:11] offset:2048\n"
    "v_mfma_f32_16x16x32_bf16 a[28:31], a[156:159], v[200:203], a[28:31]\n"
    "v_mfma_f32_16x16x32_bf16 a[32:35], a[156:159], v[204:207], a[32:35]\n"
    "ds_read_b64_tr_b16 a[168:169], v7 offset:53312\n"
    "ds_read_b64_tr_b16 a[170:171], v7 offset:58432\n"
    "v_mfma_f32_16x16x32_bf16 a[36:39], a[156:159], v[208:211], a[36:39]\n"
    "s_waitcnt lgkmcnt(4)\n"
    "v_mfma_f32_16x16x32_bf16 a[40:43], a[160:163], v[168:171], a[40:43]\n"
    "ds_write_b128 v4, a[12:15] offset:3072\n"
    "v_mfma_f32_16x16x32_bf16 a[44:47], a[160:163], v[172:175], a[44:47]\n"
    "v_mfma_f32_16x16x32_bf16 a[48:51], a[160:163], v[176:179], a[48:51]\n"
    "ds_write_b128 v4, a[16:19] offset:4096\n"
    "v_mfma_f32_16x16x32_bf16 a[52:55], a[160:163], v[180:183], a[52:55]\n"
    "ds_read_b64_tr_b16 a[172:173], v6 offset:96\n"
    "ds_read_b64_tr_b16 a[174:175], v6 offset:5216\n"
    "v_mfma_f32_16x16x32_bf16 a[56:59], a[160:163], v[184:187], a[56:59]\n"
    "v_mfma_f32_16x16x32_bf16 a[40:43], a[164:167], v[148:151], a[40:43]\n"
    "ds_write_b128 v4, a[20:23] offset:5120\n"
    "v_mfma_f32_16x16x32_bf16 a[44:47], a[164:167], v[152:155], a[44:47]\n"
    "v_mfma_f32_16x16x32_bf16 a[48:51], a[164:167], v[156:159], a[48:51]\n"
    "v_mfma_f32_16x16x32_bf16 a[52:55], a[164:167], v[160:163], a[52:55]\n"
    "ds_read_b64_tr_b16 a[176:177], v6 offset:32864\n"
    "ds_read_b64_tr_b16 a[178:179], v6 offset:37984\n"
    "v_mfma_f32_16x16x32_bf16 a[56:59], a[164:167], v[164:167], a[56:59]\n"
    "s_waitcnt lgkmcnt(3)\n"
    "v_mfma_f32_16x16x32_bf16 a[40:43], a[168:171], v[192:195], a[40:43]\n"
    "ds_write_b128 v4, a[24:27] offset:6144\n"
    "v_mfma_f32_16x16x32_bf16 a[44:47], a[168:171], v[196:199], a[44:47]\n"
    "ds_write_b128 v4, a[28:31] offset:7168\n"
    "v_mfma_f32_16x16x32_bf16 a[48:51], a[168:171], v[200:203], a[48:51]\n"
    "v_mfma_f32_16x16x32_bf16 a[52:55], a[168:171], v[204:207], a[52:55]\n"
    "ds_read_b64_tr_b16 a[180:181], v6 offset:53344\n"
    "ds_read_b64_tr_b16 a[182:183], v6 offset:58464\n"
    "v_mfma_f32_16x16x32_bf16 a[56:59], a[168:171], v[208:211], a[56:59]\n"
    "ds_write_b128 v4, a[32:35] offset:8192\n"
    "v_mfma_f32_16x16x32_bf16 a[60:63], a[172:175], v[168:171], a[60:63]\n"
    "v_mfma_f32_16x16x32_bf16 a[64:67], a[172:175], v[172:175], a[64:67]\n"
    "v_mfma_f32_16x16x32_bf16 a[68:71], a[172:175], v[176:179], a[68:71]\n"
    "ds_write_b128 v4, a[36:39] offset:9216\n"
    "v_mfma_f32_16x16x32_bf16 a[72:75], a[172:175], v[180:183], a[72:75]\n"
    "ds_read_b64_tr_b16 a[184:185], v7 offset:128\n"
    "ds_read_b64_tr_b16 a[186:187], v7 offset:5248\n"
    "v_mfma_f32_16x16x32_bf16 a[76:79], a[172:175], v[184:187], a[76:79]\n"
    "s_waitcnt lgkmcnt(4)\n"
    "v_mfma_f32_16x16x32_bf16 a[60:63], a[176:179], v[148:151], a[60:63]\n"
    "ds_write_b128 v4, a[40:43] offset:10240\n"
    "v_mfma_f32_16x16x32_bf16 a[64:67], a[176:179], v[152:155], a[64:67]\n"
    "v_mfma_f32_16x16x32_bf16 a[68:71], a[176:179], v[156:159], a[68:71]\n"
    "v_mfma_f32_16x16x32_bf16 a[72:75], a[176:179], v[160:163], a[72:75]\n"
    "ds_read_b64_tr_b16 a[188:189], v7 offset:32896\n"
    "ds_read_b64_tr_b16 a[190:191], v7 offset:38016\n"
    "v_mfma_f32_16x16x32_bf16 a[76:79], a[176:179], v[164:167], a[76:79]\n"
    "ds_write_b128 v4, a[44:47] offset:11264\n"
    "v_mfma_f32_16x16x32_bf16 a[60:63], a[180:183], v[192:195], a[60:63]\n"
    "v_mfma_f32_16x16x32_bf16 a[64:67], a[180:183], v[196:199], a[64:67]\n"
    "ds_write_b128 v4, a[48:51] offset:12288\n"
    "v_mfma_f32_16x16x32_bf16 a[68:71], a[180:183], v[200:203], a[68:71]\n"
    "v_mfma_f32_16x16x32_bf16 a[72:75], a[180:183], v[204:207], a[72:75]\n"
    "ds_read_b64_tr_b16 a[144:145], v7 offset:53376\n"
    "ds_read_b64_tr_b16 a[146:147], v7 offset:58496\n"
    "v_mfma_f32_16x16x32_bf16 a[76:79], a[180:183], v[208:211], a[76:79]\n"
    "s_waitcnt lgkmcnt(4)\n"
    "v_mfma_f32_16x16x32_bf16 a[80:83], a[184:187], v[168:171], a[80:83]\n"
    "ds_write_b128 v8, a[52:55]\n"
    "v_mfma_f32_16x16x32_bf16 a[84:87], a[184:187], v[172:175], a[84:87]\n"
    "v_mfma_f32_16x16x32_bf16 a[88:91], a[184:187], v[176:179], a[88:91]\n"
    "ds_write_b128 v8, a[56:59] offset:1024\n"
    "v_mfma_f32_16x16x32_bf16 a[92:95], a[184:187], v[180:183], a[92:95]\n"
    "ds_read_b64_tr_b16 a[148:149], v3\n"
    "ds_read_b64_tr_b16 a[150:151], v3 offset:5120\n"
    "v_mfma_f32_16x16x32_bf16 a[96:99], a[184:187], v[184:187], a[96:99]\n"
    "v_mfma_f32_16x16x32_bf16 a[80:83], a[188:191], v[148:151], a[80:83]\n"
    "ds_write_b128 v8, a[60:63] offset:2048\n"
    "v_mfma_f32_16x16x32_bf16 a[84:87], a[188:191], v[152:155], a[84:87]\n"
    "v_mfma_f32_16x16x32_bf16 a[88:91], a[188:191], v[156:159], a[88:91]\n"
    "v_mfma_f32_16x16x32_bf16 a[92:95], a[188:191], v[160:163], a[92:95]\n"
    "ds_read_b64_tr_b16 a[152:153], v3 offset:32768\n"
    "ds_read_b64_tr_b16 a[154:155], v3 offset:37888\n"
    "v_mfma_f32_16x16x32_bf16 a[96:99], a[188:191], v[164:167], a[96:99]\n"
    "s_waitcnt lgkmcnt(3)\n"
    "v_mfma_f32_16x16x32_bf16 a[80:83], a[144:147], v[192:195], a[80:83]\n"
    "ds_write_b128 v8, a[64:67] offset:3072\n"
    "v_mfma_f32_16x16x32_bf16 a[84:87], a[144:147], v[196:199], a[84:87]\n"
    "ds_write_b128 v8, a[68:71] offset:4096\n"
    "v_mfma_f32_16x16x32_bf16 a[88:91], a[144:147], v[200:203], a[88:91]\n"
    "v_mfma_f32_16x16x32_bf16 a[92:95], a[144:147], v[204:207], a[92:95]\n"
    "ds_read_b64_tr_b16 a[156:157], v3 offset:53248\n"
    "ds_read_b64_tr_b16 a[158:159], v3 offset:58368\n"
    "v_mfma_f32_16x16x32_bf16 a[96:99], a[144:147], v[208:211], a[96:99]\n"
    "ds_write_b128 v8, a[72:75] offset:5120\n"
    "v_mfma_f32_16x16x32_bf16 v[28:31], a[148:151], v[168:171], v[28:31]\n"
    "v_mfma_f32_16x16x32_bf16 v[32:35], a[148:151], v[172:175], v[32:35]\n"
    "v_mfma_f32_16x16x32_bf16 v[36:39], a[148:151], v[176:179], v[36:39]\n"
    "ds_write_b128 v8, a[76:79] offset:6144\n"
    "v_mfma_f32_16x16x32_bf16 v[40:43], a[148:151], v[180:183], v[40:43]\n"
    "ds_read_b64_tr_b16 a[160:161], v5 offset:32\n"
    "ds_read_b64_tr_b16 a[162:163], v5 offset:5152\n"
    "v_mfma_f32_16x16x32_bf16 v[44:47], a[148:151], v[184:187], v[44:47]\n"
    "s_waitcnt lgkmcnt(4)\n"
    "v_mfma_f32_16x16x32_bf16 v[28:31], a[152:155], v[148:151], v[28:31]\n"
    "ds_write_b128 v8, a[80:83] offset:7168\n"
    "v_mfma_f32_16x16x32_bf16 v[32:35], a[152:155], v[152:155], v[32:35]\n"
    "v_mfma_f32_16x16x32_bf16 v[36:39], a[152:155], v[156:159], v[36:39]\n"
    "v_mfma_f32_16x16x32_bf16 v[40:43], a[152:155], v[160:163], v[40:43]\n"
    "ds_read_b64_tr_b16 a[164:165], v5 offset:32800\n"
    "ds_read_b64_tr_b16 a[166:167], v5 offset:37920\n"
    "v_mfma_f32_16x16x32_bf16 v[44:47], a[152:155], v[164:167], v[44:47]\n"
    "ds_write_b128 v8, a[84:87] offset:8192\n"
    "v_mfma_f32_16x16x32_bf16 v[28:31], a[156:159], v[192:195], v[28:31]\n"
    "v_mfma_f32_16x16x32_bf16 v[32:35], a[156:159], v[196:199], v[32:35]\n"
    "ds_write_b128 v8, a[88:91] offset:9216\n"
    "v_mfma_f32_16x16x32_bf16 v[36:39], a[156:159], v[200:203], v[36:39]\n"
    "v_mfma_f32_16x16x32_bf16 v[40:43], a[156:159], v[204:207], v[40:43]\n"
    "ds_read_b64_tr_b16 a[168:169], v5 offset:53280\n"
    "ds_read_b64_tr_b16 a[170:171], v5 offset:58400\n"
    "v_mfma_f32_16x16x32_bf16 v[44:47], a[156:159], v[208:211], v[44:47]\n"
    "s_waitcnt lgkmcnt(4)\n"
    "ds_read_b64_tr_b16 a[172:173], v3 offset:64\n"
    "ds_read_b64_tr_b16 a[174:175], v3 offset:5184\n"
    "v_mfma_f32_16x16x32_bf16 v[48:51], a[160:163], v[168:171], v[48:51]\n"
    "ds_read_b64_tr_b16 a[176:177], v3 offset:32832\n"
    "ds_read_b64_tr_b16 a[178:179], v3 offset:37952\n"
    "v_mfma_f32_16x16x32_bf16 v[52:55], a[160:163], v[172:175], v[52:55]\n"
    "ds_read_b64_tr_b16 a[180:181], v3 offset:53312\n"
    "ds_read_b64_tr_b16 a[182:183], v3 offset:58432\n"
    "v_mfma_f32_16x16x32_bf16 v[56:59], a[160:163], v[176:179], v[56:59]\n"
    "ds_write_b128 v8, a[92:95] offset:10240\n"
    "v_mfma_f32_16x16x32_bf16 v[60:63], a[160:163], v[180:183], v[60:63]\n"
    "ds_write_b128 v8, a[96:99] offset:11264\n"
    "v_mfma_f32_16x16x32_bf16 v[64:67], a[160:163], v[184:187], v[64:67]\n"
    "v_mfma_f32_16x16x32_bf16 v[48:51], a[164:167], v[148:151], v[48:51]\n"
    "v_mfma_f32_16x16x32_bf16 v[52:55], a[164:167], v[152:155], v[52:55]\n"
    "v_mfma_f32_16x16x32_bf16 v[56:59], a[164:167], v[156:159], v[56:59]\n"
    "v_mfma_f32_16x16x32_bf16 v[60:63], a[164:167], v[160:163], v[60:63]\n"
    "s_waitcnt lgkmcnt(0)\n"
    "s_barrier\n"
    "v_mfma_f32_16x16x32_bf16 v[64:67], a[164:167], v[164:167], v[64:67]\n"
    "ds_read_b128 v[212:215], v10\n"
    "v_mfma_f32_16x16x32_bf16 v[48:51], a[168:171], v[192:195], v[48:51]\n"
    "ds_read_b128 v[216:219], v10 offset:1024\n"
    "v_mfma_f32_16x16x32_bf16 v[52:55], a[168:171], v[196:199], v[52:55]\n"
    "ds_read_b128 v[220:223], v10 offset:2048\n"
    "v_mfma_f32_16x16x32_bf16 v[56:59], a[168:171], v[200:203], v[56:59]\n"
    "ds_read_b128 v[224:227], v10 offset:3072\n"
    "v_mfma_f32_16x16x32_bf16 v[60:63], a[168:171], v[204:207], v[60:63]\n"
    "ds_read_b128 v[228:231], v10 offset:4096\n"
    "v_mfma_f32_16x16x32_bf16 v[64:67], a[168:171], v[208:211], v[64:67]\n"
    "ds_read_b128 v[232:235], v10 offset:5120\n"
    "v_mfma_f32_16x16x32_bf16 v[68:71], a[172:175], v[168:171], v[68:71]\n"
    "ds_read_b128 v[236:239], v10 offset:6144\n"
    "v_mfma_f32_16x16x32_bf16 v[72:75], a[172:175], v[172:175], v[72:75]\n"
    "ds_read_b128 v[240:243], v10 offset:7168\n"
    "v_mfma_f32_16x16x32_bf16 v[76:79], a[172:175], v[176:179], v[76:79]\n"
    "ds_read_b128 v[244:247], v10 offset:8192\n"
    "v_mfma_f32_16x16x32_bf16 v[80:83], a[172:175], v[180:183], v[80:83]\n"
    "ds_read_b64_tr_b16 a[184:185], v5 offset:96\n"
    "ds_read_b64_tr_b16 a[186:187], v5 offset:5216\n"
    "ds_read_b128 v[248:251], v10 offset:9216\n"
    "v_mfma_f32_16x16x32_bf16 v[84:87], a[172:175], v[184:187], v[84:87]\n"
    "ds_read_b128 v[252:255], v10 offset:10240\n"
    "v_mfma_f32_16x16x32_bf16 v[68:71], a[176:179], v[148:151], v[68:71]\n"
    "s_waitcnt lgkmcnt(0)\n"
    "v_add_f32 v28, v28, v212\n"
    "v_mfma_f32_16x16x32_bf16 v[72:75], a[176:179], v[152:155], v[72:75]\n"
    "v_add_f32 v29, v29, v213\n"
    "v_add_f32 v30, v30, v214\n"
    "v_mfma_f32_16x16x32_bf16 v[76:79], a[176:179], v[156:159], v[76:79]\n"
    "v_add_f32 v31, v31, v215\n"
    "v_cvt_pk_bf16_f32 v128, v28, v29\n"
    "v_mfma_f32_16x16x32_bf16 v[80:83], a[176:179], v[160:163], v[80:83]\n"
    "ds_read_b64_tr_b16 a[188:189], v5 offset:32864\n"
    "v_cvt_pk_bf16_f32 v129, v30, v31\n"
    "v_mfma_f32_16x16x32_bf16 v[84:87], a[176:179], v[164:167], v[84:87]\n"
    "ds_read_b64_tr_b16 a[190:191], v5 offset:37984\n"
    "v_add_f32 v32, v32, v216\n"
    "v_mfma_f32_16x16x32_bf16 v[68:71], a[180:183], v[192:195], v[68:71]\n"
    "v_add_f32 v33, v33, v217\n"
    "v_add_f32 v34, v34, v218\n"
    "v_mfma_f32_16x16x32_bf16 v[72:75], a[180:183], v[196:199], v[72:75]\n"
    "v_add_f32 v35, v35, v219\n"
    "v_cvt_pk_bf16_f32 v132, v32, v33\n"
    "v_mfma_f32_16x16x32_bf16 v[76:79], a[180:183], v[200:203], v[76:79]\n"
    "v_cvt_pk_bf16_f32 v133, v34, v35\n"
    "v_add_f32 v36, v36, v220\n"
    "v_mfma_f32_16x16x32_bf16 v[80:83], a[180:183], v[204:207], v[80:83]\n"
    "ds_read_b64_tr_b16 a[144:145], v5 offset:53344\n"
    "v_add_f32 v37, v37, v221\n"
    "v_mfma_f32_16x16x32_bf16 v[84:87], a[180:183], v[208:211], v[84:87]\n"
    "s_waitcnt lgkmcnt(1)\n"
    "ds_read_b64_tr_b16 a[146:147], v5 offset:58464\n"
    "v_add_f32 v38, v38, v222\n"
    "v_mfma_f32_16x16x32_bf16 v[88:91], a[184:187], v[168:171], v[88:91]\n"
    "v_add_f32 v39, v39, v223\n"
    "v_cvt_pk_bf16_f32 v136, v36, v37\n"
    "v_mfma_f32_16x16x32_bf16 v[92:95], a[184:187], v[172:175], v[92:95]\n"
    "v_cvt_pk_bf16_f32 v137, v38, v39\n"
    "v_add_f32 v40, v40, v224\n"
    "v_mfma_f32_16x16x32_bf16 v[96:99], a[184:187], v[176:179], v[96:99]\n"
    "v_add_f32 v41, v41, v225\n"
    "v_add_f32 v42, v42, v226\n"
    "v_mfma_f32_16x16x32_bf16 v[100:103], a[184:187], v[180:183], v[100:103]\n"
    "ds_read_b64_tr_b16 a[148:149], v3 offset:128\n"
    "v_add_f32 v43, v43, v227\n"
    "v_mfma_f32_16x16x32_bf16 v[104:107], a[184:187], v[184:187], v[104:107]\n"
    "ds_read_b64_tr_b16 a[150:151], v3 offset:5248\n"
    "v_cvt_pk_bf16_f32 v140, v40, v41\n"
    "v_mfma_f32_16x16x32_bf16 v[88:91], a[188:191], v[148:151], v[88:91]\n"
    "v_cvt_pk_bf16_f32 v141, v42, v43\n"
    "s_waitcnt lgkmcnt(0)\n"
    "v_mfma_f32_16x16x32_bf16 v[92:95], a[188:191], v[152:155], v[92:95]\n"
    "v_add_f32 v44, v44, v228\n"
    "v_add_f32 v45, v45, v229\n"
    "v_mfma_f32_16x16x32_bf16 v[96:99], a[188:191], v[156:159], v[96:99]\n"
    "v_add_f32 v46, v46, v230\n"
    "v_add_f32 v47, v47, v231\n"
    "v_mfma_f32_16x16x32_bf16 v[100:103], a[188:191], v[160:163], v[100:103]\n"
    "ds_read_b64_tr_b16 a[152:153], v3 offset:32896\n"
    "v_cvt_pk_bf16_f32 v130, v44, v45\n"
    "v_mfma_f32_16x16x32_bf16 v[104:107], a[188:191], v[164:167], v[104:107]\n"
    "s_waitcnt lgkmcnt(1)\n"
    "ds_read_b64_tr_b16 a[154:155], v3 offset:38016\n"
    "v_cvt_pk_bf16_f32 v131, v46, v47\n"
    "v_mfma_f32_16x16x32_bf16 v[88:91], a[144:147], v[192:195], v[88:91]\n"
    "buffer_store_dwordx4 v[128:131], v1, s[64:67], s48 offen sc1\n"
    "v_mfma_f32_16x16x32_bf16 v[92:95], a[144:147], v[196:199], v[92:95]\n"
    "v_add_f32 v48, v48, v232\n"
    "v_add_f32 v49, v49, v233\n"
    "v_mfma_f32_16x16x32_bf16 v[96:99], a[144:147], v[200:203], v[96:99]\n"
    "v_add_f32 v50, v50, v234\n"
    "v_add_f32 v51, v51, v235\n"
    "v_mfma_f32_16x16x32_bf16 v[100:103], a[144:147], v[204:207], v[100:103]\n"
    "ds_read_b64_tr_b16 a[156:157], v3 offset:53376\n"
    "v_cvt_pk_bf16_f32 v134, v48, v49\n"
    "v_mfma_f32_16x16x32_bf16 v[104:107], a[144:147], v[208:211], v[104:107]\n"
    "ds_read_b64_tr_b16 a[158:159], v3 offset:58496\n"
    "v_cvt_pk_bf16_f32 v135, v50, v51\n"
    "v_mfma_f32_16x16x32_bf16 v[108:111], a[148:151], v[168:171], v[108:111]\n"
    "buffer_store_dwordx4 v[132:135], v1, s[64:67], s49 offen sc1\n"
    "v_mfma_f32_16x16x32_bf16 v[112:115], a[148:151], v[172:175], v[112:115]\n"
    "v_add_f32 v52, v52, v236\n"
    "v_add_f32 v53, v53, v237\n"
    "v_mfma_f32_16x16x32_bf16 v[116:119], a[148:151], v[176:179], v[116:119]\n"
    "v_add_f32 v54, v54, v238\n"
    "v_add_f32 v55, v55, v239\n"
    "v_mfma_f32_16x16x32_bf16 v[120:123], a[148:151], v[180:183], v[120:123]\n"
    "v_cvt_pk_bf16_f32 v138, v52, v53\n"
    "v_cvt_pk_bf16_f32 v139, v54, v55\n"
    "v_mfma_f32_16x16x32_bf16 v[124:127], a[148:151], v[184:187], v[124:127]\n"
    "s_waitcnt lgkmcnt(0)\n"
    "v_add_f32 v56, v56, v240\n"
    "v_add_f32 v57, v57, v241\n"
    "v_mfma_f32_16x16x32_bf16 v[108:111], a[152:155], v[148:151], v[108:111]\n"
    "buffer_store_dwordx4 v[136:139], v1, s[64:67], s50 offen sc1\n"
    "v_mfma_f32_16x16x32_bf16 v[112:115], a[152:155], v[152:155], v[112:115]\n"
    "v_add_f32 v58, v58, v242\n"
    "v_add_f32 v59, v59, v243\n"
    "v_mfma_f32_16x16x32_bf16 v[116:119], a[152:155], v[156:159], v[116:119]\n"
    "v_cvt_pk_bf16_f32 v142, v56, v57\n"
    "v_cvt_pk_bf16_f32 v143, v58, v59\n"
    "v_mfma_f32_16x16x32_bf16 v[120:123], a[152:155], v[160:163], v[120:123]\n"
    "buffer_store_dwordx4 v[140:143], v1, s[64:67], s51 offen sc1\n"
    "v_mfma_f32_16x16x32_bf16 v[124:127], a[152:155], v[164:167], v[124:127]\n"
    "s_waitcnt lgkmcnt(0)\n"
    "v_add_f32 v60, v60, v244\n"
    "v_mfma_f32_16x16x32_bf16 v[108:111], a[156:159], v[192:195], v[108:111]\n"
    "v_add_f32 v61, v61, v245\n"
    "v_add_f32 v62, v62, v246\n"
    "v_mfma_f32_16x16x32_bf16 v[112:115], a[156:159], v[196:199], v[112:115]\n"
    "v_add_f32 v63, v63, v247\n"
    "v_cvt_pk_bf16_f32 v144, v60, v61\n"
    "v_mfma_f32_16x16x32_bf16 v[116:119], a[156:159], v[200:203], v[116:119]\n"
    "v_cvt_pk_bf16_f32 v145, v62, v63\n"
    "v_add_f32 v64, v64, v248\n"
    "v_mfma_f32_16x16x32_bf16 v[120:123], a[156:159], v[204:207], v[120:123]\n"
    "v_add_f32 v65, v65, v249\n"
    "v_add_f32 v66, v66, v250\n"
    "v_mfma_f32_16x16x32_bf16 v[124:127], a[156:159], v[208:211], v[124:127]\n"
    "v_add_f32 v67, v67, v251\n"
    "v_cvt_pk_bf16_f32 v188, v64, v65\n"
    "v_cvt_pk_bf16_f32 v189, v66, v67\n"
    "v_add_f32 v68, v68, v252\n"
    "v_add_f32 v69, v69, v253\n"
    "v_add_f32 v70, v70, v254\n"
    "v_add_f32 v71, v71, v255\n"
    "v_cvt_pk_bf16_f32 v128, v68, v69\n"
    "v_cvt_pk_bf16_f32 v129, v70, v71\n"
    "ds_read_b128 v[212:215], v10 offset:11264\n"
    "ds_read_b128 v[216:219], v10 offset:12288\n"
    "ds_read_b128 v[220:223], v12\n"
    "ds_read_b128 v[224:227], v12 offset:1024\n"
    "ds_read_b128 v[228:231], v12 offset:2048\n"
    "ds_read_b128 v[232:235], v12 offset:3072\n"
    "ds_read_b128 v[236:239], v12 offset:4096\n"
    "ds_read_b128 v[240:243], v12 offset:5120\n"
    "ds_read_b128 v[244:247], v12 offset:6144\n"
    "ds_read_b128 v[248:251], v12 offset:7168\n"
    "ds_read_b128 v[252:255], v12 offset:8192\n"
    "s_waitcnt lgkmcnt(10)\n"
    "v_pk_add_f32 v[72:73], v[72:73], v[212:213]\n"
    "v_pk_add_f32 v[74:75], v[74:75], v[214:215]\n"
    "v_cvt_pk_bf16_f32 v132, v72, v73\n"
    "v_cvt_pk_bf16_f32 v133, v74, v75\n"
    "ds_read_b128 v[212:215], v12 offset:9216\n"
    "s_waitcnt lgkmcnt(10)\n"
    "v_pk_add_f32 v[76:77], v[76:77], v[216:217]\n"
    "v_pk_add_f32 v[78:79], v[78:79], v[218:219]\n"
    "v_cvt_pk_bf16_f32 v146, v76, v77\n"
    "v_cvt_pk_bf16_f32 v147, v78, v79\n"
    "buffer_store_dwordx4 v[144:147], v1, s[64:67], s48 offen offset:1024 sc1\n"
    "ds_read_b128 v[216:219], v12 offset:10240\n"
    "s_waitcnt lgkmcnt(10)\n"
    "v_pk_add_f32 v[80:81], v[80:81], v[220:221]\n"
    "v_pk_add_f32 v[82:83], v[82:83], v[222:223]\n"
    "v_cvt_pk_bf16_f32 v190, v80, v81\n"
    "v_cvt_pk_bf16_f32 v191, v82, v83\n"
    "buffer_store_dwordx4 v[188:191], v1, s[64:67], s49 offen offset:1024 sc1\n"
    "ds_read_b128 v[220:223], v12 offset:11264\n"
    "s_waitcnt lgkmcnt(10)\n"
    "v_pk_add_f32 v[84:85], v[84:85], v[224:225]\n"
    "v_pk_add_f32 v[86:87], v[86:87], v[226:227]\n"
    "v_cvt_pk_bf16_f32 v130, v84, v85\n"
    "v_cvt_pk_bf16_f32 v131, v86, v87\n"
    "buffer_store_dwordx4 v[128:131], v1, s[64:67], s50 offen offset:1024 sc1\n"
    "s_waitcnt lgkmcnt(9)\n"
    "v_pk_add_f32 v[88:89], v[88:89], v[228:229]\n"
    "v_pk_add_f32 v[90:91], v[90:91], v[230:231]\n"
    "v_cvt_pk_bf16_f32 v134, v88, v89\n"
    "v_cvt_pk_bf16_f32 v135, v90, v91\n"
    "buffer_store_dwordx4 v[132:135], v1, s[64:67], s51 offen offset:1024 sc1\n"
    "s_waitcnt lgkmcnt(8)\n"
    "v_pk_add_f32 v[92:93], v[92:93], v[232:233]\n"
    "v_pk_add_f32 v[94:95], v[94:95], v[234:235]\n"
    "v_cvt_pk_bf16_f32 v136, v92, v93\n"
    "v_cvt_pk_bf16_f32 v137, v94, v95\n"
    "s_waitcnt lgkmcnt(7)\n"
    "v_pk_add_f32 v[96:97], v[96:97], v[236:237]\n"
    "v_pk_add_f32 v[98:99], v[98:99], v[238:239]\n"
    "v_cvt_pk_bf16_f32 v140, v96, v97\n"
    "v_cvt_pk_bf16_f32 v141, v98, v99\n"
    "s_waitcnt lgkmcnt(6)\n"
    "v_pk_add_f32 v[100:101], v[100:101], v[240:241]\n"
    "v_pk_add_f32 v[102:103], v[102:103], v[242:243]\n"
    "v_cvt_pk_bf16_f32 v144, v100, v101\n"
    "v_cvt_pk_bf16_f32 v145, v102, v103\n"
    "s_waitcnt lgkmcnt(5)\n"
    "v_pk_add_f32 v[104:105], v[104:105], v[244:245]\n"
    "v_pk_add_f32 v[106:107], v[106:107], v[246:247]\n"
    "v_cvt_pk_bf16_f32 v188, v104, v105\n"
    "v_cvt_pk_bf16_f32 v189, v106, v107\n"
    "s_waitcnt lgkmcnt(4)\n"
    "v_pk_add_f32 v[108:109], v[108:109], v[248:249]\n"
    "v_pk_add_f32 v[110:111], v[110:111], v[250:251]\n"
    "v_cvt_pk_bf16_f32 v138, v108, v109\n"
    "v_cvt_pk_bf16_f32 v139, v110, v111\n"
    "buffer_store_dwordx4 v[136:139], v1, s[64:67], s48 offen offset:2048 sc1\n"
    "s_waitcnt lgkmcnt(3)\n"
    "v_pk_add_f32 v[112:113], v[112:113], v[252:253]\n"
    "v_pk_add_f32 v[114:115], v[114:115], v[254:255]\n"
    "v_cvt_pk_bf16_f32 v142, v112, v113\n"
    "v_cvt_pk_bf16_f32 v143, v114, v115\n"
    "buffer_store_dwordx4 v[140:143], v1, s[64:67], s49 offen offset:2048 sc1\n"
    "s_waitcnt lgkmcnt(2)\n"
    "v_pk_add_f32 v[116:117], v[116:117], v[212:213]\n"
    "v_pk_add_f32 v[118:119], v[118:119], v[214:215]\n"
    "v_cvt_pk_bf16_f32 v146, v116, v117\n"
    "v_cvt_pk_bf16_f32 v147, v118, v119\n"
    "buffer_store_dwordx4 v[144:147], v1, s[64:67], s50 offen offset:2048 sc1\n"
    "s_waitcnt lgkmcnt(1)\n"
    "v_pk_add_f32 v[120:121], v[120:121], v[216:217]\n"
    "v_pk_add_f32 v[122:123], v[122:123], v[218:219]\n"
    "v_cvt_pk_bf16_f32 v190, v120, v121\n"
    "v_cvt_pk_bf16_f32 v191, v122, v123\n"
    "buffer_store_dwordx4 v[188:191], v1, s[64:67], s51 offen offset:2048 sc1\n"
    "s_waitcnt lgkmcnt(0)\n"
    "v_pk_add_f32 v[124:125], v[124:125], v[220:221]\n"
    "v_pk_add_f32 v[126:127], v[126:127], v[222:223]\n"
    "v_cvt_pk_bf16_f32 v128, v124, v125\n"
    "v_cvt_pk_bf16_f32 v129, v126, v127\n"
    "buffer_store_dwordx4 v[128:131], v1, s[64:67], s48 offen offset:3072 sc1\n"
    "s_lshr_b32 s74, s90, 2\n"
    "s_lshl_b32 s98, s72, 7\n"
    "s_add_u32 s77, s98, s96\n"
    "s_waitcnt vmcnt(0)\n"
    "s_lshl_b64 exec, 1, s90\n"
    "buffer_store_dwordx2 v[16:17], v15, s[68:71], s77 offen offset:256 sc1\n"
    "s_mov_b64 exec, -1\n"
    "s_mov_b32 exec_lo, s92\n"
    "s_mov_b32 exec_hi, 0\n"
    "buffer_load_dwordx2 v[18:19], v14, s[68:71], s98 offen offset:256 sc1\n"
    "s_mov_b64 exec, -1\n"
    "s_lshl_b32 s75, s74, 12\n"
    "s_add_u32 s81, s81, s75\n"
    "s_add_u32 s48, s81, 0\n"
    "s_add_u32 s49, s81, 0x10000\n"
    "s_add_u32 s50, s81, 0x20000\n"
    "s_add_u32 s51, s81, 0x30000\n"
    "s_waitcnt vmcnt(0)\n"
    "s_mov_b32 s80, 0\n"
    "LPFD_%=:\n"
    "v_cmp_ne_u64 vcc, v[18:19], v[16:17]\n"
    "s_nop 0\n"
    "s_and_b32 s99, vcc_lo, s92\n"
    "s_cbranch_scc0 LPDFD_%=\n"
    "s_sleep 1\n"
    "s_mov_b32 exec_lo, s92\n"
    "s_mov_b32 exec_hi, 0\n"
    "buffer_load_dwordx2 v[18:19], v14, s[68:71], s98 offen offset:256 sc1\n"
    "s_mov_b64 exec, -1\n"
    "s_add_u32 s80, s80, 1\n"
    "s_waitcnt vmcnt(0)\n"
    "s_cmp_lt_u32 s80, 0x40000\n"
    "s_cbranch_scc1 LPFD_%=\n"
    "LPDFD_%=:\n"
    "s_cmp_eq_u32 s74, 1\n"
    "s_cbranch_scc1 LVF1_%=\n"
    "s_cmp_eq_u32 s74, 2\n"
    "s_cbranch_scc1 LVF2_%=\n"
    "s_cmp_eq_u32 s74, 3\n"
    "s_cbranch_scc1 LVF3_%=\n"
    "buffer_load_dwordx4 v[128:131], v1, s[64:67], s49 offen sc1\n"
    "buffer_load_dwordx4 v[132:135], v1, s[64:67], s50 offen sc1\n"
    "buffer_load_dwordx4 v[136:139], v1, s[64:67], s51 offen sc1\n"
    "buffer_load_dwordx4 v[140:143], v1, s[64:67], s49 offen offset:1024 sc1\n"
    "buffer_load_dwordx4 v[144:147], v1, s[64:67], s50 offen offset:1024 sc1\n"
    "buffer_load_dwordx4 v[148:151], v1, s[64:67], s51 offen offset:1024 sc1\n"
    "buffer_load_dwordx4 v[152:155], v1, s[64:67], s49 offen offset:2048 sc1\n"
    "buffer_load_dwordx4 v[156:159], v1, s[64:67], s50 offen offset:2048 sc1\n"
    "buffer_load_dwordx4 v[160:163], v1, s[64:67], s51 offen offset:2048 sc1\n"
    "buffer_load_dwordx4 v[164:167], v1, s[64:67], s49 offen offset:3072 sc1\n"
    "buffer_load_dwordx4 v[168:171], v1, s[64:67], s50 offen offset:3072 sc1\n"
    "buffer_load_dwordx4 v[172:175], v1, s[64:67], s51 offen offset:3072 sc1\n"
    "s_waitcnt vmcnt(9)\n"
    "v_lshlrev_b32 v200, 16, v128\n"
    "v_and_b32 v201, v13, v128\n"
    "v_lshlrev_b32 v202, 16, v129\n"
    "v_and_b32 v203, v13, v129\n"
    "v_lshlrev_b32 v204, 16, v132\n"
    "v_and_b32 v205, v13, v132\n"
    "v_lshlrev_b32 v206, 16, v133\n"
    "v_and_b32 v207, v13, v133\n"
    "v_lshlrev_b32 v208, 16, v136\n"
    "v_and_b32 v209, v13, v136\n"
    "v_lshlrev_b32 v210, 16, v137\n"
    "v_and_b32 v211, v13, v137\n"
    "v_pk_add_f32 v[200:201], v[200:201], v[204:205]\n"
    "v_pk_add_f32 v[202:203], v[202:203], v[206:207]\n"
    "v_pk_add_f32 v[28:29], v[28:29], v[208:209]\n"
    "v_pk_add_f32 v[30:31], v[30:31], v[210:211]\n"
    "v_pk_add_f32 v[28:29], v[28:29], v[200:201]\n"
    "v_pk_add_f32 v[30:31], v[30:31], v[202:203]\n"
    "v_cvt_pk_bf16_f32 v28, v28, v29\n"
    "v_cvt_pk_bf16_f32 v29, v30, v31\n"
    "buffer_store_dwordx2 v[28:29], v2, s[36:39], 0 offen sc1\n"
    "v_lshlrev_b32 v212, 16, v130\n"
    "v_and_b32 v213, v13, v130\n"
    "v_lshlrev_b32 v214, 16, v131\n"
    "v_and_b32 v215, v13, v131\n"
    "v_lshlrev_b32 v216, 16, v134\n"
    "v_and_b32 v217, v13, v134\n"
    "v_lshlrev_b32 v218, 16, v135\n"
    "v_and_b32 v219, v13, v135\n"
    "v_lshlrev_b32 v220, 16, v138\n"
    "v_and_b32 v221, v13, v138\n"
    "v_lshlrev_b32 v222, 16, v139\n"
    "v_and_b32 v223, v13, v139\n"
    "v_pk_add_f32 v[212:213], v[212:213], v[216:217]\n"
    "v_pk_add_f32 v[214:215], v[214:215], v[218:219]\n"
    "v_pk_add_f32 v[44:45], v[44:45], v[220:221]\n"
    "v_pk_add_f32 v[46:47], v[46:47], v[222:223]\n"
    "v_pk_add_f32 v[44:45], v[44:45], v[212:213]\n"
    "v_pk_add_f32 v[46:47], v[46:47], v[214:215]\n"
    "v_cvt_pk_bf16_f32 v44, v44, v45\n"
    "v_cvt_pk_bf16_f32 v45, v46, v47\n"
    "buffer_store_dwordx2 v[44:45], v2, s[36:39], s47 offen sc1\n"
    "s_waitcnt vmcnt(8)\n"
    "v_lshlrev_b32 v200, 16, v140\n"
    "v_and_b32 v201, v13, v140\n"
    "v_lshlrev_b32 v202, 16, v141\n"
    "v_and_b32 v203, v13, v141\n"
    "v_lshlrev_b32 v204, 16, v144\n"
    "v_and_b32 v205, v13, v144\n"
    "v_lshlrev_b32 v206, 16, v145\n"
    "v_and_b32 v207, v13, v145\n"
    "v_lshlrev_b32 v208, 16, v148\n"
    "v_and_b32 v209, v13, v148\n"
    "v_lshlrev_b32 v210, 16, v149\n"
    "v_and_b32 v211, v13, v149\n"
    "v_pk_add_f32 v[200:201], v[200:201], v[204:205]\n"
    "v_pk_add_f32 v[202:203], v[202:203], v[206:207]\n"
    "v_pk_add_f32 v[60:61], v[60:61], v[208:209]\n"
    "v_pk_add_f32 v[62:63], v[62:63], v[210:211]\n"
    "v_pk_add_f32 v[60:61], v[60:61], v[200:201]\n"
    "v_pk_add_f32 v[62:63], v[62:63], v[202:203]\n"
    "v_cvt_pk_bf16_f32 v60, v60, v61\n"
    "v_cvt_pk_bf16_f32 v61, v62, v63\n"
    "buffer_store_dwordx2 v[60:61], v2, s[36:39], s46 offen offset:32 sc1\n"
    "v_lshlrev_b32 v212, 16, v142\n"
    "v_and_b32 v213, v13, v142\n"
    "v_lshlrev_b32 v214, 16, v143\n"
    "v_and_b32 v215, v13, v143\n"
    "v_lshlrev_b32 v216, 16, v146\n"
    "v_and_b32 v217, v13, v146\n"
    "v_lshlrev_b32 v218, 16, v147\n"
    "v_and_b32 v219, v13, v147\n"
    "v_lshlrev_b32 v220, 16, v150\n"
    "v_and_b32 v221, v13, v150\n"
    "v_lshlrev_b32 v222, 16, v151\n"
    "v_and_b32 v223, v13, v151\n"
    "v_pk_add_f32 v[212:213], v[212:213], v[216:217]\n"
    "v_pk_add_f32 v[214:215], v[214:215], v[218:219]\n"
    "v_pk_add_f32 v[76:77], v[76:77], v[220:221]\n"
    "v_pk_add_f32 v[78:79], v[78:79], v[222:223]\n"
    "v_pk_add_f32 v[76:77], v[76:77], v[212:213]\n"
    "v_pk_add_f32 v[78:79], v[78:79], v[214:215]\n"
    "v_cvt_pk_bf16_f32 v76, v76, v77\n"
    "v_cvt_pk_bf16_f32 v77, v78, v79\n"
    "buffer_store_dwordx2 v[76:77], v2, s[36:39], s45 offen offset:64 sc1\n"
    "s_waitcnt vmcnt(7)\n"
    "v_lshlrev_b32 v200, 16, v152\n"
    "v_and_b32 v201, v13, v152\n"
    "v_lshlrev_b32 v202, 16, v153\n"
    "v_and_b32 v203, v13, v153\n"
    "v_lshlrev_b32 v204, 16, v156\n"
    "v_and_b32 v205, v13, v156\n"
    "v_lshlrev_b32 v206, 16, v157\n"
    "v_and_b32 v207, v13, v157\n"
    "v_lshlrev_b32 v208, 16, v160\n"
    "v_and_b32 v209, v13, v160\n"
    "v_lshlrev_b32 v210, 16, v161\n"
    "v_and_b32 v211, v13, v161\n"
    "v_pk_add_f32 v[200:201], v[200:201], v[204:205]\n"
    "v_pk_add_f32 v[202:203], v[202:203], v[206:207]\n"
    "v_pk_add_f32 v[92:93], v[92:93], v[208:209]\n"
    "v_pk_add_f32 v[94:95], v[94:95], v[210:211]\n"
    "v_pk_add_f32 v[92:93], v[92:93], v[200:201]\n"
    "v_pk_add_f32 v[94:95], v[94:95], v[202:203]\n"
    "v_cvt_pk_bf16_f32 v92, v92, v93\n"
    "v_cvt_pk_bf16_f32 v93, v94, v95\n"
    "buffer_store_dwordx2 v[92:93], v2, s[36:39], s44 offen offset:96 sc1\n"
    "v_lshlrev_b32 v212, 16, v154\n"
    "v_and_b32 v213, v13, v154\n"
    "v_lshlrev_b32 v214, 16, v155\n"
    "v_and_b32 v215, v13, v155\n"
    "v_lshlrev_b32 v216, 16, v158\n"
    "v_and_b32 v217, v13, v158\n"
    "v_lshlrev_b32 v218, 16, v159\n"
    "v_and_b32 v219, v13, v159\n"
    "v_lshlrev_b32 v220, 16, v162\n"
    "v_and_b32 v221, v13, v162\n"
    "v_lshlrev_b32 v222, 16, v163\n"
    "v_and_b32 v223, v13, v163\n"
    "v_pk_add_f32 v[212:213], v[212:213], v[216:217]\n"
    "v_pk_add_f32 v[214:215], v[214:215], v[218:219]\n"
    "v_pk_add_f32 v[108:109], v[108:109], v[220:221]\n"
    "v_pk_add_f32 v[110:111], v[110:111], v[222:223]\n"
    "v_pk_add_f32 v[108:109], v[108:109], v[212:213]\n"
    "v_pk_add_f32 v[110:111], v[110:111], v[214:215]\n"
    "v_cvt_pk_bf16_f32 v108, v108, v109\n"
    "v_cvt_pk_bf16_f32 v109, v110, v111\n"
    "buffer_store_dwordx2 v[108:109], v2, s[36:39], 0 offen offset:128 sc1\n"
    "s_waitcnt vmcnt(6)\n"
    "v_lshlrev_b32 v200, 16, v164\n"
    "v_and_b32 v201, v13, v164\n"
    "v_lshlrev_b32 v202, 16, v165\n"
    "v_and_b32 v203, v13, v165\n"
    "v_lshlrev_b32 v204, 16, v168\n"
    "v_and_b32 v205, v13, v168\n"
    "v_lshlrev_b32 v206, 16, v169\n"
    "v_and_b32 v207, v13, v169\n"
    "v_lshlrev_b32 v208, 16, v172\n"
    "v_and_b32 v209, v13, v172\n"
    "v_lshlrev_b32 v210, 16, v173\n"
    "v_and_b32 v211, v13, v173\n"
    "v_pk_add_f32 v[200:201], v[200:201], v[204:205]\n"
    "v_pk_add_f32 v[202:203], v[202:203], v[206:207]\n"
    "v_pk_add_f32 v[124:125], v[124:125], v[208:209]\n"
    "v_pk_add_f32 v[126:127], v[126:127], v[210:211]\n"
    "v_pk_add_f32 v[124:125], v[124:125], v[200:201]\n"
    "v_pk_add_f32 v[126:127], v[126:127], v[202:203]\n"
    "v_cvt_pk_bf16_f32 v124, v124, v125\n"
    "v_cvt_pk_bf16_f32 v125, v126, v127\n"
    "buffer_store_dwordx2 v[124:125], v2, s[36:39], s47 offen offset:128 sc1\n"
    "s_branch LVEF_%=\n"
    "LVF1_%=:\n"
    "buffer_load_dwordx4 v[128:131], v1, s[64:67], s48 offen sc1\n"
    "buffer_load_dwordx4 v[132:135], v1, s[64:67], s50 offen sc1\n"
    "buffer_load_dwordx4 v[136:139], v1, s[64:67], s51 offen sc1\n"
    "buffer_load_dwordx4 v[140:143], v1, s[64:67], s48 offen offset:1024 sc1\n"
    "buffer_load_dwordx4 v[144:147], v1, s[64:67], s50 offen offset:1024 sc1\n"
    "buffer_load_dwordx4 v[148:151], v1, s[64:67], s51 offen offset:1024 sc1\n"
    "buffer_load_dwordx4 v[152:155], v1, s[64:67], s48 offen offset:2048 sc1\n"
    "buffer_load_dwordx4 v[156:159], v1, s[64:67], s50 offen offset:2048 sc1\n"
    "buffer_load_dwordx4 v[160:163], v1, s[64:67], s51 offen offset:2048 sc1\n"
    "s_waitcnt vmcnt(6)\n"
    "v_lshlrev_b32 v200, 16, v128\n"
    "v_and_b32 v201, v13, v128\n"
    "v_lshlrev_b32 v202, 16, v129\n"
    "v_and_b32 v203, v13, v129\n"
    "v_lshlrev_b32 v204, 16, v132\n"
    "v_and_b32 v205, v13, v132\n"
    "v_lshlrev_b32 v206, 16, v133\n"
    "v_and_b32 v207, v13, v133\n"
    "v_lshlrev_b32 v208, 16, v136\n"
    "v_and_b32 v209, v13, v136\n"
    "v_lshlrev_b32 v210, 16, v137\n"
    "v_and_b32 v211, v13, v137\n"
    "v_pk_add_f32 v[200:201], v[200:201], v[204:205]\n"
    "v_pk_add_f32 v[202:203], v[202:203], v[206:207]\n"
    "v_pk_add_f32 v[32:33], v[32:33], v[208:209]\n"
    "v_pk_add_f32 v[34:35], v[34:35], v[210:211]\n"
    "v_pk_add_f32 v[32:33], v[32:33], v[200:201]\n"
    "v_pk_add_f32 v[34:35], v[34:35], v[202:203]\n"
    "v_cvt_pk_bf16_f32 v32, v32, v33\n"
    "v_cvt_pk_bf16_f32 v33, v34, v35\n"
    "buffer_store_dwordx2 v[32:33], v2, s[36:39], s44 offen sc1\n"
    "v_lshlrev_b32 v212, 16, v130\n"
    "v_and_b32 v213, v13, v130\n"
    "v_lshlrev_b32 v214, 16, v131\n"
    "v_and_b32 v215, v13, v131\n"
    "v_lshlrev_b32 v216, 16, v134\n"
    "v_and_b32 v217, v13, v134\n"
    "v_lshlrev_b32 v218, 16, v135\n"
    "v_and_b32 v219, v13, v135\n"
    "v_lshlrev_b32 v220, 16, v138\n"
    "v_and_b32 v221, v13, v138\n"
    "v_lshlrev_b32 v222, 16, v139\n"
    "v_and_b32 v223, v13, v139\n"
    "v_pk_add_f32 v[212:213], v[212:213], v[216:217]\n"
    "v_pk_add_f32 v[214:215], v[214:215], v[218:219]\n"
    "v_pk_add_f32 v[48:49], v[48:49], v[220:221]\n"
    "v_pk_add_f32 v[50:51], v[50:51], v[222:223]\n"
    "v_pk_add_f32 v[48:49], v[48:49], v[212:213]\n"
    "v_pk_add_f32 v[50:51], v[50:51], v[214:215]\n"
    "v_cvt_pk_bf16_f32 v48, v48, v49\n"
    "v_cvt_pk_bf16_f32 v49, v50, v51\n"
    "buffer_store_dwordx2 v[48:49], v2, s[36:39], 0 offen offset:32 sc1\n"
    "s_waitcnt vmcnt(5)\n"
    "v_lshlrev_b32 v200, 16, v140\n"
    "v_and_b32 v201, v13, v140\n"
    "v_lshlrev_b32 v202, 16, v141\n"
    "v_and_b32 v203, v13, v141\n"
    "v_lshlrev_b32 v204, 16, v144\n"
    "v_and_b32 v205, v13, v144\n"
    "v_lshlrev_b32 v206, 16, v145\n"
    "v_and_b32 v207, v13, v145\n"
    "v_lshlrev_b32 v208, 16, v148\n"
    "v_and_b32 v209, v13, v148\n"
    "v_lshlrev_b32 v210, 16, v149\n"
    "v_and_b32 v211, v13, v149\n"
    "v_pk_add_f32 v[200:201], v[200:201], v[204:205]\n"
    "v_pk_add_f32 v[202:203], v[202:203], v[206:207]\n"
    "v_pk_add_f32 v[64:65], v[64:65], v[208:209]\n"
    "v_pk_add_f32 v[66:67], v[66:67], v[210:211]\n"
    "v_pk_add_f32 v[64:65], v[64:65], v[200:201]\n"
    "v_pk_add_f32 v[66:67], v[66:67], v[202:203]\n"
    "v_cvt_pk_bf16_f32 v64, v64, v65\n"
    "v_cvt_pk_bf16_f32 v65, v66, v67\n"
    "buffer_store_dwordx2 v[64:65], v2, s[36:39], s47 offen offset:32 sc1\n"
    "v_lshlrev_b32 v212, 16, v142\n"
    "v_and_b32 v213, v13, v142\n"
    "v_lshlrev_b32 v214, 16, v143\n"
    "v_and_b32 v215, v13, v143\n"
    "v_lshlrev_b32 v216, 16, v146\n"
    "v_and_b32 v217, v13, v146\n"
    "v_lshlrev_b32 v218, 16, v147\n"
    "v_and_b32 v219, v13, v147\n"
    "v_lshlrev_b32 v220, 16, v150\n"
    "v_and_b32 v221, v13, v150\n"
    "v_lshlrev_b32 v222, 16, v151\n"
    "v_and_b32 v223, v13, v151\n"
    "v_pk_add_f32 v[212:213], v[212:213], v[216:217]\n"
    "v_pk_add_f32 v[214:215], v[214:215], v[218:219]\n"
    "v_pk_add_f32 v[80:81], v[80:81], v[220:221]\n"
    "v_pk_add_f32 v[82:83], v[82:83], v[222:223]\n"
    "v_pk_add_f32 v[80:81], v[80:81], v[212:213]\n"
    "v_pk_add_f32 v[82:83], v[82:83], v[214:215]\n"
    "v_cvt_pk_bf16_f32 v80, v80, v81\n"
    "v_cvt_pk_bf16_f32 v81, v82, v83\n"
    "buffer_store_dwordx2 v[80:81], v2, s[36:39], s46 offen offset:64 sc1\n"
    "s_waitcnt vmcnt(4)\n"
    "v_lshlrev_b32 v200, 16, v152\n"
    "v_and_b32 v201, v13, v152\n"
    "v_lshlrev_b32 v202, 16, v153\n"
    "v_and_b32 v203, v13, v153\n"
    "v_lshlrev_b32 v204, 16, v156\n"
    "v_and_b32 v205, v13, v156\n"
    "v_lshlrev_b32 v206, 16, v157\n"
    "v_and_b32 v207, v13, v157\n"
    "v_lshlrev_b32 v208, 16, v160\n"
    "v_and_b32 v209, v13, v160\n"
    "v_lshlrev_b32 v210, 16, v161\n"
    "v_and_b32 v211, v13, v161\n"
    "v_pk_add_f32 v[200:201], v[200:201], v[204:205]\n"
    "v_pk_add_f32 v[202:203], v[202:203], v[206:207]\n"
    "v_pk_add_f32 v[96:97], v[96:97], v[208:209]\n"
    "v_pk_add_f32 v[98:99], v[98:99], v[210:211]\n"
    "v_pk_add_f32 v[96:97], v[96:97], v[200:201]\n"
    "v_pk_add_f32 v[98:99], v[98:99], v[202:203]\n"
    "v_cvt_pk_bf16_f32 v96, v96, v97\n"
    "v_cvt_pk_bf16_f32 v97, v98, v99\n"
    "buffer_store_dwordx2 v[96:97], v2, s[36:39], s45 offen offset:96 sc1\n"
    "v_lshlrev_b32 v212, 16, v154\n"
    "v_and_b32 v213, v13, v154\n"
    "v_lshlrev_b32 v214, 16, v155\n"
    "v_and_b32 v215, v13, v155\n"
    "v_lshlrev_b32 v216, 16, v158\n"
    "v_and_b32 v217, v13, v158\n"
    "v_lshlrev_b32 v218, 16, v159\n"
    "v_and_b32 v219, v13, v159\n"
    "v_lshlrev_b32 v220, 16, v162\n"
    "v_and_b32 v221, v13, v162\n"
    "v_lshlrev_b32 v222, 16, v163\n"
    "v_and_b32 v223, v13, v163\n"
    "v_pk_add_f32 v[212:213], v[212:213], v[216:217]\n"
    "v_pk_add_f32 v[214:215], v[214:215], v[218:219]\n"
    "v_pk_add_f32 v[112:113], v[112:113], v[220:221]\n"
    "v_pk_add_f32 v[114:115], v[114:115], v[222:223]\n"
    "v_pk_add_f32 v[112:113], v[112:113], v[212:213]\n"
    "v_pk_add_f32 v[114:115], v[114:115], v[214:215]\n"
    "v_cvt_pk_bf16_f32 v112, v112, v113\n"
    "v_cvt_pk_bf16_f32 v113, v114, v115\n"
    "buffer_store_dwordx2 v[112:113], v2, s[36:39], s44 offen offset:128 sc1\n"
    "s_branch LVEF_%=\n"
    "LVF2_%=:\n"
    "buffer_load_dwordx4 v[128:131], v1, s[64:67], s48 offen sc1\n"
    "buffer_load_dwordx4 v[132:135], v1, s[64:67], s49 offen sc1\n"
    "buffer_load_dwordx4 v[136:139], v1, s[64:67], s51 offen sc1\n"
    "buffer_load_dwordx4 v[140:143], v1, s[64:67], s48 offen offset:1024 sc1\n"
    "buffer_load_dwordx4 v[144:147], v1, s[64:67], s49 offen offset:1024 sc1\n"
    "buffer_load_dwordx4 v[148:151], v1, s[64:67], s51 offen offset:1024 sc1\n"
    "buffer_load_dwordx4 v[152:155], v1, s[64:67], s48 offen offset:2048 sc1\n"
    "buffer_load_dwordx4 v[156:159], v1, s[64:67], s49 offen offset:2048 sc1\n"
    "buffer_load_dwordx4 v[160:163], v1, s[64:67], s51 offen offset:2048 sc1\n"
    "s_waitcnt vmcnt(6)\n"
    "v_lshlrev_b32 v200, 16, v128\n"
    "v_and_b32 v201, v13, v128\n"
    "v_lshlrev_b32 v202, 16, v129\n"
    "v_and_b32 v203, v13, v129\n"
    "v_lshlrev_b32 v204, 16, v132\n"
    "v_and_b32 v205, v13, v132\n"
    "v_lshlrev_b32 v206, 16, v133\n"
    "v_and_b32 v207, v13, v133\n"
    "v_lshlrev_b32 v208, 16, v136\n"
    "v_and_b32 v209, v13, v136\n"
    "v_lshlrev_b32 v210, 16, v137\n"
    "v_and_b32 v211, v13, v137\n"
    "v_pk_add_f32 v[200:201], v[200:201], v[204:205]\n"
    "v_pk_add_f32 v[202:203], v[202:203], v[206:207]\n"
    "v_pk_add_f32 v[36:37], v[36:37], v[208:209]\n"
    "v_pk_add_f32 v[38:39], v[38:39], v[210:211]\n"
    "v_pk_add_f32 v[36:37], v[36:37], v[200:201]\n"
    "v_pk_add_f32 v[38:39], v[38:39], v[202:203]\n"
    "v_cvt_pk_bf16_f32 v36, v36, v37\n"
    "v_cvt_pk_bf16_f32 v37, v38, v39\n"
    "buffer_store_dwordx2 v[36:37], v2, s[36:39], s45 offen sc1\n"
    "v_lshlrev_b32 v212, 16, v130\n"
    "v_and_b32 v213, v13, v130\n"
    "v_lshlrev_b32 v214, 16, v131\n"
    "v_and_b32 v215, v13, v131\n"
    "v_lshlrev_b32 v216, 16, v134\n"
    "v_and_b32 v217, v13, v134\n"
    "v_lshlrev_b32 v218, 16, v135\n"
    "v_and_b32 v219, v13, v135\n"
    "v_lshlrev_b32 v220, 16, v138\n"
    "v_and_b32 v221, v13, v138\n"
    "v_lshlrev_b32 v222, 16, v139\n"
    "v_and_b32 v223, v13, v139\n"
    "v_pk_add_f32 v[212:213], v[212:213], v[216:217]\n"
    "v_pk_add_f32 v[214:215], v[214:215], v[218:219]\n"
    "v_pk_add_f32 v[52:53], v[52:53], v[220:221]\n"
    "v_pk_add_f32 v[54:55], v[54:55], v[222:223]\n"
    "v_pk_add_f32 v[52:53], v[52:53], v[212:213]\n"
    "v_pk_add_f32 v[54:55], v[54:55], v[214:215]\n"
    "v_cvt_pk_bf16_f32 v52, v52, v53\n"
    "v_cvt_pk_bf16_f32 v53, v54, v55\n"
    "buffer_store_dwordx2 v[52:53], v2, s[36:39], s44 offen offset:32 sc1\n"
    "s_waitcnt vmcnt(5)\n"
    "v_lshlrev_b32 v200, 16, v140\n"
    "v_and_b32 v201, v13, v140\n"
    "v_lshlrev_b32 v202, 16, v141\n"
    "v_and_b32 v203, v13, v141\n"
    "v_lshlrev_b32 v204, 16, v144\n"
    "v_and_b32 v205, v13, v144\n"
    "v_lshlrev_b32 v206, 16, v145\n"
    "v_and_b32 v207, v13, v145\n"
    "v_lshlrev_b32 v208, 16, v148\n"
    "v_and_b32 v209, v13, v148\n"
    "v_lshlrev_b32 v210, 16, v149\n"
    "v_and_b32 v211, v13, v149\n"
    "v_pk_add_f32 v[200:201], v[200:201], v[204:205]\n"
    "v_pk_add_f32 v[202:203], v[202:203], v[206:207]\n"
    "v_pk_add_f32 v[68:69], v[68:69], v[208:209]\n"
    "v_pk_add_f32 v[70:71], v[70:71], v[210:211]\n"
    "v_pk_add_f32 v[68:69], v[68:69], v[200:201]\n"
    "v_pk_add_f32 v[70:71], v[70:71], v[202:203]\n"
    "v_cvt_pk_bf16_f32 v68, v68, v69\n"
    "v_cvt_pk_bf16_f32 v69, v70, v71\n"
    "buffer_store_dwordx2 v[68:69], v2, s[36:39], 0 offen offset:64 sc1\n"
    "v_lshlrev_b32 v212, 16, v142\n"
    "v_and_b32 v213, v13, v142\n"
    "v_lshlrev_b32 v214, 16, v143\n"
    "v_and_b32 v215, v13, v143\n"
    "v_lshlrev_b32 v216, 16, v146\n"
    "v_and_b32 v217, v13, v146\n"
    "v_lshlrev_b32 v218, 16, v147\n"
    "v_and_b32 v219, v13, v147\n"
    "v_lshlrev_b32 v220, 16, v150\n"
    "v_and_b32 v221, v13, v150\n"
    "v_lshlrev_b32 v222, 16, v151\n"
    "v_and_b32 v223, v13, v151\n"
    "v_pk_add_f32 v[212:213], v[212:213], v[216:217]\n"
    "v_pk_add_f32 v[214:215], v[214:215], v[218:219]\n"
    "v_pk_add_f32 v[84:85], v[84:85], v[220:221]\n"
    "v_pk_add_f32 v[86:87], v[86:87], v[222:223]\n"
    "v_pk_add_f32 v[84:85], v[84:85], v[212:213]\n"
    "v_pk_add_f32 v[86:87], v[86:87], v[214:215]\n"
    "v_cvt_pk_bf16_f32 v84, v84, v85\n"
    "v_cvt_pk_bf16_f32 v85, v86, v87\n"
    "buffer_store_dwordx2 v[84:85], v2, s[36:39], s47 offen offset:64 sc1\n"
    "s_waitcnt vmcnt(4)\n"
    "v_lshlrev_b32 v200, 16, v152\n"
    "v_and_b32 v201, v13, v152\n"
    "v_lshlrev_b32 v202, 16, v153\n"
    "v_and_b32 v203, v13, v153\n"
    "v_lshlrev_b32 v204, 16, v156\n"
    "v_and_b32 v205, v13, v156\n"
    "v_lshlrev_b32 v206, 16, v157\n"
    "v_and_b32 v207, v13, v157\n"
    "v_lshlrev_b32 v208, 16, v160\n"
    "v_and_b32 v209, v13, v160\n"
    "v_lshlrev_b32 v210, 16, v161\n"
    "v_and_b32 v211, v13, v161\n"
    "v_pk_add_f32 v[200:201], v[200:201], v[204:205]\n"
    "v_pk_add_f32 v[202:203], v[202:203], v[206:207]\n"
    "v_pk_add_f32 v[100:101], v[100:101], v[208:209]\n"
    "v_pk_add_f32 v[102:103], v[102:103], v[210:211]\n"
    "v_pk_add_f32 v[100:101], v[100:101], v[200:201]\n"
    "v_pk_add_f32 v[102:103], v[102:103], v[202:203]\n"
    "v_cvt_pk_bf16_f32 v100, v100, v101\n"
    "v_cvt_pk_bf16_f32 v101, v102, v103\n"
    "buffer_store_dwordx2 v[100:101], v2, s[36:39], s46 offen offset:96 sc1\n"
    "v_lshlrev_b32 v212, 16, v154\n"
    "v_and_b32 v213, v13, v154\n"
    "v_lshlrev_b32 v214, 16, v155\n"
    "v_and_b32 v215, v13, v155\n"
    "v_lshlrev_b32 v216, 16, v158\n"
    "v_and_b32 v217, v13, v158\n"
    "v_lshlrev_b32 v218, 16, v159\n"
    "v_and_b32 v219, v13, v159\n"
    "v_lshlrev_b32 v220, 16, v162\n"
    "v_and_b32 v221, v13, v162\n"
    "v_lshlrev_b32 v222, 16, v163\n"
    "v_and_b32 v223, v13, v163\n"
    "v_pk_add_f32 v[212:213], v[212:213], v[216:217]\n"
    "v_pk_add_f32 v[214:215], v[214:215], v[218:219]\n"
    "v_pk_add_f32 v[116:117], v[116:117], v[220:221]\n"
    "v_pk_add_f32 v[118:119], v[118:119], v[222:223]\n"
    "v_pk_add_f32 v[116:117], v[116:117], v[212:213]\n"
    "v_pk_add_f32 v[118:119], v[118:119], v[214:215]\n"
    "v_cvt_pk_bf16_f32 v116, v116, v117\n"
    "v_cvt_pk_bf16_f32 v117, v118, v119\n"
    "buffer_store_dwordx2 v[116:117], v2, s[36:39], s45 offen offset:128 sc1\n"
    "s_branch LVEF_%=\n"
    "LVF3_%=:\n"
    "buffer_load_dwordx4 v[128:131], v1, s[64:67], s48 offen sc1\n"
    "buffer_load_dwordx4 v[132:135], v1, s[64:67], s49 offen sc1\n"
    "buffer_load_dwordx4 v[136:139], v1, s[64:67], s50 offen sc1\n"
    "buffer_load_dwordx4 v[140:143], v1, s[64:67], s48 offen offset:1024 sc1\n"
    "buffer_load_dwordx4 v[144:147], v1, s[64:67], s49 offen offset:1024 sc1\n"
    "buffer_load_dwordx4 v[148:151], v1, s[64:67], s50 offen offset:1024 sc1\n"
    "buffer_load_dwordx4 v[152:155], v1, s[64:67], s48 offen offset:2048 sc1\n"
    "buffer_load_dwordx4 v[156:159], v1, s[64:67], s49 offen offset:2048 sc1\n"
    "buffer_load_dwordx4 v[160:163], v1, s[64:67], s50 offen offset:2048 sc1\n"
    "s_waitcnt vmcnt(6)\n"
    "v_lshlrev_b32 v200, 16, v128\n"
    "v_and_b32 v201, v13, v128\n"
    "v_lshlrev_b32 v202, 16, v129\n"
    "v_and_b32 v203, v13, v129\n"
    "v_lshlrev_b32 v204, 16, v132\n"
    "v_and_b32 v205, v13, v132\n"
    "v_lshlrev_b32 v206, 16, v133\n"
    "v_and_b32 v207, v13, v133\n"
    "v_lshlrev_b32 v208, 16, v136\n"
    "v_and_b32 v209, v13, v136\n"
    "v_lshlrev_b32 v210, 16, v137\n"
    "v_and_b32 v211, v13, v137\n"
    "v_pk_add_f32 v[200:201], v[200:201], v[204:205]\n"
    "v_pk_add_f32 v[202:203], v[202:203], v[206:207]\n"
    "v_pk_add_f32 v[40:41], v[40:41], v[208:209]\n"
    "v_pk_add_f32 v[42:43], v[42:43], v[210:211]\n"
    "v_pk_add_f32 v[40:41], v[40:41], v[200:201]\n"
    "v_pk_add_f32 v[42:43], v[42:43], v[202:203]\n"
    "v_cvt_pk_bf16_f32 v40, v40, v41\n"
    "v_cvt_pk_bf16_f32 v41, v42, v43\n"
    "buffer_store_dwordx2 v[40:41], v2, s[36:39], s46 offen sc1\n"
    "v_lshlrev_b32 v212, 16, v130\n"
    "v_and_b32 v213, v13, v130\n"
    "v_lshlrev_b32 v214, 16, v131\n"
    "v_and_b32 v215, v13, v131\n"
    "v_lshlrev_b32 v216, 16, v134\n"
    "v_and_b32 v217, v13, v134\n"
    "v_lshlrev_b32 v218, 16, v135\n"
    "v_and_b32 v219, v13, v135\n"
    "v_lshlrev_b32 v220, 16, v138\n"
    "v_and_b32 v221, v13, v138\n"
    "v_lshlrev_b32 v222, 16, v139\n"
    "v_and_b32 v223, v13, v139\n"
    "v_pk_add_f32 v[212:213], v[212:213], v[216:217]\n"
    "v_pk_add_f32 v[214:215], v[214:215], v[218:219]\n"
    "v_pk_add_f32 v[56:57], v[56:57], v[220:221]\n"
    "v_pk_add_f32 v[58:59], v[58:59], v[222:223]\n"
    "v_pk_add_f32 v[56:57], v[56:57], v[212:213]\n"
    "v_pk_add_f32 v[58:59], v[58:59], v[214:215]\n"
    "v_cvt_pk_bf16_f32 v56, v56, v57\n"
    "v_cvt_pk_bf16_f32 v57, v58, v59\n"
    "buffer_store_dwordx2 v[56:57], v2, s[36:39], s45 offen offset:32 sc1\n"
    "s_waitcnt vmcnt(5)\n"
    "v_lshlrev_b32 v200, 16, v140\n"
    "v_and_b32 v201, v13, v140\n"
    "v_lshlrev_b32 v202, 16, v141\n"
    "v_and_b32 v203, v13, v141\n"
    "v_lshlrev_b32 v204, 16, v144\n"
    "v_and_b32 v205, v13, v144\n"
    "v_lshlrev_b32 v206, 16, v145\n"
    "v_and_b32 v207, v13, v145\n"
    "v_lshlrev_b32 v208, 16, v148\n"
    "v_and_b32 v209, v13, v148\n"
    "v_lshlrev_b32 v210, 16, v149\n"
    "v_and_b32 v211, v13, v149\n"
    "v_pk_add_f32 v[200:201], v[200:201], v[204:205]\n"
    "v_pk_add_f32 v[202:203], v[202:203], v[206:207]\n"
    "v_pk_add_f32 v[72:73], v[72:73], v[208:209]\n"
    "v_pk_add_f32 v[74:75], v[74:75], v[210:211]\n"
    "v_pk_add_f32 v[72:73], v[72:73], v[200:201]\n"
    "v_pk_add_f32 v[74:75], v[74:75], v[202:203]\n"
    "v_cvt_pk_bf16_f32 v72, v72, v73\n"
    "v_cvt_pk_bf16_f32 v73, v74, v75\n"
    "buffer_store_dwordx2 v[72:73], v2, s[36:39], s44 offen offset:64 sc1\n"
    "v_lshlrev_b32 v212, 16, v142\n"
    "v_and_b32 v213, v13, v142\n"
    "v_lshlrev_b32 v214, 16, v143\n"
    "v_and_b32 v215, v13, v143\n"
    "v_lshlrev_b32 v216, 16, v146\n"
    "v_and_b32 v217, v13, v146\n"
    "v_lshlrev_b32 v218, 16, v147\n"
    "v_and_b32 v219, v13, v147\n"
    "v_lshlrev_b32 v220, 16, v150\n"
    "v_and_b32 v221, v13, v150\n"
    "v_lshlrev_b32 v222, 16, v151\n"
    "v_and_b32 v223, v13, v151\n"
    "v_pk_add_f32 v[212:213], v[212:213], v[216:217]\n"
    "v_pk_add_f32 v[214:215], v[214:215], v[218:219]\n"
    "v_pk_add_f32 v[88:89], v[88:89], v[220:221]\n"
    "v_pk_add_f32 v[90:91], v[90:91], v[222:223]\n"
    "v_pk_add_f32 v[88:89], v[88:89], v[212:213]\n"
    "v_pk_add_f32 v[90:91], v[90:91], v[214:215]\n"
    "v_cvt_pk_bf16_f32 v88, v88, v89\n"
    "v_cvt_pk_bf16_f32 v89, v90, v91\n"
    "buffer_store_dwordx2 v[88:89], v2, s[36:39], 0 offen offset:96 sc1\n"
    "s_waitcnt vmcnt(4)\n"
    "v_lshlrev_b32 v200, 16, v152\n"
    "v_and_b32 v201, v13, v152\n"
    "v_lshlrev_b32 v202, 16, v153\n"
    "v_and_b32 v203, v13, v153\n"
    "v_lshlrev_b32 v204, 16, v156\n"
    "v_and_b32 v205, v13, v156\n"
    "v_lshlrev_b32 v206, 16, v157\n"
    "v_and_b32 v207, v13, v157\n"
    "v_lshlrev_b32 v208, 16, v160\n"
    "v_and_b32 v209, v13, v160\n"
    "v_lshlrev_b32 v210, 16, v161\n"
    "v_and_b32 v211, v13, v161\n"
    "v_pk_add_f32 v[200:201], v[200:201], v[204:205]\n"
    "v_pk_add_f32 v[202:203], v[202:203], v[206:207]\n"
    "v_pk_add_f32 v[104:105], v[104:105], v[208:209]\n"
    "v_pk_add_f32 v[106:107], v[106:107], v[210:211]\n"
    "v_pk_add_f32 v[104:105], v[104:105], v[200:201]\n"
    "v_pk_add_f32 v[106:107], v[106:107], v[202:203]\n"
    "v_cvt_pk_bf16_f32 v104, v104, v105\n"
    "v_cvt_pk_bf16_f32 v105, v106, v107\n"
    "buffer_store_dwordx2 v[104:105], v2, s[36:39], s47 offen offset:96 sc1\n"
    "v_lshlrev_b32 v212, 16, v154\n"
    "v_and_b32 v213, v13, v154\n"
    "v_lshlrev_b32 v214, 16, v155\n"
    "v_and_b32 v215, v13, v155\n"
    "v_lshlrev_b32 v216, 16, v158\n"
    "v_and_b32 v217, v13, v158\n"
    "v_lshlrev_b32 v218, 16, v159\n"
    "v_and_b32 v219, v13, v159\n"
    "v_lshlrev_b32 v220, 16, v162\n"
    "v_and_b32 v221, v13, v162\n"
    "v_lshlrev_b32 v222, 16, v163\n"
    "v_and_b32 v223, v13, v163\n"
    "v_pk_add_f32 v[212:213], v[212:213], v[216:217]\n"
    "v_pk_add_f32 v[214:215], v[214:215], v[218:219]\n"
    "v_pk_add_f32 v[120:121], v[120:121], v[220:221]\n"
    "v_pk_add_f32 v[122:123], v[122:123], v[222:223]\n"
    "v_pk_add_f32 v[120:121], v[120:121], v[212:213]\n"
    "v_pk_add_f32 v[122:123], v[122:123], v[214:215]\n"
    "v_cvt_pk_bf16_f32 v120, v120, v121\n"
    "v_cvt_pk_bf16_f32 v121, v122, v123\n"
    "buffer_store_dwordx2 v[120:121], v2, s[36:39], s46 offen offset:128 sc1\n"
    "LVEF_%=:\n"
    "s_cmp_lg_u32 s72, 0\n"
    "s_cbranch_scc1 LNPFE_%=\n"
    "s_lshl_b64 exec, 1, s90\n"
    "buffer_store_dwordx2 v[16:17], v15, s[68:71], s96 offen sc1\n"
    "s_mov_b64 exec, -1\n"
    "LNPFE_%=:\n"
    "s_branch LCODE1_%=\n"
    "LSLOW_%=:\n"
    "s_waitcnt vmcnt(0)\n"
    "v_mov_b32 %[t0], v16\n"
    "v_mov_b32 %[t1], v17\n"
    "LCODE1_%=:\n"
    : [flag] "=&s"(flag), [t0] "=&v"(t0), [t1] "=&v"(t1)
    : [ka] "s"(ka), [wid] "s"(wid), [blk] "s"(blockIdx.x), [lds] "s"((unsigned)(size_t)lds)
    : "v0", "v1", "v2", "v3", "v4", "v5", "v6", "v7", "v8", "v9", "v10", "v11", "v12", "v13", "v14", "v15", "v16", "v17", "v18", "v19", "v20", "v21", "v22", "v23", "v24", "v25", "v26", "v27", "v28", "v29", "v30", "v31", "v32", "v33", "v34", "v35", "v36", "v37", "v38", "v39", "v40", "v41", "v42", "v43", "v44", "v45", "v46", "v47", "v48", "v49", "v50", "v51", "v52", "v53", "v54", "v55", "v56", "v57", "v58", "v59", "v60", "v61", "v62", "v63", "v64", "v65", "v66", "v67", "v68", "v69", "v70", "v71", "v72", "v73", "v74", "v75", "v76", "v77", "v78", "v79", "v80", "v81", "v82", "v83", "v84", "v85", "v86", "v87", "v88", "v89", "v90", "v91", "v92", "v93", "v94", "v95", "v96", "v97", "v98", "v99", "v100", "v101", "v102", "v103", "v104", "v105", "v106", "v107", "v108", "v109", "v110", "v111", "v112", "v113", "v114", "v115", "v116", "v117", "v118", "v119", "v120", "v121", "v122", "v123", "v124", "v125", "v126", "v127", "v128", "v129", "v130", "v131", "v132", "v133", "v134", "v135", "v136", "v137", "v138", "v139", "v140", "v141", "v142", "v143", "v144", "v145", "v146", "v147", "v148", "v149", "v150", "v151", "v152", "v153", "v154", "v155", "v156", "v157", "v158", "v159", "v160", "v161", "v162", "v163", "v164", "v165", "v166", "v167", "v168", "v169", "v170", "v171", "v172", "v173", "v174", "v175", "v176", "v177", "v178", "v179", "v180", "v181", "v182", "v183", "v184", "v185", "v186", "v187", "v188", "v189", "v190", "v191", "v192", "v193", "v194", "v195", "v196", "v197", "v198", "v199", "v200", "v201", "v202", "v203", "v204", "v205", "v206", "v207", "v208", "v209", "v210", "v211", "v212", "v213", "v214", "v215", "v216", "v217", "v218", "v219", "v220", "v221", "v222", "v223", "v224", "v225", "v226", "v227", "v228", "v229", "v230", "v231", "v232", "v233", "v234", "v235", "v236", "v237", "v238", "v239", "v240", "v241", "v242", "v243", "v244", "v245", "v246", "v247", "v248", "v249", "v250", "v251", "v252", "v253", "a0", "a1", "a2", "a3", "a4", "a5", "a6", "a7", "a8", "a9", "a10", "a11", "a12", "a13", "a14", "a15", "a16", "a17", "a18", "a19", "a20", "a21", "a22", "a23", "a24", "a25", "a26", "a27", "a28", "a29", "a30", "a31", "a32", "a33", "a34", "a35", "a36", "a37", "a38", "a39", "a40", "a41", "a42", "a43", "a44", "a45", "a46", "a47", "a48", "a49", "a50", "a51", "a52", "a53", "a54", "a55", "a56", "a57", "a58", "a59", "a60", "a61", "a62", "a63", "a64", "a65", "a66", "a67", "a68", "a69", "a70", "a71", "a72", "a73", "a74", "a75", "a76", "a77", "a78", "a79", "a80", "a81", "a82", "a83", "a84", "a85", "a86", "a87", "a88", "a89", "a90", "a91", "a92", "a93", "a94", "a95", "a96", "a97", "a98", "a99", "a100", "a101", "a102", "a103", "a104", "a105", "a106", "a107", "a108", "a109", "a110", "a111", "a112", "a113", "a114", "a115", "a116", "a117", "a118", "a119", "a120", "a121", "a122", "a123", "a124", "a125", "a126", "a127", "a128", "a129", "a130", "a131", "a132", "a133", "a134", "a135", "a136", "a137", "a138", "a139", "a140", "a141", "a142", "a143", "a144", "a145", "a146", "a147", "a148", "a149", "a150", "a151", "a152", "a153", "a154", "a155", "a156", "a157", "a158", "a159", "a160", "a161", "a162", "a163", "a164", "a165", "a166", "a167", "a168", "a169", "a170", "a171", "a172", "a173", "a174", "a175", "a176", "a177", "a178", "a179", "a180", "a181", "a182", "a183", "a184", "a185", "a186", "a187", "a188", "a189", "a190", "a191", "a192", "a193", "a194", "a195", "a196", "a197", "a198", "a199", "a200", "a201", "a202", "a203", "a204", "a205", "a206", "a207", "a208", "a209", "a210", "a211", "a212", "a213", "a214", "a215", "a216", "a217", "a218", "a219", "a220", "a221", "a222", "a223", "a224", "a225", "a226", "a227", "a228", "a229", "a230", "a231", "a232", "a233", "a234", "a235", "a236", "a237", "a238", "a239", "a240", "a241", "a242", "a243", "a244", "a245", "a246", "a247", "a248", "a249", "a250", "a251", "a252", "a253", "a254", "a255", "s20", "s21", "s22", "s23", "s24", "s25", "s26", "s27", "s28", "s29", "s30", "s31", "s32", "s33", "s34", "s35", "s36", "s37", "s38", "s39", "s40", "s41", "s42", "s43", "s44", "s45", "s46", "s47", "s48", "s49", "s50", "s51", "s52", "s53", "s54", "s55", "s56", "s57", "s58", "s59", "s60", "s61", "s62", "s63", "s64", "s65", "s66", "s67", "s68", "s69", "s70", "s71", "s72", "s73", "s74", "s75", "s76", "s77", "s78", "s79", "s80", "s81", "s82", "s83", "s84", "s85", "s86", "s87", "s88", "s89", "s90", "s91", "s92", "s93", "s94", "s95", "s96", "s97", "s98", "s99", "vcc", "scc", "memory");
  if (__builtin_expect(flag != 0, 0)) {
    const u16* x = (const u16*)ka[0]; const u16* K = (const u16*)ka[1]; const u16* V = (const u16*)ka[2]; const u16* wq = (const u16*)ka[3];
    const u16* wo = (const u16*)ka[4]; char* ws = (char*)ka[5]; u16* out = (u16*)ka[6];
    unsigned lane;
    asm volatile("v_mbcnt_lo_u32_b32 %0, -1, 0\n\tv_mbcnt_hi_u32_b32 %0, -1, %0" : "=v"(lane));
    const int blk = blockIdx.x, b = 8 * (blk & 1) + ((blk >> 3) & 7), h0 = 2 * (4 * ((blk >> 1) & 3) + (blk >> 6));
    for (int i = 0; i < 2; ++i) slow_item(x, K, V, wq, ws, b, h0 + i, (float*)lds, (int)(wid * 64 + lane));
  asm volatile(
    "v_mbcnt_lo_u32_b32 v0, -1, 0\n"
    "v_mbcnt_hi_u32_b32 v0, -1, v0\n"
    "s_mov_b32 s72, %[wid]\n"
    "s_bfe_u32 s74, %[blk], 0x10000\n"
    "s_lshl_b32 s74, s74, 3\n"
    "s_bfe_u32 s75, %[blk], 0x30003\n"
    "s_add_u32 s89, s74, s75\n"
    "s_bfe_u32 s74, %[blk], 0x20001\n"
    "s_lshl_b32 s75, s74, 2\n"
    "s_bfe_u32 s76, %[blk], 0x20006\n"
    "s_add_u32 s90, s75, s76\n"
    "s_lshl_b32 s73, s72, 12\n"
    "v_mov_b32 v16, %[t0]\n"
    "v_mov_b32 v17, %[t1]\n"
    "s_mul_i32 s74, s89, 0x140000\n"
    "s_mov_b64 s[52:53], %[ws]\n"
    "s_add_u32 s52, s52, s74\n"
    "s_addc_u32 s53, s53, 0\n"
    "s_mov_b32 s54, 0x140000\n"
    "s_mov_b32 s55, 0x20000\n"
    "s_mov_b64 s[68:69], %[ws]\n"
    "s_add_u32 s68, s68, 0x3400000\n"
    "s_addc_u32 s69, s69, 0\n"
    "s_mov_b32 s70, 0x40000\n"
    "s_mov_b32 s71, 0x20000\n"
    "s_lshr_b32 s74, s89, 3\n"
    "s_and_b32 s75, s89, 7\n"
    "s_lshl_b32 s75, s75, 3\n"
    "s_add_u32 s74, s74, s75\n"
    "v_bfe_u32 v44, v0, 2, 2\n"
    "v_lshlrev_b32 v44, 1, v44\n"
    "v_and_b32 v45, 3, v0\n"
    "v_lshl_add_u32 v44, v45, 6, v44\n"
    "v_add_u32 v44, s74, v44\n"
    "v_lshlrev_b32 v14, 10, v44\n"
    "v_lshlrev_b32 v1, 4, v0\n"
    "v_mov_b32 v15, 0\n"
    "v_add_u32 v44, 0, v0\n"
    "v_mul_u32_u24 v45, 0xccd, v44\n"
    "v_lshrrev_b32 v45, 16, v45\n"
    "v_mul_u32_u24 v46, 20, v45\n"
    "v_sub_u32 v46, v44, v46\n"
    "v_lshrrev_b32 v47, 1, v45\n"
    "v_and_b32 v47, 2, v47\n"
    "v_xor_b32 v46, v46, v47\n"
    "v_mul_u32_u24 v45, 0x500, v45\n"
    "v_lshl_add_u32 v45, v46, 4, v45\n"
    "v_add_u32 v23, 0xc00, v45\n"
    "v_add_u32 v44, 64, v0\n"
    "v_mul_u32_u24 v45, 0xccd, v44\n"
    "v_lshrrev_b32 v45, 16, v45\n"
    "v_mul_u32_u24 v46, 20, v45\n"
    "v_sub_u32 v46, v44, v46\n"
    "v_lshrrev_b32 v47, 1, v45\n"
    "v_and_b32 v47, 2, v47\n"
    "v_xor_b32 v46, v46, v47\n"
    "v_mul_u32_u24 v45, 0x500, v45\n"
    "v_lshl_add_u32 v45, v46, 4, v45\n"
    "v_add_u32 v24, 0x800, v45\n"
    "v_add_u32 v44, 0x80, v0\n"
    "v_mul_u32_u24 v45, 0xccd, v44\n"
    "v_lshrrev_b32 v45, 16, v45\n"
    "v_mul_u32_u24 v46, 20, v45\n"
    "v_sub_u32 v46, v44, v46\n"
    "v_lshrrev_b32 v47, 1, v45\n"
    "v_and_b32 v47, 2, v47\n"
    "v_xor_b32 v46, v46, v47\n"
    "v_mul_u32_u24 v45, 0x500, v45\n"
    "v_lshl_add_u32 v45, v46, 4, v45\n"
    "v_add_u32 v25, 0x400, v45\n"
    "v_add_u32 v44, 0xc0, v0\n"
    "v_mul_u32_u24 v45, 0xccd, v44\n"
    "v_lshrrev_b32 v45, 16, v45\n"
    "v_mul_u32_u24 v46, 20, v45\n"
    "v_sub_u32 v46, v44, v46\n"
    "v_lshrrev_b32 v47, 1, v45\n"
    "v_and_b32 v47, 2, v47\n"
    "v_xor_b32 v46, v46, v47\n"
    "v_mul_u32_u24 v45, 0x500, v45\n"
    "v_lshl_add_u32 v45, v46, 4, v45\n"
    "v_add_u32 v26, 0, v45\n"
    "v_add_u32 v44, 0x100, v0\n"
    "v_mul_u32_u24 v45, 0xccd, v44\n"
    "v_lshrrev_b32 v45, 16, v45\n"
    "v_mul_u32_u24 v46, 20, v45\n"
    "v_sub_u32 v46, v44, v46\n"
    "v_lshrrev_b32 v47, 1, v45\n"
    "v_and_b32 v47, 2, v47\n"
    "v_xor_b32 v46, v46, v47\n"
    "v_mul_u32_u24 v45, 0x500, v45\n"
    "v_lshl_add_u32 v45, v46, 4, v45\n"
    "v_add_u32 v27, 0xc00, v45\n"
    "s_and_b32 s76, s90, 3\n"
    "s_and_b32 s77, s90, 12\n"
    "s_lshr_b32 s98, s72, 1\n"
    "s_and_b32 s99, s72, 1\n"
    "s_mul_i32 s74, s76, 0x140\n"
    "s_mov_b64 s[60:61], %[wo]\n"
    "s_add_u32 s60, s60, s74\n"
    "s_addc_u32 s61, s61, 0\n"
    "s_sub_u32 s60, s60, 0xc00\n"
    "s_subb_u32 s61, s61, 0\n"
    "s_sub_u32 s62, 0x500c00, s74\n"
    "s_mov_b32 s63, 0x20000\n"
    "s_xor_b32 s74, s90, 0\n"
    "s_lshl_b32 s74, s74, 1\n"
    "s_add_u32 s74, s74, s98\n"
    "s_mul_i32 s40, s74, 0x28000\n"
    "s_mul_i32 s75, s99, 0x5000\n"
    "s_add_u32 s40, s40, s75\n"
    "s_mul_i32 s58, s74, 0xa000\n"
    "s_mul_i32 s75, s99, 0x1400\n"
    "s_add_u32 s58, s58, s75\n"
    "s_cmp_le_u32 s76, 0\n"
    "s_addc_u32 s74, s77, 0\n"
    "s_lshl_b32 s74, s74, 1\n"
    "s_add_u32 s74, s74, s98\n"
    "s_mul_i32 s41, s74, 0x28000\n"
    "s_mul_i32 s75, s99, 0x5000\n"
    "s_add_u32 s41, s41, s75\n"
    "s_mul_i32 s59, s74, 0xa000\n"
    "s_mul_i32 s75, s99, 0x1400\n"
    "s_add_u32 s59, s59, s75\n"
    "s_cmp_le_u32 s76, 1\n"
    "s_addc_u32 s74, s77, 1\n"
    "s_lshl_b32 s74, s74, 1\n"
    "s_add_u32 s74, s74, s98\n"
    "s_mul_i32 s42, s74, 0x28000\n"
    "s_mul_i32 s75, s99, 0x5000\n"
    "s_add_u32 s42, s42, s75\n"
    "s_mul_i32 s78, s74, 0xa000\n"
    "s_mul_i32 s75, s99, 0x1400\n"
    "s_add_u32 s78, s78, s75\n"
    "s_cmp_le_u32 s76, 2\n"
    "s_addc_u32 s74, s77, 2\n"
    "s_lshl_b32 s74, s74, 1\n"
    "s_add_u32 s74, s74, s98\n"
    "s_mul_i32 s43, s74, 0x28000\n"
    "s_mul_i32 s75, s99, 0x5000\n"
    "s_add_u32 s43, s43, s75\n"
    "s_mul_i32 s79, s74, 0xa000\n"
    "s_mul_i32 s75, s99, 0x1400\n"
    "s_add_u32 s79, s79, s75\n"
    "s_mul_i32 s85, s72, 0x1400\n"
    "s_mul_i32 s74, s89, 0x32000\n"
    "s_mul_i32 s75, s99, 0x19000\n"
    "s_add_u32 s74, s74, s75\n"
    "s_mul_i32 s75, s76, 0x140\n"
    "s_add_u32 s74, s74, s75\n"
    "s_mul_i32 s75, s98, 0xa0\n"
    "s_add_u32 s74, s74, s75\n"
    "s_mov_b64 s[36:37], %[out]\n"
    "s_add_u32 s36, s36, s74\n"
    "s_addc_u32 s37, s37, 0\n"
    "s_sub_u32 s38, 0x320000, s74\n"
    "s_mov_b32 s39, 0x20000\n"
    "s_lshl_b32 s74, s89, 2\n"
    "s_add_u32 s74, s74, s76\n"
    "s_lshl_b32 s74, s74, 18\n"
    "s_add_u32 s74, s74, 0x1600000\n"
    "s_mov_b64 s[64:65], %[ws]\n"
    "s_add_u32 s64, s64, s74\n"
    "s_addc_u32 s65, s65, 0\n"
    "s_mov_b32 s66, 0x40000\n"
    "s_mov_b32 s67, 0x20000\n"
    "s_lshl_b32 s74, 1, s90\n"
    "s_lshl_b32 s91, 15, s77\n"
    "s_andn2_b32 s91, s91, s74\n"
    "s_lshl_b32 s92, 0x1111, s76\n"
    "s_andn2_b32 s92, s92, s74\n"
    "s_lshl_b32 s96, %[blk], 10\n"
    "s_lshr_b32 s76, s72, 1\n"
    "v_lshrrev_b32 v9, 2, v0\n"
    "v_mul_u32_u24 v9, 0x140, v9\n"
    "v_and_b32 v13, 3, v0\n"
    "s_mul_i32 s74, s76, 0x2800\n"
    "s_add_u32 s74, s74, 0x8000\n"
    "v_lshl_add_u32 v9, v13, 3, v9\n"
    "v_add_u32 v9, s74, v9\n"
    "s_lshl_b32 s75, s76, 6\n"
    "s_sub_u32 s75, 32, s75\n"
    "v_bfe_u32 v13, v0, 4, 1\n"
    "v_mul_lo_u32 v13, v13, s75\n"
    "s_mul_i32 s74, s76, 0xa0\n"
    "s_sub_u32 s75, 0xa0, s74\n"
    "v_add_u32 v11, s74, v9\n"
    "v_add_u32 v3, v11, v13\n"
    "v_sub_u32 v5, v11, v13\n"
    "v_add_u32 v11, s75, v9\n"
    "v_add_u32 v6, v11, v13\n"
    "v_sub_u32 v7, v11, v13\n"
    "v_mov_b32 v9, 0x1c000\n"
    "s_cmp_lg_u32 s72, 0\n"
    "s_cbranch_scc1 LNPSO_%=\n"
    "s_lshl_b64 exec, 1, s90\n"
    "buffer_store_dwordx2 v[16:17], v15, s[68:71], s96 offen offset:128 sc1\n"
    "s_mov_b64 exec, -1\n"
    "LNPSO_%=:\n"
    "s_add_u32 m0, s85, 0x8000\n"
    "s_add_u32 s98, s40, 0\n"
    "buffer_load_dwordx4 v23, s[60:63], s98 offen lds\n"
    "buffer_load_dwordx4 v24, s[60:63], s98 offen offset:1024 lds\n"
    "buffer_load_dwordx4 v25, s[60:63], s98 offen offset:2048 lds\n"
    "buffer_load_dwordx4 v26, s[60:63], s98 offen offset:3072 lds\n"
    "s_add_u32 m0, s85, 0x9000\n"
    "s_nop 0\n"
    "buffer_load_dwordx4 v27, s[60:63], s98 offen lds\n"
    "s_add_u32 m0, s85, 0xd000\n"
    "s_add_u32 s98, s40, 0xa000\n"
    "buffer_load_dwordx4 v23, s[60:63], s98 offen lds\n"
    "buffer_load_dwordx4 v24, s[60:63], s98 offen offset:1024 lds\n"
    "buffer_load_dwordx4 v25, s[60:63], s98 offen offset:2048 lds\n"
    "buffer_load_dwordx4 v26, s[60:63], s98 offen offset:3072 lds\n"
    "s_add_u32 m0, s85, 0xe000\n"
    "s_nop 0\n"
    "buffer_load_dwordx4 v27, s[60:63], s98 offen lds\n"
    "s_add_u32 m0, s85, 0x12000\n"
    "s_add_u32 s98, s40, 0x14000\n"
    "buffer_load_dwordx4 v23, s[60:63], s98 offen lds\n"
    "buffer_load_dwordx4 v24, s[60:63], s98 offen offset:1024 lds\n"
    "buffer_load_dwordx4 v25, s[60:63], s98 offen offset:2048 lds\n"
    "buffer_load_dwordx4 v26, s[60:63], s98 offen offset:3072 lds\n"
    "s_add_u32 m0, s85, 0x13000\n"
    "s_nop 0\n"
    "buffer_load_dwordx4 v27, s[60:63], s98 offen lds\n"
    "s_add_u32 m0, s85, 0x17000\n"
    "s_add_u32 s98, s40, 0x1e000\n"
    "buffer_load_dwordx4 v23, s[60:63], s98 offen lds\n"
    "buffer_load_dwordx4 v24, s[60:63], s98 offen offset:1024 lds\n"
    "buffer_load_dwordx4 v25, s[60:63], s98 offen offset:2048 lds\n"
    "buffer_load_dwordx4 v26, s[60:63], s98 offen offset:3072 lds\n"
    "s_add_u32 m0, s85, 0x18000\n"
    "s_nop 0\n"
    "buffer_load_dwordx4 v27, s[60:63], s98 offen lds\n"
    "s_add_u32 m0, s85, 0x1d000\n"
    "s_add_u32 s98, s41, 0\n"
    "buffer_load_dwordx4 v23, s[60:63], s98 offen lds\n"
    "buffer_load_dwordx4 v24, s[60:63], s98 offen offset:1024 lds\n"
    "buffer_load_dwordx4 v25, s[60:63], s98 offen offset:2048 lds\n"
    "buffer_load_dwordx4 v26, s[60:63], s98 offen offset:3072 lds\n"
    "s_add_u32 m0, s85, 0x1e000\n"
    "s_nop 0\n"
    "buffer_load_dwordx4 v27, s[60:63], s98 offen lds\n"
    "s_add_u32 m0, s85, 0x22000\n"
    "s_add_u32 s98, s41, 0xa000\n"
    "buffer_load_dwordx4 v23, s[60:63], s98 offen lds\n"
    "buffer_load_dwordx4 v24, s[60:63], s98 offen offset:1024 lds\n"
    "buffer_load_dwordx4 v25, s[60:63], s98 offen offset:2048 lds\n"
    "buffer_load_dwordx4 v26, s[60:63], s98 offen offset:3072 lds\n"
    "s_add_u32 m0, s85, 0x23000\n"
    "s_nop 0\n"
    "buffer_load_dwordx4 v27, s[60:63], s98 offen lds\n"
    "s_mov_b32 exec_lo, s91\n"
    "s_mov_b32 exec_hi, 0\n"
    "buffer_load_dwordx2 v[18:19], v14, s[68:71], 0 offen offset:128 sc1\n"
    "s_mov_b64 exec, -1\n"
    "s_waitcnt vmcnt(0)\n"
    "s_mov_b32 s80, 0\n"
    "LPSO_%=:\n"
    "v_cmp_ne_u64 vcc, v[18:19], v[16:17]\n"
    "s_nop 0\n"
    "s_and_b32 s99, vcc_lo, s91\n"
    "s_cbranch_scc0 LPDSO_%=\n"
    "s_sleep 1\n"
    "s_mov_b32 exec_lo, s91\n"
    "s_mov_b32 exec_hi, 0\n"
    "buffer_load_dwordx2 v[18:19], v14, s[68:71], 0 offen offset:128 sc1\n"
    "s_mov_b64 exec, -1\n"
    "s_add_u32 s80, s80, 1\n"
    "s_waitcnt vmcnt(0)\n"
    "s_cmp_lt_u32 s80, 0x40000\n"
    "s_cbranch_scc1 LPSO_%=\n"
    "LPDSO_%=:\n"
    "s_add_u32 s76, s58, 0\n"
    "s_add_u32 s77, s58, 0x1000\n"
    "buffer_load_dwordx4 v[128:131], v1, s[52:55], s76 offen sc1\n"
    "buffer_load_dwordx4 v[132:135], v1, s[52:55], s76 offen offset:1024 sc1\n"
    "buffer_load_dwordx4 v[136:139], v1, s[52:55], s76 offen offset:2048 sc1\n"
    "buffer_load_dwordx4 v[140:143], v1, s[52:55], s76 offen offset:3072 sc1\n"
    "buffer_load_dwordx4 v[144:147], v1, s[52:55], s77 offen sc1\n"
    "s_add_u32 s76, s58, 0x2800\n"
    "s_add_u32 s77, s58, 0x3800\n"
    "buffer_load_dwordx4 v[168:171], v1, s[52:55], s76 offen sc1\n"
    "buffer_load_dwordx4 v[172:175], v1, s[52:55], s76 offen offset:1024 sc1\n"
    "buffer_load_dwordx4 v[176:179], v1, s[52:55], s76 offen offset:2048 sc1\n"
    "buffer_load_dwordx4 v[180:183], v1, s[52:55], s76 offen offset:3072 sc1\n"
    "buffer_load_dwordx4 v[184:187], v1, s[52:55], s77 offen sc1\n"
    "s_add_u32 s76, s58, 0x5000\n"
    "s_add_u32 s77, s58, 0x6000\n"
    "buffer_load_dwordx4 v[148:151], v1, s[52:55], s76 offen sc1\n"
    "buffer_load_dwordx4 v[152:155], v1, s[52:55], s76 offen offset:1024 sc1\n"
    "buffer_load_dwordx4 v[156:159], v1, s[52:55], s76 offen offset:2048 sc1\n"
    "buffer_load_dwordx4 v[160:163], v1, s[52:55], s76 offen offset:3072 sc1\n"
    "buffer_load_dwordx4 v[164:167], v1, s[52:55], s77 offen sc1\n"
    "s_add_u32 s76, s58, 0x7800\n"
    "s_add_u32 s77, s58, 0x8800\n"
    "buffer_load_dwordx4 v[192:195], v1, s[52:55], s76 offen sc1\n"
    "buffer_load_dwordx4 v[196:199], v1, s[52:55], s76 offen offset:1024 sc1\n"
    "buffer_load_dwordx4 v[200:203], v1, s[52:55], s76 offen offset:2048 sc1\n"
    "buffer_load_dwordx4 v[204:207], v1, s[52:55], s76 offen offset:3072 sc1\n"
    "buffer_load_dwordx4 v[208:211], v1, s[52:55], s77 offen sc1\n"
    "s_add_u32 s76, s59, 0\n"
    "s_add_u32 s77, s59, 0x1000\n"
    "buffer_load_dwordx4 v[212:215], v1, s[52:55], s76 offen sc1\n"
    "buffer_load_dwordx4 v[216:219], v1, s[52:55], s76 offen offset:1024 sc1\n"
    "buffer_load_dwordx4 v[220:223], v1, s[52:55], s76 offen offset:2048 sc1\n"
    "buffer_load_dwordx4 v[224:227], v1, s[52:55], s76 offen offset:3072 sc1\n"
    "buffer_load_dwordx4 v[228:231], v1, s[52:55], s77 offen sc1\n"
    "s_waitcnt vmcnt(20) lgkmcnt(0)\n"
    "s_barrier\n"
    "ds_read_b64_tr_b16 a[144:145], v3\n"
    "ds_read_b64_tr_b16 a[146:147], v3 offset:5120\n"
    "ds_read_b64_tr_b16 a[148:149], v5 offset:32\n"
    "ds_read_b64_tr_b16 a[150:151], v5 offset:5152\n"
    "s_waitcnt lgkmcnt(2)\n"
    "v_mfma_f32_16x16x32_bf16 v[28:31], a[144:147], v[128:131], 0\n"
    "s_add_u32 m0, s85, 0\n"
    "ds_read_b64_tr_b16 a[152:153], v3 offset:64\n"
    "ds_read_b64_tr_b16 a[154:155], v3 offset:5184\n"
    "v_mfma_f32_16x16x32_bf16 v[32:35], a[144:147], v[132:135], 0\n"
    "s_add_u32 s98, s41, 0x14000\n"
    "v_mfma_f32_16x16x32_bf16 v[36:39], a[144:147], v[136:139], 0\n"
    "v_mfma_f32_16x16x32_bf16 v[40:43], a[144:147], v[140:143], 0\n"
    "ds_read_b64_tr_b16 a[156:157], v5 offset:96\n"
    "ds_read_b64_tr_b16 a[158:159], v5 offset:5216\n"
    "v_add_u32 v5, 0x5000, v5\n"
    "v_mfma_f32_16x16x32_bf16 v[44:47], a[144:147], v[144:147], 0\n"
    "s_waitcnt lgkmcnt(4)\n"
    "v_mfma_f32_16x16x32_bf16 v[48:51], a[148:151], v[128:131], 0\n"
    "buffer_load_dwordx4 v23, s[60:63], s98 offen lds\n"
    "v_mfma_f32_16x16x32_bf16 v[52:55], a[148:151], v[132:135], 0\n"
    "v_mfma_f32_16x16x32_bf16 v[56:59], a[148:151], v[136:139], 0\n"
    "buffer_load_dwordx4 v24, s[60:63], s98 offen offset:1024 lds\n"
    "v_mfma_f32_16x16x32_bf16 v[60:63], a[148:151], v[140:143], 0\n"
    "ds_read_b64_tr_b16 a[160:161], v3 offset:128\n"
    "ds_read_b64_tr_b16 a[162:163], v3 offset:5248\n"
    "v_add_u32 v3, 0x5000, v3\n"
    "v_mfma_f32_16x16x32_bf16 v[64:67], a[148:151], v[144:147], 0\n"
    "s_waitcnt lgkmcnt(2)\n"
    "v_mfma_f32_16x16x32_bf16 v[68:71], a[152:155], v[128:131], 0\n"
    "v_mfma_f32_16x16x32_bf16 v[72:75], a[152:155], v[132:135], 0\n"
    "buffer_load_dwordx4 v25, s[60:63], s98 offen offset:2048 lds\n"
    "v_mfma_f32_16x16x32_bf16 v[76:79], a[152:155], v[136:139], 0\n"
    "v_mfma_f32_16x16x32_bf16 v[80:83], a[152:155], v[140:143], 0\n"
    "ds_read_b64_tr_b16 a[164:165], v7\n"
    "ds_read_b64_tr_b16 a[166:167], v7 offset:5120\n"
    "v_mfma_f32_16x16x32_bf16 v[84:87], a[152:155], v[144:147], 0\n"
    "v_mfma_f32_16x16x32_bf16 v[88:91], a[156:159], v[128:131], 0\n"
    "buffer_load_dwordx4 v26, s[60:63], s98 offen offset:3072 lds\n"
    "v_mfma_f32_16x16x32_bf16 v[92:95], a[156:159], v[132:135], 0\n"
    "s_add_u32 m0, s85, 0x1000\n"
    "v_mfma_f32_16x16x32_bf16 v[96:99], a[156:159], v[136:139], 0\n"
    "v_mfma_f32_16x16x32_bf16 v[100:103], a[156:159], v[140:143], 0\n"
    "ds_read_b64_tr_b16 a[168:169], v6 offset:32\n"
    "ds_read_b64_tr_b16 a[170:171], v6 offset:5152\n"
    "v_mfma_f32_16x16x32_bf16 v[104:107], a[156:159], v[144:147], 0\n"
    "s_waitcnt lgkmcnt(2)\n"
    "v_mfma_f32_16x16x32_bf16 v[108:111], a[160:163], v[128:131], 0\n"
    "buffer_load_dwordx4 v27, s[60:63], s98 offen lds\n"
    "v_mfma_f32_16x16x32_bf16 v[112:115], a[160:163], v[132:135], 0\n"
    "v_mfma_f32_16x16x32_bf16 v[116:119], a[160:163], v[136:139], 0\n"
    "v_mfma_f32_16x16x32_bf16 v[120:123], a[160:163], v[140:143], 0\n"
    "ds_read_b64_tr_b16 a[172:173], v7 offset:64\n"
    "ds_read_b64_tr_b16 a[174:175], v7 offset:5184\n"
    "v_mfma_f32_16x16x32_bf16 v[124:127], a[160:163], v[144:147], 0\n"
    "v_mfma_f32_16x16x32_bf16 a[0:3], a[164:167], v[128:131], 0\n"
    "v_mfma_f32_16x16x32_bf16 a[4:7], a[164:167], v[132:135], 0\n"
    "v_mfma_f32_16x16x32_bf16 a[8:11], a[164:167], v[136:139], 0\n"
    "v_mfma_f32_16x16x32_bf16 a[12:15], a[164:167], v[140:143], 0\n"
    "ds_read_b64_tr_b16 a[176:177], v6 offset:96\n"
    "ds_read_b64_tr_b16 a[178:179], v6 offset:5216\n"
    "v_add_u32 v6, 0x5000, v6\n"
    "v_mfma_f32_16x16x32_bf16 a[16:19], a[164:167], v[144:147], 0\n"
    "s_waitcnt lgkmcnt(2)\n"
    "v_mfma_f32_16x16x32_bf16 a[20:23], a[168:171], v[128:131], 0\n"
    "ds_read_b64_tr_b16 a[180:181], v7 offset:128\n"
    "ds_read_b64_tr_b16 a[182:183], v7 offset:5248\n"
    "v_add_u32 v7, 0x5000, v7\n"
    "v_mfma_f32_16x16x32_bf16 a[24:27], a[168:171], v[132:135], 0\n"
    "v_mfma_f32_16x16x32_bf16 a[28:31], a[168:171], v[136:139], 0\n"
    "v_mfma_f32_16x16x32_bf16 a[32:35], a[168:171], v[140:143], 0\n"
    "v_mfma_f32_16x16x32_bf16 a[36:39], a[168:171], v[144:147], 0\n"
    "v_mfma_f32_16x16x32_bf16 a[40:43], a[172:175], v[128:131], 0\n"
    "v_mfma_f32_16x16x32_bf16 a[44:47], a[172:175], v[132:135], 0\n"
    "v_mfma_f32_16x16x32_bf16 a[48:51], a[172:175], v[136:139], 0\n"
    "v_mfma_f32_16x16x32_bf16 a[52:55], a[172:175], v[140:143], 0\n"
    "v_mfma_f32_16x16x32_bf16 a[56:59], a[172:175], v[144:147], 0\n"
    "s_waitcnt vmcnt(20) lgkmcnt(0)\n"
    "s_barrier\n"
    "v_mfma_f32_16x16x32_bf16 a[60:63], a[176:179], v[128:131], 0\n"
    "ds_read_b64_tr_b16 a[184:185], v3\n"
    "ds_read_b64_tr_b16 a[186:187], v3 offset:5120\n"
    "v_mfma_f32_16x16x32_bf16 a[64:67], a[176:179], v[132:135], 0\n"
    "v_mfma_f32_16x16x32_bf16 a[68:71], a[176:179], v[136:139], 0\n"
    "v_mfma_f32_16x16x32_bf16 a[72:75], a[176:179], v[140:143], 0\n"
    "ds_read_b64_tr_b16 a[188:189], v5 offset:32\n"
    "ds_read_b64_tr_b16 a[190:191], v5 offset:5152\n"
    "v_mfma_f32_16x16x32_bf16 a[76:79], a[176:179], v[144:147], 0\n"
    "v_mfma_f32_16x16x32_bf16 a[80:83], a[180:183], v[128:131], 0\n"
    "v_mfma_f32_16x16x32_bf16 a[84:87], a[180:183], v[132:135], 0\n"
    "v_mfma_f32_16x16x32_bf16 a[88:91], a[180:183], v[136:139], 0\n"
    "v_mfma_f32_16x16x32_bf16 a[92:95], a[180:183], v[140:143], 0\n"
    "ds_read_b64_tr_b16 a[144:145], v3 offset:64\n"
    "ds_read_b64_tr_b16 a[146:147], v3 offset:5184\n"
    "v_mfma_f32_16x16x32_bf16 a[96:99], a[180:183], v[144:147], 0\n"
    "s_waitcnt lgkmcnt(2)\n"
    "v_mfma_f32_16x16x32_bf16 v[28:31], a[184:187], v[168:171], v[28:31]\n"
    "v_mfma_f32_16x16x32_bf16 v[32:35], a[184:187], v[172:175], v[32:35]\n"
    "v_mfma_f32_16x16x32_bf16 v[36:39], a[184:187], v[176:179], v[36:39]\n"
    "v_mfma_f32_16x16x32_bf16 v[40:43], a[184:187], v[180:183], v[40:43]\n"
    "ds_read_b64_tr_b16 a[148:149], v5 offset:96\n"
    "ds_read_b64_tr_b16 a[150:151], v5 offset:5216\n"
    "v_add_u32 v5, 0x5000, v5\n"
    "v_mfma_f32_16x16x32_bf16 v[44:47], a[184:187], v[184:187], v[44:47]\n"
    "v_mfma_f32_16x16x32_bf16 v[48:51], a[188:191], v[168:171], v[48:51]\n"
    "v_mfma_f32_16x16x32_bf16 v[52:55], a[188:191], v[172:175], v[52:55]\n"
    "v_mfma_f32_16x16x32_bf16 v[56:59], a[188:191], v[176:179], v[56:59]\n"
    "v_mfma_f32_16x16x32_bf16 v[60:63], a[188:191], v[180:183], v[60:63]\n"
    "ds_read_b64_tr_b16 a[152:153], v3 offset:128\n"
    "ds_read_b64_tr_b16 a[154:155], v3 offset:5248\n"
    "v_add_u32 v3, 0x5000, v3\n"
    "v_mfma_f32_16x16x32_bf16 v[64:67], a[188:191], v[184:187], v[64:67]\n"
    "s_waitcnt lgkmcnt(2)\n"
    "v_mfma_f32_16x16x32_bf16 v[68:71], a[144:147], v[168:171], v[68:71]\n"
    "s_mov_b32 s44, 0x5000\n"
    "v_mfma_f32_16x16x32_bf16 v[72:75], a[144:147], v[172:175], v[72:75]\n"
    "s_mov_b32 s45, 0xa000\n"
    "v_mfma_f32_16x16x32_bf16 v[76:79], a[144:147], v[176:179], v[76:79]\n"
    "s_mov_b32 s46, 0xf000\n"
    "v_mfma_f32_16x16x32_bf16 v[80:83], a[144:147], v[180:183], v[80:83]\n"
    "s_mov_b32 s47, 0x14000\n"
    "ds_read_b64_tr_b16 a[156:157], v7\n"
    "ds_read_b64_tr_b16 a[158:159], v7 offset:5120\n"
    "v_mfma_f32_16x16x32_bf16 v[84:87], a[144:147], v[184:187], v[84:87]\n"
    "v_mfma_f32_16x16x32_bf16 v[88:91], a[148:151], v[168:171], v[88:91]\n"
    "v_mfma_f32_16x16x32_bf16 v[92:95], a[148:151], v[172:175], v[92:95]\n"
    "v_mfma_f32_16x16x32_bf16 v[96:99], a[148:151], v[176:179], v[96:99]\n"
    "v_mfma_f32_16x16x32_bf16 v[100:103], a[148:151], v[180:183], v[100:103]\n"
    "ds_read_b64_tr_b16 a[160:161], v6 offset:32\n"
    "ds_read_b64_tr_b16 a[162:163], v6 offset:5152\n"
    "v_mfma_f32_16x16x32_bf16 v[104:107], a[148:151], v[184:187], v[104:107]\n"
    "s_waitcnt lgkmcnt(2)\n"
    "v_mfma_f32_16x16x32_bf16 v[108:111], a[152:155], v[168:171], v[108:111]\n"
    "v_mfma_f32_16x16x32_bf16 v[112:115], a[152:155], v[172:175], v[112:115]\n"
    "v_mfma_f32_16x16x32_bf16 v[116:119], a[152:155], v[176:179], v[116:119]\n"
    "v_mfma_f32_16x16x32_bf16 v[120:123], a[152:155], v[180:183], v[120:123]\n"
    "ds_read_b64_tr_b16 a[164:165], v7 offset:64\n"
    "ds_read_b64_tr_b16 a[166:167], v7 offset:5184\n"
    "v_mfma_f32_16x16x32_bf16 v[124:127], a[152:155], v[184:187], v[124:127]\n"
    "v_mfma_f32_16x16x32_bf16 a[0:3], a[156:159], v[168:171], a[0:3]\n"
    "v_mfma_f32_16x16x32_bf16 a[4:7], a[156:159], v[172:175], a[4:7]\n"
    "v_mfma_f32_16x16x32_bf16 a[8:11], a[156:159], v[176:179], a[8:11]\n"
    "v_mfma_f32_16x16x32_bf16 a[12:15], a[156:159], v[180:183], a[12:15]\n"
    "ds_read_b64_tr_b16 a[168:169], v6 offset:96\n"
    "ds_read_b64_tr_b16 a[170:171], v6 offset:5216\n"
    "v_add_u32 v6, 0x5000, v6\n"
    "v_mfma_f32_16x16x32_bf16 a[16:19], a[156:159], v[184:187], a[16:19]\n"
    "s_waitcnt lgkmcnt(2)\n"
    "v_mfma_f32_16x16x32_bf16 a[20:23], a[160:163], v[168:171], a[20:23]\n"
    "ds_read_b64_tr_b16 a[172:173], v7 offset:128\n"
    "ds_read_b64_tr_b16 a[174:175], v7 offset:5248\n"
    "v_add_u32 v7, 0x5000, v7\n"
    "v_mfma_f32_16x16x32_bf16 a[24:27], a[160:163], v[172:175], a[24:27]\n"
    "v_mfma_f32_16x16x32_bf16 a[28:31], a[160:163], v[176:179], a[28:31]\n"
    "v_mfma_f32_16x16x32_bf16 a[32:35], a[160:163], v[180:183], a[32:35]\n"
    "v_mfma_f32_16x16x32_bf16 a[36:39], a[160:163], v[184:187], a[36:39]\n"
    "v_mfma_f32_16x16x32_bf16 a[40:43], a[164:167], v[168:171], a[40:43]\n"
    "v_mfma_f32_16x16x32_bf16 a[44:47], a[164:167], v[172:175], a[44:47]\n"
    "v_mfma_f32_16x16x32_bf16 a[48:51], a[164:167], v[176:179], a[48:51]\n"
    "v_mfma_f32_16x16x32_bf16 a[52:55], a[164:167], v[180:183], a[52:55]\n"
    "v_mfma_f32_16x16x32_bf16 a[56:59], a[164:167], v[184:187], a[56:59]\n"
    "s_waitcnt vmcnt(15) lgkmcnt(0)\n"
    "s_barrier\n"
    "v_mfma_f32_16x16x32_bf16 a[60:63], a[168:171], v[168:171], a[60:63]\n"
    "ds_read_b64_tr_b16 a[176:177], v3\n"
    "ds_read_b64_tr_b16 a[178:179], v3 offset:5120\n"
    "v_mfma_f32_16x16x32_bf16 a[64:67], a[168:171], v[172:175], a[64:67]\n"
    "v_mfma_f32_16x16x32_bf16 a[68:71], a[168:171], v[176:179], a[68:71]\n"
    "v_mfma_f32_16x16x32_bf16 a[72:75], a[168:171], v[180:183], a[72:75]\n"
    "ds_read_b64_tr_b16 a[180:181], v5 offset:32\n"
    "ds_read_b64_tr_b16 a[182:183], v5 offset:5152\n"
    "v_mfma_f32_16x16x32_bf16 a[76:79], a[168:171], v[184:187], a[76:79]\n"
    "v_mfma_f32_16x16x32_bf16 a[80:83], a[172:175], v[168:171], a[80:83]\n"
    "v_mfma_f32_16x16x32_bf16 a[84:87], a[172:175], v[172:175], a[84:87]\n"
    "v_mfma_f32_16x16x32_bf16 a[88:91], a[172:175], v[176:179], a[88:91]\n"
    "v_mfma_f32_16x16x32_bf16 a[92:95], a[172:175], v[180:183], a[92:95]\n"
    "ds_read_b64_tr_b16 a[184:185], v3 offset:64\n"
    "ds_read_b64_tr_b16 a[186:187], v3 offset:5184\n"
    "v_mfma_f32_16x16x32_bf16 a[96:99], a[172:175], v[184:187], a[96:99]\n"
    "s_waitcnt lgkmcnt(2)\n"
    "v_mfma_f32_16x16x32_bf16 v[28:31], a[176:179], v[148:151], v[28:31]\n"
    "s_add_u32 s76, s59, 0x2800\n"
    "v_mfma_f32_16x16x32_bf16 v[32:35], a[176:179], v[152:155], v[32:35]\n"
    "s_add_u32 s77, s59, 0x3800\n"
    "v_mfma_f32_16x16x32_bf16 v[36:39], a[176:179], v[156:159], v[36:39]\n"
    "buffer_load_dwordx4 v[232:235], v1, s[52:55], s76 offen sc1\n"
    "v_mfma_f32_16x16x32_bf16 v[40:43], a[176:179], v[160:163], v[40:43]\n"
    "ds_read_b64_tr_b16 a[188:189], v5 offset:96\n"
    "ds_read_b64_tr_b16 a[190:191], v5 offset:5216\n"
    "v_add_u32 v5, 0x5000, v5\n"
    "v_mfma_f32_16x16x32_bf16 v[44:47], a[176:179], v[164:167], v[44:47]\n"
    "v_mfma_f32_16x16x32_bf16 v[48:51], a[180:183], v[148:151], v[48:51]\n"
    "buffer_load_dwordx4 v[236:239], v1, s[52:55], s76 offen offset:1024 sc1\n"
    "v_mfma_f32_16x16x32_bf16 v[52:55], a[180:183], v[152:155], v[52:55]\n"
    "v_mfma_f32_16x16x32_bf16 v[56:59], a[180:183], v[156:159], v[56:59]\n"
    "v_mfma_f32_16x16x32_bf16 v[60:63], a[180:183], v[160:163], v[60:63]\n"
    "ds_read_b64_tr_b16 a[144:145], v3 offset:128\n"
    "ds_read_b64_tr_b16 a[146:147], v3 offset:5248\n"
    "v_add_u32 v3, 0x5000, v3\n"
    "v_mfma_f32_16x16x32_bf16 v[64:67], a[180:183], v[164:167], v[64:67]\n"
    "s_waitcnt lgkmcnt(2)\n"
    "v_mfma_f32_16x16x32_bf16 v[68:71], a[184:187], v[148:151], v[68:71]\n"
    "buffer_load_dwordx4 v[240:243], v1, s[52:55], s76 offen offset:2048 sc1\n"
    "v_mfma_f32_16x16x32_bf16 v[72:75], a[184:187], v[152:155], v[72:75]\n"
    "buffer_load_dwordx4 v[244:247], v1, s[52:55], s76 offen offset:3072 sc1\n"
    "v_mfma_f32_16x16x32_bf16 v[76:79], a[184:187], v[156:159], v[76:79]\n"
    "v_mfma_f32_16x16x32_bf16 v[80:83], a[184:187], v[160:163], v[80:83]\n"
    "ds_read_b64_tr_b16 a[148:149], v7\n"
    "ds_read_b64_tr_b16 a[150:151], v7 offset:5120\n"
    "v_mfma_f32_16x16x32_bf16 v[84:87], a[184:187], v[164:167], v[84:87]\n"
    "buffer_load_dwordx4 v[248:251], v1, s[52:55], s77 offen sc1\n"
    "v_mfma_f32_16x16x32_bf16 v[88:91], a[188:191], v[148:151], v[88:91]\n"
    "v_mfma_f32_16x16x32_bf16 v[92:95], a[188:191], v[152:155], v[92:95]\n"
    "v_mfma_f32_16x16x32_bf16 v[96:99], a[188:191], v[156:159], v[96:99]\n"
    "v_mfma_f32_16x16x32_bf16 v[100:103], a[188:191], v[160:163], v[100:103]\n"
    "ds_read_b64_tr_b16 a[152:153], v6 offset:32\n"
    "ds_read_b64_tr_b16 a[154:155], v6 offset:5152\n"
    "v_mfma_f32_16x16x32_bf16 v[104:107], a[188:191], v[164:167], v[104:107]\n"
    "s_waitcnt lgkmcnt(2)\n"
    "v_mfma_f32_16x16x32_bf16 v[108:111], a[144:147], v[148:151], v[108:111]\n"
    "v_mfma_f32_16x16x32_bf16 v[112:115], a[144:147], v[152:155], v[112:115]\n"
    "v_mfma_f32_16x16x32_bf16 v[116:119], a[144:147], v[156:159], v[116:119]\n"
    "v_mfma_f32_16x16x32_bf16 v[120:123], a[144:147], v[160:163], v[120:123]\n"
    "ds_read_b64_tr_b16 a[156:157], v7 offset:64\n"
    "ds_read_b64_tr_b16 a[158:159], v7 offset:5184\n"
    "v_mfma_f32_16x16x32_bf16 v[124:127], a[144:147], v[164:167], v[124:127]\n"
    "v_mfma_f32_16x16x32_bf16 a[0:3], a[148:151], v[148:151], a[0:3]\n"
    "v_mfma_f32_16x16x32_bf16 a[4:7], a[148:151], v[152:155], a[4:7]\n"
    "v_mfma_f32_16x16x32_bf16 a[8:11], a[148:151], v[156:159], a[8:11]\n"
    "v_mfma_f32_16x16x32_bf16 a[12:15], a[148:151], v[160:163], a[12:15]\n"
    "ds_read_b64_tr_b16 a[160:161], v6 offset:96\n"
    "ds_read_b64_tr_b16 a[162:163], v6 offset:5216\n"
    "v_add_u32 v6, 0x5000, v6\n"
    "v_mfma_f32_16x16x32_bf16 a[16:19], a[148:151], v[164:167], a[16:19]\n"
    "s_waitcnt lgkmcnt(2)\n"
    "v_mfma_f32_16x16x32_bf16 a[20:23], a[152:155], v[148:151], a[20:23]\n"
    "ds_read_b64_tr_b16 a[164:165], v7 offset:128\n"
    "ds_read_b64_tr_b16 a[166:167], v7 offset:5248\n"
    "v_add_u32 v7, 0x5000, v7\n"
    "v_mfma_f32_16x16x32_bf16 a[24:27], a[152:155], v[152:155], a[24:27]\n"
    "v_mfma_f32_16x16x32_bf16 a[28:31], a[152:155], v[156:159], a[28:31]\n"
    "v_mfma_f32_16x16x32_bf16 a[32:35], a[152:155], v[160:163], a[32:35]\n"
    "v_mfma_f32_16x16x32_bf16 a[36:39], a[152:155], v[164:167], a[36:39]\n"
    "v_mfma_f32_16x16x32_bf16 a[40:43], a[156:159], v[148:151], a[40:43]\n"
    "v_mfma_f32_16x16x32_bf16 a[44:47], a[156:159], v[152:155], a[44:47]\n"
    "v_mfma_f32_16x16x32_bf16 a[48:51], a[156:159], v[156:159], a[48:51]\n"
    "v_mfma_f32_16x16x32_bf16 a[52:55], a[156:159], v[160:163], a[52:55]\n"
    "v_mfma_f32_16x16x32_bf16 a[56:59], a[156:159], v[164:167], a[56:59]\n"
    "s_waitcnt vmcnt(15) lgkmcnt(0)\n"
    "s_barrier\n"
    "v_mfma_f32_16x16x32_bf16 a[60:63], a[160:163], v[148:151], a[60:63]\n"
    "ds_read_b64_tr_b16 a[168:169], v3\n"
    "ds_read_b64_tr_b16 a[170:171], v3 offset:5120\n"
    "v_mfma_f32_16x16x32_bf16 a[64:67], a[160:163], v[152:155], a[64:67]\n"
    "v_mfma_f32_16x16x32_bf16 a[68:71], a[160:163], v[156:159], a[68:71]\n"
    "v_mfma_f32_16x16x32_bf16 a[72:75], a[160:163], v[160:163], a[72:75]\n"
    "ds_read_b64_tr_b16 a[172:173], v5 offset:32\n"
    "ds_read_b64_tr_b16 a[174:175], v5 offset:5152\n"
    "v_mfma_f32_16x16x32_bf16 a[76:79], a[160:163], v[164:167], a[76:79]\n"
    "v_mfma_f32_16x16x32_bf16 a[80:83], a[164:167], v[148:151], a[80:83]\n"
    "v_mfma_f32_16x16x32_bf16 a[84:87], a[164:167], v[152:155], a[84:87]\n"
    "v_mfma_f32_16x16x32_bf16 a[88:91], a[164:167], v[156:159], a[88:91]\n"
    "v_mfma_f32_16x16x32_bf16 a[92:95], a[164:167], v[160:163], a[92:95]\n"
    "ds_read_b64_tr_b16 a[176:177], v3 offset:64\n"
    "ds_read_b64_tr_b16 a[178:179], v3 offset:5184\n"
    "v_mfma_f32_16x16x32_bf16 a[96:99], a[164:167], v[164:167], a[96:99]\n"
    "s_waitcnt lgkmcnt(2)\n"
    "v_mfma_f32_16x16x32_bf16 v[28:31], a[168:171], v[192:195], v[28:31]\n"
    "s_add_u32 s76, s59, 0x5000\n"
    "v_mfma_f32_16x16x32_bf16 v[32:35], a[168:171], v[196:199], v[32:35]\n"
    "s_add_u32 s77, s59, 0x6000\n"
    "v_mfma_f32_16x16x32_bf16 v[36:39], a[168:171], v[200:203], v[36:39]\n"
    "buffer_load_dwordx4 v[128:131], v1, s[52:55], s76 offen sc1\n"
    "v_mfma_f32_16x16x32_bf16 v[40:43], a[168:171], v[204:207], v[40:43]\n"
    "ds_read_b64_tr_b16 a[180:181], v5 offset:96\n"
    "ds_read_b64_tr_b16 a[182:183], v5 offset:5216\n"
    "v_add_u32 v5, 0x6000, v5\n"
    "v_mfma_f32_16x16x32_bf16 v[44:47], a[168:171], v[208:211], v[44:47]\n"
    "v_mfma_f32_16x16x32_bf16 v[48:51], a[172:175], v[192:195], v[48:51]\n"
    "buffer_load_dwordx4 v[132:135], v1, s[52:55], s76 offen offset:1024 sc1\n"
    "v_mfma_f32_16x16x32_bf16 v[52:55], a[172:175], v[196:199], v[52:55]\n"
    "v_mfma_f32_16x16x32_bf16 v[56:59], a[172:175], v[200:203], v[56:59]\n"
    "v_mfma_f32_16x16x32_bf16 v[60:63], a[172:175], v[204:207], v[60:63]\n"
    "ds_read_b64_tr_b16 a[184:185], v3 offset:128\n"
    "ds_read_b64_tr_b16 a[186:187], v3 offset:5248\n"
    "v_add_u32 v3, 0x6000, v3\n"
    "v_mfma_f32_16x16x32_bf16 v[64:67], a[172:175], v[208:211], v[64:67]\n"
    "s_waitcnt lgkmcnt(2)\n"
    "v_mfma_f32_16x16x32_bf16 v[68:71], a[176:179], v[192:195], v[68:71]\n"
    "buffer_load_dwordx4 v[136:139], v1, s[52:55], s76 offen offset:2048 sc1\n"
    "v_mfma_f32_16x16x32_bf16 v[72:75], a[176:179], v[196:199], v[72:75]\n"
    "buffer_load_dwordx4 v[140:143], v1, s[52:55], s76 offen offset:3072 sc1\n"
    "v_mfma_f32_16x16x32_bf16 v[76:79], a[176:179], v[200:203], v[76:79]\n"
    "v_mfma_f32_16x16x32_bf16 v[80:83], a[176:179], v[204:207], v[80:83]\n"
    "ds_read_b64_tr_b16 a[188:189], v7\n"
    "ds_read_b64_tr_b16 a[190:191], v7 offset:5120\n"
    "v_mfma_f32_16x16x32_bf16 v[84:87], a[176:179], v[208:211], v[84:87]\n"
    "buffer_load_dwordx4 v[144:147], v1, s[52:55], s77 offen sc1\n"
    "v_mfma_f32_16x16x32_bf16 v[88:91], a[180:183], v[192:195], v[88:91]\n"
    "v_mfma_f32_16x16x32_bf16 v[92:95], a[180:183], v[196:199], v[92:95]\n"
    "v_mfma_f32_16x16x32_bf16 v[96:99], a[180:183], v[200:203], v[96:99]\n"
    "v_mfma_f32_16x16x32_bf16 v[100:103], a[180:183], v[204:207], v[100:103]\n"
    "ds_read_b64_tr_b16 a[144:145], v6 offset:32\n"
    "ds_read_b64_tr_b16 a[146:147], v6 offset:5152\n"
    "v_mfma_f32_16x16x32_bf16 v[104:107], a[180:183], v[208:211], v[104:107]\n"
    "s_waitcnt lgkmcnt(2)\n"
    "v_mfma_f32_16x16x32_bf16 v[108:111], a[184:187], v[192:195], v[108:111]\n"
    "v_mfma_f32_16x16x32_bf16 v[112:115], a[184:187], v[196:199], v[112:115]\n"
    "v_mfma_f32_16x16x32_bf16 v[116:119], a[184:187], v[200:203], v[116:119]\n"
    "v_mfma_f32_16x16x32_bf16 v[120:123], a[184:187], v[204:207], v[120:123]\n"
    "ds_read_b64_tr_b16 a[148:149], v7 offset:64\n"
    "ds_read_b64_tr_b16 a[150:151], v7 offset:5184\n"
    "v_mfma_f32_16x16x32_bf16 v[124:127], a[184:187], v[208:211], v[124:127]\n"
    "v_mfma_f32_16x16x32_bf16 a[0:3], a[188:191], v[192:195], a[0:3]\n"
    "v_mfma_f32_16x16x32_bf16 a[4:7], a[188:191], v[196:199], a[4:7]\n"
    "v_mfma_f32_16x16x32_bf16 a[8:11], a[188:191], v[200:203], a[8:11]\n"
    "v_mfma_f32_16x16x32_bf16 a[12:15], a[188:191], v[204:207], a[12:15]\n"
    "ds_read_b64_tr_b16 a[152:153], v6 offset:96\n"
    "ds_read_b64_tr_b16 a[154:155], v6 offset:5216\n"
    "v_add_u32 v6, 0x6000, v6\n"
    "v_mfma_f32_16x16x32_bf16 a[16:19], a[188:191], v[208:211], a[16:19]\n"
    "s_waitcnt lgkmcnt(2)\n"
    "v_mfma_f32_16x16x32_bf16 a[20:23], a[144:147], v[192:195], a[20:23]\n"
    "ds_read_b64_tr_b16 a[156:157], v7 offset:128\n"
    "ds_read_b64_tr_b16 a[158:159], v7 offset:5248\n"
    "v_add_u32 v7, 0x6000, v7\n"
    "v_mfma_f32_16x16x32_bf16 a[24:27], a[144:147], v[196:199], a[24:27]\n"
    "v_mfma_f32_16x16x32_bf16 a[28:31], a[144:147], v[200:203], a[28:31]\n"
    "v_mfma_f32_16x16x32_bf16 a[32:35], a[144:147], v[204:207], a[32:35]\n"
    "v_mfma_f32_16x16x32_bf16 a[36:39], a[144:147], v[208:211], a[36:39]\n"
    "v_mfma_f32_16x16x32_bf16 a[40:43], a[148:151], v[192:195], a[40:43]\n"
    "v_mfma_f32_16x16x32_bf16 a[44:47], a[148:151], v[196:199], a[44:47]\n"
    "v_mfma_f32_16x16x32_bf16 a[48:51], a[148:151], v[200:203], a[48:51]\n"
    "v_mfma_f32_16x16x32_bf16 a[52:55], a[148:151], v[204:207], a[52:55]\n"
    "v_mfma_f32_16x16x32_bf16 a[56:59], a[148:151], v[208:211], a[56:59]\n"
    "s_waitcnt vmcnt(15) lgkmcnt(0)\n"
    "s_barrier\n"
    "v_mfma_f32_16x16x32_bf16 a[60:63], a[152:155], v[192:195], a[60:63]\n"
    "ds_read_b64_tr_b16 a[160:161], v3\n"
    "ds_read_b64_tr_b16 a[162:163], v3 offset:5120\n"
    "v_mfma_f32_16x16x32_bf16 a[64:67], a[152:155], v[196:199], a[64:67]\n"
    "v_mfma_f32_16x16x32_bf16 a[68:71], a[152:155], v[200:203], a[68:71]\n"
    "v_mfma_f32_16x16x32_bf16 a[72:75], a[152:155], v[204:207], a[72:75]\n"
    "ds_read_b64_tr_b16 a[164:165], v5 offset:32\n"
    "ds_read_b64_tr_b16 a[166:167], v5 offset:5152\n"
    "v_mfma_f32_16x16x32_bf16 a[76:79], a[152:155], v[208:211], a[76:79]\n"
    "v_mfma_f32_16x16x32_bf16 a[80:83], a[156:159], v[192:195], a[80:83]\n"
    "v_mfma_f32_16x16x32_bf16 a[84:87], a[156:159], v[196:199], a[84:87]\n"
    "v_mfma_f32_16x16x32_bf16 a[88:91], a[156:159], v[200:203], a[88:91]\n"
    "v_mfma_f32_16x16x32_bf16 a[92:95], a[156:159], v[204:207], a[92:95]\n"
    "ds_read_b64_tr_b16 a[168:169], v3 offset:64\n"
    "ds_read_b64_tr_b16 a[170:171], v3 offset:5184\n"
    "v_mfma_f32_16x16x32_bf16 a[96:99], a[156:159], v[208:211], a[96:99]\n"
    "s_waitcnt lgkmcnt(2)\n"
    "v_mfma_f32_16x16x32_bf16 v[28:31], a[160:163], v[212:215], v[28:31]\n"
    "s_add_u32 s76, s59, 0x7800\n"
    "v_mfma_f32_16x16x32_bf16 v[32:35], a[160:163], v[216:219], v[32:35]\n"
    "s_add_u32 s77, s59, 0x8800\n"
    "v_mfma_f32_16x16x32_bf16 v[36:39], a[160:163], v[220:223], v[36:39]\n"
    "buffer_load_dwordx4 v[168:171], v1, s[52:55], s76 offen sc1\n"
    "v_mfma_f32_16x16x32_bf16 v[40:43], a[160:163], v[224:227], v[40:43]\n"
    "ds_read_b64_tr_b16 a[172:173], v5 offset:96\n"
    "ds_read_b64_tr_b16 a[174:175], v5 offset:5216\n"
    "v_add_u32 v5, 0x5000, v5\n"
    "v_mfma_f32_16x16x32_bf16 v[44:47], a[160:163], v[228:231], v[44:47]\n"
    "v_mfma_f32_16x16x32_bf16 v[48:51], a[164:167], v[212:215], v[48:51]\n"
    "buffer_load_dwordx4 v[172:175], v1, s[52:55], s76 offen offset:1024 sc1\n"
    "v_mfma_f32_16x16x32_bf16 v[52:55], a[164:167], v[216:219], v[52:55]\n"
    "v_mfma_f32_16x16x32_bf16 v[56:59], a[164:167], v[220:223], v[56:59]\n"
    "v_mfma_f32_16x16x32_bf16 v[60:63], a[164:167], v[224:227], v[60:63]\n"
    "ds_read_b64_tr_b16 a[176:177], v3 offset:128\n"
    "ds_read_b64_tr_b16 a[178:179], v3 offset:5248\n"
    "v_add_u32 v3, 0x5000, v3\n"
    "v_mfma_f32_16x16x32_bf16 v[64:67], a[164:167], v[228:231], v[64:67]\n"
    "s_waitcnt lgkmcnt(2)\n"
    "v_mfma_f32_16x16x32_bf16 v[68:71], a[168:171], v[212:215], v[68:71]\n"
    "buffer_load_dwordx4 v[176:179], v1, s[52:55], s76 offen offset:2048 sc1\n"
    "v_mfma_f32_16x16x32_bf16 v[72:75], a[168:171], v[216:219], v[72:75]\n"
    "buffer_load_dwordx4 v[180:183], v1, s[52:55], s76 offen offset:3072 sc1\n"
    "v_mfma_f32_16x16x32_bf16 v[76:79], a[168:171], v[220:223], v[76:79]\n"
    "v_mfma_f32_16x16x32_bf16 v[80:83], a[168:171], v[224:227], v[80:83]\n"
    "ds_read_b64_tr_b16 a[180:181], v7\n"
    "ds_read_b64_tr_b16 a[182:183], v7 offset:5120\n"
    "v_mfma_f32_16x16x32_bf16 v[84:87], a[168:171], v[228:231], v[84:87]\n"
    "buffer_load_dwordx4 v[184:187], v1, s[52:55], s77 offen sc1\n"
    "v_mfma_f32_16x16x32_bf16 v[88:91], a[172:175], v[212:215], v[88:91]\n"
    "s_add_u32 m0, s85, 0x8000\n"
    "v_mfma_f32_16x16x32_bf16 v[92:95], a[172:175], v[216:219], v[92:95]\n"
    "s_add_u32 s98, s41, 0x1e000\n"
    "v_mfma_f32_16x16x32_bf16 v[96:99], a[172:175], v[220:223], v[96:99]\n"
    "buffer_load_dwordx4 v23, s[60:63], s98 offen lds\n"
    "v_mfma_f32_16x16x32_bf16 v[100:103], a[172:175], v[224:227], v[100:103]\n"
    "ds_read_b64_tr_b16 a[184:185], v6 offset:32\n"
    "ds_read_b64_tr_b16 a[186:187], v6 offset:5152\n"
    "v_mfma_f32_16x16x32_bf16 v[104:107], a[172:175], v[228:231], v[104:107]\n"
    "s_waitcnt lgkmcnt(2)\n"
    "v_mfma_f32_16x16x32_bf16 v[108:111], a[176:179], v[212:215], v[108:111]\n"
    "buffer_load_dwordx4 v24, s[60:63], s98 offen offset:1024 lds\n"
    "v_mfma_f32_16x16x32_bf16 v[112:115], a[176:179], v[216:219], v[112:115]\n"
    "v_mfma_f32_16x16x32_bf16 v[116:119], a[176:179], v[220:223], v[116:119]\n"
    "v_mfma_f32_16x16x32_bf16 v[120:123], a[176:179], v[224:227], v[120:123]\n"
    "ds_read_b64_tr_b16 a[188:189], v7 offset:64\n"
    "ds_read_b64_tr_b16 a[190:191], v7 offset:5184\n"
    "v_mfma_f32_16x16x32_bf16 v[124:127], a[176:179], v[228:231], v[124:127]\n"
    "buffer_load_dwordx4 v25, s[60:63], s98 offen offset:2048 lds\n"
    "v_mfma_f32_16x16x32_bf16 a[0:3], a[180:183], v[212:215], a[0:3]\n"
    "v_mfma_f32_16x16x32_bf16 a[4:7], a[180:183], v[216:219], a[4:7]\n"
    "buffer_load_dwordx4 v26, s[60:63], s98 offen offset:3072 lds\n"
    "v_mfma_f32_16x16x32_bf16 a[8:11], a[180:183], v[220:223], a[8:11]\n"
    "s_add_u32 m0, s85, 0x9000\n"
    "v_mfma_f32_16x16x32_bf16 a[12:15], a[180:183], v[224:227], a[12:15]\n"
    "ds_read_b64_tr_b16 a[144:145], v6 offset:96\n"
    "ds_read_b64_tr_b16 a[146:147], v6 offset:5216\n"
    "v_add_u32 v6, 0x5000, v6\n"
    "v_mfma_f32_16x16x32_bf16 a[16:19], a[180:183], v[228:231], a[16:19]\n"
    "s_waitcnt lgkmcnt(2)\n"
    "v_mfma_f32_16x16x32_bf16 a[20:23], a[184:187], v[212:215], a[20:23]\n"
    "ds_read_b64_tr_b16 a[148:149], v7 offset:128\n"
    "ds_read_b64_tr_b16 a[150:151], v7 offset:5248\n"
    "v_add_u32 v7, 0x5000, v7\n"
    "v_mfma_f32_16x16x32_bf16 a[24:27], a[184:187], v[216:219], a[24:27]\n"
    "buffer_load_dwordx4 v27, s[60:63], s98 offen lds\n"
    "v_mfma_f32_16x16x32_bf16 a[28:31], a[184:187], v[220:223], a[28:31]\n"
    "v_mfma_f32_16x16x32_bf16 a[32:35], a[184:187], v[224:227], a[32:35]\n"
    "v_mfma_f32_16x16x32_bf16 a[36:39], a[184:187], v[228:231], a[36:39]\n"
    "v_mfma_f32_16x16x32_bf16 a[40:43], a[188:191], v[212:215], a[40:43]\n"
    "v_mfma_f32_16x16x32_bf16 a[44:47], a[188:191], v[216:219], a[44:47]\n"
    "v_mfma_f32_16x16x32_bf16 a[48:51], a[188:191], v[220:223], a[48:51]\n"
    "v_mfma_f32_16x16x32_bf16 a[52:55], a[188:191], v[224:227], a[52:55]\n"
    "v_mfma_f32_16x16x32_bf16 a[56:59], a[188:191], v[228:231], a[56:59]\n"
    "s_waitcnt vmcnt(15) lgkmcnt(0)\n"
    "s_barrier\n"
    "v_mfma_f32_16x16x32_bf16 a[60:63], a[144:147], v[212:215], a[60:63]\n"
    "ds_read_b64_tr_b16 a[152:153], v3\n"
    "ds_read_b64_tr_b16 a[154:155], v3 offset:5120\n"
    "v_mfma_f32_16x16x32_bf16 a[64:67], a[144:147], v[216:219], a[64:67]\n"
    "v_mfma_f32_16x16x32_bf16 a[68:71], a[144:147], v[220:223], a[68:71]\n"
    "v_mfma_f32_16x16x32_bf16 a[72:75], a[144:147], v[224:227], a[72:75]\n"
    "ds_read_b64_tr_b16 a[156:157], v5 offset:32\n"
    "ds_read_b64_tr_b16 a[158:159], v5 offset:5152\n"
    "v_mfma_f32_16x16x32_bf16 a[76:79], a[144:147], v[228:231], a[76:79]\n"
    "v_mfma_f32_16x16x32_bf16 a[80:83], a[148:151], v[212:215], a[80:83]\n"
    "v_mfma_f32_16x16x32_bf16 a[84:87], a[148:151], v[216:219], a[84:87]\n"
    "v_mfma_f32_16x16x32_bf16 a[88:91], a[148:151], v[220:223], a[88:91]\n"
    "v_mfma_f32_16x16x32_bf16 a[92:95], a[148:151], v[224:227], a[92:95]\n"
    "ds_read_b64_tr_b16 a[160:161], v3 offset:64\n"
    "ds_read_b64_tr_b16 a[162:163], v3 offset:5184\n"
    "v_mfma_f32_16x16x32_bf16 a[96:99], a[148:151], v[228:231], a[96:99]\n"
    "s_waitcnt lgkmcnt(2)\n"
    "v_mfma_f32_16x16x32_bf16 v[28:31], a[152:155], v[232:235], v[28:31]\n"
    "s_add_u32 s76, s78, 0\n"
    "v_mfma_f32_16x16x32_bf16 v[32:35], a[152:155], v[236:239], v[32:35]\n"
    "s_add_u32 s77, s78, 0x1000\n"
    "v_mfma_f32_16x16x32_bf16 v[36:39], a[152:155], v[240:243], v[36:39]\n"
    "buffer_load_dwordx4 v[148:151], v1, s[52:55], s76 offen sc1\n"
    "v_mfma_f32_16x16x32_bf16 v[40:43], a[152:155], v[244:247], v[40:43]\n"
    "ds_read_b64_tr_b16 a[164:165], v5 offset:96\n"
    "ds_read_b64_tr_b16 a[166:167], v5 offset:5216\n"
    "v_add_u32 v5, 0xfffde000, v5\n"
    "v_mfma_f32_16x16x32_bf16 v[44:47], a[152:155], v[248:251], v[44:47]\n"
    "v_mfma_f32_16x16x32_bf16 v[48:51], a[156:159], v[232:235], v[48:51]\n"
    "buffer_load_dwordx4 v[152:155], v1, s[52:55], s76 offen offset:1024 sc1\n"
    "v_mfma_f32_16x16x32_bf16 v[52:55], a[156:159], v[236:239], v[52:55]\n"
    "v_mfma_f32_16x16x32_bf16 v[56:59], a[156:159], v[240:243], v[56:59]\n"
    "v_mfma_f32_16x16x32_bf16 v[60:63], a[156:159], v[244:247], v[60:63]\n"
    "ds_read_b64_tr_b16 a[168:169], v3 offset:128\n"
    "ds_read_b64_tr_b16 a[170:171], v3 offset:5248\n"
    "v_add_u32 v3, 0xfffde000, v3\n"
    "v_mfma_f32_16x16x32_bf16 v[64:67], a[156:159], v[248:251], v[64:67]\n"
    "s_waitcnt lgkmcnt(2)\n"
    "v_mfma_f32_16x16x32_bf16 v[68:71], a[160:163], v[232:235], v[68:71]\n"
    "buffer_load_dwordx4 v[156:159], v1, s[52:55], s76 offen offset:2048 sc1\n"
    "v_mfma_f32_16x16x32_bf16 v[72:75], a[160:163], v[236:239], v[72:75]\n"
    "buffer_load_dwordx4 v[160:163], v1, s[52:55], s76 offen offset:3072 sc1\n"
    "v_mfma_f32_16x16x32_bf16 v[76:79], a[160:163], v[240:243], v[76:79]\n"
    "v_mfma_f32_16x16x32_bf16 v[80:83], a[160:163], v[244:247], v[80:83]\n"
    "ds_read_b64_tr_b16 a[172:173], v7\n"
    "ds_read_b64_tr_b16 a[174:175], v7 offset:5120\n"
    "v_mfma_f32_16x16x32_bf16 v[84:87], a[160:163], v[248:251], v[84:87]\n"
    "buffer_load_dwordx4 v[164:167], v1, s[52:55], s77 offen sc1\n"
    "v_mfma_f32_16x16x32_bf16 v[88:91], a[164:167], v[232:235], v[88:91]\n"
    "s_add_u32 m0, s85, 0xd000\n"
    "v_mfma_f32_16x16x32_bf16 v[92:95], a[164:167], v[236:239], v[92:95]\n"
    "s_add_u32 s98, s42, 0\n"
    "v_mfma_f32_16x16x32_bf16 v[96:99], a[164:167], v[240:243], v[96:99]\n"
    "buffer_load_dwordx4 v23, s[60:63], s98 offen lds\n"
    "v_mfma_f32_16x16x32_bf16 v[100:103], a[164:167], v[244:247], v[100:103]\n"
    "ds_read_b64_tr_b16 a[176:177], v6 offset:32\n"
    "ds_read_b64_tr_b16 a[178:179], v6 offset:5152\n"
    "v_mfma_f32_16x16x32_bf16 v[104:107], a[164:167], v[248:251], v[104:107]\n"
    "s_waitcnt lgkmcnt(2)\n"
    "v_mfma_f32_16x16x32_bf16 v[108:111], a[168:171], v[232:235], v[108:111]\n"
    "buffer_load_dwordx4 v24, s[60:63], s98 offen offset:1024 lds\n"
    "v_mfma_f32_16x16x32_bf16 v[112:115], a[168:171], v[236:239], v[112:115]\n"
    "v_mfma_f32_16x16x32_bf16 v[116:119], a[168:171], v[240:243], v[116:119]\n"
    "v_mfma_f32_16x16x32_bf16 v[120:123], a[168:171], v[244:247], v[120:123]\n"
    "ds_read_b64_tr_b16 a[180:181], v7 offset:64\n"
    "ds_read_b64_tr_b16 a[182:183], v7 offset:5184\n"
    "v_mfma_f32_16x16x32_bf16 v[124:127], a[168:171], v[248:251], v[124:127]\n"
    "buffer_load_dwordx4 v25, s[60:63], s98 offen offset:2048 lds\n"
    "v_mfma_f32_16x16x32_bf16 a[0:3], a[172:175], v[232:235], a[0:3]\n"
    "v_mfma_f32_16x16x32_bf16 a[4:7], a[172:175], v[236:239], a[4:7]\n"
    "buffer_load_dwordx4 v26, s[60:63], s98 offen offset:3072 lds\n"
    "v_mfma_f32_16x16x32_bf16 a[8:11], a[172:175], v[240:243], a[8:11]\n"
    "s_add_u32 m0, s85, 0xe000\n"
    "v_mfma_f32_16x16x32_bf16 a[12:15], a[172:175], v[244:247], a[12:15]\n"
    "ds_read_b64_tr_b16 a[184:185], v6 offset:96\n"
    "ds_read_b64_tr_b16 a[186:187], v6 offset:5216\n"
    "v_add_u32 v6, 0xfffde000, v6\n"
    "v_mfma_f32_16x16x32_bf16 a[16:19], a[172:175], v[248:251], a[16:19]\n"
    "s_waitcnt lgkmcnt(2)\n"
    "v_mfma_f32_16x16x32_bf16 a[20:23], a[176:179], v[232:235], a[20:23]\n"
    "ds_read_b64_tr_b16 a[188:189], v7 offset:128\n"
    "ds_read_b64_tr_b16 a[190:191], v7 offset:5248\n"
    "v_add_u32 v7, 0xfffde000, v7\n"
    "v_mfma_f32_16x16x32_bf16 a[24:27], a[176:179], v[236:239], a[24:27]\n"
    "buffer_load_dwordx4 v27, s[60:63], s98 offen lds\n"
    "v_mfma_f32_16x16x32_bf16 a[28:31], a[176:179], v[240:243], a[28:31]\n"
    "v_mfma_f32_16x16x32_bf16 a[32:35], a[176:179], v[244:247], a[32:35]\n"
    "v_mfma_f32_16x16x32_bf16 a[36:39], a[176:179], v[248:251], a[36:39]\n"
    "v_mfma_f32_16x16x32_bf16 a[40:43], a[180:183], v[232:235], a[40:43]\n"
    "v_mfma_f32_16x16x32_bf16 a[44:47], a[180:183], v[236:239], a[44:47]\n"
    "v_mfma_f32_16x16x32_bf16 a[48:51], a[180:183], v[240:243], a[48:51]\n"
    "v_mfma_f32_16x16x32_bf16 a[52:55], a[180:183], v[244:247], a[52:55]\n"
    "v_mfma_f32_16x16x32_bf16 a[56:59], a[180:183], v[248:251], a[56:59]\n"
    "s_waitcnt vmcnt(20) lgkmcnt(0)\n"
    "s_barrier\n"
    "v_mfma_f32_16x16x32_bf16 a[60:63], a[184:187], v[232:235], a[60:63]\n"
    "ds_read_b64_tr_b16 a[144:145], v3\n"
    "ds_read_b64_tr_b16 a[146:147], v3 offset:5120\n"
    "v_mfma_f32_16x16x32_bf16 a[64:67], a[184:187], v[236:239], a[64:67]\n"
    "v_mfma_f32_16x16x32_bf16 a[68:71], a[184:187], v[240:243], a[68:71]\n"
    "v_mfma_f32_16x16x32_bf16 a[72:75], a[184:187], v[244:247], a[72:75]\n"
    "ds_read_b64_tr_b16 a[148:149], v5 offset:32\n"
    "ds_read_b64_tr_b16 a[150:151], v5 offset:5152\n"
    "v_mfma_f32_16x16x32_bf16 a[76:79], a[184:187], v[248:251], a[76:79]\n"
    "v_mfma_f32_16x16x32_bf16 a[80:83], a[188:191], v[232:235], a[80:83]\n"
    "v_mfma_f32_16x16x32_bf16 a[84:87], a[188:191], v[236:239], a[84:87]\n"
    "v_mfma_f32_16x16x32_bf16 a[88:91], a[188:191], v[240:243], a[88:91]\n"
    "v_mfma_f32_16x16x32_bf16 a[92:95], a[188:191], v[244:247], a[92:95]\n"
    "ds_read_b64_tr_b16 a[152:153], v3 offset:64\n"
    "ds_read_b64_tr_b16 a[154:155], v3 offset:5184\n"
    "v_mfma_f32_16x16x32_bf16 a[96:99], a[188:191], v[248:251], a[96:99]\n"
    "s_waitcnt lgkmcnt(2)\n"
    "v_mfma_f32_16x16x32_bf16 v[28:31], a[144:147], v[128:131], v[28:31]\n"
    "s_add_u32 s76, s78, 0x2800\n"
    "v_mfma_f32_16x16x32_bf16 v[32:35], a[144:147], v[132:135], v[32:35]\n"
    "s_add_u32 s77, s78, 0x3800\n"
    "v_mfma_f32_16x16x32_bf16 v[36:39], a[144:147], v[136:139], v[36:39]\n"
    "buffer_load_dwordx4 v[192:195], v1, s[52:55], s76 offen sc1\n"
    "v_mfma_f32_16x16x32_bf16 v[40:43], a[144:147], v[140:143], v[40:43]\n"
    "s_lshr_b32 s74, s90, 2\n"
    "ds_read_b64_tr_b16 a[156:157], v5 offset:96\n"
    "ds_read_b64_tr_b16 a[158:159], v5 offset:5216\n"
    "v_add_u32 v5, 0x8000, v5\n"
    "v_mfma_f32_16x16x32_bf16 v[44:47], a[144:147], v[144:147], v[44:47]\n"
    "s_lshl_b32 s74, s74, 16\n"
    "v_mfma_f32_16x16x32_bf16 v[48:51], a[148:151], v[128:131], v[48:51]\n"
    "buffer_load_dwordx4 v[196:199], v1, s[52:55], s76 offen offset:1024 sc1\n"
    "v_mfma_f32_16x16x32_bf16 v[52:55], a[148:151], v[132:135], v[52:55]\n"
    "s_lshl_b32 s81, s72, 14\n"
    "v_mfma_f32_16x16x32_bf16 v[56:59], a[148:151], v[136:139], v[56:59]\n"
    "s_add_u32 s48, s74, s81\n"
    "v_mfma_f32_16x16x32_bf16 v[60:63], a[148:151], v[140:143], v[60:63]\n"
    "ds_read_b64_tr_b16 a[160:161], v3 offset:128\n"
    "ds_read_b64_tr_b16 a[162:163], v3 offset:5248\n"
    "v_add_u32 v3, 0x8000, v3\n"
    "v_mfma_f32_16x16x32_bf16 v[64:67], a[148:151], v[144:147], v[64:67]\n"
    "s_waitcnt lgkmcnt(2)\n"
    "v_mfma_f32_16x16x32_bf16 v[68:71], a[152:155], v[128:131], v[68:71]\n"
    "buffer_load_dwordx4 v[200:203], v1, s[52:55], s76 offen offset:2048 sc1\n"
    "v_mfma_f32_16x16x32_bf16 v[72:75], a[152:155], v[132:135], v[72:75]\n"
    "buffer_load_dwordx4 v[204:207], v1, s[52:55], s76 offen offset:3072 sc1\n"
    "v_mfma_f32_16x16x32_bf16 v[76:79], a[152:155], v[136:139], v[76:79]\n"
    "s_add_u32 s49, s48, 0x1000\n"
    "v_mfma_f32_16x16x32_bf16 v[80:83], a[152:155], v[140:143], v[80:83]\n"
    "s_add_u32 s50, s48, 0x2000\n"
    "ds_read_b64_tr_b16 a[164:165], v7\n"
    "ds_read_b64_tr_b16 a[166:167], v7 offset:5120\n"
    "v_mfma_f32_16x16x32_bf16 v[84:87], a[152:155], v[144:147], v[84:87]\n"
    "buffer_load_dwordx4 v[208:211], v1, s[52:55], s77 offen sc1\n"
    "v_mfma_f32_16x16x32_bf16 v[88:91], a[156:159], v[128:131], v[88:91]\n"
    "s_add_u32 m0, s85, 0x12000\n"
    "v_mfma_f32_16x16x32_bf16 v[92:95], a[156:159], v[132:135], v[92:95]\n"
    "s_add_u32 s98, s42, 0xa000\n"
    "v_mfma_f32_16x16x32_bf16 v[96:99], a[156:159], v[136:139], v[96:99]\n"
    "buffer_load_dwordx4 v23, s[60:63], s98 offen lds\n"
    "v_mfma_f32_16x16x32_bf16 v[100:103], a[156:159], v[140:143], v[100:103]\n"
    "s_add_u32 s51, s48, 0x3000\n"
    "ds_read_b64_tr_b16 a[168:169], v6 offset:32\n"
    "ds_read_b64_tr_b16 a[170:171], v6 offset:5152\n"
    "v_mfma_f32_16x16x32_bf16 v[104:107], a[156:159], v[144:147], v[104:107]\n"
    "s_waitcnt lgkmcnt(2)\n"
    "v_mfma_f32_16x16x32_bf16 v[108:111], a[160:163], v[128:131], v[108:111]\n"
    "buffer_load_dwordx4 v24, s[60:63], s98 offen offset:1024 lds\n"
    "v_mfma_f32_16x16x32_bf16 v[112:115], a[160:163], v[132:135], v[112:115]\n"
    "s_lshr_b32 s74, s90, 2\n"
    "v_mfma_f32_16x16x32_bf16 v[116:119], a[160:163], v[136:139], v[116:119]\n"
    "s_cmp_eq_u32 s74, 0\n"
    "s_cselect_b32 s48, 0x40000, s48\n"
    "v_mfma_f32_16x16x32_bf16 v[120:123], a[160:163], v[140:143], v[120:123]\n"
    "ds_read_b64_tr_b16 a[172:173], v7 offset:64\n"
    "ds_read_b64_tr_b16 a[174:175], v7 offset:5184\n"
    "v_mfma_f32_16x16x32_bf16 v[124:127], a[160:163], v[144:147], v[124:127]\n"
    "buffer_load_dwordx4 v25, s[60:63], s98 offen offset:2048 lds\n"
    "v_mfma_f32_16x16x32_bf16 a[0:3], a[164:167], v[128:131], a[0:3]\n"
    "s_cmp_eq_u32 s74, 1\n"
    "s_cselect_b32 s49, 0x40000, s49\n"
    "v_mfma_f32_16x16x32_bf16 a[4:7], a[164:167], v[132:135], a[4:7]\n"
    "buffer_load_dwordx4 v26, s[60:63], s98 offen offset:3072 lds\n"
    "v_mfma_f32_16x16x32_bf16 a[8:11], a[164:167], v[136:139], a[8:11]\n"
    "s_add_u32 m0, s85, 0x13000\n"
    "v_mfma_f32_16x16x32_bf16 a[12:15], a[164:167], v[140:143], a[12:15]\n"
    "s_cmp_eq_u32 s74, 2\n"
    "s_cselect_b32 s50, 0x40000, s50\n"
    "ds_read_b64_tr_b16 a[176:177], v6 offset:96\n"
    "v_mfma_f32_16x16x32_bf16 a[16:19], a[164:167], v[144:147], a[16:19]\n"
    "s_waitcnt lgkmcnt(1)\n"
    "ds_read_b64_tr_b16 a[178:179], v6 offset:5216\n"
    "ds_read_b64_tr_b16 a[180:181], v7 offset:128\n"
    "v_add_u32 v6, 0x8000, v6\n"
    "v_mfma_f32_16x16x32_bf16 a[20:23], a[168:171], v[128:131], a[20:23]\n"
    "ds_read_b64_tr_b16 a[182:183], v7 offset:5248\n"
    "v_add_u32 v7, 0x8000, v7\n"
    "v_mfma_f32_16x16x32_bf16 a[24:27], a[168:171], v[132:135], a[24:27]\n"
    "buffer_load_dwordx4 v27, s[60:63], s98 offen lds\n"
    "v_mfma_f32_16x16x32_bf16 a[28:31], a[168:171], v[136:139], a[28:31]\n"
    "s_cmp_eq_u32 s74, 3\n"
    "s_cselect_b32 s51, 0x40000, s51\n"
    "v_mfma_f32_16x16x32_bf16 a[32:35], a[168:171], v[140:143], a[32:35]\n"
    "v_mfma_f32_16x16x32_bf16 a[36:39], a[168:171], v[144:147], a[36:39]\n"
    "v_mfma_f32_16x16x32_bf16 a[40:43], a[172:175], v[128:131], a[40:43]\n"
    "v_mfma_f32_16x16x32_bf16 a[44:47], a[172:175], v[132:135], a[44:47]\n"
    "v_mfma_f32_16x16x32_bf16 a[48:51], a[172:175], v[136:139], a[48:51]\n"
    "v_mfma_f32_16x16x32_bf16 a[52:55], a[172:175], v[140:143], a[52:55]\n"
    "v_mfma_f32_16x16x32_bf16 a[56:59], a[172:175], v[144:147], a[56:59]\n"
    "s_waitcnt vmcnt(20) lgkmcnt(0)\n"
    "s_barrier\n"
    "v_mfma_f32_16x16x32_bf16 a[60:63], a[176:179], v[128:131], a[60:63]\n"
    "ds_read_b64_tr_b16 a[184:185], v3\n"
    "ds_read_b64_tr_b16 a[186:187], v3 offset:5120\n"
    "v_mfma_f32_16x16x32_bf16 a[64:67], a[176:179], v[132:135], a[64:67]\n"
    "v_mfma_f32_16x16x32_bf16 a[68:71], a[176:179], v[136:139], a[68:71]\n"
    "v_mfma_f32_16x16x32_bf16 a[72:75], a[176:179], v[140:143], a[72:75]\n"
    "ds_read_b64_tr_b16 a[188:189], v5 offset:32\n"
    "ds_read_b64_tr_b16 a[190:191], v5 offset:5152\n"
    "v_mfma_f32_16x16x32_bf16 a[76:79], a[176:179], v[144:147], a[76:79]\n"
    "v_mfma_f32_16x16x32_bf16 a[80:83], a[180:183], v[128:131], a[80:83]\n"
    "v_mfma_f32_16x16x32_bf16 a[84:87], a[180:183], v[132:135], a[84:87]\n"
    "v_mfma_f32_16x16x32_bf16 a[88:91], a[180:183], v[136:139], a[88:91]\n"
    "v_mfma_f32_16x16x32_bf16 a[92:95], a[180:183], v[140:143], a[92:95]\n"
    "ds_read_b64_tr_b16 a[144:145], v3 offset:64\n"
    "ds_read_b64_tr_b16 a[146:147], v3 offset:5184\n"
    "v_mfma_f32_16x16x32_bf16 a[96:99], a[180:183], v[144:147], a[96:99]\n"
    "s_waitcnt lgkmcnt(2)\n"
    "v_mfma_f32_16x16x32_bf16 v[28:31], a[184:187], v[168:171], v[28:31]\n"
    "s_add_u32 s76, s78, 0x5000\n"
    "v_mfma_f32_16x16x32_bf16 v[32:35], a[184:187], v[172:175], v[32:35]\n"
    "s_add_u32 s77, s78, 0x6000\n"
    "v_mfma_f32_16x16x32_bf16 v[36:39], a[184:187], v[176:179], v[36:39]\n"
    "buffer_load_dwordx4 v[212:215], v1, s[52:55], s76 offen sc1\n"
    "v_mfma_f32_16x16x32_bf16 v[40:43], a[184:187], v[180:183], v[40:43]\n"
    "ds_read_b64_tr_b16 a[148:149], v5 offset:96\n"
    "ds_read_b64_tr_b16 a[150:151], v5 offset:5216\n"
    "v_add_u32 v5, 0x5000, v5\n"
    "v_mfma_f32_16x16x32_bf16 v[44:47], a[184:187], v[184:187], v[44:47]\n"
    "v_mfma_f32_16x16x32_bf16 v[48:51], a[188:191], v[168:171], v[48:51]\n"
    "buffer_load_dwordx4 v[216:219], v1, s[52:55], s76 offen offset:1024 sc1\n"
    "v_mfma_f32_16x16x32_bf16 v[52:55], a[188:191], v[172:175], v[52:55]\n"
    "v_mfma_f32_16x16x32_bf16 v[56:59], a[188:191], v[176:179], v[56:59]\n"
    "v_mfma_f32_16x16x32_bf16 v[60:63], a[188:191], v[180:183], v[60:63]\n"
    "ds_read_b64_tr_b16 a[152:153], v3 offset:128\n"
    "ds_read_b64_tr_b16 a[154:155], v3 offset:5248\n"
    "v_add_u32 v3, 0x5000, v3\n"
    "v_mfma_f32_16x16x32_bf16 v[64:67], a[188:191], v[184:187], v[64:67]\n"
    "s_waitcnt lgkmcnt(2)\n"
    "v_mfma_f32_16x16x32_bf16 v[68:71], a[144:147], v[168:171], v[68:71]\n"
    "buffer_load_dwordx4 v[220:223], v1, s[52:55], s76 offen offset:2048 sc1\n"
    "v_mfma_f32_16x16x32_bf16 v[72:75], a[144:147], v[172:175], v[72:75]\n"
    "buffer_load_dwordx4 v[224:227], v1, s[52:55], s76 offen offset:3072 sc1\n"
    "v_mfma_f32_16x16x32_bf16 v[76:79], a[144:147], v[176:179], v[76:79]\n"
    "v_mfma_f32_16x16x32_bf16 v[80:83], a[144:147], v[180:183], v[80:83]\n"
    "ds_read_b64_tr_b16 a[156:157], v7\n"
    "ds_read_b64_tr_b16 a[158:159], v7 offset:5120\n"
    "v_mfma_f32_16x16x32_bf16 v[84:87], a[144:147], v[184:187], v[84:87]\n"
    "buffer_load_dwordx4 v[228:231], v1, s[52:55], s77 offen sc1\n"
    "v_mfma_f32_16x16x32_bf16 v[88:91], a[148:151], v[168:171], v[88:91]\n"
    "s_add_u32 m0, s85, 0x17000\n"
    "v_mfma_f32_16x16x32_bf16 v[92:95], a[148:151], v[172:175], v[92:95]\n"
    "s_add_u32 s98, s42, 0x14000\n"
    "v_mfma_f32_16x16x32_bf16 v[96:99], a[148:151], v[176:179], v[96:99]\n"
    "buffer_load_dwordx4 v23, s[60:63], s98 offen lds\n"
    "v_mfma_f32_16x16x32_bf16 v[100:103], a[148:151], v[180:183], v[100:103]\n"
    "ds_read_b64_tr_b16 a[160:161], v6 offset:32\n"
    "ds_read_b64_tr_b16 a[162:163], v6 offset:5152\n"
    "v_mfma_f32_16x16x32_bf16 v[104:107], a[148:151], v[184:187], v[104:107]\n"
    "s_waitcnt lgkmcnt(2)\n"
    "v_mfma_f32_16x16x32_bf16 v[108:111], a[152:155], v[168:171], v[108:111]\n"
    "buffer_load_dwordx4 v24, s[60:63], s98 offen offset:1024 lds\n"
    "v_mfma_f32_16x16x32_bf16 v[112:115], a[152:155], v[172:175], v[112:115]\n"
    "v_mfma_f32_16x16x32_bf16 v[116:119], a[152:155], v[176:179], v[116:119]\n"
    "v_mfma_f32_16x16x32_bf16 v[120:123], a[152:155], v[180:183], v[120:123]\n"
    "ds_read_b64_tr_b16 a[164:165], v7 offset:64\n"
    "ds_read_b64_tr_b16 a[166:167], v7 offset:5184\n"
    "v_mfma_f32_16x16x32_bf16 v[124:127], a[152:155], v[184:187], v[124:127]\n"
    "buffer_load_dwordx4 v25, s[60:63], s98 offen offset:2048 lds\n"
    "v_mfma_f32_16x16x32_bf16 a[0:3], a[156:159], v[168:171], a[0:3]\n"
    "v_mfma_f32_16x16x32_bf16 a[4:7], a[156:159], v[172:175], a[4:7]\n"
    "buffer_load_dwordx4 v26, s[60:63], s98 offen offset:3072 lds\n"
    "v_mfma_f32_16x16x32_bf16 a[8:11], a[156:159], v[176:179], a[8:11]\n"
    "s_add_u32 m0, s85, 0x18000\n"
    "v_mfma_f32_16x16x32_bf16 a[12:15], a[156:159], v[180:183], a[12:15]\n"
    "ds_read_b64_tr_b16 a[168:169], v6 offset:96\n"
    "ds_read_b64_tr_b16 a[170:171], v6 offset:5216\n"
    "v_add_u32 v6, 0x5000, v6\n"
    "v_mfma_f32_16x16x32_bf16 a[16:19], a[156:159], v[184:187], a[16:19]\n"
    "s_waitcnt lgkmcnt(2)\n"
    "v_mfma_f32_16x16x32_bf16 a[20:23], a[160:163], v[168:171], a[20:23]\n"
    "ds_read_b64_tr_b16 a[172:173], v7 offset:128\n"
    "ds_read_b64_tr_b16 a[174:175], v7 offset:5248\n"
    "v_add_u32 v7, 0x5000, v7\n"
    "v_mfma_f32_16x16x32_bf16 a[24:27], a[160:163], v[172:175], a[24:27]\n"
    "buffer_load_dwordx4 v27, s[60:63], s98 offen lds\n"
    "v_mfma_f32_16x16x32_bf16 a[28:31], a[160:163], v[176:179], a[28:31]\n"
    "v_mfma_f32_16x16x32_bf16 a[32:35], a[160:163], v[180:183], a[32:35]\n"
    "v_mfma_f32_16x16x32_bf16 a[36:39], a[160:163], v[184:187], a[36:39]\n"
    "v_mfma_f32_16x16x32_bf16 a[40:43], a[164:167], v[168:171], a[40:43]\n"
    "v_mfma_f32_16x16x32_bf16 a[44:47], a[164:167], v[172:175], a[44:47]\n"
    "v_mfma_f32_16x16x32_bf16 a[48:51], a[164:167], v[176:179], a[48:51]\n"
    "v_mfma_f32_16x16x32_bf16 a[52:55], a[164:167], v[180:183], a[52:55]\n"
    "v_mfma_f32_16x16x32_bf16 a[56:59], a[164:167], v[184:187], a[56:59]\n"
    "s_waitcnt vmcnt(20) lgkmcnt(0)\n"
    "s_barrier\n"
    "v_mfma_f32_16x16x32_bf16 a[60:63], a[168:171], v[168:171], a[60:63]\n"
    "ds_read_b64_tr_b16 a[176:177], v3\n"
    "ds_read_b64_tr_b16 a[178:179], v3 offset:5120\n"
    "v_mfma_f32_16x16x32_bf16 a[64:67], a[168:171], v[172:175], a[64:67]\n"
    "v_mfma_f32_16x16x32_bf16 a[68:71], a[168:171], v[176:179], a[68:71]\n"
    "v_mfma_f32_16x16x32_bf16 a[72:75], a[168:171], v[180:183], a[72:75]\n"
    "ds_read_b64_tr_b16 a[180:181], v5 offset:32\n"
    "ds_read_b64_tr_b16 a[182:183], v5 offset:5152\n"
    "v_mfma_f32_16x16x32_bf16 a[76:79], a[168:171], v[184:187], a[76:79]\n"
    "v_mfma_f32_16x16x32_bf16 a[80:83], a[172:175], v[168:171], a[80:83]\n"
    "v_mfma_f32_16x16x32_bf16 a[84:87], a[172:175], v[172:175], a[84:87]\n"
    "v_mfma_f32_16x16x32_bf16 a[88:91], a[172:175], v[176:179], a[88:91]\n"
    "v_mfma_f32_16x16x32_bf16 a[92:95], a[172:175], v[180:183], a[92:95]\n"
    "ds_read_b64_tr_b16 a[184:185], v3 offset:64\n"
    "ds_read_b64_tr_b16 a[186:187], v3 offset:5184\n"
    "v_mfma_f32_16x16x32_bf16 a[96:99], a[172:175], v[184:187], a[96:99]\n"
    "s_waitcnt lgkmcnt(2)\n"
    "v_mfma_f32_16x16x32_bf16 v[28:31], a[176:179], v[148:151], v[28:31]\n"
    "s_add_u32 s76, s78, 0x7800\n"
    "v_mfma_f32_16x16x32_bf16 v[32:35], a[176:179], v[152:155], v[32:35]\n"
    "s_add_u32 s77, s78, 0x8800\n"
    "v_mfma_f32_16x16x32_bf16 v[36:39], a[176:179], v[156:159], v[36:39]\n"
    "buffer_load_dwordx4 v[232:235], v1, s[52:55], s76 offen sc1\n"
    "v_mfma_f32_16x16x32_bf16 v[40:43], a[176:179], v[160:163], v[40:43]\n"
    "ds_read_b64_tr_b16 a[188:189], v5 offset:96\n"
    "ds_read_b64_tr_b16 a[190:191], v5 offset:5216\n"
    "v_add_u32 v5, 0x5000, v5\n"
    "v_mfma_f32_16x16x32_bf16 v[44:47], a[176:179], v[164:167], v[44:47]\n"
    "v_mfma_f32_16x16x32_bf16 v[48:51], a[180:183], v[148:151], v[48:51]\n"
    "buffer_load_dwordx4 v[236:239], v1, s[52:55], s76 offen offset:1024 sc1\n"
    "v_mfma_f32_16x16x32_bf16 v[52:55], a[180:183], v[152:155], v[52:55]\n"
    "v_mfma_f32_16x16x32_bf16 v[56:59], a[180:183], v[156:159], v[56:59]\n"
    "v_mfma_f32_16x16x32_bf16 v[60:63], a[180:183], v[160:163], v[60:63]\n"
    "ds_read_b64_tr_b16 a[144:145], v3 offset:128\n"
    "ds_read_b64_tr_b16 a[146:147], v3 offset:5248\n"
    "v_add_u32 v3, 0x5000, v3\n"
    "v_mfma_f32_16x16x32_bf16 v[64:67], a[180:183], v[164:167], v[64:67]\n"
    "s_waitcnt lgkmcnt(2)\n"
    "v_mfma_f32_16x16x32_bf16 v[68:71], a[184:187], v[148:151], v[68:71]\n"
    "buffer_load_dwordx4 v[240:243], v1, s[52:55], s76 offen offset:2048 sc1\n"
    "v_mfma_f32_16x16x32_bf16 v[72:75], a[184:187], v[152:155], v[72:75]\n"
    "buffer_load_dwordx4 v[244:247], v1, s[52:55], s76 offen offset:3072 sc1\n"
    "v_mfma_f32_16x16x32_bf16 v[76:79], a[184:187], v[156:159], v[76:79]\n"
    "v_mfma_f32_16x16x32_bf16 v[80:83], a[184:187], v[160:163], v[80:83]\n"
    "ds_read_b64_tr_b16 a[148:149], v7\n"
    "ds_read_b64_tr_b16 a[150:151], v7 offset:5120\n"
    "v_mfma_f32_16x16x32_bf16 v[84:87], a[184:187], v[164:167], v[84:87]\n"
    "buffer_load_dwordx4 v[248:251], v1, s[52:55], s77 offen sc1\n"
    "v_mfma_f32_16x16x32_bf16 v[88:91], a[188:191], v[148:151], v[88:91]\n"
    "s_add_u32 m0, s85, 0x1d000\n"
    "v_mfma_f32_16x16x32_bf16 v[92:95], a[188:191], v[152:155], v[92:95]\n"
    "s_add_u32 s98, s42, 0x1e000\n"
    "v_mfma_f32_16x16x32_bf16 v[96:99], a[188:191], v[156:159], v[96:99]\n"
    "buffer_load_dwordx4 v23, s[60:63], s98 offen lds\n"
    "v_mfma_f32_16x16x32_bf16 v[100:103], a[188:191], v[160:163], v[100:103]\n"
    "ds_read_b64_tr_b16 a[152:153], v6 offset:32\n"
    "ds_read_b64_tr_b16 a[154:155], v6 offset:5152\n"
    "v_mfma_f32_16x16x32_bf16 v[104:107], a[188:191], v[164:167], v[104:107]\n"
    "s_waitcnt lgkmcnt(2)\n"
    "v_mfma_f32_16x16x32_bf16 v[108:111], a[144:147], v[148:151], v[108:111]\n"
    "buffer_load_dwordx4 v24, s[60:63], s98 offen offset:1024 lds\n"
    "v_mfma_f32_16x16x32_bf16 v[112:115], a[144:147], v[152:155], v[112:115]\n"
    "v_mfma_f32_16x16x32_bf16 v[116:119], a[144:147], v[156:159], v[116:119]\n"
    "v_mfma_f32_16x16x32_bf16 v[120:123], a[144:147], v[160:163], v[120:123]\n"
    "ds_read_b64_tr_b16 a[156:157], v7 offset:64\n"
    "ds_read_b64_tr_b16 a[158:159], v7 offset:5184\n"
    "v_mfma_f32_16x16x32_bf16 v[124:127], a[144:147], v[164:167], v[124:127]\n"
    "buffer_load_dwordx4 v25, s[60:63], s98 offen offset:2048 lds\n"
    "v_mfma_f32_16x16x32_bf16 a[0:3], a[148:151], v[148:151], a[0:3]\n"
    "v_mfma_f32_16x16x32_bf16 a[4:7], a[148:151], v[152:155], a[4:7]\n"
    "buffer_load_dwordx4 v26, s[60:63], s98 offen offset:3072 lds\n"
    "v_mfma_f32_16x16x32_bf16 a[8:11], a[148:151], v[156:159], a[8:11]\n"
    "s_add_u32 m0, s85, 0x1e000\n"
    "v_mfma_f32_16x16x32_bf16 a[12:15], a[148:151], v[160:163], a[12:15]\n"
    "ds_read_b64_tr_b16 a[160:161], v6 offset:96\n"
    "ds_read_b64_tr_b16 a[162:163], v6 offset:5216\n"
    "v_add_u32 v6, 0x5000, v6\n"
    "v_mfma_f32_16x16x32_bf16 a[16:19], a[148:151], v[164:167], a[16:19]\n"
    "s_waitcnt lgkmcnt(2)\n"
    "v_mfma_f32_16x16x32_bf16 a[20:23], a[152:155], v[148:151], a[20:23]\n"
    "ds_read_b64_tr_b16 a[164:165], v7 offset:128\n"
    "ds_read_b64_tr_b16 a[166:167], v7 offset:5248\n"
    "v_add_u32 v7, 0x5000, v7\n"
    "v_mfma_f32_16x16x32_bf16 a[24:27], a[152:155], v[152:155], a[24:27]\n"
    "buffer_load_dwordx4 v27, s[60:63], s98 offen lds\n"
    "v_mfma_f32_16x16x32_bf16 a[28:31], a[152:155], v[156:159], a[28:31]\n"
    "v_mfma_f32_16x16x32_bf16 a[32:35], a[152:155], v[160:163], a[32:35]\n"
    "v_mfma_f32_16x16x32_bf16 a[36:39], a[152:155], v[164:167], a[36:39]\n"
    "v_mfma_f32_16x16x32_bf16 a[40:43], a[156:159], v[148:151], a[40:43]\n"
    "v_mfma_f32_16x16x32_bf16 a[44:47], a[156:159], v[152:155], a[44:47]\n"
    "v_mfma_f32_16x16x32_bf16 a[48:51], a[156:159], v[156:159], a[48:51]\n"
    "v_mfma_f32_16x16x32_bf16 a[52:55], a[156:159], v[160:163], a[52:55]\n"
    "v_mfma_f32_16x16x32_bf16 a[56:59], a[156:159], v[164:167], a[56:59]\n"
    "s_waitcnt vmcnt(20) lgkmcnt(0)\n"
    "s_barrier\n"
    "v_mfma_f32_16x16x32_bf16 a[60:63], a[160:163], v[148:151], a[60:63]\n"
    "ds_read_b64_tr_b16 a[168:169], v3\n"
    "ds_read_b64_tr_b16 a[170:171], v3 offset:5120\n"
    "v_mfma_f32_16x16x32_bf16 a[64:67], a[160:163], v[152:155], a[64:67]\n"
    "v_mfma_f32_16x16x32_bf16 a[68:71], a[160:163], v[156:159], a[68:71]\n"
    "v_mfma_f32_16x16x32_bf16 a[72:75], a[160:163], v[160:163], a[72:75]\n"
    "ds_read_b64_tr_b16 a[172:173], v5 offset:32\n"
    "ds_read_b64_tr_b16 a[174:175], v5 offset:5152\n"
    "v_mfma_f32_16x16x32_bf16 a[76:79], a[160:163], v[164:167], a[76:79]\n"
    "v_mfma_f32_16x16x32_bf16 a[80:83], a[164:167], v[148:151], a[80:83]\n"
    "v_mfma_f32_16x16x32_bf16 a[84:87], a[164:167], v[152:155], a[84:87]\n"
    "v_mfma_f32_16x16x32_bf16 a[88:91], a[164:167], v[156:159], a[88:91]\n"
    "v_mfma_f32_16x16x32_bf16 a[92:95], a[164:167], v[160:163], a[92:95]\n"
    "ds_read_b64_tr_b16 a[176:177], v3 offset:64\n"
    "ds_read_b64_tr_b16 a[178:179], v3 offset:5184\n"
    "v_mfma_f32_16x16x32_bf16 a[96:99], a[164:167], v[164:167], a[96:99]\n"
    "s_waitcnt lgkmcnt(2)\n"
    "v_mfma_f32_16x16x32_bf16 v[28:31], a[168:171], v[192:195], v[28:31]\n"
    "s_add_u32 s76, s79, 0\n"
    "v_mfma_f32_16x16x32_bf16 v[32:35], a[168:171], v[196:199], v[32:35]\n"
    "s_add_u32 s77, s79, 0x1000\n"
    "v_mfma_f32_16x16x32_bf16 v[36:39], a[168:171], v[200:203], v[36:39]\n"
    "buffer_load_dwordx4 v[128:131], v1, s[52:55], s76 offen sc1\n"
    "v_mfma_f32_16x16x32_bf16 v[40:43], a[168:171], v[204:207], v[40:43]\n"
    "ds_read_b64_tr_b16 a[180:181], v5 offset:96\n"
    "ds_read_b64_tr_b16 a[182:183], v5 offset:5216\n"
    "v_add_u32 v5, 0x5000, v5\n"
    "v_mfma_f32_16x16x32_bf16 v[44:47], a[168:171], v[208:211], v[44:47]\n"
    "v_mfma_f32_16x16x32_bf16 v[48:51], a[172:175], v[192:195], v[48:51]\n"
    "buffer_load_dwordx4 v[132:135], v1, s[52:55], s76 offen offset:1024 sc1\n"
    "v_mfma_f32_16x16x32_bf16 v[52:55], a[172:175], v[196:199], v[52:55]\n"
    "v_mfma_f32_16x16x32_bf16 v[56:59], a[172:175], v[200:203], v[56:59]\n"
    "v_mfma_f32_16x16x32_bf16 v[60:63], a[172:175], v[204:207], v[60:63]\n"
    "ds_read_b64_tr_b16 a[184:185], v3 offset:128\n"
    "ds_read_b64_tr_b16 a[186:187], v3 offset:5248\n"
    "v_add_u32 v3, 0x5000, v3\n"
    "v_mfma_f32_16x16x32_bf16 v[64:67], a[172:175], v[208:211], v[64:67]\n"
    "s_waitcnt lgkmcnt(2)\n"
    "v_mfma_f32_16x16x32_bf16 v[68:71], a[176:179], v[192:195], v[68:71]\n"
    "buffer_load_dwordx4 v[136:139], v1, s[52:55], s76 offen offset:2048 sc1\n"
    "v_mfma_f32_16x16x32_bf16 v[72:75], a[176:179], v[196:199], v[72:75]\n"
    "buffer_load_dwordx4 v[140:143], v1, s[52:55], s76 offen offset:3072 sc1\n"
    "v_mfma_f32_16x16x32_bf16 v[76:79], a[176:179], v[200:203], v[76:79]\n"
    "v_mfma_f32_16x16x32_bf16 v[80:83], a[176:179], v[204:207], v[80:83]\n"
    "ds_read_b64_tr_b16 a[188:189], v7\n"
    "ds_read_b64_tr_b16 a[190:191], v7 offset:5120\n"
    "v_mfma_f32_16x16x32_bf16 v[84:87], a[176:179], v[208:211], v[84:87]\n"
    "buffer_load_dwordx4 v[144:147], v1, s[52:55], s77 offen sc1\n"
    "v_mfma_f32_16x16x32_bf16 v[88:91], a[180:183], v[192:195], v[88:91]\n"
    "s_add_u32 m0, s85, 0x22000\n"
    "v_mfma_f32_16x16x32_bf16 v[92:95], a[180:183], v[196:199], v[92:95]\n"
    "s_add_u32 s98, s43, 0\n"
    "v_mfma_f32_16x16x32_bf16 v[96:99], a[180:183], v[200:203], v[96:99]\n"
    "buffer_load_dwordx4 v23, s[60:63], s98 offen lds\n"
    "v_mfma_f32_16x16x32_bf16 v[100:103], a[180:183], v[204:207], v[100:103]\n"
    "ds_read_b64_tr_b16 a[144:145], v6 offset:32\n"
    "ds_read_b64_tr_b16 a[146:147], v6 offset:5152\n"
    "v_mfma_f32_16x16x32_bf16 v[104:107], a[180:183], v[208:211], v[104:107]\n"
    "s_waitcnt lgkmcnt(2)\n"
    "v_mfma_f32_16x16x32_bf16 v[108:111], a[184:187], v[192:195], v[108:111]\n"
    "buffer_load_dwordx4 v24, s[60:63], s98 offen offset:1024 lds\n"
    "s_xor_b32 s74, s72, 0\n"
    "s_mul_i32 s75, s74, 0x6400\n"
    "s_add_u32 s75, s75, 0x12000\n"
    "v_add_u32 v4, s75, v1\n"
    "s_add_u32 s75, s75, 0x3400\n"
    "s_cmp_eq_u32 s74, 3\n"
    "s_cselect_b32 s75, 20480, s75\n"
    "v_add_u32 v8, s75, v1\n"
    "s_xor_b32 s74, s72, 2\n"
    "s_mul_i32 s75, s74, 0x6400\n"
    "s_add_u32 s75, s75, 0x12000\n"
    "v_add_u32 v10, s75, v1\n"
    "s_add_u32 s75, s75, 0x3400\n"
    "s_cmp_eq_u32 s74, 3\n"
    "s_cselect_b32 s75, 20480, s75\n"
    "v_add_u32 v12, s75, v1\n"
    "v_and_b32 v9, 15, v0\n"
    "v_lshrrev_b32 v11, 4, v0\n"
    "v_mul_u32_u24 v2, 0x500, v9\n"
    "v_lshl_add_u32 v2, v11, 3, v2\n"
    "v_mov_b32 v13, 0xffff0000\n"
    "v_mfma_f32_16x16x32_bf16 v[112:115], a[184:187], v[196:199], v[112:115]\n"
    "v_mfma_f32_16x16x32_bf16 v[116:119], a[184:187], v[200:203], v[116:119]\n"
    "v_mfma_f32_16x16x32_bf16 v[120:123], a[184:187], v[204:207], v[120:123]\n"
    "ds_read_b64_tr_b16 a[148:149], v7 offset:64\n"
    "ds_read_b64_tr_b16 a[150:151], v7 offset:5184\n"
    "v_mfma_f32_16x16x32_bf16 v[124:127], a[184:187], v[208:211], v[124:127]\n"
    "buffer_load_dwordx4 v25, s[60:63], s98 offen offset:2048 lds\n"
    "v_mfma_f32_16x16x32_bf16 a[0:3], a[188:191], v[192:195], a[0:3]\n"
    "v_mfma_f32_16x16x32_bf16 a[4:7], a[188:191], v[196:199], a[4:7]\n"
    "buffer_load_dwordx4 v26, s[60:63], s98 offen offset:3072 lds\n"
    "v_mfma_f32_16x16x32_bf16 a[8:11], a[188:191], v[200:203], a[8:11]\n"
    "s_add_u32 m0, s85, 0x23000\n"
    "v_mfma_f32_16x16x32_bf16 a[12:15], a[188:191], v[204:207], a[12:15]\n"
    "ds_read_b64_tr_b16 a[152:153], v6 offset:96\n"
    "ds_read_b64_tr_b16 a[154:155], v6 offset:5216\n"
    "v_add_u32 v6, 0x5000, v6\n"
    "v_mfma_f32_16x16x32_bf16 a[16:19], a[188:191], v[208:211], a[16:19]\n"
    "s_waitcnt lgkmcnt(2)\n"
    "v_mfma_f32_16x16x32_bf16 a[20:23], a[144:147], v[192:195], a[20:23]\n"
    "ds_read_b64_tr_b16 a[156:157], v7 offset:128\n"
    "ds_read_b64_tr_b16 a[158:159], v7 offset:5248\n"
    "v_add_u32 v7, 0x5000, v7\n"
    "v_mfma_f32_16x16x32_bf16 a[24:27], a[144:147], v[196:199], a[24:27]\n"
    "buffer_load_dwordx4 v27, s[60:63], s98 offen lds\n"
    "v_mfma_f32_16x16x32_bf16 a[28:31], a[144:147], v[200:203], a[28:31]\n"
    "v_mfma_f32_16x16x32_bf16 a[32:35], a[144:147], v[204:207], a[32:35]\n"
    "v_mfma_f32_16x16x32_bf16 a[36:39], a[144:147], v[208:211], a[36:39]\n"
    "v_mfma_f32_16x16x32_bf16 a[40:43], a[148:151], v[192:195], a[40:43]\n"
    "v_mfma_f32_16x16x32_bf16 a[44:47], a[148:151], v[196:199], a[44:47]\n"
    "v_mfma_f32_16x16x32_bf16 a[48:51], a[148:151], v[200:203], a[48:51]\n"
    "v_mfma_f32_16x16x32_bf16 a[52:55], a[148:151], v[204:207], a[52:55]\n"
    "v_mfma_f32_16x16x32_bf16 a[56:59], a[148:151], v[208:211], a[56:59]\n"
    "s_waitcnt vmcnt(20) lgkmcnt(0)\n"
    "s_barrier\n"
    "v_mfma_f32_16x16x32_bf16 a[60:63], a[152:155], v[192:195], a[60:63]\n"
    "ds_read_b64_tr_b16 a[160:161], v3\n"
    "ds_read_b64_tr_b16 a[162:163], v3 offset:5120\n"
    "v_mfma_f32_16x16x32_bf16 a[64:67], a[152:155], v[196:199], a[64:67]\n"
    "v_mfma_f32_16x16x32_bf16 a[68:71], a[152:155], v[200:203], a[68:71]\n"
    "v_mfma_f32_16x16x32_bf16 a[72:75], a[152:155], v[204:207], a[72:75]\n"
    "ds_read_b64_tr_b16 a[164:165], v5 offset:32\n"
    "ds_read_b64_tr_b16 a[166:167], v5 offset:5152\n"
    "v_mfma_f32_16x16x32_bf16 a[76:79], a[152:155], v[208:211], a[76:79]\n"
    "v_mfma_f32_16x16x32_bf16 a[80:83], a[156:159], v[192:195], a[80:83]\n"
    "v_mfma_f32_16x16x32_bf16 a[84:87], a[156:159], v[196:199], a[84:87]\n"
    "v_mfma_f32_16x16x32_bf16 a[88:91], a[156:159], v[200:203], a[88:91]\n"
    "v_mfma_f32_16x16x32_bf16 a[92:95], a[156:159], v[204:207], a[92:95]\n"
    "ds_read_b64_tr_b16 a[168:169], v3 offset:64\n"
    "ds_read_b64_tr_b16 a[170:171], v3 offset:5184\n"
    "v_mfma_f32_16x16x32_bf16 a[96:99], a[156:159], v[208:211], a[96:99]\n"
    "s_waitcnt lgkmcnt(2)\n"
    "v_mfma_f32_16x16x32_bf16 v[28:31], a[160:163], v[212:215], v[28:31]\n"
    "s_add_u32 s76, s79, 0x2800\n"
    "v_mfma_f32_16x16x32_bf16 v[32:35], a[160:163], v[216:219], v[32:35]\n"
    "s_add_u32 s77, s79, 0x3800\n"
    "v_mfma_f32_16x16x32_bf16 v[36:39], a[160:163], v[220:223], v[36:39]\n"
    "buffer_load_dwordx4 v[168:171], v1, s[52:55], s76 offen sc1\n"
    "v_mfma_f32_16x16x32_bf16 v[40:43], a[160:163], v[224:227], v[40:43]\n"
    "ds_read_b64_tr_b16 a[172:173], v5 offset:96\n"
    "ds_read_b64_tr_b16 a[174:175], v5 offset:5216\n"
    "v_add_u32 v5, 0x6000, v5\n"
    "v_mfma_f32_16x16x32_bf16 v[44:47], a[160:163], v[228:231], v[44:47]\n"
    "v_mfma_f32_16x16x32_bf16 v[48:51], a[164:167], v[212:215], v[48:51]\n"
    "buffer_load_dwordx4 v[172:175], v1, s[52:55], s76 offen offset:1024 sc1\n"
    "v_mfma_f32_16x16x32_bf16 v[52:55], a[164:167], v[216:219], v[52:55]\n"
    "v_mfma_f32_16x16x32_bf16 v[56:59], a[164:167], v[220:223], v[56:59]\n"
    "v_mfma_f32_16x16x32_bf16 v[60:63], a[164:167], v[224:227], v[60:63]\n"
    "ds_read_b64_tr_b16 a[176:177], v3 offset:128\n"
    "ds_read_b64_tr_b16 a[178:179], v3 offset:5248\n"
    "v_add_u32 v3, 0x6000, v3\n"
    "v_mfma_f32_16x16x32_bf16 v[64:67], a[164:167], v[228:231], v[64:67]\n"
    "s_waitcnt lgkmcnt(2)\n"
    "v_mfma_f32_16x16x32_bf16 v[68:71], a[168:171], v[212:215], v[68:71]\n"
    "buffer_load_dwordx4 v[176:179], v1, s[52:55], s76 offen offset:2048 sc1\n"
    "v_mfma_f32_16x16x32_bf16 v[72:75], a[168:171], v[216:219], v[72:75]\n"
    "buffer_load_dwordx4 v[180:183], v1, s[52:55], s76 offen offset:3072 sc1\n"
    "v_mfma_f32_16x16x32_bf16 v[76:79], a[168:171], v[220:223], v[76:79]\n"
    "v_mfma_f32_16x16x32_bf16 v[80:83], a[168:171], v[224:227], v[80:83]\n"
    "ds_read_b64_tr_b16 a[180:181], v7\n"
    "ds_read_b64_tr_b16 a[182:183], v7 offset:5120\n"
    "v_mfma_f32_16x16x32_bf16 v[84:87], a[168:171], v[228:231], v[84:87]\n"
    "buffer_load_dwordx4 v[184:187], v1, s[52:55], s77 offen sc1\n"
    "v_mfma_f32_16x16x32_bf16 v[88:91], a[172:175], v[212:215], v[88:91]\n"
    "s_add_u32 s76, s79, 0x5000\n"
    "v_mfma_f32_16x16x32_bf16 v[92:95], a[172:175], v[216:219], v[92:95]\n"
    "s_add_u32 s77, s79, 0x6000\n"
    "v_mfma_f32_16x16x32_bf16 v[96:99], a[172:175], v[220:223], v[96:99]\n"
    "buffer_load_dwordx4 v[148:151], v1, s[52:55], s76 offen sc1\n"
    "v_mfma_f32_16x16x32_bf16 v[100:103], a[172:175], v[224:227], v[100:103]\n"
    "ds_read_b64_tr_b16 a[184:185], v6 offset:32\n"
    "ds_read_b64_tr_b16 a[186:187], v6 offset:5152\n"
    "v_mfma_f32_16x16x32_bf16 v[104:107], a[172:175], v[228:231], v[104:107]\n"
    "s_waitcnt lgkmcnt(2)\n"
    "v_mfma_f32_16x16x32_bf16 v[108:111], a[176:179], v[212:215], v[108:111]\n"
    "buffer_load_dwordx4 v[152:155], v1, s[52:55], s76 offen offset:1024 sc1\n"
    "v_mfma_f32_16x16x32_bf16 v[112:115], a[176:179], v[216:219], v[112:115]\n"
    "v_mfma_f32_16x16x32_bf16 v[116:119], a[176:179], v[220:223], v[116:119]\n"
    "v_mfma_f32_16x16x32_bf16 v[120:123], a[176:179], v[224:227], v[120:123]\n"
    "ds_read_b64_tr_b16 a[188:189], v7 offset:64\n"
    "ds_read_b64_tr_b16 a[190:191], v7 offset:5184\n"
    "v_mfma_f32_16x16x32_bf16 v[124:127], a[176:179], v[228:231], v[124:127]\n"
    "buffer_load_dwordx4 v[156:159], v1, s[52:55], s76 offen offset:2048 sc1\n"
    "v_mfma_f32_16x16x32_bf16 a[0:3], a[180:183], v[212:215], a[0:3]\n"
    "v_mfma_f32_16x16x32_bf16 a[4:7], a[180:183], v[216:219], a[4:7]\n"
    "buffer_load_dwordx4 v[160:163], v1, s[52:55], s76 offen offset:3072 sc1\n"
    "v_mfma_f32_16x16x32_bf16 a[8:11], a[180:183], v[220:223], a[8:11]\n"
    "v_mfma_f32_16x16x32_bf16 a[12:15], a[180:183], v[224:227], a[12:15]\n"
    "ds_read_b64_tr_b16 a[144:145], v6 offset:96\n"
    "ds_read_b64_tr_b16 a[146:147], v6 offset:5216\n"
    "v_add_u32 v6, 0x6000, v6\n"
    "v_mfma_f32_16x16x32_bf16 a[16:19], a[180:183], v[228:231], a[16:19]\n"
    "s_waitcnt lgkmcnt(2)\n"
    "v_mfma_f32_16x16x32_bf16 a[20:23], a[184:187], v[212:215], a[20:23]\n"
    "ds_read_b64_tr_b16 a[148:149], v7 offset:128\n"
    "ds_read_b64_tr_b16 a[150:151], v7 offset:5248\n"
    "v_add_u32 v7, 0x6000, v7\n"
    "v_mfma_f32_16x16x32_bf16 a[24:27], a[184:187], v[216:219], a[24:27]\n"
    "buffer_load_dwordx4 v[164:167], v1, s[52:55], s77 offen sc1\n"
    "v_mfma_f32_16x16x32_bf16 a[28:31], a[184:187], v[220:223], a[28:31]\n"
    "s_add_u32 s76, s79, 0x7800\n"
    "v_mfma_f32_16x16x32_bf16 a[32:35], a[184:187], v[224:227], a[32:35]\n"
    "s_add_u32 s77, s79, 0x8800\n"
    "v_mfma_f32_16x16x32_bf16 a[36:39], a[184:187], v[228:231], a[36:39]\n"
    "buffer_load_dwordx4 v[192:195], v1, s[52:55], s76 offen sc1\n"
    "v_mfma_f32_16x16x32_bf16 a[40:43], a[188:191], v[212:215], a[40:43]\n"
    "buffer_load_dwordx4 v[196:199], v1, s[52:55], s76 offen offset:1024 sc1\n"
    "v_mfma_f32_16x16x32_bf16 a[44:47], a[188:191], v[216:219], a[44:47]\n"
    "v_mfma_f32_16x16x32_bf16 a[48:51], a[188:191], v[220:223], a[48:51]\n"
    "v_mfma_f32_16x16x32_bf16 a[52:55], a[188:191], v[224:227], a[52:55]\n"
    "buffer_load_dwordx4 v[200:203], v1, s[52:55], s76 offen offset:2048 sc1\n"
    "v_mfma_f32_16x16x32_bf16 a[56:59], a[188:191], v[228:231], a[56:59]\n"
    "s_waitcnt vmcnt(23) lgkmcnt(0)\n"
    "s_barrier\n"
    "v_mfma_f32_16x16x32_bf16 a[60:63], a[144:147], v[212:215], a[60:63]\n"
    "ds_read_b64_tr_b16 a[152:153], v3\n"
    "ds_read_b64_tr_b16 a[154:155], v3 offset:5120\n"
    "v_mfma_f32_16x16x32_bf16 a[64:67], a[144:147], v[216:219], a[64:67]\n"
    "buffer_load_dwordx4 v[204:207], v1, s[52:55], s76 offen offset:3072 sc1\n"
    "v_mfma_f32_16x16x32_bf16 a[68:71], a[144:147], v[220:223], a[68:71]\n"
    "v_mfma_f32_16x16x32_bf16 a[72:75], a[144:147], v[224:227], a[72:75]\n"
    "ds_read_b64_tr_b16 a[156:157], v5 offset:32\n"
    "ds_read_b64_tr_b16 a[158:159], v5 offset:5152\n"
    "v_mfma_f32_16x16x32_bf16 a[76:79], a[144:147], v[228:231], a[76:79]\n"
    "buffer_load_dwordx4 v[208:211], v1, s[52:55], s77 offen sc1\n"
    "v_mfma_f32_16x16x32_bf16 a[80:83], a[148:151], v[212:215], a[80:83]\n"
    "s_add_u32 m0, s85, 0\n"
    "v_mfma_f32_16x16x32_bf16 a[84:87], a[148:151], v[216:219], a[84:87]\n"
    "s_add_u32 s98, s43, 0xa000\n"
    "v_mfma_f32_16x16x32_bf16 a[88:91], a[148:151], v[220:223], a[88:91]\n"
    "buffer_load_dwordx4 v23, s[60:63], s98 offen lds\n"
    "v_mfma_f32_16x16x32_bf16 a[92:95], a[148:151], v[224:227], a[92:95]\n"
    "ds_read_b64_tr_b16 a[160:161], v3 offset:64\n"
    "ds_read_b64_tr_b16 a[162:163], v3 offset:5184\n"
    "v_mfma_f32_16x16x32_bf16 a[96:99], a[148:151], v[228:231], a[96:99]\n"
    "s_waitcnt lgkmcnt(2)\n"
    "v_mfma_f32_16x16x32_bf16 v[28:31], a[152:155], v[232:235], v[28:31]\n"
    "buffer_load_dwordx4 v24, s[60:63], s98 offen offset:1024 lds\n"
    "v_mfma_f32_16x16x32_bf16 v[32:35], a[152:155], v[236:239], v[32:35]\n"
    "v_mfma_f32_16x16x32_bf16 v[36:39], a[152:155], v[240:243], v[36:39]\n"
    "v_mfma_f32_16x16x32_bf16 v[40:43], a[152:155], v[244:247], v[40:43]\n"
    "ds_read_b64_tr_b16 a[164:165], v5 offset:96\n"
    "ds_read_b64_tr_b16 a[166:167], v5 offset:5216\n"
    "v_add_u32 v5, 0x5000, v5\n"
    "v_mfma_f32_16x16x32_bf16 v[44:47], a[152:155], v[248:251], v[44:47]\n"
    "buffer_load_dwordx4 v25, s[60:63], s98 offen offset:2048 lds\n"
    "v_mfma_f32_16x16x32_bf16 v[48:51], a[156:159], v[232:235], v[48:51]\n"
    "v_mfma_f32_16x16x32_bf16 v[52:55], a[156:159], v[236:239], v[52:55]\n"
    "buffer_load_dwordx4 v26, s[60:63], s98 offen offset:3072 lds\n"
    "v_mfma_f32_16x16x32_bf16 v[56:59], a[156:159], v[240:243], v[56:59]\n"
    "s_add_u32 m0, s85, 0x1000\n"
    "v_mfma_f32_16x16x32_bf16 v[60:63], a[156:159], v[244:247], v[60:63]\n"
    "ds_read_b64_tr_b16 a[168:169], v3 offset:128\n"
    "ds_read_b64_tr_b16 a[170:171], v3 offset:5248\n"
    "v_add_u32 v3, 0x5000, v3\n"
    "v_mfma_f32_16x16x32_bf16 v[64:67], a[156:159], v[248:251], v[64:67]\n"
    "s_waitcnt lgkmcnt(2)\n"
    "v_mfma_f32_16x16x32_bf16 v[68:71], a[160:163], v[232:235], v[68:71]\n"
    "buffer_load_dwordx4 v27, s[60:63], s98 offen lds\n"
    "v_mfma_f32_16x16x32_bf16 v[72:75], a[160:163], v[236:239], v[72:75]\n"
    "s_add_u32 m0, s85, 0x8000\n"
    "v_mfma_f32_16x16x32_bf16 v[76:79], a[160:163], v[240:243], v[76:79]\n"
    "s_add_u32 s98, s43, 0x14000\n"
    "v_mfma_f32_16x16x32_bf16 v[80:83], a[160:163], v[244:247], v[80:83]\n"
    "ds_read_b64_tr_b16 a[172:173], v7\n"
    "ds_read_b64_tr_b16 a[174:175], v7 offset:5120\n"
    "v_mfma_f32_16x16x32_bf16 v[84:87], a[160:163], v[248:251], v[84:87]\n"
    "buffer_load_dwordx4 v23, s[60:63], s98 offen lds\n"
    "v_mfma_f32_16x16x32_bf16 v[88:91], a[164:167], v[232:235], v[88:91]\n"
    "buffer_load_dwordx4 v24, s[60:63], s98 offen offset:1024 lds\n"
    "v_mfma_f32_16x16x32_bf16 v[92:95], a[164:167], v[236:239], v[92:95]\n"
    "v_mfma_f32_16x16x32_bf16 v[96:99], a[164:167], v[240:243], v[96:99]\n"
    "v_mfma_f32_16x16x32_bf16 v[100:103], a[164:167], v[244:247], v[100:103]\n"
    "ds_read_b64_tr_b16 a[176:177], v6 offset:32\n"
    "ds_read_b64_tr_b16 a[178:179], v6 offset:5152\n"
    "v_mfma_f32_16x16x32_bf16 v[104:107], a[164:167], v[248:251], v[104:107]\n"
    "s_waitcnt lgkmcnt(2)\n"
    "v_mfma_f32_16x16x32_bf16 v[108:111], a[168:171], v[232:235], v[108:111]\n"
    "buffer_load_dwordx4 v25, s[60:63], s98 offen offset:2048 lds\n"
    "v_mfma_f32_16x16x32_bf16 v[112:115], a[168:171], v[236:239], v[112:115]\n"
    "buffer_load_dwordx4 v26, s[60:63], s98 offen offset:3072 lds\n"
    "v_mfma_f32_16x16x32_bf16 v[116:119], a[168:171], v[240:243], v[116:119]\n"
    "s_add_u32 m0, s85, 0x9000\n"
    "v_mfma_f32_16x16x32_bf16 v[120:123], a[168:171], v[244:247], v[120:123]\n"
    "ds_read_b64_tr_b16 a[180:181], v7 offset:64\n"
    "ds_read_b64_tr_b16 a[182:183], v7 offset:5184\n"
    "v_mfma_f32_16x16x32_bf16 v[124:127], a[168:171], v[248:251], v[124:127]\n"
    "buffer_load_dwordx4 v27, s[60:63], s98 offen lds\n"
    "v_mfma_f32_16x16x32_bf16 a[0:3], a[172:175], v[232:235], a[0:3]\n"
    "s_add_u32 m0, s85, 0xd000\n"
    "v_mfma_f32_16x16x32_bf16 a[4:7], a[172:175], v[236:239], a[4:7]\n"
    "s_add_u32 s98, s43, 0x1e000\n"
    "v_mfma_f32_16x16x32_bf16 a[8:11], a[172:175], v[240:243], a[8:11]\n"
    "buffer_load_dwordx4 v23, s[60:63], s98 offen lds\n"
    "v_mfma_f32_16x16x32_bf16 a[12:15], a[172:175], v[244:247], a[12:15]\n"
    "ds_read_b64_tr_b16 a[184:185], v6 offset:96\n"
    "ds_read_b64_tr_b16 a[186:187], v6 offset:5216\n"
    "v_add_u32 v6, 0x5000, v6\n"
    "v_mfma_f32_16x16x32_bf16 a[16:19], a[172:175], v[248:251], a[16:19]\n"
    "s_waitcnt lgkmcnt(2)\n"
    "v_mfma_f32_16x16x32_bf16 a[20:23], a[176:179], v[232:235], a[20:23]\n"
    "ds_read_b64_tr_b16 a[188:189], v7 offset:128\n"
    "ds_read_b64_tr_b16 a[190:191], v7 offset:5248\n"
    "v_add_u32 v7, 0x5000, v7\n"
    "v_mfma_f32_16x16x32_bf16 a[24:27], a[176:179], v[236:239], a[24:27]\n"
    "buffer_load_dwordx4 v24, s[60:63], s98 offen offset:1024 lds\n"
    "v_mfma_f32_16x16x32_bf16 a[28:31], a[176:179], v[240:243], a[28:31]\n"
    "buffer_load_dwordx4 v25, s[60:63], s98 offen offset:2048 lds\n"
    "v_mfma_f32_16x16x32_bf16 a[32:35], a[176:179], v[244:247], a[32:35]\n"
    "buffer_load_dwordx4 v26, s[60:63], s98 offen offset:3072 lds\n"
    "v_mfma_f32_16x16x32_bf16 a[36:39], a[176:179], v[248:251], a[36:39]\n"
    "s_add_u32 m0, s85, 0xe000\n"
    "v_mfma_f32_16x16x32_bf16 a[40:43], a[180:183], v[232:235], a[40:43]\n"
    "buffer_load_dwordx4 v27, s[60:63], s98 offen lds\n"
    "v_mfma_f32_16x16x32_bf16 a[44:47], a[180:183], v[236:239], a[44:47]\n"
    "v_mfma_f32_16x16x32_bf16 a[48:51], a[180:183], v[240:243], a[48:51]\n"
    "v_mfma_f32_16x16x32_bf16 a[52:55], a[180:183], v[244:247], a[52:55]\n"
    "v_mfma_f32_16x16x32_bf16 a[56:59], a[180:183], v[248:251], a[56:59]\n"
    "s_waitcnt vmcnt(30) lgkmcnt(0)\n"
    "s_barrier\n"
    "v_mfma_f32_16x16x32_bf16 a[60:63], a[184:187], v[232:235], a[60:63]\n"
    "ds_read_b64_tr_b16 a[144:145], v3\n"
    "ds_read_b64_tr_b16 a[146:147], v3 offset:5120\n"
    "v_mfma_f32_16x16x32_bf16 a[64:67], a[184:187], v[236:239], a[64:67]\n"
    "v_mfma_f32_16x16x32_bf16 a[68:71], a[184:187], v[240:243], a[68:71]\n"
    "v_mfma_f32_16x16x32_bf16 a[72:75], a[184:187], v[244:247], a[72:75]\n"
    "ds_read_b64_tr_b16 a[148:149], v5 offset:32\n"
    "ds_read_b64_tr_b16 a[150:151], v5 offset:5152\n"
    "v_mfma_f32_16x16x32_bf16 a[76:79], a[184:187], v[248:251], a[76:79]\n"
    "v_mfma_f32_16x16x32_bf16 a[80:83], a[188:191], v[232:235], a[80:83]\n"
    "v_mfma_f32_16x16x32_bf16 a[84:87], a[188:191], v[236:239], a[84:87]\n"
    "v_mfma_f32_16x16x32_bf16 a[88:91], a[188:191], v[240:243], a[88:91]\n"
    "v_mfma_f32_16x16x32_bf16 a[92:95], a[188:191], v[244:247], a[92:95]\n"
    "ds_read_b64_tr_b16 a[152:153], v3 offset:64\n"
    "ds_read_b64_tr_b16 a[154:155], v3 offset:5184\n"
    "v_mfma_f32_16x16x32_bf16 a[96:99], a[188:191], v[248:251], a[96:99]\n"
    "s_waitcnt lgkmcnt(2)\n"
    "v_mfma_f32_16x16x32_bf16 v[28:31], a[144:147], v[128:131], v[28:31]\n"
    "v_mfma_f32_16x16x32_bf16 v[32:35], a[144:147], v[132:135], v[32:35]\n"
    "v_mfma_f32_16x16x32_bf16 v[36:39], a[144:147], v[136:139], v[36:39]\n"
    "v_mfma_f32_16x16x32_bf16 v[40:43], a[144:147], v[140:143], v[40:43]\n"
    "ds_read_b64_tr_b16 a[156:157], v5 offset:96\n"
    "ds_read_b64_tr_b16 a[158:159], v5 offset:5216\n"
    "v_add_u32 v5, 0xfffde000, v5\n"
    "v_mfma_f32_16x16x32_bf16 v[44:47], a[144:147], v[144:147], v[44:47]\n"
    "v_mfma_f32_16x16x32_bf16 v[48:51], a[148:151], v[128:131], v[48:51]\n"
    "v_mfma_f32_16x16x32_bf16 v[52:55], a[148:151], v[132:135], v[52:55]\n"
    "v_mfma_f32_16x16x32_bf16 v[56:59], a[148:151], v[136:139], v[56:59]\n"
    "v_mfma_f32_16x16x32_bf16 v[60:63], a[148:151], v[140:143], v[60:63]\n"
    "ds_read_b64_tr_b16 a[160:161], v3 offset:128\n"
    "ds_read_b64_tr_b16 a[162:163], v3 offset:5248\n"
    "v_add_u32 v3, 0xfffde000, v3\n"
    "v_mfma_f32_16x16x32_bf16 v[64:67], a[148:151], v[144:147], v[64:67]\n"
    "s_waitcnt lgkmcnt(2)\n"
    "v_mfma_f32_16x16x32_bf16 v[68:71], a[152:155], v[128:131], v[68:71]\n"
    "v_mfma_f32_16x16x32_bf16 v[72:75], a[152:155], v[132:135], v[72:75]\n"
    "v_mfma_f32_16x16x32_bf16 v[76:79], a[152:155], v[136:139], v[76:79]\n"
    "v_mfma_f32_16x16x32_bf16 v[80:83], a[152:155], v[140:143], v[80:83]\n"
    "ds_read_b64_tr_b16 a[164:165], v7\n"
    "ds_read_b64_tr_b16 a[166:167], v7 offset:5120\n"
    "v_mfma_f32_16x16x32_bf16 v[84:87], a[152:155], v[144:147], v[84:87]\n"
    "v_mfma_f32_16x16x32_bf16 v[88:91], a[156:159], v[128:131], v[88:91]\n"
    "v_mfma_f32_16x16x32_bf16 v[92:95], a[156:159], v[132:135], v[92:95]\n"
    "v_mfma_f32_16x16x32_bf16 v[96:99], a[156:159], v[136:139], v[96:99]\n"
    "v_mfma_f32_16x16x32_bf16 v[100:103], a[156:159], v[140:143], v[100:103]\n"
    "ds_read_b64_tr_b16 a[168:169], v6 offset:32\n"
    "ds_read_b64_tr_b16 a[170:171], v6 offset:5152\n"
    "v_mfma_f32_16x16x32_bf16 v[104:107], a[156:159], v[144:147], v[104:107]\n"
    "s_waitcnt lgkmcnt(2)\n"
    "v_mfma_f32_16x16x32_bf16 v[108:111], a[160:163], v[128:131], v[108:111]\n"
    "v_mfma_f32_16x16x32_bf16 v[112:115], a[160:163], v[132:135], v[112:115]\n"
    "v_mfma_f32_16x16x32_bf16 v[116:119], a[160:163], v[136:139], v[116:119]\n"
    "v_mfma_f32_16x16x32_bf16 v[120:123], a[160:163], v[140:143], v[120:123]\n"
    "ds_read_b64_tr_b16 a[172:173], v7 offset:64\n"
    "ds_read_b64_tr_b16 a[174:175], v7 offset:5184\n"
    "v_mfma_f32_16x16x32_bf16 v[124:127], a[160:163], v[144:147], v[124:127]\n"
    "v_mfma_f32_16x16x32_bf16 a[0:3], a[164:167], v[128:131], a[0:3]\n"
    "v_mfma_f32_16x16x32_bf16 a[4:7], a[164:167], v[132:135], a[4:7]\n"
    "v_mfma_f32_16x16x32_bf16 a[8:11], a[164:167], v[136:139], a[8:11]\n"
    "v_mfma_f32_16x16x32_bf16 a[12:15], a[164:167], v[140:143], a[12:15]\n"
    "ds_read_b64_tr_b16 a[176:177], v6 offset:96\n"
    "ds_read_b64_tr_b16 a[178:179], v6 offset:5216\n"
    "v_add_u32 v6, 0xfffde000, v6\n"
    "v_mfma_f32_16x16x32_bf16 a[16:19], a[164:167], v[144:147], a[16:19]\n"
    "s_waitcnt lgkmcnt(2)\n"
    "v_mfma_f32_16x16x32_bf16 a[20:23], a[168:171], v[128:131], a[20:23]\n"
    "ds_read_b64_tr_b16 a[180:181], v7 offset:128\n"
    "ds_read_b64_tr_b16 a[182:183], v7 offset:5248\n"
    "v_add_u32 v7, 0xfffde000, v7\n"
    "v_mfma_f32_16x16x32_bf16 a[24:27], a[168:171], v[132:135], a[24:27]\n"
    "v_mfma_f32_16x16x32_bf16 a[28:31], a[168:171], v[136:139], a[28:31]\n"
    "v_mfma_f32_16x16x32_bf16 a[32:35], a[168:171], v[140:143], a[32:35]\n"
    "v_mfma_f32_16x16x32_bf16 a[36:39], a[168:171], v[144:147], a[36:39]\n"
    "v_mfma_f32_16x16x32_bf16 a[40:43], a[172:175], v[128:131], a[40:43]\n"
    "v_mfma_f32_16x16x32_bf16 a[44:47], a[172:175], v[132:135], a[44:47]\n"
    "v_mfma_f32_16x16x32_bf16 a[48:51], a[172:175], v[136:139], a[48:51]\n"
    "v_mfma_f32_16x16x32_bf16 a[52:55], a[172:175], v[140:143], a[52:55]\n"
    "v_mfma_f32_16x16x32_bf16 a[56:59], a[172:175], v[144:147], a[56:59]\n"
    "s_waitcnt vmcnt(0) lgkmcnt(0)\n"
    "s_barrier\n"
    "v_mfma_f32_16x16x32_bf16 a[60:63], a[176:179], v[128:131], a[60:63]\n"
    "ds_read_b64_tr_b16 a[184:185], v7\n"
    "ds_read_b64_tr_b16 a[186:187], v7 offset:5120\n"
    "v_mfma_f32_16x16x32_bf16 a[64:67], a[176:179], v[132:135], a[64:67]\n"
    "v_mfma_f32_16x16x32_bf16 a[68:71], a[176:179], v[136:139], a[68:71]\n"
    "v_mfma_f32_16x16x32_bf16 a[72:75], a[176:179], v[140:143], a[72:75]\n"
    "ds_read_b64_tr_b16 a[188:189], v7 offset:32768\n"
    "ds_read_b64_tr_b16 a[190:191], v7 offset:37888\n"
    "v_mfma_f32_16x16x32_bf16 a[76:79], a[176:179], v[144:147], a[76:79]\n"
    "v_mfma_f32_16x16x32_bf16 a[80:83], a[180:183], v[128:131], a[80:83]\n"
    "v_mfma_f32_16x16x32_bf16 a[84:87], a[180:183], v[132:135], a[84:87]\n"
    "v_mfma_f32_16x16x32_bf16 a[88:91], a[180:183], v[136:139], a[88:91]\n"
    "v_mfma_f32_16x16x32_bf16 a[92:95], a[180:183], v[140:143], a[92:95]\n"
    "ds_read_b64_tr_b16 a[144:145], v7 offset:53248\n"
    "ds_read_b64_tr_b16 a[146:147], v7 offset:58368\n"
    "v_mfma_f32_16x16x32_bf16 a[96:99], a[180:183], v[144:147], a[96:99]\n"
    "s_waitcnt lgkmcnt(2)\n"
    "v_mfma_f32_16x16x32_bf16 a[0:3], a[184:187], v[168:171], a[0:3]\n"
    "v_mfma_f32_16x16x32_bf16 a[4:7], a[184:187], v[172:175], a[4:7]\n"
    "v_mfma_f32_16x16x32_bf16 a[8:11], a[184:187], v[176:179], a[8:11]\n"
    "v_mfma_f32_16x16x32_bf16 a[12:15], a[184:187], v[180:183], a[12:15]\n"
    "ds_read_b64_tr_b16 a[148:149], v6 offset:32\n"
    "ds_read_b64_tr_b16 a[150:151], v6 offset:5152\n"
    "v_mfma_f32_16x16x32_bf16 a[16:19], a[184:187], v[184:187], a[16:19]\n"
    "v_mfma_f32_16x16x32_bf16 a[0:3], a[188:191], v[148:151], a[0:3]\n"
    "v_mfma_f32_16x16x32_bf16 a[4:7], a[188:191], v[152:155], a[4:7]\n"
    "v_mfma_f32_16x16x32_bf16 a[8:11], a[188:191], v[156:159], a[8:11]\n"
    "v_mfma_f32_16x16x32_bf16 a[12:15], a[188:191], v[160:163], a[12:15]\n"
    "ds_read_b64_tr_b16 a[152:153], v6 offset:32800\n"
    "ds_read_b64_tr_b16 a[154:155], v6 offset:37920\n"
    "v_mfma_f32_16x16x32_bf16 a[16:19], a[188:191], v[164:167], a[16:19]\n"
    "s_waitcnt lgkmcnt(2)\n"
    "v_mfma_f32_16x16x32_bf16 a[0:3], a[144:147], v[192:195], a[0:3]\n"
    "v_mfma_f32_16x16x32_bf16 a[4:7], a[144:147], v[196:199], a[4:7]\n"
    "v_mfma_f32_16x16x32_bf16 a[8:11], a[144:147], v[200:203], a[8:11]\n"
    "v_mfma_f32_16x16x32_bf16 a[12:15], a[144:147], v[204:207], a[12:15]\n"
    "ds_read_b64_tr_b16 a[156:157], v6 offset:53280\n"
    "ds_read_b64_tr_b16 a[158:159], v6 offset:58400\n"
    "v_mfma_f32_16x16x32_bf16 a[16:19], a[144:147], v[208:211], a[16:19]\n"
    "v_mfma_f32_16x16x32_bf16 a[20:23], a[148:151], v[168:171], a[20:23]\n"
    "v_mfma_f32_16x16x32_bf16 a[24:27], a[148:151], v[172:175], a[24:27]\n"
    "v_mfma_f32_16x16x32_bf16 a[28:31], a[148:151], v[176:179], a[28:31]\n"
    "v_mfma_f32_16x16x32_bf16 a[32:35], a[148:151], v[180:183], a[32:35]\n"
    "ds_read_b64_tr_b16 a[160:161], v7 offset:64\n"
    "ds_read_b64_tr_b16 a[162:163], v7 offset:5184\n"
    "v_mfma_f32_16x16x32_bf16 a[36:39], a[148:151], v[184:187], a[36:39]\n"
    "s_waitcnt lgkmcnt(2)\n"
    "v_mfma_f32_16x16x32_bf16 a[20:23], a[152:155], v[148:151], a[20:23]\n"
    "ds_write_b128 v4, a[0:3]\n"
    "v_mfma_f32_16x16x32_bf16 a[24:27], a[152:155], v[152:155], a[24:27]\n"
    "v_mfma_f32_16x16x32_bf16 a[28:31], a[152:155], v[156:159], a[28:31]\n"
    "v_mfma_f32_16x16x32_bf16 a[32:35], a[152:155], v[160:163], a[32:35]\n"
    "ds_read_b64_tr_b16 a[164:165], v7 offset:32832\n"
    "ds_read_b64_tr_b16 a[166:167], v7 offset:37952\n"
    "v_mfma_f32_16x16x32_bf16 a[36:39], a[152:155], v[164:167], a[36:39]\n"
    "ds_write_b128 v4, a[4:7] offset:1024\n"
    "v_mfma_f32_16x16x32_bf16 a[20:23], a[156:159], v[192:195], a[20:23]\n"
    "v_mfma_f32_16x16x32_bf16 a[24:27], a[156:159], v[196:199], a[24:27]\n"
    "ds_write_b128 v4, a[8:11] offset:2048\n"
    "v_mfma_f32_16x16x32_bf16 a[28:31], a[156:159], v[200:203], a[28:31]\n"
    "v_mfma_f32_16x16x32_bf16 a[32:35], a[156:159], v[204:207], a[32:35]\n"
    "ds_read_b64_tr_b16 a[168:169], v7 offset:53312\n"
    "ds_read_b64_tr_b16 a[170:171], v7 offset:58432\n"
    "v_mfma_f32_16x16x32_bf16 a[36:39], a[156:159], v[208:211], a[36:39]\n"
    "s_waitcnt lgkmcnt(4)\n"
    "v_mfma_f32_16x16x32_bf16 a[40:43], a[160:163], v[168:171], a[40:43]\n"
    "ds_write_b128 v4, a[12:15] offset:3072\n"
    "v_mfma_f32_16x16x32_bf16 a[44:47], a[160:163], v[172:175], a[44:47]\n"
    "v_mfma_f32_16x16x32_bf16 a[48:51], a[160:163], v[176:179], a[48:51]\n"
    "ds_write_b128 v4, a[16:19] offset:4096\n"
    "v_mfma_f32_16x16x32_bf16 a[52:55], a[160:163], v[180:183], a[52:55]\n"
    "ds_read_b64_tr_b16 a[172:173], v6 offset:96\n"
    "ds_read_b64_tr_b16 a[174:175], v6 offset:5216\n"
    "v_mfma_f32_16x16x32_bf16 a[56:59], a[160:163], v[184:187], a[56:59]\n"
    "v_mfma_f32_16x16x32_bf16 a[40:43], a[164:167], v[148:151], a[40:43]\n"
    "ds_write_b128 v4, a[20:23] offset:5120\n"
    "v_mfma_f32_16x16x32_bf16 a[44:47], a[164:167], v[152:155], a[44:47]\n"
    "v_mfma_f32_16x16x32_bf16 a[48:51], a[164:167], v[156:159], a[48:51]\n"
    "v_mfma_f32_16x16x32_bf16 a[52:55], a[164:167], v[160:163], a[52:55]\n"
    "ds_read_b64_tr_b16 a[176:177], v6 offset:32864\n"
    "ds_read_b64_tr_b16 a[178:179], v6 offset:37984\n"
    "v_mfma_f32_16x16x32_bf16 a[56:59], a[164:167], v[164:167], a[56:59]\n"
    "s_waitcnt lgkmcnt(3)\n"
    "v_mfma_f32_16x16x32_bf16 a[40:43], a[168:171], v[192:195], a[40:43]\n"
    "ds_write_b128 v4, a[24:27] offset:6144\n"
    "v_mfma_f32_16x16x32_bf16 a[44:47], a[168:171], v[196:199], a[44:47]\n"
    "ds_write_b128 v4, a[28:31] offset:7168\n"
    "v_mfma_f32_16x16x32_bf16 a[48:51], a[168:171], v[200:203], a[48:51]\n"
    "v_mfma_f32_16x16x32_bf16 a[52:55], a[168:171], v[204:207], a[52:55]\n"
    "ds_read_b64_tr_b16 a[180:181], v6 offset:53344\n"
    "ds_read_b64_tr_b16 a[182:183], v6 offset:58464\n"
    "v_mfma_f32_16x16x32_bf16 a[56:59], a[168:171], v[208:211], a[56:59]\n"
    "ds_write_b128 v4, a[32:35] offset:8192\n"
    "v_mfma_f32_16x16x32_bf16 a[60:63], a[172:175], v[168:171], a[60:63]\n"
    "v_mfma_f32_16x16x32_bf16 a[64:67], a[172:175], v[172:175], a[64:67]\n"
    "v_mfma_f32_16x16x32_bf16 a[68:71], a[172:175], v[176:179], a[68:71]\n"
    "ds_write_b128 v4, a[36:39] offset:9216\n"
    "v_mfma_f32_16x16x32_bf16 a[72:75], a[172:175], v[180:183], a[72:75]\n"
    "ds_read_b64_tr_b16 a[184:185], v7 offset:128\n"
    "ds_read_b64_tr_b16 a[186:187], v7 offset:5248\n"
    "v_mfma_f32_16x16x32_bf16 a[76:79], a[172:175], v[184:187], a[76:79]\n"
    "s_waitcnt lgkmcnt(4)\n"
    "v_mfma_f32_16x16x32_bf16 a[60:63], a[176:179], v[148:151], a[60:63]\n"
    "ds_write_b128 v4, a[40:43] offset:10240\n"
    "v_mfma_f32_16x16x32_bf16 a[64:67], a[176:179], v[152:155], a[64:67]\n"
    "v_mfma_f32_16x16x32_bf16 a[68:71], a[176:179], v[156:159], a[68:71]\n"
    "v_mfma_f32_16x16x32_bf16 a[72:75], a[176:179], v[160:163], a[72:75]\n"
    "ds_read_b64_tr_b16 a[188:189], v7 offset:32896\n"
    "ds_read_b64_tr_b16 a[190:191], v7 offset:38016\n"
    "v_mfma_f32_16x16x32_bf16 a[76:79], a[176:179], v[164:167], a[76:79]\n"
    "ds_write_b128 v4, a[44:47] offset:11264\n"
    "v_mfma_f32_16x16x32_bf16 a[60:63], a[180:183], v[192:195], a[60:63]\n"
    "v_mfma_f32_16x16x32_bf16 a[64:67], a[180:183], v[196:199], a[64:67]\n"
    "ds_write_b128 v4, a[48:51] offset:12288\n"
    "v_mfma_f32_16x16x32_bf16 a[68:71], a[180:183], v[200:203], a[68:71]\n"
    "v_mfma_f32_16x16x32_bf16 a[72:75], a[180:183], v[204:207], a[72:75]\n"
    "ds_read_b64_tr_b16 a[144:145], v7 offset:53376\n"
    "ds_read_b64_tr_b16 a[146:147], v7 offset:58496\n"
    "v_mfma_f32_16x16x32_bf16 a[76:79], a[180:183], v[208:211], a[76:79]\n"
    "s_waitcnt lgkmcnt(4)\n"
    "v_mfma_f32_16x16x32_bf16 a[80:83], a[184:187], v[168:171], a[80:83]\n"
    "ds_write_b128 v8, a[52:55]\n"
    "v_mfma_f32_16x16x32_bf16 a[84:87], a[184:187], v[172:175], a[84:87]\n"
    "v_mfma_f32_16x16x32_bf16 a[88:91], a[184:187], v[176:179], a[88:91]\n"
    "ds_write_b128 v8, a[56:59] offset:1024\n"
    "v_mfma_f32_16x16x32_bf16 a[92:95], a[184:187], v[180:183], a[92:95]\n"
    "ds_read_b64_tr_b16 a[148:149], v3\n"
    "ds_read_b64_tr_b16 a[150:151], v3 offset:5120\n"
    "v_mfma_f32_16x16x32_bf16 a[96:99], a[184:187], v[184:187], a[96:99]\n"
    "v_mfma_f32_16x16x32_bf16 a[80:83], a[188:191], v[148:151], a[80:83]\n"
    "ds_write_b128 v8, a[60:63] offset:2048\n"
    "v_mfma_f32_16x16x32_bf16 a[84:87], a[188:191], v[152:155], a[84:87]\n"
    "v_mfma_f32_16x16x32_bf16 a[88:91], a[188:191], v[156:159], a[88:91]\n"
    "v_mfma_f32_16x16x32_bf16 a[92:95], a[188:191], v[160:163], a[92:95]\n"
    "ds_read_b64_tr_b16 a[152:153], v3 offset:32768\n"
    "ds_read_b64_tr_b16 a[154:155], v3 offset:37888\n"
    "v_mfma_f32_16x16x32_bf16 a[96:99], a[188:191], v[164:167], a[96:99]\n"
    "s_waitcnt lgkmcnt(3)\n"
    "v_mfma_f32_16x16x32_bf16 a[80:83], a[144:147], v[192:195], a[80:83]\n"
    "ds_write_b128 v8, a[64:67] offset:3072\n"
    "v_mfma_f32_16x16x32_bf16 a[84:87], a[144:147], v[196:199], a[84:87]\n"
    "ds_write_b128 v8, a[68:71] offset:4096\n"
    "v_mfma_f32_16x16x32_bf16 a[88:91], a[144:147], v[200:203], a[88:91]\n"
    "v_mfma_f32_16x16x32_bf16 a[92:95], a[144:147], v[204:207], a[92:95]\n"
    "ds_read_b64_tr_b16 a[156:157], v3 offset:53248\n"
    "ds_read_b64_tr_b16 a[158:159], v3 offset:58368\n"
    "v_mfma_f32_16x16x32_bf16 a[96:99], a[144:147], v[208:211], a[96:99]\n"
    "ds_write_b128 v8, a[72:75] offset:5120\n"
    "v_mfma_f32_16x16x32_bf16 v[28:31], a[148:151], v[168:171], v[28:31]\n"
    "v_mfma_f32_16x16x32_bf16 v[32:35], a[148:151], v[172:175], v[32:35]\n"
    "v_mfma_f32_16x16x32_bf16 v[36:39], a[148:151], v[176:179], v[36:39]\n"
    "ds_write_b128 v8, a[76:79] offset:6144\n"
    "v_mfma_f32_16x16x32_bf16 v[40:43], a[148:151], v[180:183], v[40:43]\n"
    "ds_read_b64_tr_b16 a[160:161], v5 offset:32\n"
    "ds_read_b64_tr_b16 a[162:163], v5 offset:5152\n"
    "v_mfma_f32_16x16x32_bf16 v[44:47], a[148:151], v[184:187], v[44:47]\n"
    "s_waitcnt lgkmcnt(4)\n"
    "v_mfma_f32_16x16x32_bf16 v[28:31], a[152:155], v[148:151], v[28:31]\n"
    "ds_write_b128 v8, a[80:83] offset:7168\n"
    "v_mfma_f32_16x16x32_bf16 v[32:35], a[152:155], v[152:155], v[32:35]\n"
    "v_mfma_f32_16x16x32_bf16 v[36:39], a[152:155], v[156:159], v[36:39]\n"
    "v_mfma_f32_16x16x32_bf16 v[40:43], a[152:155], v[160:163], v[40:43]\n"
    "ds_read_b64_tr_b16 a[164:165], v5 offset:32800\n"
    "ds_read_b64_tr_b16 a[166:167], v5 offset:37920\n"
    "v_mfma_f32_16x16x32_bf16 v[44:47], a[152:155], v[164:167], v[44:47]\n"
    "ds_write_b128 v8, a[84:87] offset:8192\n"
    "v_mfma_f32_16x16x32_bf16 v[28:31], a[156:159], v[192:195], v[28:31]\n"
    "v_mfma_f32_16x16x32_bf16 v[32:35], a[156:159], v[196:199], v[32:35]\n"
    "ds_write_b128 v8, a[88:91] offset:9216\n"
    "v_mfma_f32_16x16x32_bf16 v[36:39], a[156:159], v[200:203], v[36:39]\n"
    "v_mfma_f32_16x16x32_bf16 v[40:43], a[156:159], v[204:207], v[40:43]\n"
    "ds_read_b64_tr_b16 a[168:169], v5 offset:53280\n"
    "ds_read_b64_tr_b16 a[170:171], v5 offset:58400\n"
    "v_mfma_f32_16x16x32_bf16 v[44:47], a[156:159], v[208:211], v[44:47]\n"
    "s_waitcnt lgkmcnt(4)\n"
    "ds_read_b64_tr_b16 a[172:173], v3 offset:64\n"
    "ds_read_b64_tr_b16 a[174:175], v3 offset:5184\n"
    "v_mfma_f32_16x16x32_bf16 v[48:51], a[160:163], v[168:171], v[48:51]\n"
    "ds_read_b64_tr_b16 a[176:177], v3 offset:32832\n"
    "ds_read_b64_tr_b16 a[178:179], v3 offset:37952\n"
    "v_mfma_f32_16x16x32_bf16 v[52:55], a[160:163], v[172:175], v[52:55]\n"
    "ds_read_b64_tr_b16 a[180:181], v3 offset:53312\n"
    "ds_read_b64_tr_b16 a[182:183], v3 offset:58432\n"
    "v_mfma_f32_16x16x32_bf16 v[56:59], a[160:163], v[176:179], v[56:59]\n"
    "ds_write_b128 v8, a[92:95] offset:10240\n"
    "v_mfma_f32_16x16x32_bf16 v[60:63], a[160:163], v[180:183], v[60:63]\n"
    "ds_write_b128 v8, a[96:99] offset:11264\n"
    "v_mfma_f32_16x16x32_bf16 v[64:67], a[160:163], v[184:187], v[64:67]\n"
    "v_mfma_f32_16x16x32_bf16 v[48:51], a[164:167], v[148:151], v[48:51]\n"
    "v_mfma_f32_16x16x32_bf16 v[52:55], a[164:167], v[152:155], v[52:55]\n"
    "v_mfma_f32_16x16x32_bf16 v[56:59], a[164:167], v[156:159], v[56:59]\n"
    "v_mfma_f32_16x16x32_bf16 v[60:63], a[164:167], v[160:163], v[60:63]\n"
    "s_waitcnt lgkmcnt(0)\n"
    "s_barrier\n"
    "v_mfma_f32_16x16x32_bf16 v[64:67], a[164:167], v[164:167], v[64:67]\n"
    "ds_read_b128 v[212:215], v10\n"
    "v_mfma_f32_16x16x32_bf16 v[48:51], a[168:171], v[192:195], v[48:51]\n"
    "ds_read_b128 v[216:219], v10 offset:1024\n"
    "v_mfma_f32_16x16x32_bf16 v[52:55], a[168:171], v[196:199], v[52:55]\n"
    "ds_read_b128 v[220:223], v10 offset:2048\n"
    "v_mfma_f32_16x16x32_bf16 v[56:59], a[168:171], v[200:203], v[56:59]\n"
    "ds_read_b128 v[224:227], v10 offset:3072\n"
    "v_mfma_f32_16x16x32_bf16 v[60:63], a[168:171], v[204:207], v[60:63]\n"
    "ds_read_b128 v[228:231], v10 offset:4096\n"
    "v_mfma_f32_16x16x32_bf16 v[64:67], a[168:171], v[208:211], v[64:67]\n"
    "ds_read_b128 v[232:235], v10 offset:5120\n"
    "v_mfma_f32_16x16x32_bf16 v[68:71], a[172:175], v[168:171], v[68:71]\n"
    "ds_read_b128 v[236:239], v10 offset:6144\n"
    "v_mfma_f32_16x16x32_bf16 v[72:75], a[172:175], v[172:175], v[72:75]\n"
    "ds_read_b128 v[240:243], v10 offset:7168\n"
    "v_mfma_f32_16x16x32_bf16 v[76:79], a[172:175], v[176:179], v[76:79]\n"
    "ds_read_b128 v[244:247], v10 offset:8192\n"
    "v_mfma_f32_16x16x32_bf16 v[80:83], a[172:175], v[180:183], v[80:83]\n"
    "ds_read_b64_tr_b16 a[184:185], v5 offset:96\n"
    "ds_read_b64_tr_b16 a[186:187], v5 offset:5216\n"
    "ds_read_b128 v[248:251], v10 offset:9216\n"
    "v_mfma_f32_16x16x32_bf16 v[84:87], a[172:175], v[184:187], v[84:87]\n"
    "ds_read_b128 v[252:255], v10 offset:10240\n"
    "v_mfma_f32_16x16x32_bf16 v[68:71], a[176:179], v[148:151], v[68:71]\n"
    "s_waitcnt lgkmcnt(0)\n"
    "v_add_f32 v28, v28, v212\n"
    "v_mfma_f32_16x16x32_bf16 v[72:75], a[176:179], v[152:155], v[72:75]\n"
    "v_add_f32 v29, v29, v213\n"
    "v_add_f32 v30, v30, v214\n"
    "v_mfma_f32_16x16x32_bf16 v[76:79], a[176:179], v[156:159], v[76:79]\n"
    "v_add_f32 v31, v31, v215\n"
    "v_cvt_pk_bf16_f32 v128, v28, v29\n"
    "v_mfma_f32_16x16x32_bf16 v[80:83], a[176:179], v[160:163], v[80:83]\n"
    "ds_read_b64_tr_b16 a[188:189], v5 offset:32864\n"
    "v_cvt_pk_bf16_f32 v129, v30, v31\n"
    "v_mfma_f32_16x16x32_bf16 v[84:87], a[176:179], v[164:167], v[84:87]\n"
    "ds_read_b64_tr_b16 a[190:191], v5 offset:37984\n"
    "v_add_f32 v32, v32, v216\n"
    "v_mfma_f32_16x16x32_bf16 v[68:71], a[180:183], v[192:195], v[68:71]\n"
    "v_add_f32 v33, v33, v217\n"
    "v_add_f32 v34, v34, v218\n"
    "v_mfma_f32_16x16x32_bf16 v[72:75], a[180:183], v[196:199], v[72:75]\n"
    "v_add_f32 v35, v35, v219\n"
    "v_cvt_pk_bf16_f32 v132, v32, v33\n"
    "v_mfma_f32_16x16x32_bf16 v[76:79], a[180:183], v[200:203], v[76:79]\n"
    "v_cvt_pk_bf16_f32 v133, v34, v35\n"
    "v_add_f32 v36, v36, v220\n"
    "v_mfma_f32_16x16x32_bf16 v[80:83], a[180:183], v[204:207], v[80:83]\n"
    "ds_read_b64_tr_b16 a[144:145], v5 offset:53344\n"
    "v_add_f32 v37, v37, v221\n"
    "v_mfma_f32_16x16x32_bf16 v[84:87], a[180:183], v[208:211], v[84:87]\n"
    "s_waitcnt lgkmcnt(1)\n"
    "ds_read_b64_tr_b16 a[146:147], v5 offset:58464\n"
    "v_add_f32 v38, v38, v222\n"
    "v_mfma_f32_16x16x32_bf16 v[88:91], a[184:187], v[168:171], v[88:91]\n"
    "v_add_f32 v39, v39, v223\n"
    "v_cvt_pk_bf16_f32 v136, v36, v37\n"
    "v_mfma_f32_16x16x32_bf16 v[92:95], a[184:187], v[172:175], v[92:95]\n"
    "v_cvt_pk_bf16_f32 v137, v38, v39\n"
    "v_add_f32 v40, v40, v224\n"
    "v_mfma_f32_16x16x32_bf16 v[96:99], a[184:187], v[176:179], v[96:99]\n"
    "v_add_f32 v41, v41, v225\n"
    "v_add_f32 v42, v42, v226\n"
    "v_mfma_f32_16x16x32_bf16 v[100:103], a[184:187], v[180:183], v[100:103]\n"
    "ds_read_b64_tr_b16 a[148:149], v3 offset:128\n"
    "v_add_f32 v43, v43, v227\n"
    "v_mfma_f32_16x16x32_bf16 v[104:107], a[184:187], v[184:187], v[104:107]\n"
    "ds_read_b64_tr_b16 a[150:151], v3 offset:5248\n"
    "v_cvt_pk_bf16_f32 v140, v40, v41\n"
    "v_mfma_f32_16x16x32_bf16 v[88:91], a[188:191], v[148:151], v[88:91]\n"
    "v_cvt_pk_bf16_f32 v141, v42, v43\n"
    "s_waitcnt lgkmcnt(0)\n"
    "v_mfma_f32_16x16x32_bf16 v[92:95], a[188:191], v[152:155], v[92:95]\n"
    "v_add_f32 v44, v44, v228\n"
    "v_add_f32 v45, v45, v229\n"
    "v_mfma_f32_16x16x32_bf16 v[96:99], a[188:191], v[156:159], v[96:99]\n"
    "v_add_f32 v46, v46, v230\n"
    "v_add_f32 v47, v47, v231\n"
    "v_mfma_f32_16x16x32_bf16 v[100:103], a[188:191], v[160:163], v[100:103]\n"
    "ds_read_b64_tr_b16 a[152:153], v3 offset:32896\n"
    "v_cvt_pk_bf16_f32 v130, v44, v45\n"
    "v_mfma_f32_16x16x32_bf16 v[104:107], a[188:191], v[164:167], v[104:107]\n"
    "s_waitcnt lgkmcnt(1)\n"
    "ds_read_b64_tr_b16 a[154:155], v3 offset:38016\n"
    "v_cvt_pk_bf16_f32 v131, v46, v47\n"
    "v_mfma_f32_16x16x32_bf16 v[88:91], a[144:147], v[192:195], v[88:91]\n"
    "buffer_store_dwordx4 v[128:131], v1, s[64:67], s48 offen sc1\n"
    "v_mfma_f32_16x16x32_bf16 v[92:95], a[144:147], v[196:199], v[92:95]\n"
    "v_add_f32 v48, v48, v232\n"
    "v_add_f32 v49, v49, v233\n"
    "v_mfma_f32_16x16x32_bf16 v[96:99], a[144:147], v[200:203], v[96:99]\n"
    "v_add_f32 v50, v50, v234\n"
    "v_add_f32 v51, v51, v235\n"
    "v_mfma_f32_16x16x32_bf16 v[100:103], a[144:147], v[204:207], v[100:103]\n"
    "ds_read_b64_tr_b16 a[156:157], v3 offset:53376\n"
    "v_cvt_pk_bf16_f32 v134, v48, v49\n"
    "v_mfma_f32_16x16x32_bf16 v[104:107], a[144:147], v[208:211], v[104:107]\n"
    "ds_read_b64_tr_b16 a[158:159], v3 offset:58496\n"
    "v_cvt_pk_bf16_f32 v135, v50, v51\n"
    "v_mfma_f32_16x16x32_bf16 v[108:111], a[148:151], v[168:171], v[108:111]\n"
    "buffer_store_dwordx4 v[132:135], v1, s[64:67], s49 offen sc1\n"
    "v_mfma_f32_16x16x32_bf16 v[112:115], a[148:151], v[172:175], v[112:115]\n"
    "v_add_f32 v52, v52, v236\n"
    "v_add_f32 v53, v53, v237\n"
    "v_mfma_f32_16x16x32_bf16 v[116:119], a[148:151], v[176:179], v[116:119]\n"
    "v_add_f32 v54, v54, v238\n"
    "v_add_f32 v55, v55, v239\n"
    "v_mfma_f32_16x16x32_bf16 v[120:123], a[148:151], v[180:183], v[120:123]\n"
    "v_cvt_pk_bf16_f32 v138, v52, v53\n"
    "v_cvt_pk_bf16_f32 v139, v54, v55\n"
    "v_mfma_f32_16x16x32_bf16 v[124:127], a[148:151], v[184:187], v[124:127]\n"
    "s_waitcnt lgkmcnt(0)\n"
    "v_add_f32 v56, v56, v240\n"
    "v_add_f32 v57, v57, v241\n"
    "v_mfma_f32_16x16x32_bf16 v[108:111], a[152:155], v[148:151], v[108:111]\n"
    "buffer_store_dwordx4 v[136:139], v1, s[64:67], s50 offen sc1\n"
    "v_mfma_f32_16x16x32_bf16 v[112:115], a[152:155], v[152:155], v[112:115]\n"
    "v_add_f32 v58, v58, v242\n"
    "v_add_f32 v59, v59, v243\n"
    "v_mfma_f32_16x16x32_bf16 v[116:119], a[152:155], v[156:159], v[116:119]\n"
    "v_cvt_pk_bf16_f32 v142, v56, v57\n"
    "v_cvt_pk_bf16_f32 v143, v58, v59\n"
    "v_mfma_f32_16x16x32_bf16 v[120:123], a[152:155], v[160:163], v[120:123]\n"
    "buffer_store_dwordx4 v[140:143], v1, s[64:67], s51 offen sc1\n"
    "v_mfma_f32_16x16x32_bf16 v[124:127], a[152:155], v[164:167], v[124:127]\n"
    "s_waitcnt lgkmcnt(0)\n"
    "v_add_f32 v60, v60, v244\n"
    "v_mfma_f32_16x16x32_bf16 v[108:111], a[156:159], v[192:195], v[108:111]\n"
    "v_add_f32 v61, v61, v245\n"
    "v_add_f32 v62, v62, v246\n"
    "v_mfma_f32_16x16x32_bf16 v[112:115], a[156:159], v[196:199], v[112:115]\n"
    "v_add_f32 v63, v63, v247\n"
    "v_cvt_pk_bf16_f32 v144, v60, v61\n"
    "v_mfma_f32_16x16x32_bf16 v[116:119], a[156:159], v[200:203], v[116:119]\n"
    "v_cvt_pk_bf16_f32 v145, v62, v63\n"
    "v_add_f32 v64, v64, v248\n"
    "v_mfma_f32_16x16x32_bf16 v[120:123], a[156:159], v[204:207], v[120:123]\n"
    "v_add_f32 v65, v65, v249\n"
    "v_add_f32 v66, v66, v250\n"
    "v_mfma_f32_16x16x32_bf16 v[124:127], a[156:159], v[208:211], v[124:127]\n"
    "v_add_f32 v67, v67, v251\n"
    "v_cvt_pk_bf16_f32 v188, v64, v65\n"
    "v_cvt_pk_bf16_f32 v189, v66, v67\n"
    "v_add_f32 v68, v68, v252\n"
    "v_add_f32 v69, v69, v253\n"
    "v_add_f32 v70, v70, v254\n"
    "v_add_f32 v71, v71, v255\n"
    "v_cvt_pk_bf16_f32 v128, v68, v69\n"
    "v_cvt_pk_bf16_f32 v129, v70, v71\n"
    "ds_read_b128 v[212:215], v10 offset:11264\n"
    "ds_read_b128 v[216:219], v10 offset:12288\n"
    "ds_read_b128 v[220:223], v12\n"
    "ds_read_b128 v[224:227], v12 offset:1024\n"
    "ds_read_b128 v[228:231], v12 offset:2048\n"
    "ds_read_b128 v[232:235], v12 offset:3072\n"
    "ds_read_b128 v[236:239], v12 offset:4096\n"
    "ds_read_b128 v[240:243], v12 offset:5120\n"
    "ds_read_b128 v[244:247], v12 offset:6144\n"
    "ds_read_b128 v[248:251], v12 offset:7168\n"
    "ds_read_b128 v[252:255], v12 offset:8192\n"
    "s_waitcnt lgkmcnt(10)\n"
    "v_pk_add_f32 v[72:73], v[72:73], v[212:213]\n"
    "v_pk_add_f32 v[74:75], v[74:75], v[214:215]\n"
    "v_cvt_pk_bf16_f32 v132, v72, v73\n"
    "v_cvt_pk_bf16_f32 v133, v74, v75\n"
    "ds_read_b128 v[212:215], v12 offset:9216\n"
    "s_waitcnt lgkmcnt(10)\n"
    "v_pk_add_f32 v[76:77], v[76:77], v[216:217]\n"
    "v_pk_add_f32 v[78:79], v[78:79], v[218:219]\n"
    "v_cvt_pk_bf16_f32 v146, v76, v77\n"
    "v_cvt_pk_bf16_f32 v147, v78, v79\n"
    "buffer_store_dwordx4 v[144:147], v1, s[64:67], s48 offen offset:1024 sc1\n"
    "ds_read_b128 v[216:219], v12 offset:10240\n"
    "s_waitcnt lgkmcnt(10)\n"
    "v_pk_add_f32 v[80:81], v[80:81], v[220:221]\n"
    "v_pk_add_f32 v[82:83], v[82:83], v[222:223]\n"
    "v_cvt_pk_bf16_f32 v190, v80, v81\n"
    "v_cvt_pk_bf16_f32 v191, v82, v83\n"
    "buffer_store_dwordx4 v[188:191], v1, s[64:67], s49 offen offset:1024 sc1\n"
    "ds_read_b128 v[220:223], v12 offset:11264\n"
    "s_waitcnt lgkmcnt(10)\n"
    "v_pk_add_f32 v[84:85], v[84:85], v[224:225]\n"
    "v_pk_add_f32 v[86:87], v[86:87], v[226:227]\n"
    "v_cvt_pk_bf16_f32 v130, v84, v85\n"
    "v_cvt_pk_bf16_f32 v131, v86, v87\n"
    "buffer_store_dwordx4 v[128:131], v1, s[64:67], s50 offen offset:1024 sc1\n"
    "s_waitcnt lgkmcnt(9)\n"
    "v_pk_add_f32 v[88:89], v[88:89], v[228:229]\n"
    "v_pk_add_f32 v[90:91], v[90:91], v[230:231]\n"
    "v_cvt_pk_bf16_f32 v134, v88, v89\n"
    "v_cvt_pk_bf16_f32 v135, v90, v91\n"
    "buffer_store_dwordx4 v[132:135], v1, s[64:67], s51 offen offset:1024 sc1\n"
    "s_waitcnt lgkmcnt(8)\n"
    "v_pk_add_f32 v[92:93], v[92:93], v[232:233]\n"
    "v_pk_add_f32 v[94:95], v[94:95], v[234:235]\n"
    "v_cvt_pk_bf16_f32 v136, v92, v93\n"
    "v_cvt_pk_bf16_f32 v137, v94, v95\n"
    "s_waitcnt lgkmcnt(7)\n"
    "v_pk_add_f32 v[96:97], v[96:97], v[236:237]\n"
    "v_pk_add_f32 v[98:99], v[98:99], v[238:239]\n"
    "v_cvt_pk_bf16_f32 v140, v96, v97\n"
    "v_cvt_pk_bf16_f32 v141, v98, v99\n"
    "s_waitcnt lgkmcnt(6)\n"
    "v_pk_add_f32 v[100:101], v[100:101], v[240:241]\n"
    "v_pk_add_f32 v[102:103], v[102:103], v[242:243]\n"
    "v_cvt_pk_bf16_f32 v144, v100, v101\n"
    "v_cvt_pk_bf16_f32 v145, v102, v103\n"
    "s_waitcnt lgkmcnt(5)\n"
    "v_pk_add_f32 v[104:105], v[104:105], v[244:245]\n"
    "v_pk_add_f32 v[106:107], v[106:107], v[246:247]\n"
    "v_cvt_pk_bf16_f32 v188, v104, v105\n"
    "v_cvt_pk_bf16_f32 v189, v106, v107\n"
    "s_waitcnt lgkmcnt(4)\n"
    "v_pk_add_f32 v[108:109], v[108:109], v[248:249]\n"
    "v_pk_add_f32 v[110:111], v[110:111], v[250:251]\n"
    "v_cvt_pk_bf16_f32 v138, v108, v109\n"
    "v_cvt_pk_bf16_f32 v139, v110, v111\n"
    "buffer_store_dwordx4 v[136:139], v1, s[64:67], s48 offen offset:2048 sc1\n"
    "s_waitcnt lgkmcnt(3)\n"
    "v_pk_add_f32 v[112:113], v[112:113], v[252:253]\n"
    "v_pk_add_f32 v[114:115], v[114:115], v[254:255]\n"
    "v_cvt_pk_bf16_f32 v142, v112, v113\n"
    "v_cvt_pk_bf16_f32 v143, v114, v115\n"
    "buffer_store_dwordx4 v[140:143], v1, s[64:67], s49 offen offset:2048 sc1\n"
    "s_waitcnt lgkmcnt(2)\n"
    "v_pk_add_f32 v[116:117], v[116:117], v[212:213]\n"
    "v_pk_add_f32 v[118:119], v[118:119], v[214:215]\n"
    "v_cvt_pk_bf16_f32 v146, v116, v117\n"
    "v_cvt_pk_bf16_f32 v147, v118, v119\n"
    "buffer_store_dwordx4 v[144:147], v1, s[64:67], s50 offen offset:2048 sc1\n"
    "s_waitcnt lgkmcnt(1)\n"
    "v_pk_add_f32 v[120:121], v[120:121], v[216:217]\n"
    "v_pk_add_f32 v[122:123], v[122:123], v[218:219]\n"
    "v_cvt_pk_bf16_f32 v190, v120, v121\n"
    "v_cvt_pk_bf16_f32 v191, v122, v123\n"
    "buffer_store_dwordx4 v[188:191], v1, s[64:67], s51 offen offset:2048 sc1\n"
    "s_waitcnt lgkmcnt(0)\n"
    "v_pk_add_f32 v[124:125], v[124:125], v[220:221]\n"
    "v_pk_add_f32 v[126:127], v[126:127], v[222:223]\n"
    "v_cvt_pk_bf16_f32 v128, v124, v125\n"
    "v_cvt_pk_bf16_f32 v129, v126, v127\n"
    "buffer_store_dwordx4 v[128:131], v1, s[64:67], s48 offen offset:3072 sc1\n"
    "s_lshr_b32 s74, s90, 2\n"
    "s_lshl_b32 s98, s72, 7\n"
    "s_add_u32 s77, s98, s96\n"
    "s_waitcnt vmcnt(0)\n"
    "s_lshl_b64 exec, 1, s90\n"
    "buffer_store_dwordx2 v[16:17], v15, s[68:71], s77 offen offset:256 sc1\n"
    "s_mov_b64 exec, -1\n"
    "s_mov_b32 exec_lo, s92\n"
    "s_mov_b32 exec_hi, 0\n"
    "buffer_load_dwordx2 v[18:19], v14, s[68:71], s98 offen offset:256 sc1\n"
    "s_mov_b64 exec, -1\n"
    "s_lshl_b32 s75, s74, 12\n"
    "s_add_u32 s81, s81, s75\n"
    "s_add_u32 s48, s81, 0\n"
    "s_add_u32 s49, s81, 0x10000\n"
    "s_add_u32 s50, s81, 0x20000\n"
    "s_add_u32 s51, s81, 0x30000\n"
    "s_waitcnt vmcnt(0)\n"
    "s_mov_b32 s80, 0\n"
    "LPSD_%=:\n"
    "v_cmp_ne_u64 vcc, v[18:19], v[16:17]\n"
    "s_nop 0\n"
    "s_and_b32 s99, vcc_lo, s92\n"
    "s_cbranch_scc0 LPDSD_%=\n"
    "s_sleep 1\n"
    "s_mov_b32 exec_lo, s92\n"
    "s_mov_b32 exec_hi, 0\n"
    "buffer_load_dwordx2 v[18:19], v14, s[68:71], s98 offen offset:256 sc1\n"
    "s_mov_b64 exec, -1\n"
    "s_add_u32 s80, s80, 1\n"
    "s_waitcnt vmcnt(0)\n"
    "s_cmp_lt_u32 s80, 0x40000\n"
    "s_cbranch_scc1 LPSD_%=\n"
    "LPDSD_%=:\n"
    "s_cmp_eq_u32 s74, 1\n"
    "s_cbranch_scc1 LVS1_%=\n"
    "s_cmp_eq_u32 s74, 2\n"
    "s_cbranch_scc1 LVS2_%=\n"
    "s_cmp_eq_u32 s74, 3\n"
    "s_cbranch_scc1 LVS3_%=\n"
    "buffer_load_dwordx4 v[128:131], v1, s[64:67], s49 offen sc1\n"
    "buffer_load_dwordx4 v[132:135], v1, s[64:67], s50 offen sc1\n"
    "buffer_load_dwordx4 v[136:139], v1, s[64:67], s51 offen sc1\n"
    "buffer_load_dwordx4 v[140:143], v1, s[64:67], s49 offen offset:1024 sc1\n"
    "buffer_load_dwordx4 v[144:147], v1, s[64:67], s50 offen offset:1024 sc1\n"
    "buffer_load_dwordx4 v[148:151], v1, s[64:67], s51 offen offset:1024 sc1\n"
    "buffer_load_dwordx4 v[152:155], v1, s[64:67], s49 offen offset:2048 sc1\n"
    "buffer_load_dwordx4 v[156:159], v1, s[64:67], s50 offen offset:2048 sc1\n"
    "buffer_load_dwordx4 v[160:163], v1, s[64:67], s51 offen offset:2048 sc1\n"
    "buffer_load_dwordx4 v[164:167], v1, s[64:67], s49 offen offset:3072 sc1\n"
    "buffer_load_dwordx4 v[168:171], v1, s[64:67], s50 offen offset:3072 sc1\n"
    "buffer_load_dwordx4 v[172:175], v1, s[64:67], s51 offen offset:3072 sc1\n"
    "s_waitcnt vmcnt(9)\n"
    "v_lshlrev_b32 v200, 16, v128\n"
    "v_and_b32 v201, v13, v128\n"
    "v_lshlrev_b32 v202, 16, v129\n"
    "v_and_b32 v203, v13, v129\n"
    "v_lshlrev_b32 v204, 16, v132\n"
    "v_and_b32 v205, v13, v132\n"
    "v_lshlrev_b32 v206, 16, v133\n"
    "v_and_b32 v207, v13, v133\n"
    "v_lshlrev_b32 v208, 16, v136\n"
    "v_and_b32 v209, v13, v136\n"
    "v_lshlrev_b32 v210, 16, v137\n"
    "v_and_b32 v211, v13, v137\n"
    "v_pk_add_f32 v[200:201], v[200:201], v[204:205]\n"
    "v_pk_add_f32 v[202:203], v[202:203], v[206:207]\n"
    "v_pk_add_f32 v[28:29], v[28:29], v[208:209]\n"
    "v_pk_add_f32 v[30:31], v[30:31], v[210:211]\n"
    "v_pk_add_f32 v[28:29], v[28:29], v[200:201]\n"
    "v_pk_add_f32 v[30:31], v[30:31], v[202:203]\n"
    "v_cvt_pk_bf16_f32 v28, v28, v29\n"
    "v_cvt_pk_bf16_f32 v29, v30, v31\n"
    "buffer_store_dwordx2 v[28:29], v2, s[36:39], 0 offen sc1\n"
    "v_lshlrev_b32 v212, 16, v130\n"
    "v_and_b32 v213, v13, v130\n"
    "v_lshlrev_b32 v214, 16, v131\n"
    "v_and_b32 v215, v13, v131\n"
    "v_lshlrev_b32 v216, 16, v134\n"
    "v_and_b32 v217, v13, v134\n"
    "v_lshlrev_b32 v218, 16, v135\n"
    "v_and_b32 v219, v13, v135\n"
    "v_lshlrev_b32 v220, 16, v138\n"
    "v_and_b32 v221, v13, v138\n"
    "v_lshlrev_b32 v222, 16, v139\n"
    "v_and_b32 v223, v13, v139\n"
    "v_pk_add_f32 v[212:213], v[212:213], v[216:217]\n"
    "v_pk_add_f32 v[214:215], v[214:215], v[218:219]\n"
    "v_pk_add_f32 v[44:45], v[44:45], v[220:221]\n"
    "v_pk_add_f32 v[46:47], v[46:47], v[222:223]\n"
    "v_pk_add_f32 v[44:45], v[44:45], v[212:213]\n"
    "v_pk_add_f32 v[46:47], v[46:47], v[214:215]\n"
    "v_cvt_pk_bf16_f32 v44, v44, v45\n"
    "v_cvt_pk_bf16_f32 v45, v46, v47\n"
    "buffer_store_dwordx2 v[44:45], v2, s[36:39], s47 offen sc1\n"
    "s_waitcnt vmcnt(8)\n"
    "v_lshlrev_b32 v200, 16, v140\n"
    "v_and_b32 v201, v13, v140\n"
    "v_lshlrev_b32 v202, 16, v141\n"
    "v_and_b32 v203, v13, v141\n"
    "v_lshlrev_b32 v204, 16, v144\n"
    "v_and_b32 v205, v13, v144\n"
    "v_lshlrev_b32 v206, 16, v145\n"
    "v_and_b32 v207, v13, v145\n"
    "v_lshlrev_b32 v208, 16, v148\n"
    "v_and_b32 v209, v13, v148\n"
    "v_lshlrev_b32 v210, 16, v149\n"
    "v_and_b32 v211, v13, v149\n"
    "v_pk_add_f32 v[200:201], v[200:201], v[204:205]\n"
    "v_pk_add_f32 v[202:203], v[202:203], v[206:207]\n"
    "v_pk_add_f32 v[60:61], v[60:61], v[208:209]\n"
    "v_pk_add_f32 v[62:63], v[62:63], v[210:211]\n"
    "v_pk_add_f32 v[60:61], v[60:61], v[200:201]\n"
    "v_pk_add_f32 v[62:63], v[62:63], v[202:203]\n"
    "v_cvt_pk_bf16_f32 v60, v60, v61\n"
    "v_cvt_pk_bf16_f32 v61, v62, v63\n"
    "buffer_store_dwordx2 v[60:61], v2, s[36:39], s46 offen offset:32 sc1\n"
    "v_lshlrev_b32 v212, 16, v142\n"
    "v_and_b32 v213, v13, v142\n"
    "v_lshlrev_b32 v214, 16, v143\n"
    "v_and_b32 v215, v13, v143\n"
    "v_lshlrev_b32 v216, 16, v146\n"
    "v_and_b32 v217, v13, v146\n"
    "v_lshlrev_b32 v218, 16, v147\n"
    "v_and_b32 v219, v13, v147\n"
    "v_lshlrev_b32 v220, 16, v150\n"
    "v_and_b32 v221, v13, v150\n"
    "v_lshlrev_b32 v222, 16, v151\n"
    "v_and_b32 v223, v13, v151\n"
    "v_pk_add_f32 v[212:213], v[212:213], v[216:217]\n"
    "v_pk_add_f32 v[214:215], v[214:215], v[218:219]\n"
    "v_pk_add_f32 v[76:77], v[76:77], v[220:221]\n"
    "v_pk_add_f32 v[78:79], v[78:79], v[222:223]\n"
    "v_pk_add_f32 v[76:77], v[76:77], v[212:213]\n"
    "v_pk_add_f32 v[78:79], v[78:79], v[214:215]\n"
    "v_cvt_pk_bf16_f32 v76, v76, v77\n"
    "v_cvt_pk_bf16_f32 v77, v78, v79\n"
    "buffer_store_dwordx2 v[76:77], v2, s[36:39], s45 offen offset:64 sc1\n"
    "s_waitcnt vmcnt(7)\n"
    "v_lshlrev_b32 v200, 16, v152\n"
    "v_and_b32 v201, v13, v152\n"
    "v_lshlrev_b32 v202, 16, v153\n"
    "v_and_b32 v203, v13, v153\n"
    "v_lshlrev_b32 v204, 16, v156\n"
    "v_and_b32 v205, v13, v156\n"
    "v_lshlrev_b32 v206, 16, v157\n"
    "v_and_b32 v207, v13, v157\n"
    "v_lshlrev_b32 v208, 16, v160\n"
    "v_and_b32 v209, v13, v160\n"
    "v_lshlrev_b32 v210, 16, v161\n"
    "v_and_b32 v211, v13, v161\n"
    "v_pk_add_f32 v[200:201], v[200:201], v[204:205]\n"
    "v_pk_add_f32 v[202:203], v[202:203], v[206:207]\n"
    "v_pk_add_f32 v[92:93], v[92:93], v[208:209]\n"
    "v_pk_add_f32 v[94:95], v[94:95], v[210:211]\n"
    "v_pk_add_f32 v[92:93], v[92:93], v[200:201]\n"
    "v_pk_add_f32 v[94:95], v[94:95], v[202:203]\n"
    "v_cvt_pk_bf16_f32 v92, v92, v93\n"
    "v_cvt_pk_bf16_f32 v93, v94, v95\n"
    "buffer_store_dwordx2 v[92:93], v2, s[36:39], s44 offen offset:96 sc1\n"
    "v_lshlrev_b32 v212, 16, v154\n"
    "v_and_b32 v213, v13, v154\n"
    "v_lshlrev_b32 v214, 16, v155\n"
    "v_and_b32 v215, v13, v155\n"
    "v_lshlrev_b32 v216, 16, v158\n"
    "v_and_b32 v217, v13, v158\n"
    "v_lshlrev_b32 v218, 16, v159\n"
    "v_and_b32 v219, v13, v159\n"
    "v_lshlrev_b32 v220, 16, v162\n"
    "v_and_b32 v221, v13, v162\n"
    "v_lshlrev_b32 v222, 16, v163\n"
    "v_and_b32 v223, v13, v163\n"
    "v_pk_add_f32 v[212:213], v[212:213], v[216:217]\n"
    "v_pk_add_f32 v[214:215], v[214:215], v[218:219]\n"
    "v_pk_add_f32 v[108:109], v[108:109], v[220:221]\n"
    "v_pk_add_f32 v[110:111], v[110:111], v[222:223]\n"
    "v_pk_add_f32 v[108:109], v[108:109], v[212:213]\n"
    "v_pk_add_f32 v[110:111], v[110:111], v[214:215]\n"
    "v_cvt_pk_bf16_f32 v108, v108, v109\n"
    "v_cvt_pk_bf16_f32 v109, v110, v111\n"
    "buffer_store_dwordx2 v[108:109], v2, s[36:39], 0 offen offset:128 sc1\n"
    "s_waitcnt vmcnt(6)\n"
    "v_lshlrev_b32 v200, 16, v164\n"
    "v_and_b32 v201, v13, v164\n"
    "v_lshlrev_b32 v202, 16, v165\n"
    "v_and_b32 v203, v13, v165\n"
    "v_lshlrev_b32 v204, 16, v168\n"
    "v_and_b32 v205, v13, v168\n"
    "v_lshlrev_b32 v206, 16, v169\n"
    "v_and_b32 v207, v13, v169\n"
    "v_lshlrev_b32 v208, 16, v172\n"
    "v_and_b32 v209, v13, v172\n"
    "v_lshlrev_b32 v210, 16, v173\n"
    "v_and_b32 v211, v13, v173\n"
    "v_pk_add_f32 v[200:201], v[200:201], v[204:205]\n"
    "v_pk_add_f32 v[202:203], v[202:203], v[206:207]\n"
    "v_pk_add_f32 v[124:125], v[124:125], v[208:209]\n"
    "v_pk_add_f32 v[126:127], v[126:127], v[210:211]\n"
    "v_pk_add_f32 v[124:125], v[124:125], v[200:201]\n"
    "v_pk_add_f32 v[126:127], v[126:127], v[202:203]\n"
    "v_cvt_pk_bf16_f32 v124, v124, v125\n"
    "v_cvt_pk_bf16_f32 v125, v126, v127\n"
    "buffer_store_dwordx2 v[124:125], v2, s[36:39], s47 offen offset:128 sc1\n"
    "s_branch LVES_%=\n"
    "LVS1_%=:\n"
    "buffer_load_dwordx4 v[128:131], v1, s[64:67], s48 offen sc1\n"
    "buffer_load_dwordx4 v[132:135], v1, s[64:67], s50 offen sc1\n"
    "buffer_load_dwordx4 v[136:139], v1, s[64:67], s51 offen sc1\n"
    "buffer_load_dwordx4 v[140:143], v1, s[64:67], s48 offen offset:1024 sc1\n"
    "buffer_load_dwordx4 v[144:147], v1, s[64:67], s50 offen offset:1024 sc1\n"
    "buffer_load_dwordx4 v[148:151], v1, s[64:67], s51 offen offset:1024 sc1\n"
    "buffer_load_dwordx4 v[152:155], v1, s[64:67], s48 offen offset:2048 sc1\n"
    "buffer_load_dwordx4 v[156:159], v1, s[64:67], s50 offen offset:2048 sc1\n"
    "buffer_load_dwordx4 v[160:163], v1, s[64:67], s51 offen offset:2048 sc1\n"
    "s_waitcnt vmcnt(6)\n"
    "v_lshlrev_b32 v200, 16, v128\n"
    "v_and_b32 v201, v13, v128\n"
    "v_lshlrev_b32 v202, 16, v129\n"
    "v_and_b32 v203, v13, v129\n"
    "v_lshlrev_b32 v204, 16, v132\n"
    "v_and_b32 v205, v13, v132\n"
    "v_lshlrev_b32 v206, 16, v133\n"
    "v_and_b32 v207, v13, v133\n"
    "v_lshlrev_b32 v208, 16, v136\n"
    "v_and_b32 v209, v13, v136\n"
    "v_lshlrev_b32 v210, 16, v137\n"
    "v_and_b32 v211, v13, v137\n"
    "v_pk_add_f32 v[200:201], v[200:201], v[204:205]\n"
    "v_pk_add_f32 v[202:203], v[202:203], v[206:207]\n"
    "v_pk_add_f32 v[32:33], v[32:33], v[208:209]\n"
    "v_pk_add_f32 v[34:35], v[34:35], v[210:211]\n"
    "v_pk_add_f32 v[32:33], v[32:33], v[200:201]\n"
    "v_pk_add_f32 v[34:35], v[34:35], v[202:203]\n"
    "v_cvt_pk_bf16_f32 v32, v32, v33\n"
    "v_cvt_pk_bf16_f32 v33, v34, v35\n"
    "buffer_store_dwordx2 v[32:33], v2, s[36:39], s44 offen sc1\n"
    "v_lshlrev_b32 v212, 16, v130\n"
    "v_and_b32 v213, v13, v130\n"
    "v_lshlrev_b32 v214, 16, v131\n"
    "v_and_b32 v215, v13, v131\n"
    "v_lshlrev_b32 v216, 16, v134\n"
    "v_and_b32 v217, v13, v134\n"
    "v_lshlrev_b32 v218, 16, v135\n"
    "v_and_b32 v219, v13, v135\n"
    "v_lshlrev_b32 v220, 16, v138\n"
    "v_and_b32 v221, v13, v138\n"
    "v_lshlrev_b32 v222, 16, v139\n"
    "v_and_b32 v223, v13, v139\n"
    "v_pk_add_f32 v[212:213], v[212:213], v[216:217]\n"
    "v_pk_add_f32 v[214:215], v[214:215], v[218:219]\n"
    "v_pk_add_f32 v[48:49], v[48:49], v[220:221]\n"
    "v_pk_add_f32 v[50:51], v[50:51], v[222:223]\n"
    "v_pk_add_f32 v[48:49], v[48:49], v[212:213]\n"
    "v_pk_add_f32 v[50:51], v[50:51], v[214:215]\n"
    "v_cvt_pk_bf16_f32 v48, v48, v49\n"
    "v_cvt_pk_bf16_f32 v49, v50, v51\n"
    "buffer_store_dwordx2 v[48:49], v2, s[36:39], 0 offen offset:32 sc1\n"
    "s_waitcnt vmcnt(5)\n"
    "v_lshlrev_b32 v200, 16, v140\n"
    "v_and_b32 v201, v13, v140\n"
    "v_lshlrev_b32 v202, 16, v141\n"
    "v_and_b32 v203, v13, v141\n"
    "v_lshlrev_b32 v204, 16, v144\n"
    "v_and_b32 v205, v13, v144\n"
    "v_lshlrev_b32 v206, 16, v145\n"
    "v_and_b32 v207, v13, v145\n"
    "v_lshlrev_b32 v208, 16, v148\n"
    "v_and_b32 v209, v13, v148\n"
    "v_lshlrev_b32 v210, 16, v149\n"
    "v_and_b32 v211, v13, v149\n"
    "v_pk_add_f32 v[200:201], v[200:201], v[204:205]\n"
    "v_pk_add_f32 v[202:203], v[202:203], v[206:207]\n"
    "v_pk_add_f32 v[64:65], v[64:65], v[208:209]\n"
    "v_pk_add_f32 v[66:67], v[66:67], v[210:211]\n"
    "v_pk_add_f32 v[64:65], v[64:65], v[200:201]\n"
    "v_pk_add_f32 v[66:67], v[66:67], v[202:203]\n"
    "v_cvt_pk_bf16_f32 v64, v64, v65\n"
    "v_cvt_pk_bf16_f32 v65, v66, v67\n"
    "buffer_store_dwordx2 v[64:65], v2, s[36:39], s47 offen offset:32 sc1\n"
    "v_lshlrev_b32 v212, 16, v142\n"
    "v_and_b32 v213, v13, v142\n"
    "v_lshlrev_b32 v214, 16, v143\n"
    "v_and_b32 v215, v13, v143\n"
    "v_lshlrev_b32 v216, 16, v146\n"
    "v_and_b32 v217, v13, v146\n"
    "v_lshlrev_b32 v218, 16, v147\n"
    "v_and_b32 v219, v13, v147\n"
    "v_lshlrev_b32 v220, 16, v150\n"
    "v_and_b32 v221, v13, v150\n"
    "v_lshlrev_b32 v222, 16, v151\n"
    "v_and_b32 v223, v13, v151\n"
    "v_pk_add_f32 v[212:213], v[212:213], v[216:217]\n"
    "v_pk_add_f32 v[214:215], v[214:215], v[218:219]\n"
    "v_pk_add_f32 v[80:81], v[80:81], v[220:221]\n"
    "v_pk_add_f32 v[82:83], v[82:83], v[222:223]\n"
    "v_pk_add_f32 v[80:81], v[80:81], v[212:213]\n"
    "v_pk_add_f32 v[82:83], v[82:83], v[214:215]\n"
    "v_cvt_pk_bf16_f32 v80, v80, v81\n"
    "v_cvt_pk_bf16_f32 v81, v82, v83\n"
    "buffer_store_dwordx2 v[80:81], v2, s[36:39], s46 offen offset:64 sc1\n"
    "s_waitcnt vmcnt(4)\n"
    "v_lshlrev_b32 v200, 16, v152\n"
    "v_and_b32 v201, v13, v152\n"
    "v_lshlrev_b32 v202, 16, v153\n"
    "v_and_b32 v203, v13, v153\n"
    "v_lshlrev_b32 v204, 16, v156\n"
    "v_and_b32 v205, v13, v156\n"
    "v_lshlrev_b32 v206, 16, v157\n"
    "v_and_b32 v207, v13, v157\n"
    "v_lshlrev_b32 v208, 16, v160\n"
    "v_and_b32 v209, v13, v160\n"
    "v_lshlrev_b32 v210, 16, v161\n"
    "v_and_b32 v211, v13, v161\n"
    "v_pk_add_f32 v[200:201], v[200:201], v[204:205]\n"
    "v_pk_add_f32 v[202:203], v[202:203], v[206:207]\n"
    "v_pk_add_f32 v[96:97], v[96:97], v[208:209]\n"
    "v_pk_add_f32 v[98:99], v[98:99], v[210:211]\n"
    "v_pk_add_f32 v[96:97], v[96:97], v[200:201]\n"
    "v_pk_add_f32 v[98:99], v[98:99], v[202:203]\n"
    "v_cvt_pk_bf16_f32 v96, v96, v97\n"
    "v_cvt_pk_bf16_f32 v97, v98, v99\n"
    "buffer_store_dwordx2 v[96:97], v2, s[36:39], s45 offen offset:96 sc1\n"
    "v_lshlrev_b32 v212, 16, v154\n"
    "v_and_b32 v213, v13, v154\n"
    "v_lshlrev_b32 v214, 16, v155\n"
    "v_and_b32 v215, v13, v155\n"
    "v_lshlrev_b32 v216, 16, v158\n"
    "v_and_b32 v217, v13, v158\n"
    "v_lshlrev_b32 v218, 16, v159\n"
    "v_and_b32 v219, v13, v159\n"
    "v_lshlrev_b32 v220, 16, v162\n"
    "v_and_b32 v221, v13, v162\n"
    "v_lshlrev_b32 v222, 16, v163\n"
    "v_and_b32 v223, v13, v163\n"
    "v_pk_add_f32 v[212:213], v[212:213], v[216:217]\n"
    "v_pk_add_f32 v[214:215], v[214:215], v[218:219]\n"
    "v_pk_add_f32 v[112:113], v[112:113], v[220:221]\n"
    "v_pk_add_f32 v[114:115], v[114:115], v[222:223]\n"
    "v_pk_add_f32 v[112:113], v[112:113], v[212:213]\n"
    "v_pk_add_f32 v[114:115], v[114:115], v[214:215]\n"
    "v_cvt_pk_bf16_f32 v112, v112, v113\n"
    "v_cvt_pk_bf16_f32 v113, v114, v115\n"
    "buffer_store_dwordx2 v[112:113], v2, s[36:39], s44 offen offset:128 sc1\n"
    "s_branch LVES_%=\n"
    "LVS2_%=:\n"
    "buffer_load_dwordx4 v[128:131], v1, s[64:67], s48 offen sc1\n"
    "buffer_load_dwordx4 v[132:135], v1, s[64:67], s49 offen sc1\n"
    "buffer_load_dwordx4 v[136:139], v1, s[64:67], s51 offen sc1\n"
    "buffer_load_dwordx4 v[140:143], v1, s[64:67], s48 offen offset:1024 sc1\n"
    "buffer_load_dwordx4 v[144:147], v1, s[64:67], s49 offen offset:1024 sc1\n"
    "buffer_load_dwordx4 v[148:151], v1, s[64:67], s51 offen offset:1024 sc1\n"
    "buffer_load_dwordx4 v[152:155], v1, s[64:67], s48 offen offset:2048 sc1\n"
    "buffer_load_dwordx4 v[156:159], v1, s[64:67], s49 offen offset:2048 sc1\n"
    "buffer_load_dwordx4 v[160:163], v1, s[64:67], s51 offen offset:2048 sc1\n"
    "s_waitcnt vmcnt(6)\n"
    "v_lshlrev_b32 v200, 16, v128\n"
    "v_and_b32 v201, v13, v128\n"
    "v_lshlrev_b32 v202, 16, v129\n"
    "v_and_b32 v203, v13, v129\n"
    "v_lshlrev_b32 v204, 16, v132\n"
    "v_and_b32 v205, v13, v132\n"
    "v_lshlrev_b32 v206, 16, v133\n"
    "v_and_b32 v207, v13, v133\n"
    "v_lshlrev_b32 v208, 16, v136\n"
    "v_and_b32 v209, v13, v136\n"
    "v_lshlrev_b32 v210, 16, v137\n"
    "v_and_b32 v211, v13, v137\n"
    "v_pk_add_f32 v[200:201], v[200:201], v[204:205]\n"
    "v_pk_add_f32 v[202:203], v[202:203], v[206:207]\n"
    "v_pk_add_f32 v[36:37], v[36:37], v[208:209]\n"
    "v_pk_add_f32 v[38:39], v[38:39], v[210:211]\n"
    "v_pk_add_f32 v[36:37], v[36:37], v[200:201]\n"
    "v_pk_add_f32 v[38:39], v[38:39], v[202:203]\n"
    "v_cvt_pk_bf16_f32 v36, v36, v37\n"
    "v_cvt_pk_bf16_f32 v37, v38, v39\n"
    "buffer_store_dwordx2 v[36:37], v2, s[36:39], s45 offen sc1\n"
    "v_lshlrev_b32 v212, 16, v130\n"
    "v_and_b32 v213, v13, v130\n"
    "v_lshlrev_b32 v214, 16, v131\n"
    "v_and_b32 v215, v13, v131\n"
    "v_lshlrev_b32 v216, 16, v134\n"
    "v_and_b32 v217, v13, v134\n"
    "v_lshlrev_b32 v218, 16, v135\n"
    "v_and_b32 v219, v13, v135\n"
    "v_lshlrev_b32 v220, 16, v138\n"
    "v_and_b32 v221, v13, v138\n"
    "v_lshlrev_b32 v222, 16, v139\n"
    "v_and_b32 v223, v13, v139\n"
    "v_pk_add_f32 v[212:213], v[212:213], v[216:217]\n"
    "v_pk_add_f32 v[214:215], v[214:215], v[218:219]\n"
    "v_pk_add_f32 v[52:53], v[52:53], v[220:221]\n"
    "v_pk_add_f32 v[54:55], v[54:55], v[222:223]\n"
    "v_pk_add_f32 v[52:53], v[52:53], v[212:213]\n"
    "v_pk_add_f32 v[54:55], v[54:55], v[214:215]\n"
    "v_cvt_pk_bf16_f32 v52, v52, v53\n"
    "v_cvt_pk_bf16_f32 v53, v54, v55\n"
    "buffer_store_dwordx2 v[52:53], v2, s[36:39], s44 offen offset:32 sc1\n"
    "s_waitcnt vmcnt(5)\n"
    "v_lshlrev_b32 v200, 16, v140\n"
    "v_and_b32 v201, v13, v140\n"
    "v_lshlrev_b32 v202, 16, v141\n"
    "v_and_b32 v203, v13, v141\n"
    "v_lshlrev_b32 v204, 16, v144\n"
    "v_and_b32 v205, v13, v144\n"
    "v_lshlrev_b32 v206, 16, v145\n"
    "v_and_b32 v207, v13, v145\n"
    "v_lshlrev_b32 v208, 16, v148\n"
    "v_and_b32 v209, v13, v148\n"
    "v_lshlrev_b32 v210, 16, v149\n"
    "v_and_b32 v211, v13, v149\n"
    "v_pk_add_f32 v[200:201], v[200:201], v[204:205]\n"
    "v_pk_add_f32 v[202:203], v[202:203], v[206:207]\n"
    "v_pk_add_f32 v[68:69], v[68:69], v[208:209]\n"
    "v_pk_add_f32 v[70:71], v[70:71], v[210:211]\n"
    "v_pk_add_f32 v[68:69], v[68:69], v[200:201]\n"
    "v_pk_add_f32 v[70:71], v[70:71], v[202:203]\n"
    "v_cvt_pk_bf16_f32 v68, v68, v69\n"
    "v_cvt_pk_bf16_f32 v69, v70, v71\n"
    "buffer_store_dwordx2 v[68:69], v2, s[36:39], 0 offen offset:64 sc1\n"
    "v_lshlrev_b32 v212, 16, v142\n"
    "v_and_b32 v213, v13, v142\n"
    "v_lshlrev_b32 v214, 16, v143\n"
    "v_and_b32 v215, v13, v143\n"
    "v_lshlrev_b32 v216, 16, v146\n"
    "v_and_b32 v217, v13, v146\n"
    "v_lshlrev_b32 v218, 16, v147\n"
    "v_and_b32 v219, v13, v147\n"
    "v_lshlrev_b32 v220, 16, v150\n"
    "v_and_b32 v221, v13, v150\n"
    "v_lshlrev_b32 v222, 16, v151\n"
    "v_and_b32 v223, v13, v151\n"
    "v_pk_add_f32 v[212:213], v[212:213], v[216:217]\n"
    "v_pk_add_f32 v[214:215], v[214:215], v[218:219]\n"
    "v_pk_add_f32 v[84:85], v[84:85], v[220:221]\n"
    "v_pk_add_f32 v[86:87], v[86:87], v[222:223]\n"
    "v_pk_add_f32 v[84:85], v[84:85], v[212:213]\n"
    "v_pk_add_f32 v[86:87], v[86:87], v[214:215]\n"
    "v_cvt_pk_bf16_f32 v84, v84, v85\n"
    "v_cvt_pk_bf16_f32 v85, v86, v87\n"
    "buffer_store_dwordx2 v[84:85], v2, s[36:39], s47 offen offset:64 sc1\n"
    "s_waitcnt vmcnt(4)\n"
    "v_lshlrev_b32 v200, 16, v152\n"
    "v_and_b32 v201, v13, v152\n"
    "v_lshlrev_b32 v202, 16, v153\n"
    "v_and_b32 v203, v13, v153\n"
    "v_lshlrev_b32 v204, 16, v156\n"
    "v_and_b32 v205, v13, v156\n"
    "v_lshlrev_b32 v206, 16, v157\n"
    "v_and_b32 v207, v13, v157\n"
    "v_lshlrev_b32 v208, 16, v160\n"
    "v_and_b32 v209, v13, v160\n"
    "v_lshlrev_b32 v210, 16, v161\n"
    "v_and_b32 v211, v13, v161\n"
    "v_pk_add_f32 v[200:201], v[200:201], v[204:205]\n"
    "v_pk_add_f32 v[202:203], v[202:203], v[206:207]\n"
    "v_pk_add_f32 v[100:101], v[100:101], v[208:209]\n"
    "v_pk_add_f32 v[102:103], v[102:103], v[210:211]\n"
    "v_pk_add_f32 v[100:101], v[100:101], v[200:201]\n"
    "v_pk_add_f32 v[102:103], v[102:103], v[202:203]\n"
    "v_cvt_pk_bf16_f32 v100, v100, v101\n"
    "v_cvt_pk_bf16_f32 v101, v102, v103\n"
    "buffer_store_dwordx2 v[100:101], v2, s[36:39], s46 offen offset:96 sc1\n"
    "v_lshlrev_b32 v212, 16, v154\n"
    "v_and_b32 v213, v13, v154\n"
    "v_lshlrev_b32 v214, 16, v155\n"
    "v_and_b32 v215, v13, v155\n"
    "v_lshlrev_b32 v216, 16, v158\n"
    "v_and_b32 v217, v13, v158\n"
    "v_lshlrev_b32 v218, 16, v159\n"
    "v_and_b32 v219, v13, v159\n"
    "v_lshlrev_b32 v220, 16, v162\n"
    "v_and_b32 v221, v13, v162\n"
    "v_lshlrev_b32 v222, 16, v163\n"
    "v_and_b32 v223, v13, v163\n"
    "v_pk_add_f32 v[212:213], v[212:213], v[216:217]\n"
    "v_pk_add_f32 v[214:215], v[214:215], v[218:219]\n"
    "v_pk_add_f32 v[116:117], v[116:117], v[220:221]\n"
    "v_pk_add_f32 v[118:119], v[118:119], v[222:223]\n"
    "v_pk_add_f32 v[116:117], v[116:117], v[212:213]\n"
    "v_pk_add_f32 v[118:119], v[118:119], v[214:215]\n"
    "v_cvt_pk_bf16_f32 v116, v116, v117\n"
    "v_cvt_pk_bf16_f32 v117, v118, v119\n"
    "buffer_store_dwordx2 v[116:117], v2, s[36:39], s45 offen offset:128 sc1\n"
    "s_branch LVES_%=\n"
    "LVS3_%=:\n"
    "buffer_load_dwordx4 v[128:131], v1, s[64:67], s48 offen sc1\n"
    "buffer_load_dwordx4 v[132:135], v1, s[64:67], s49 offen sc1\n"
    "buffer_load_dwordx4 v[136:139], v1, s[64:67], s50 offen sc1\n"
    "buffer_load_dwordx4 v[140:143], v1, s[64:67], s48 offen offset:1024 sc1\n"
    "buffer_load_dwordx4 v[144:147], v1, s[64:67], s49 offen offset:1024 sc1\n"
    "buffer_load_dwordx4 v[148:151], v1, s[64:67], s50 offen offset:1024 sc1\n"
    "buffer_load_dwordx4 v[152:155], v1, s[64:67], s48 offen offset:2048 sc1\n"
    "buffer_load_dwordx4 v[156:159], v1, s[64:67], s49 offen offset:2048 sc1\n"
    "buffer_load_dwordx4 v[160:163], v1, s[64:67], s50 offen offset:2048 sc1\n"
    "s_waitcnt vmcnt(6)\n"
    "v_lshlrev_b32 v200, 16, v128\n"
    "v_and_b32 v201, v13, v128\n"
    "v_lshlrev_b32 v202, 16, v129\n"
    "v_and_b32 v203, v13, v129\n"
    "v_lshlrev_b32 v204, 16, v132\n"
    "v_and_b32 v205, v13, v132\n"
    "v_lshlrev_b32 v206, 16, v133\n"
    "v_and_b32 v207, v13, v133\n"
    "v_lshlrev_b32 v208, 16, v136\n"
    "v_and_b32 v209, v13, v136\n"
    "v_lshlrev_b32 v210, 16, v137\n"
    "v_and_b32 v211, v13, v137\n"
    "v_pk_add_f32 v[200:201], v[200:201], v[204:205]\n"
    "v_pk_add_f32 v[202:203], v[202:203], v[206:207]\n"
    "v_pk_add_f32 v[40:41], v[40:41], v[208:209]\n"
    "v_pk_add_f32 v[42:43], v[42:43], v[210:211]\n"
    "v_pk_add_f32 v[40:41], v[40:41], v[200:201]\n"
    "v_pk_add_f32 v[42:43], v[42:43], v[202:203]\n"
    "v_cvt_pk_bf16_f32 v40, v40, v41\n"
    "v_cvt_pk_bf16_f32 v41, v42, v43\n"
    "buffer_store_dwordx2 v[40:41], v2, s[36:39], s46 offen sc1\n"
    "v_lshlrev_b32 v212, 16, v130\n"
    "v_and_b32 v213, v13, v130\n"
    "v_lshlrev_b32 v214, 16, v131\n"
    "v_and_b32 v215, v13, v131\n"
    "v_lshlrev_b32 v216, 16, v134\n"
    "v_and_b32 v217, v13, v134\n"
    "v_lshlrev_b32 v218, 16, v135\n"
    "v_and_b32 v219, v13, v135\n"
    "v_lshlrev_b32 v220, 16, v138\n"
    "v_and_b32 v221, v13, v138\n"
    "v_lshlrev_b32 v222, 16, v139\n"
    "v_and_b32 v223, v13, v139\n"
    "v_pk_add_f32 v[212:213], v[212:213], v[216:217]\n"
    "v_pk_add_f32 v[214:215], v[214:215], v[218:219]\n"
    "v_pk_add_f32 v[56:57], v[56:57], v[220:221]\n"
    "v_pk_add_f32 v[58:59], v[58:59], v[222:223]\n"
    "v_pk_add_f32 v[56:57], v[56:57], v[212:213]\n"
    "v_pk_add_f32 v[58:59], v[58:59], v[214:215]\n"
    "v_cvt_pk_bf16_f32 v56, v56, v57\n"
    "v_cvt_pk_bf16_f32 v57, v58, v59\n"
    "buffer_store_dwordx2 v[56:57], v2, s[36:39], s45 offen offset:32 sc1\n"
    "s_waitcnt vmcnt(5)\n"
    "v_lshlrev_b32 v200, 16, v140\n"
    "v_and_b32 v201, v13, v140\n"
    "v_lshlrev_b32 v202, 16, v141\n"
    "v_and_b32 v203, v13, v141\n"
    "v_lshlrev_b32 v204, 16, v144\n"
    "v_and_b32 v205, v13, v144\n"
    "v_lshlrev_b32 v206, 16, v145\n"
    "v_and_b32 v207, v13, v145\n"
    "v_lshlrev_b32 v208, 16, v148\n"
    "v_and_b32 v209, v13, v148\n"
    "v_lshlrev_b32 v210, 16, v149\n"
    "v_and_b32 v211, v13, v149\n"
    "v_pk_add_f32 v[200:201], v[200:201], v[204:205]\n"
    "v_pk_add_f32 v[202:203], v[202:203], v[206:207]\n"
    "v_pk_add_f32 v[72:73], v[72:73], v[208:209]\n"
    "v_pk_add_f32 v[74:75], v[74:75], v[210:211]\n"
    "v_pk_add_f32 v[72:73], v[72:73], v[200:201]\n"
    "v_pk_add_f32 v[74:75], v[74:75], v[202:203]\n"
    "v_cvt_pk_bf16_f32 v72, v72, v73\n"
    "v_cvt_pk_bf16_f32 v73, v74, v75\n"
    "buffer_store_dwordx2 v[72:73], v2, s[36:39], s44 offen offset:64 sc1\n"
    "v_lshlrev_b32 v212, 16, v142\n"
    "v_and_b32 v213, v13, v142\n"
    "v_lshlrev_b32 v214, 16, v143\n"
    "v_and_b32 v215, v13, v143\n"
    "v_lshlrev_b32 v216, 16, v146\n"
    "v_and_b32 v217, v13, v146\n"
    "v_lshlrev_b32 v218, 16, v147\n"
    "v_and_b32 v219, v13, v147\n"
    "v_lshlrev_b32 v220, 16, v150\n"
    "v_and_b32 v221, v13, v150\n"
    "v_lshlrev_b32 v222, 16, v151\n"
    "v_and_b32 v223, v13, v151\n"
    "v_pk_add_f32 v[212:213], v[212:213], v[216:217]\n"
    "v_pk_add_f32 v[214:215], v[214:215], v[218:219]\n"
    "v_pk_add_f32 v[88:89], v[88:89], v[220:221]\n"
    "v_pk_add_f32 v[90:91], v[90:91], v[222:223]\n"
    "v_pk_add_f32 v[88:89], v[88:89], v[212:213]\n"
    "v_pk_add_f32 v[90:91], v[90:91], v[214:215]\n"
    "v_cvt_pk_bf16_f32 v88, v88, v89\n"
    "v_cvt_pk_bf16_f32 v89, v90, v91\n"
    "buffer_store_dwordx2 v[88:89], v2, s[36:39], 0 offen offset:96 sc1\n"
    "s_waitcnt vmcnt(4)\n"
    "v_lshlrev_b32 v200, 16, v152\n"
    "v_and_b32 v201, v13, v152\n"
    "v_lshlrev_b32 v202, 16, v153\n"
    "v_and_b32 v203, v13, v153\n"
    "v_lshlrev_b32 v204, 16, v156\n"
    "v_and_b32 v205, v13, v156\n"
    "v_lshlrev_b32 v206, 16, v157\n"
    "v_and_b32 v207, v13, v157\n"
    "v_lshlrev_b32 v208, 16, v160\n"
    "v_and_b32 v209, v13, v160\n"
    "v_lshlrev_b32 v210, 16, v161\n"
    "v_and_b32 v211, v13, v161\n"
    "v_pk_add_f32 v[200:201], v[200:201], v[204:205]\n"
    "v_pk_add_f32 v[202:203], v[202:203], v[206:207]\n"
    "v_pk_add_f32 v[104:105], v[104:105], v[208:209]\n"
    "v_pk_add_f32 v[106:107], v[106:107], v[210:211]\n"
    "v_pk_add_f32 v[104:105], v[104:105], v[200:201]\n"
    "v_pk_add_f32 v[106:107], v[106:107], v[202:203]\n"
    "v_cvt_pk_bf16_f32 v104, v104, v105\n"
    "v_cvt_pk_bf16_f32 v105, v106, v107\n"
    "buffer_store_dwordx2 v[104:105], v2, s[36:39], s47 offen offset:96 sc1\n"
    "v_lshlrev_b32 v212, 16, v154\n"
    "v_and_b32 v213, v13, v154\n"
    "v_lshlrev_b32 v214, 16, v155\n"
    "v_and_b32 v215, v13, v155\n"
    "v_lshlrev_b32 v216, 16, v158\n"
    "v_and_b32 v217, v13, v158\n"
    "v_lshlrev_b32 v218, 16, v159\n"
    "v_and_b32 v219, v13, v159\n"
    "v_lshlrev_b32 v220, 16, v162\n"
    "v_and_b32 v221, v13, v162\n"
    "v_lshlrev_b32 v222, 16, v163\n"
    "v_and_b32 v223, v13, v163\n"
    "v_pk_add_f32 v[212:213], v[212:213], v[216:217]\n"
    "v_pk_add_f32 v[214:215], v[214:215], v[218:219]\n"
    "v_pk_add_f32 v[120:121], v[120:121], v[220:221]\n"
    "v_pk_add_f32 v[122:123], v[122:123], v[222:223]\n"
    "v_pk_add_f32 v[120:121], v[120:121], v[212:213]\n"
    "v_pk_add_f32 v[122:123], v[122:123], v[214:215]\n"
    "v_cvt_pk_bf16_f32 v120, v120, v121\n"
    "v_cvt_pk_bf16_f32 v121, v122, v123\n"
    "buffer_store_dwordx2 v[120:121], v2, s[36:39], s46 offen offset:128 sc1\n"
    "LVES_%=:\n"
    "s_cmp_lg_u32 s72, 0\n"
    "s_cbranch_scc1 LNPSE_%=\n"
    "s_lshl_b64 exec, 1, s90\n"
    "buffer_store_dwordx2 v[16:17], v15, s[68:71], s96 offen sc1\n"
    "s_mov_b64 exec, -1\n"
    "LNPSE_%=:\n"
    : [t0] "+v"(t0), [t1] "+v"(t1)
    : [ws] "s"(ws), [out] "s"(out), [wo] "s"(wo), [wid] "s"(wid), [blk] "s"(blockIdx.x)
    : "v0", "v1", "v2", "v3", "v4", "v5", "v6", "v7", "v8", "v9", "v10", "v11", "v12", "v13", "v14", "v15", "v16", "v17", "v18", "v19", "v20", "v21", "v22", "v23", "v24", "v25", "v26", "v27", "v28", "v29", "v30", "v31", "v32", "v33", "v34", "v35", "v36", "v37", "v38", "v39", "v40", "v41", "v42", "v43", "v44", "v45", "v46", "v47", "v48", "v49", "v50", "v51", "v52", "v53", "v54", "v55", "v56", "v57", "v58", "v59", "v60", "v61", "v62", "v63", "v64", "v65", "v66", "v67", "v68", "v69", "v70", "v71", "v72", "v73", "v74", "v75", "v76", "v77", "v78", "v79", "v80", "v81", "v82", "v83", "v84", "v85", "v86", "v87", "v88", "v89", "v90", "v91", "v92", "v93", "v94", "v95", "v96", "v97", "v98", "v99", "v100", "v101", "v102", "v103", "v104", "v105", "v106", "v107", "v108", "v109", "v110", "v111", "v112", "v113", "v114", "v115", "v116", "v117", "v118", "v119", "v120", "v121", "v122", "v123", "v124", "v125", "v126", "v127", "v128", "v129", "v130", "v131", "v132", "v133", "v134", "v135", "v136", "v137", "v138", "v139", "v140", "v141", "v142", "v143", "v144", "v145", "v146", "v147", "v148", "v149", "v150", "v151", "v152", "v153", "v154", "v155", "v156", "v157", "v158", "v159", "v160", "v161", "v162", "v163", "v164", "v165", "v166", "v167", "v168", "v169", "v170", "v171", "v172", "v173", "v174", "v175", "v176", "v177", "v178", "v179", "v180", "v181", "v182", "v183", "v184", "v185", "v186", "v187", "v188", "v189", "v190", "v191", "v192", "v193", "v194", "v195", "v196", "v197", "v198", "v199", "v200", "v201", "v202", "v203", "v204", "v205", "v206", "v207", "v208", "v209", "v210", "v211", "v212", "v213", "v214", "v215", "v216", "v217", "v218", "v219", "v220", "v221", "v222", "v223", "v224", "v225", "v226", "v227", "v228", "v229", "v230", "v231", "v232", "v233", "v234", "v235", "v236", "v237", "v238", "v239", "v240", "v241", "v242", "v243", "v244", "v245", "v246", "v247", "v248", "v249", "v250", "v251", "v252", "v253", "a0", "a1", "a2", "a3", "a4", "a5", "a6", "a7", "a8", "a9", "a10", "a11", "a12", "a13", "a14", "a15", "a16", "a17", "a18", "a19", "a20", "a21", "a22", "a23", "a24", "a25", "a26", "a27", "a28", "a29", "a30", "a31", "a32", "a33", "a34", "a35", "a36", "a37", "a38", "a39", "a40", "a41", "a42", "a43", "a44", "a45", "a46", "a47", "a48", "a49", "a50", "a51", "a52", "a53", "a54", "a55", "a56", "a57", "a58", "a59", "a60", "a61", "a62", "a63", "a64", "a65", "a66", "a67", "a68", "a69", "a70", "a71", "a72", "a73", "a74", "a75", "a76", "a77", "a78", "a79", "a80", "a81", "a82", "a83", "a84", "a85", "a86", "a87", "a88", "a89", "a90", "a91", "a92", "a93", "a94", "a95", "a96", "a97", "a98", "a99", "a100", "a101", "a102", "a103", "a104", "a105", "a106", "a107", "a108", "a109", "a110", "a111", "a112", "a113", "a114", "a115", "a116", "a117", "a118", "a119", "a120", "a121", "a122", "a123", "a124", "a125", "a126", "a127", "a128", "a129", "a130", "a131", "a132", "a133", "a134", "a135", "a136", "a137", "a138", "a139", "a140", "a141", "a142", "a143", "a144", "a145", "a146", "a147", "a148", "a149", "a150", "a151", "a152", "a153", "a154", "a155", "a156", "a157", "a158", "a159", "a160", "a161", "a162", "a163", "a164", "a165", "a166", "a167", "a168", "a169", "a170", "a171", "a172", "a173", "a174", "a175", "a176", "a177", "a178", "a179", "a180", "a181", "a182", "a183", "a184", "a185", "a186", "a187", "a188", "a189", "a190", "a191", "a192", "a193", "a194", "a195", "a196", "a197", "a198", "a199", "a200", "a201", "a202", "a203", "a204", "a205", "a206", "a207", "a208", "a209", "a210", "a211", "a212", "a213", "a214", "a215", "a216", "a217", "a218", "a219", "a220", "a221", "a222", "a223", "a224", "a225", "a226", "a227", "a228", "a229", "a230", "a231", "a232", "a233", "a234", "a235", "a236", "a237", "a238", "a239", "a240", "a241", "a242", "a243", "a244", "a245", "a246", "a247", "a248", "a249", "a250", "a251", "a252", "a253", "a254", "a255", "s36", "s37", "s38", "s39", "s40", "s41", "s42", "s43", "s44", "s45", "s46", "s47", "s48", "s49", "s50", "s51", "s52", "s53", "s54", "s55", "s56", "s57", "s58", "s59", "s60", "s61", "s62", "s63", "s64", "s65", "s66", "s67", "s68", "s69", "s70", "s71", "s72", "s73", "s74", "s75", "s76", "s77", "s78", "s79", "s80", "s81", "s82", "s83", "s84", "s85", "s86", "s87", "s88", "s89", "s90", "s91", "s92", "s93", "s94", "s95", "s96", "s97", "s98", "s99", "vcc", "scc", "memory");
  }
}

extern "C" void kernel_launch(void* const* d_in, const int* in_sizes, int n_in, void* d_out, int out_size, void* d_ws, size_t ws_size, hipStream_t stream) {
  mha_fused<<<256, 256, 0, stream>>>((const u16*)d_in[0], (const u16*)d_in[1], (const u16*)d_in[2], (const u16*)d_in[3], (const u16*)d_in[4], (char*)d_ws, (u16*)d_out);
}
```
